```python
import torch

def _softmax(x, axis=-1):
    return torch.softmax(x, dim=axis)
def _gelu(x):
    return torch.nn.functional.gelu(x, approximate="tanh")
def _silu(x):
    return torch.nn.functional.silu(x)
def _sigmoid(x):
    return torch.sigmoid(x)
def _layernorm(x, axis=-1, eps=1e-5):
    m = x.mean(dim=axis, keepdim=True)
    v = x.var(dim=axis, keepdim=True, unbiased=False)
    return (x - m) / torch.sqrt(v + eps)
def _scatter_add(tgt, idx, src, axis=0):
    return tgt.clone().index_add_(axis, idx, src)
def _segment_sum(src, seg, n):
    out = torch.zeros((n,) + src.shape[1:], dtype=src.dtype)
    return out.index_add_(0, seg, src)
def _pad(x, pw):
    flat = tuple(v for pair in reversed(pw) for v in pair)
    return torch.nn.functional.pad(x, flat)

kernel_name = 'parallel_sum__ssm_conv_s4d__mixer_channel_mlp__prompt'

def setup_inputs():

    _w = (lambda a: (lambda t: t.to(torch.bfloat16) if t.is_floating_point() else t)(torch.as_tensor(a)))
    def _A_setup():
        Bsz, L, D, N = 1, 2048, 512, 128
        return {
            "u": _w((lambda *s: torch.randn(*s, dtype=torch.bfloat16) * 0.05)(Bsz, L, D)),
            "log_A_real": _w((lambda *s: torch.randn(*s, dtype=torch.bfloat16) * 3.2)(D, N) * 0.5),
            "A_imag": _w((lambda *s: torch.randn(*s, dtype=torch.bfloat16) * 0.05)(D, N) * 3.14),
            "C_real": _w((lambda *s: torch.randn(*s, dtype=torch.bfloat16) * 0.1)(D, N) / N ** 0.5),
            "C_imag": _w((lambda *s: torch.randn(*s, dtype=torch.bfloat16) * 0.2)(D, N) / N ** 0.5),
            "log_dt": _w((lambda *s: torch.randn(*s, dtype=torch.bfloat16) * 0.05)(D) * 0.5 - 4.0),
            "D_skip": _w((lambda *s: torch.randn(*s, dtype=torch.bfloat16) * 0.05)(D)),
        }
    _A_in = _A_setup()

    def _B_setup():
        B, S, D, F = 1, 2048, 512, 1376
        return {
            "x": _w((lambda *s: torch.randn(*s, dtype=torch.bfloat16) * 0.05)(B, S, D)),
            "w1": _w((lambda *s: torch.randn(*s, dtype=torch.bfloat16) * 1.6)(D, F) * (D ** -0.5)),
            "b1": _w((lambda *s: torch.randn(*s, dtype=torch.bfloat16) * 0.8)(F)),
            "w2": _w((lambda *s: torch.randn(*s, dtype=torch.bfloat16) * 0.05)(F, D) * (F ** -0.5)),
            "b2": _w((lambda *s: torch.randn(*s, dtype=torch.bfloat16) * 0.05)(D)),
        }
    _B_in = _B_setup()

    return {
        "A_u": _A_in["u"],
        "A_log_A_real": _A_in["log_A_real"],
        "A_A_imag": _A_in["A_imag"],
        "A_C_real": _A_in["C_real"],
        "A_C_imag": _A_in["C_imag"],
        "A_log_dt": _A_in["log_dt"],
        "A_D_skip": _A_in["D_skip"],
        "B_x": _B_in["x"],
        "B_w1": _B_in["w1"],
        "B_b1": _B_in["b1"],
        "B_w2": _B_in["w2"],
        "B_b2": _B_in["b2"],
    }

def reference(A_u, A_log_A_real, A_A_imag, A_C_real, A_C_imag, A_log_dt, A_D_skip, B_x, B_w1, B_b1, B_w2, B_b2):
    def _A_ref(u, log_A_real, A_imag, C_real, C_imag, log_dt, D_skip):
        L = u.shape[1]
        dt = torch.exp(log_dt)
        A = -torch.exp(log_A_real) + 1j * A_imag
        C = (C_real + 1j * C_imag) * dt[:, None]
        pos = (lambda x, dt: x.to(dt))(torch.arange(L), log_dt.dtype)
        P = torch.exp((A * dt[:, None])[:, :, None] * pos[None, None, :])
        K = 2.0 * torch.einsum("dn,dnl->dl", C, P).real
        L2 = 2 * L
        u_t = (lambda x, *d: x.permute(*d))(u, 0, 2, 1)
        Kf = (lambda x, *a, **k: torch.fft.rfft(x.float(), *a, **k))(K, L2)
        Uf = (lambda x, *a, **k: torch.fft.rfft(x.float(), *a, **k))(u_t, L2)
        y = (lambda x, *a, **k: torch.fft.irfft(x, *a, **k).to(torch.bfloat16))(Uf * Kf[None, :, :], L2)[:, :, :L]
        y = (lambda x, *d: x.permute(*d))(y, 0, 2, 1)
        return y + u * D_skip[None, None, :]

    def _B_ref(x, w1, b1, w2, b2):
        y = _layernorm(x, axis=-1)
        h = _gelu(torch.einsum("bsd,df->bsf", y, w1) + b1)
        return torch.einsum("bsf,fd->bsd", h, w2) + b2

    x = A_u
    xn = _layernorm(x, axis=-1)
    return x + _A_ref(xn, A_log_A_real, A_A_imag, A_C_real, A_C_imag, A_log_dt, A_D_skip) + _B_ref(xn, B_w1, B_b1, B_w2, B_b2)
```

```cpp
#include <hip/hip_runtime.h>
#include <hip/hip_bf16.h>
typedef unsigned short u16;
__device__ inline float bf2f(u16 b){ return __uint_as_float(((unsigned)b)<<16); }
__device__ inline u16 f2bf(float f){ unsigned u=__float_as_uint(f); u += 0x7FFFu + ((u>>16)&1u); return (u16)(u>>16); }
__device__ inline float rbf(float f){ return bf2f(f2bf(f)); }
constexpr int L=2048, D=512, N=128, F=1376;

__global__ void k_ln(const u16* x, u16* xn, u16* yn){
  int t=blockIdx.x; int tid=threadIdx.x;
  float v[8];
  for(int i=0;i<8;i++) v[i]=bf2f(x[t*D+tid*8+i]);
  for(int pass=0;pass<2;pass++){
    float s=0; for(int i=0;i<8;i++) s+=v[i];
    for(int o=32;o;o>>=1) s+=__shfl_xor(s,o);
    float mean=s/D;
    float q=0; for(int i=0;i<8;i++){float d=v[i]-mean; q+=d*d;}
    for(int o=32;o;o>>=1) q+=__shfl_xor(q,o);
    float var=rbf(q/D); float m=rbf(mean);
    float sd=rbf(sqrtf(rbf(var+1e-5f)));
    for(int i=0;i<8;i++) v[i]=rbf(rbf(v[i]-m)/sd);
    u16* o = pass==0? xn: yn;
    for(int i=0;i<8;i++) o[t*D+tid*8+i]=f2bf(v[i]);
  }
}
__global__ void k_kgen(const u16* lar,const u16* aim,const u16* cre,const u16* cim,const u16* ldt,float* K){
  int d=blockIdx.y; int l=blockIdx.x*256+threadIdx.x;
  float dt=rbf(expf(bf2f(ldt[d])));
  float p=rbf((float)l);
  float s=0;
  for(int n=0;n<N;n++){
    float ar=-rbf(expf(bf2f(lar[d*N+n]))); float ai=bf2f(aim[d*N+n]);
    float zr=ar*dt, zi=ai*dt;
    float cr=bf2f(cre[d*N+n])*dt, ci=bf2f(cim[d*N+n])*dt;
    float e=expf(zr*p); float c=cosf(zi*p), sn=sinf(zi*p);
    s += e*(cr*c-ci*sn);
  }
  K[d*L+l]=2.f*s;
}
__global__ void k_conv(const u16* xn,const float* K,const u16* dsk,u16* A){
  int d=blockIdx.x*64+threadIdx.x%64; int l=blockIdx.y*4+threadIdx.x/64;
  float s=0;
  for(int j=0;j<=l;j++) s+=K[d*L+j]*bf2f(xn[(l-j)*D+d]);
  float y=rbf(s);
  float u=bf2f(xn[l*D+d]);
  A[l*D+d]=f2bf(y+rbf(u*bf2f(dsk[d])));
}
__global__ void k_g1(const u16* yn,const u16* w1,const u16* b1,u16* h){
  int f=blockIdx.x*32+threadIdx.x%32; int t=blockIdx.y*8+threadIdx.x/32;
  float s=0;
  for(int k=0;k<D;k++) s+=bf2f(yn[t*D+k])*bf2f(w1[k*F+f]);
  float v=rbf(rbf(s)+bf2f(b1[f]));
  float g=0.5f*v*(1.f+tanhf(0.7978845608028654f*(v+0.044715f*v*v*v)));
  h[t*F+f]=f2bf(g);
}
__global__ void k_g2(const u16* h,const u16* w2,const u16* b2,const u16* x,const u16* A,u16* out){
  int d=blockIdx.x*64+threadIdx.x%64; int t=blockIdx.y*4+threadIdx.x/64;
  float s=0;
  for(int k=0;k<F;k++) s+=bf2f(h[t*F+k])*bf2f(w2[k*D+d]);
  float B=rbf(rbf(s)+bf2f(b2[d]));
  float r=rbf(bf2f(x[t*D+d])+bf2f(A[t*D+d]));
  out[t*D+d]=f2bf(r+B);
}
extern "C" void kernel_launch(void* const* d_in, const int* in_sizes, int n_in,
                              void* d_out, int out_size, void* d_ws, size_t ws_size,
                              hipStream_t stream) {
  const u16* u=(const u16*)d_in[0]; const u16* lar=(const u16*)d_in[1]; const u16* aim=(const u16*)d_in[2];
  const u16* cre=(const u16*)d_in[3]; const u16* cim=(const u16*)d_in[4]; const u16* ldt=(const u16*)d_in[5];
  const u16* dsk=(const u16*)d_in[6]; const u16* w1=(const u16*)d_in[8]; const u16* b1=(const u16*)d_in[9];
  const u16* w2=(const u16*)d_in[10]; const u16* b2=(const u16*)d_in[11];
  char* ws=(char*)d_ws;
  u16* xn=(u16*)ws; u16* yn=(u16*)(ws+(2<<20)); u16* A=(u16*)(ws+(4<<20)); float* K=(float*)(ws+(6<<20)); u16* h=(u16*)(ws+(12<<20));
  k_ln<<<L,64,0,stream>>>(u,xn,yn);
  k_kgen<<<dim3(L/256,D),256,0,stream>>>(lar,aim,cre,cim,ldt,K);
  k_conv<<<dim3(D/64,L/4),256,0,stream>>>(xn,K,dsk,A);
  k_g1<<<dim3(F/32,L/8),256,0,stream>>>(yn,w1,b1,h);
  k_g2<<<dim3(D/64,L/4),256,0,stream>>>(h,w2,b2,u,A,(u16*)d_out);
}
```

```cpp
#include <hip/hip_runtime.h>
#include <stdint.h>
#include <utility>

namespace {
typedef unsigned short u16;
typedef unsigned u32;
typedef unsigned long long u64;
typedef float f32x2 __attribute__((ext_vector_type(2)));
typedef float f32x4 __attribute__((ext_vector_type(4)));
typedef u32 u32x2 __attribute__((ext_vector_type(2)));
typedef u32 u32x4 __attribute__((ext_vector_type(4)));
typedef short s16x4 __attribute__((ext_vector_type(4)));
typedef short s16x8 __attribute__((ext_vector_type(8)));
typedef __bf16 bf16x2 __attribute__((ext_vector_type(2)));
typedef __bf16 bf16x8 __attribute__((ext_vector_type(8)));
typedef _Float16 f16x2 __attribute__((ext_vector_type(2)));
typedef _Float16 f16x8 __attribute__((ext_vector_type(8)));
#define LAS __attribute__((address_space(3)))
#define DEV __device__ __forceinline__
typedef __amdgpu_buffer_rsrc_t rsrc_t;

constexpr int SEQ = 2048, DM = 512, NMODE = 128, FF = 1376, NUNIT = 43;
constexpr u32 WS_E = 0, WS_LNF = 0x1000, WS_HF = 0x2000, WS_AF = 0x4000, WS_STATS = 0x8000, WS_XN = 0x10000,
              WS_A = 0x210000, WS_H = 0x410000, WS_END = WS_H + 128 * NUNIT * 1024, WS_STAMP = 0xA00000;
constexpr u32 SLOT = 28672, SLOT_W = 16384;
constexpr u32 W2R = 0, W2_NS = 14, W2_HALF = W2_NS * 4096;
constexpr u32 S0 = 114688;
constexpr u32 S_LN = S0, LN_ROW = 1088;
constexpr u32 S_TAB = S0, TAB_ROW = 288, TAB_WAVE = 32 * TAB_ROW;
constexpr u32 S_KP = S0 + 36864;
constexpr u32 S_KC = S0 + 43008, KC_CH = 1296;
constexpr u32 S_CS = S0 + 45600;
constexpr u32 S_MISC = S0 + 47520;
constexpr u32 S_G = S0, G_PAR = 4160, G_CH = 8320;
constexpr u32 S_U = S0 + 16640, U_CH = 4608;
constexpr u32 S_EX = S0;
constexpr u32 S_AT = S0 + 25856;
constexpr u32 LDS_BYTES = 163840;
constexpr u32 OOB = 0x80000000u;

#define STAMP(k) do {} while (0)

struct Params {
  const u16 *x, *lar, *aim, *cre, *cim, *ldt, *dsk, *w1, *b1, *w2, *b2;
  u16* out;
  char* ws;
};

DEV float asf(u32 v) { return __builtin_bit_cast(float, v); }
DEV u32 asu(float v) { return __builtin_bit_cast(u32, v); }
DEV f16x2 ash2(u32 v) { return __builtin_bit_cast(f16x2, v); }
DEV float bflo(u32 v) { return asf(v << 16); }
DEV float bfhi(u32 v) { return asf(v & 0xffff0000u); }
DEV u32 pk_bf16(float a, float b) { f32x2 v = {a, b}; return __builtin_bit_cast(u32, __builtin_convertvector(v, bf16x2)); }
DEV u32 pk_f16(float a, float b) { f32x2 v = {a, b}; return __builtin_bit_cast(u32, __builtin_convertvector(v, f16x2)); }
DEV float rbf(float a) { return bflo(pk_bf16(a, a)); }
DEV float bf1(u16 v) { return asf((u32)v << 16); }

template <int N> DEV void wait_vm() { asm volatile("s_waitcnt vmcnt(%0)" ::"n"(N) : "memory"); }
DEV void bar() { asm volatile("s_waitcnt lgkmcnt(0)\n\ts_barrier" ::: "memory"); }
DEV u32x4 mk_srd(const void* base, u32 bytes) {
  u64 b = (u64)base;
  u32x4 s;
  s.x = __builtin_amdgcn_readfirstlane((u32)b);
  s.y = __builtin_amdgcn_readfirstlane((u32)(b >> 32)) & 0xffffu;
  s.z = bytes;
  s.w = 0x00020000u;
  return s;
}
DEV rsrc_t mk_rsrc(const void* base, u32 bytes) { return __builtin_amdgcn_make_buffer_rsrc((void*)base, 0, bytes, 0x00020000); }

#define BLOAD(INS, TY, NAME, FLAGS)                                                                          \
  DEV TY NAME(u32x4 srd, u32 voff, u32 soff) {                                                               \
    TY r;                                                                                                    \
    asm volatile(INS " %0, %1, %2, %3 offen" FLAGS : "=v"(r) : "v"(voff), "s"(srd), "s"(soff) : "memory");    \
    return r;                                                                                                \
  }
BLOAD("buffer_load_dwordx4", u32x4, ld128, "")
BLOAD("buffer_load_dwordx4", u32x4, ld128c, " sc1")
BLOAD("buffer_load_dwordx2", u32x2, ld64, "")
BLOAD("buffer_load_dword", u32, ld32c, " sc1")
BLOAD("buffer_load_ushort", u32, ld16, "")
template <class T> DEV void tie(T& v) { asm volatile("" : "+v"(v)); }

DEV void dma_m0(u32 lds_addr) { asm volatile("s_mov_b32 m0, %0" ::"s"(lds_addr) : "memory"); }
DEV void dma_go(u32x4 srd, u32 voff, u32 soff) { asm volatile("buffer_load_dwordx4 %0, %1, %2 offen lds" ::"v"(voff), "s"(srd), "s"(soff) : "memory"); }
DEV void dma16(u32x4 srd, u32 lds_addr, u32 voff, u32 soff) {
  asm volatile("s_mov_b32 m0, %0\n\ts_nop 0\n\tbuffer_load_dwordx4 %1, %2, %3 offen lds" ::"s"(lds_addr), "v"(voff), "s"(srd), "s"(soff) : "memory");
}
template <int I> using ic = std::integral_constant<int, I>;
template <class F, int... Is> DEV void sfor_impl(F&& f, std::integer_sequence<int, Is...>) { (f(ic<Is>{}), ...); }
template <int N, class F> DEV void sfor(F&& f) { sfor_impl(f, std::make_integer_sequence<int, N>{}); }
template <int OFF> DEV u32x4 a_ds128(u32 addr) { u32x4 r; asm volatile("ds_read_b128 %0, %1 offset:%2" : "=v"(r) : "v"(addr), "n"(OFF)); return r; }
template <int OFF> DEV u32x2 a_dstr(u32 addr) { u32x2 r; asm volatile("ds_read_b64_tr_b16 %0, %1 offset:%2" : "=v"(r) : "v"(addr), "n"(OFF)); return r; }
template <int O0, int O1> DEV u32x2 a_ds2x32(u32 addr) { u32x2 r; asm volatile("ds_read2_b32 %0, %1 offset0:%2 offset1:%3" : "=v"(r) : "v"(addr), "n"(O0), "n"(O1)); return r; }
template <int N> DEV void wait_lgkm() { asm volatile("s_waitcnt lgkmcnt(%0)" ::"n"(N)); }
DEV u32x4 cat4(u32x2 a, u32x2 b) { return __builtin_shufflevector(a, b, 0, 1, 2, 3); }
template <bool INIT> DEV void a_mfma_bf(f32x4& c, u32x4 a, u32x4 b) {
  if constexpr (INIT) asm volatile("v_mfma_f32_16x16x32_bf16 %0, %1, %2, 0" : "=&v"(c) : "v"(a), "v"(b));
  else asm volatile("v_mfma_f32_16x16x32_bf16 %0, %1, %2, %0" : "+v"(c) : "v"(a), "v"(b));
}
template <bool INIT> DEV void a_mfma_h(f32x4& c, u32x4 a, u32x4 b) {
  if constexpr (INIT) asm volatile("v_mfma_f32_16x16x32_f16 %0, %1, %2, 0" : "=&v"(c) : "v"(a), "v"(b));
  else asm volatile("v_mfma_f32_16x16x32_f16 %0, %1, %2, %0" : "+v"(c) : "v"(a), "v"(b));
}
DEV void a_dot2_ones(float& acc, u32 v) { asm volatile("v_dot2c_f32_bf16 %0, 0x3f803f80, %1" : "+v"(acc) : "v"(v)); }
DEV void mfma_settle4(f32x4& a, f32x4& b, f32x4& c, f32x4& d) { asm volatile("s_nop 7\n\ts_nop 1" : "+v"(a), "+v"(b), "+v"(c), "+v"(d)); }
DEV void st128c(rsrc_t r, u32x4 v, u32 voff) { __builtin_amdgcn_raw_buffer_store_b128(v, r, voff, 0, 16); }
DEV void st64c(rsrc_t r, u32x2 v, u32 voff) { __builtin_amdgcn_raw_buffer_store_b64(v, r, voff, 0, 16); }
DEV void st32c(rsrc_t r, u32 v, u32 voff) { __builtin_amdgcn_raw_buffer_store_b32(v, r, voff, 0, 16); }
DEV void st64(rsrc_t r, u32x2 v, u32 voff) { __builtin_amdgcn_raw_buffer_store_b64(v, r, voff, 0, 0); }

template <class T> DEV T lds_ld(const char* smem, u32 off) { return *(const LAS T*)(smem + off); }
template <class T> DEV void lds_st(char* smem, u32 off, T v) { *(LAS T*)(smem + off) = v; }
DEV s16x4 lds_tr(const char* smem, u32 off) { return __builtin_amdgcn_ds_read_tr16_b64_v4i16((LAS s16x4*)(smem + off)); }
DEV s16x8 cat8(s16x4 a, s16x4 b) { return __builtin_shufflevector(a, b, 0, 1, 2, 3, 4, 5, 6, 7); }
DEV f32x4 mfma_bf(s16x8 a, u32x4 b, f32x4 c) {
  return __builtin_amdgcn_mfma_f32_16x16x32_bf16(__builtin_bit_cast(bf16x8, a), __builtin_bit_cast(bf16x8, b), c, 0, 0, 0);
}
DEV f32x4 mfma_h(u32x4 a, u32x4 b, f32x4 c) {
  return __builtin_amdgcn_mfma_f32_16x16x32_f16(__builtin_bit_cast(f16x8, a), __builtin_bit_cast(f16x8, b), c, 0, 0, 0);
}

template <int CTRL, int RM> DEV float dpp_add(float v) {
  return v + asf(__builtin_amdgcn_update_dpp(0, asu(v), CTRL, RM, 0xf, true));
}
DEV float wave_sum(float v) {
  v = dpp_add<0x111, 0xf>(v); v = dpp_add<0x112, 0xf>(v); v = dpp_add<0x114, 0xf>(v); v = dpp_add<0x118, 0xf>(v);
  v = dpp_add<0x142, 0xa>(v); v = dpp_add<0x143, 0xc>(v);
  return asf(__builtin_amdgcn_readlane(asu(v), 63));
}
DEV float sum_rows(float v) {
  auto a = __builtin_amdgcn_permlane16_swap(asu(v), asu(v), false, false);
  v = asf(a[0]) + asf(a[1]);
  auto b = __builtin_amdgcn_permlane32_swap(asu(v), asu(v), false, false);
  return asf(b[0]) + asf(b[1]);
}
DEV f32x2 cmul(f32x2 a, f32x2 b) {
  f32x2 t, r;
  asm("v_pk_mul_f32 %0, %2, %3 op_sel_hi:[1,0]\n\tv_pk_fma_f32 %1, %2, %3, %0 op_sel:[1,1,0] op_sel_hi:[0,1,1] neg_lo:[1,0,0]"
      : "=&v"(t), "=v"(r) : "v"(a), "v"(b));
  return r;
}
DEV float gelu_tanh(float v) {
  const float c1 = -2.3022081986f, c2 = -0.10294323970f;
  float s = v * __builtin_fmaf(v * v, c2, c1);
  return v * __builtin_amdgcn_rcpf(1.0f + __builtin_amdgcn_exp2f(s));
}

DEV void spin_flag(u32x4 srd, u32 voff, u32 epoch, u32 val, bool need) {
  for (int it = 0; it < (1 << 16); ++it) {
    if (__all(!need || val == epoch)) break;
    __builtin_amdgcn_s_sleep(1);
    val = ld32c(srd, voff, 0);
    asm volatile("s_waitcnt vmcnt(0)" : "+v"(val)::"memory");
  }
}

}

__global__ __launch_bounds__(256, 1) void s4d_mixer_fused(Params p) {
  __shared__ __attribute__((aligned(16))) char smem[LDS_BYTES];
  const int tid = threadIdx.x, lane = tid & 63;
  const int w = __builtin_amdgcn_readfirstlane(tid >> 6);
  const int b = blockIdx.x, g8 = b & 7, r = b >> 3;
  const int c15 = lane & 15, g = lane >> 4;
  const int mt = 4 * (g8 >> 1) + (r >> 3), nt = 8 * (g8 & 1) + (r & 7);
  const int ustart = (nt * NUNIT) >> 4, unext = ((nt + 1) * NUNIT) >> 4;
  const int tg = 16 * mt + nt;
  const int cp = 32 * g8 + r;
  const int mt2 = 4 * g8 + (r >> 3), nt2 = r & 7;

  const u32x4 sX = mk_srd(p.x, SEQ * DM * 2), sWS = mk_srd(p.ws, WS_END), sW1 = mk_srd(p.w1, DM * FF * 2), sW2 = mk_srd(p.w2, FF * DM * 2);
  const rsrc_t rWS = mk_rsrc(p.ws, WS_END), rOUT = mk_rsrc(p.out, SEQ * DM * 2);
  const u32 lb = (u32)(uintptr_t)(LAS char*)smem;
  STAMP(0);

  u32 eword = ld32c(sWS, WS_E, 0);
  u32x4 xr0 = ld128(sX, lane * 16, (8 * tg + 2 * w) * 1024), xr1 = ld128(sX, lane * 16, (8 * tg + 2 * w + 1) * 1024);
  u32 vx[2], vw[3];
  {
    const int rr = lane >> 3;
    vx[0] = rr * 1024 + (((lane & 7) ^ ((lane >> 4) & 7)) * 16);
    vx[1] = rr * 1024 + (((lane & 7) ^ ((4 + (lane >> 4)) & 7)) * 16);
#pragma unroll
    for (int j = 0; j < 3; ++j) {
      const int P = (3 * w + j) * 64 + lane, k = P / 12, sl = P - 12 * k;
      const int c = (sl >> 1) ^ ((k >> 3) & 1);
      vw[j] = k * (FF * 2) + (16 * c + 8 * (sl & 1)) * 2;
    }
  }
  const u32 x_so = (mt * 128 + 32 * w) * 1024, x_lo = lb + 32 * w * 128, w_so = ustart * 64, w_lo = lb + SLOT_W + 3 * w * 1024;
  auto st1_lds = [&](int kc, int j) -> u32 { return (kc & 3) * SLOT + (j < 4 ? x_lo + j * 1024 : w_lo + (j - 4) * 1024); };
  auto st1_go = [&](int kc, int j) {
    if (j < 4) dma_go(sX, vx[j & 1], x_so + j * 8192 + kc * 128);
    else dma_go(sW1, vw[j - 4], w_so + kc * 64 * (FF * 2));
  };
  sfor<4>([&](auto KC) { sfor<7>([&](auto J) {
    dma_m0(st1_lds(KC, J)); asm volatile("s_nop 0"); st1_go(KC, J); }); });
  asm volatile("s_waitcnt vmcnt(28)" : "+v"(eword), "+v"(xr0), "+v"(xr1)::"memory");
  const u32 epoch = __builtin_amdgcn_readfirstlane(eword) * 0x9E3779B1u + 0x7F4A7C15u;
  STAMP(1);

  {
    u32x4 xr[2] = {xr0, xr1};
    float al[2], be[2];
#pragma unroll
    for (int t = 0; t < 2; ++t) {
      float x[8];
#pragma unroll
      for (int i = 0; i < 4; ++i) { x[2 * i] = bflo(xr[t][i]); x[2 * i + 1] = bfhi(xr[t][i]); }
      const float x0 = asf(__builtin_amdgcn_readfirstlane(asu(x[0])));
      float s = 0.f, q = 0.f;
#pragma unroll
      for (int i = 0; i < 8; ++i) { const float d = x[i] - x0; s += d; q = __builtin_fmaf(d, d, q); }
      const float md = wave_sum(s) * (1.0f / DM);
      const float var = fmaxf(__builtin_fmaf(-md, md, wave_sum(q) * (1.0f / DM)), 0.f);
      const float m1 = rbf(x0 + md);
      const float s1 = rbf(__builtin_amdgcn_sqrtf(rbf(rbf(var) + 1e-5f)));
      const float r1 = __builtin_amdgcn_rcpf(s1);
      float s2 = 0.f, q2 = 0.f;
      u32x4 hv;
#pragma unroll
      for (int i = 0; i < 4; ++i) {
        const u32 a = pk_bf16(x[2 * i] - m1, x[2 * i + 1] - m1);
        const u32 n = pk_bf16(bflo(a) * r1, bfhi(a) * r1);
        const float n0 = bflo(n), n1 = bfhi(n);
        s2 += n0 + n1; q2 = __builtin_fmaf(n0, n0, q2); q2 = __builtin_fmaf(n1, n1, q2);
        hv[i] = pk_f16(n0, n1);
      }
      lds_st<u32x4>(smem, S_LN + (2 * w + t) * LN_ROW + lane * 16, hv);
      const float mean2 = wave_sum(s2) * (1.0f / DM);
      const float var2 = fmaxf(__builtin_fmaf(-mean2, mean2, wave_sum(q2) * (1.0f / DM)), 0.f);
      const float m2 = rbf(mean2);
      const float r2 = __builtin_amdgcn_rcpf(rbf(__builtin_amdgcn_sqrtf(rbf(rbf(var2) + 1e-5f))));
      al[t] = r1 * r2;
      be[t] = __builtin_fmaf(m1, r1, m2) * r2;
    }
    {
      u32x4 sv = {asu(lane ? al[1] : al[0]), epoch, asu(lane ? be[1] : be[0]), epoch};
      st128c(rWS, sv, lane < 2 ? WS_STATS + (8 * tg + 2 * w + lane) * 16 : OOB);
    }
    bar();
#pragma unroll
    for (int i = 0; i < 2; ++i) {
      const int ch0 = (2 * w + i) * 64 + 16 * g;
      const u32 a = S_LN + ((lane & 15) >> 2) * LN_ROW + (ch0 + 4 * (lane & 3)) * 2;
      const s16x8 v = cat8(lds_tr(smem, a), lds_tr(smem, a + 4 * LN_ROW));
      st128c(rWS, __builtin_bit_cast(u32x4, v), WS_XN + tg * 8192 + ((2 * w + i) * 64 + lane) * 16);
    }
  }
  STAMP(2);
  const u32x4 sB1 = mk_srd(p.b1, FF * 2);
  u32x2 b1v[6];
#pragma unroll
  for (int T = 0; T < 6; ++T) b1v[T] = ld64(sB1, (16 * T + 4 * g) * 2, ustart * 64);
  const int kch = w >> 1, kd = 2 * cp + kch;
  u32 q_lar, q_aim, q_cre, q_cim;
  {
    const u32 vo = (64 * (w & 1) + lane) * 2, so = kd * NMODE * 2;
    q_lar = ld16(mk_srd(p.lar, DM * NMODE * 2), vo, so); q_aim = ld16(mk_srd(p.aim, DM * NMODE * 2), vo, so);
    q_cre = ld16(mk_srd(p.cre, DM * NMODE * 2), vo, so); q_cim = ld16(mk_srd(p.cim, DM * NMODE * 2), vo, so);
  }

  f32x4 acc1[6][2];
  float csp[3] = {0.f, 0.f, 0.f};
  {
    const int q4 = (lane & 15) >> 2, p4 = lane & 3;
    const u32 xa0 = lb + (32 * w + c15) * 128 + ((g ^ (c15 >> 1)) * 16);
    const u32 wrow = lb + SLOT_W + (8 * g + q4) * 192 + 8 * p4;
    const u32 xa[2][2] = {{xa0, xa0 ^ 64}, {xa0 + 2 * SLOT, (xa0 ^ 64) + 2 * SLOT}};
    const u32 wa[2][2] = {{wrow + (g & 1) * 32, wrow - (g & 1) * 32}, {wrow + (g & 1) * 32 + 2 * SLOT, wrow - (g & 1) * 32 + 2 * SLOT}};
    u32 wc[2][3];
#pragma unroll
    for (int i = 0; i < 3; ++i) { wc[0][i] = wrow + (((3 * (w & 1) + i) ^ (g & 1)) * 32) + (w >> 1) * 32 * 192; wc[1][i] = wc[0][i] + 2 * SLOT; }
    u32x4 xf[2][2];
    u32x2 wl[2][6], wh[2][6], cl[3], chh[3];
    auto rd = [&](auto S, auto R) {
      constexpr int s = S, r = R, buf = s & 1, sl = (s >> 1) & 3, so = (sl & 1) * SLOT, sp = s & 1;
      if constexpr (r < 2) xf[buf][r] = a_ds128<so + r * 2048>(xa[sl >> 1][sp]);
      else {
        constexpr int T = (r - 2) >> 1, hf = (r - 2) & 1, off = so + T * 32 + (sp * 32 + hf * 4) * 192;
        if constexpr (hf == 0) wl[buf][T] = a_dstr<off>(wa[sl >> 1][T & 1]); else wh[buf][T] = a_dstr<off>(wa[sl >> 1][T & 1]);
      }
    };
    wait_vm<34>();
    bar();
    sfor<14>([&](auto R) { rd(ic<0>{}, R); });
    sfor<16>([&](auto S) {
      constexpr int s = S, buf = s & 1, kc = (s + 1) >> 1;
      if constexpr ((s & 1) && kc < 8) {
        wait_vm<(kc <= 3 ? 27 : (kc <= 5 ? 14 : (kc == 6 ? 7 : 0)))>();
        bar();
        if constexpr (kc == 4) st32c(rWS, epoch, tid == 0 ? WS_LNF + tg * 4 : OOB);
      } else wait_lgkm<0>();
      sfor<12>([&](auto I) {
        constexpr int i = I, T = i >> 1, tt = i & 1;
        constexpr int sc = (s & 1) ? kc + 3 : (s >> 1) + 3;
        constexpr int pj = (s & 1) ? ((i % 3 == 0) ? i / 3 : -1) : ((i % 4 == 1) ? 4 + i / 4 : -1);
        constexpr bool st = pj >= 0 && pj < 7 && sc >= 4 && sc < 8 && s >= 1;
        if constexpr (st) dma_m0(st1_lds(sc, pj));
        a_mfma_bf<s == 0>(acc1[T][tt], cat4(wl[buf][T], wh[buf][T]), xf[buf][tt]);
        if constexpr (st) st1_go(sc, pj);
        if constexpr (s + 1 < 16 && i < 7) { rd(ic<s + 1>{}, ic<2 * i>{}); rd(ic<s + 1>{}, ic<2 * i + 1>{}); }
        if constexpr (!(s & 1) && i >= 7 && i < 10) {
          constexpr int sl = (s >> 1) & 3, so = (sl & 1) * SLOT;
          cl[i - 7] = a_dstr<so>(wc[sl >> 1][i - 7]); chh[i - 7] = a_dstr<so + 4 * 192>(wc[sl >> 1][i - 7]);
        }
        if constexpr ((s & 1) && i >= 7 && i < 10) {
          a_dot2_ones(csp[i - 7], cl[i - 7].x); a_dot2_ones(csp[i - 7], cl[i - 7].y);
          a_dot2_ones(csp[i - 7], chh[i - 7].x); a_dot2_ones(csp[i - 7], chh[i - 7].y);
        }
      });
    });
    mfma_settle4(acc1[0][0], acc1[0][1], acc1[1][0], acc1[1][1]); mfma_settle4(acc1[2][0], acc1[2][1], acc1[3][0], acc1[3][1]);
    mfma_settle4(acc1[4][0], acc1[4][1], acc1[5][0], acc1[5][1]);
    asm volatile("" : "+v"(csp[0]), "+v"(csp[1]), "+v"(csp[2]));
  }
  STAMP(3);
#pragma unroll
  for (int i = 0; i < 3; ++i) {
    const float v = sum_rows(csp[i]);
    if (lane < 16) lds_st<float>(smem, S_CS + (w * 48 + i * 16 + lane) * 4, v);
  }
  u32x4 st0 = ld128c(sWS, WS_STATS + (128 * mt + 32 * w + c15) * 16, 0), st1 = ld128c(sWS, WS_STATS + (128 * mt + 32 * w + 16 + c15) * 16, 0);
  u32 lnf = ld32c(sWS, WS_LNF + tid * 4, 0);
  asm volatile("s_waitcnt vmcnt(0)" : "+v"(st0), "+v"(st1), "+v"(lnf), "+v"(b1v[0]), "+v"(b1v[1]), "+v"(b1v[2]), "+v"(b1v[3]), "+v"(b1v[4]), "+v"(b1v[5]),
               "+v"(q_lar), "+v"(q_aim), "+v"(q_cre), "+v"(q_cim)::"memory");
  for (int it = 0; it < (1 << 16); ++it) {
    if (__all(st0.y == epoch && st0.w == epoch && st1.y == epoch && st1.w == epoch)) break;
    __builtin_amdgcn_s_sleep(1);
    st0 = ld128c(sWS, WS_STATS + (128 * mt + 32 * w + c15) * 16, 0);
    st1 = ld128c(sWS, WS_STATS + (128 * mt + 32 * w + 16 + c15) * 16, 0);
    asm volatile("s_waitcnt vmcnt(0)" : "+v"(st0), "+v"(st1)::"memory");
  }
  spin_flag(sWS, WS_LNF + tid * 4, epoch, lnf, true);
  u32x4 ug0 = ld128c(sWS, WS_XN + tid * 8192 + cp * 32, 0), ug1 = ld128c(sWS, WS_XN + tid * 8192 + cp * 32 + 16, 0);
  bar();
  st32c(rWS, epoch, (b == 0 && tid == 0) ? WS_E : OOB);
  STAMP(4);

  {
    const float al[2] = {asf(st0.x), asf(st1.x)}, be[2] = {asf(st0.z), asf(st1.z)};
#pragma unroll
    for (int uu = 0; uu < 3; ++uu) {
      float bias[8], cs[8];
#pragma unroll
      for (int hh = 0; hh < 2; ++hh) {
        const int T = 2 * uu + hh;
        const u32 o = S_CS + ((T / 3) * 48 + (T % 3) * 16 + 4 * g) * 4;
        const f32x4 c0 = lds_ld<f32x4>(smem, o), c1 = lds_ld<f32x4>(smem, o + 2 * 48 * 4);
#pragma unroll
        for (int i = 0; i < 4; ++i) cs[4 * hh + i] = c0[i] + c1[i];
        bias[4 * hh + 0] = bflo(b1v[T].x); bias[4 * hh + 1] = bfhi(b1v[T].x);
        bias[4 * hh + 2] = bflo(b1v[T].y); bias[4 * hh + 3] = bfhi(b1v[T].y);
      }
#pragma unroll
      for (int tt = 0; tt < 2; ++tt) {
        float hv[8];
#pragma unroll
        for (int j = 0; j < 8; ++j) {
          const float pre = __builtin_fmaf(al[tt], acc1[2 * uu + (j >> 2)][tt][j & 3], __builtin_fmaf(-be[tt], cs[j], bias[j]));
          hv[j] = gelu_tanh(pre);
        }
        const u32x4 o = {pk_bf16(hv[0], hv[1]), pk_bf16(hv[2], hv[3]), pk_bf16(hv[4], hv[5]), pk_bf16(hv[6], hv[7])};
        const u32 off = WS_H + ((mt * 8 + 2 * w + tt) * NUNIT + ustart + uu) * 1024 + lane * 16;
        st128c(rWS, o, ustart + uu < unext ? off : OOB);
      }
    }
  }
  const int kh = w >> 1, mh = w & 1;
  const u32 vw2 = (lane >> 3) * 1024 + ((((lane & 7) >> 1) ^ ((lane >> 4) & 3)) * 32) + (lane & 1) * 16;
  auto stage2 = [&](int j) {
    const int u = kh * 22 + j;
#pragma unroll
    for (int i = 0; i < 2; ++i)
      dma16(sW2, lb + W2R + kh * W2_HALF + (j % W2_NS) * 4096 + (2 * mh + i) * 1024, u < NUNIT ? vw2 : OOB, (u * 32 + (2 * mh + i) * 8) * 1024 + nt2 * 128);
  };
#pragma unroll
  for (int j = 0; j < 14; ++j) stage2(j);
  wait_vm<28>();
  st32c(rWS, epoch, lane == 0 ? WS_HF + ((mt * 4 + w) * 16 + nt) * 4 : OOB);
  STAMP(5);

  float inv_sk;
  {
    const int mh2 = w & 1;
    const float dt = rbf(expf(bf1(p.ldt[kd])));
    const float zr = -rbf(expf(bf1((u16)q_lar))) * dt, zi = bf1((u16)q_aim) * dt;
    f32x2 C = {2.0f * bf1((u16)q_cre) * dt, 2.0f * bf1((u16)q_cim) * dt};
    const float bw = wave_sum(__builtin_fabsf(C.x) + __builtin_fabsf(C.y));
    int se = 267 - (int)((asu(bw) >> 23) & 0xff);
    se = se < 1 ? 1 : (se > 253 ? 253 : se);
    C *= asf((u32)se << 23);
    const float inv_sx = asf((u32)(254 - se) << 23);
    const float e1 = zr * 1.44269504089f;
    const float ph = zi * 0.15915494309f;
    const float pl = __builtin_fmaf(zi, 0.15915494309f, -ph) + zi * 6.4206383e-9f;
    auto cexp = [&](float pw) {
      const float m = __builtin_amdgcn_exp2f(e1 * pw);
      const float a = __builtin_amdgcn_fractf(ph * pw) + pl * pw;
      return f32x2{m * __builtin_amdgcn_cosf(a), m * __builtin_amdgcn_sinf(a)};
    };
    f32x2 P[8], Y[16], E[5], CE[17];
    P[1] = cexp(1.f); Y[1] = cexp(8.f); E[0] = cexp(128.f);
    P[2] = cmul(P[1], P[1]); P[3] = cmul(P[2], P[1]); P[4] = cmul(P[2], P[2]);
    P[5] = cmul(P[4], P[1]); P[6] = cmul(P[4], P[2]); P[7] = cmul(P[4], P[3]);
    Y[0] = f32x2{1.f, 0.f};
    Y[2] = cmul(Y[1], Y[1]); Y[3] = cmul(Y[2], Y[1]); Y[4] = cmul(Y[2], Y[2]);
    Y[5] = cmul(Y[4], Y[1]); Y[6] = cmul(Y[4], Y[2]); Y[7] = cmul(Y[4], Y[3]); Y[8] = cmul(Y[4], Y[4]);
#pragma unroll
    for (int i = 1; i < 8; ++i) Y[8 + i] = cmul(Y[8], Y[i]);
#pragma unroll
    for (int i = 1; i < 5; ++i) E[i] = cmul(E[i - 1], E[i - 1]);
    CE[0] = C; CE[1] = cmul(C, E[0]);
    CE[2] = cmul(CE[0], E[1]); CE[3] = cmul(CE[1], E[1]);
#pragma unroll
    for (int i = 0; i < 4; ++i) CE[4 + i] = cmul(CE[i], E[2]);
#pragma unroll
    for (int i = 0; i < 8; ++i) CE[8 + i] = cmul(CE[i], E[3]);
    CE[16] = cmul(C, E[4]);
    const u32 tab = S_TAB + w * TAB_WAVE, tl = tab + lane * 4;
#pragma unroll
    for (int i = 0; i < 16; ++i) lds_st<u32>(smem, tl + i * TAB_ROW, pk_f16(Y[i].x, -Y[i].y));
    const u32 rd = tab + c15 * TAB_ROW + g * 16;
    u32x4 yf[4];
#pragma unroll
    for (int s = 0; s < 4; ++s) yf[s] = lds_ld<u32x4>(smem, rd + s * 64);
    f32x4 kd3[3];
#pragma unroll
    for (int tl3 = 0; tl3 < 3; ++tl3) {
#pragma unroll
      for (int i = 0; i < 16; ++i) {
        f32x2 v;
        if (tl3 == 0) v = (i & 7) ? cmul(CE[i >> 3], P[i & 7]) : CE[i >> 3];
        else if (tl3 == 1) {
          if (i < 8) v = (i & 3) ? cmul(CE[2 + (i >> 2)], P[2 * (i & 3)]) : CE[2 + (i >> 2)];
          else v = (i & 1) ? cmul(CE[4 + ((i - 8) >> 1)], P[4]) : CE[4 + ((i - 8) >> 1)];
        } else v = i < 8 ? CE[8 + i] : (i == 8 ? CE[16] : f32x2{0.f, 0.f});
        lds_st<u32>(smem, tl + (16 + i) * TAB_ROW, pk_f16(v.x, v.y));
      }
      f32x4 a = {0, 0, 0, 0};
#pragma unroll
      for (int s = 0; s < 4; ++s) a = mfma_h(lds_ld<u32x4>(smem, rd + 16 * TAB_ROW + s * 64), yf[s], a);
      kd3[tl3] = a * inv_sx;
    }
    if (mh2) {
#pragma unroll
      for (int t = 0; t < 3; ++t) lds_st<f32x4>(smem, S_KP + kch * 3072 + t * 1024 + lane * 16, kd3[t]);
      if (lane == 0) lds_st<float>(smem, S_MISC + kch * 4, bw);
    }
    bar();
    if (!mh2) {
      const float bd = bw + lds_ld<float>(smem, S_MISC + kch * 4);
      int sk = 267 - (int)((asu(bd) >> 23) & 0xff);
      sk = sk < 1 ? 1 : (sk > 253 ? 253 : sk);
      const float scale = asf((u32)sk << 23);
      if (lane == 0) lds_st<float>(smem, S_MISC + 8 + kch * 4, asf((u32)(254 - sk) << 23));
      const u32 kc = S_KC + kch * KC_CH;
#pragma unroll
      for (int t = 0; t < 3; ++t) {
        const f32x4 v = (kd3[t] + lds_ld<f32x4>(smem, S_KP + kch * 3072 + t * 1024 + lane * 16)) * scale;
#pragma unroll
        for (int i = 0; i < 4; ++i) {
          int idx;
          if (t == 0) idx = 128 * (g >> 1) + 8 * c15 + 4 * (g & 1) + i;
          else if (t == 1) idx = g < 2 ? 256 + 64 * g + 4 * c15 + i : 384 + 64 * (g - 2) + 32 * (i >> 1) + (i & 1) + 2 * c15;
          else idx = g < 2 ? 512 + 64 * g + 16 * i + c15 : ((g == 2 && i == 0 && c15 == 0) ? 640 : 644);
          lds_st<_Float16>(smem, kc + idx * 2, (_Float16)v[i]);
        }
      }
      if (lane == 0) lds_st<u16>(smem, kc + 641 * 2, (u16)0);
    }
    bar();
    u32 ix[9];
#pragma unroll
    for (int e = 0; e < 9; ++e) {
      const int l = 2047 - (8 * tid + e);
      const u32 fb = asu((float)l);
      const u32 rb = (fb + 0x7fffu + ((fb >> 16) & 1u)) >> 16;
      ix[e] = (l < 0 ? 641u : (l < 128 ? (u32)l : rb - 17024u)) * 2;
    }
#pragma unroll
    for (int ch = 0; ch < 2; ++ch) {
      u32 e[9];
#pragma unroll
      for (int i = 0; i < 9; ++i) e[i] = lds_ld<u16>(smem, S_KC + ch * KC_CH + ix[i]);
      const u32x4 a0 = {e[0] | (e[1] << 16), e[2] | (e[3] << 16), e[4] | (e[5] << 16), e[6] | (e[7] << 16)};
      const u32x4 a1 = {e[1] | (e[2] << 16), e[3] | (e[4] << 16), e[5] | (e[6] << 16), e[7] | (e[8] << 16)};
      lds_st<u32x4>(smem, S_G + ch * G_CH + tid * 16, a0);
      lds_st<u32x4>(smem, S_G + ch * G_CH + G_PAR + tid * 16, a1);
    }
    const u32x4 z4 = {0, 0, 0, 0};
    if (tid < 8) lds_st<u32x4>(smem, S_G + (tid >> 2) * G_CH + ((tid >> 1) & 1) * G_PAR + 4096 + (tid & 1) * 16, z4);
    if (tid < 60) lds_st<u32x4>(smem, S_U + (tid / 30) * U_CH + (tid % 30) * 16, z4);
    tie(ug0); tie(ug1);
    lds_st<u32x4>(smem, S_U + 480 + tid * 16, ug0);
    lds_st<u32x4>(smem, S_U + U_CH + 480 + tid * 16, ug1);
    bar();
    inv_sk = lds_ld<float>(smem, S_MISC + 8 + kch * 4);
  }
  STAMP(6);

  {
    const int par = w & 1;
    const u32 ub = lb + S_U + kch * U_CH + 64 - 64 * par + 32 * c15 + 16 * g;
    const u32 gb0 = lb + S_G + kch * G_CH + ((c15 & 1) ? 0 : G_PAR) + 4 * (1015 - (c15 >> 1) - 16 * par + 4 * g - 32 * 31);
    const u32 gb[4] = {gb0, gb0 + 1024, gb0 + 2048, gb0 + 3072};
    u32x4 bq[32];
    u32x2 al[32], ah[32];
    f32x4 y[8];
    auto rdB = [&](auto M, auto T) { constexpr int q = 4 * T - M + 3; bq[q] = a_ds128<128 * q>(ub); };
    auto rdA = [&](auto M, auto H) {
      constexpr int k = (31 - M) >> 3, o = 32 * ((31 - M) & 7);
      if constexpr (H == 0) al[M] = a_ds2x32<o, o + 1>(gb[k]); else ah[M] = a_ds2x32<o + 2, o + 3>(gb[k]);
    };
    sfor<2>([&](auto M) { sfor<8>([&](auto T) { rdB(M, T); }); rdA(M, ic<0>{}); rdA(M, ic<1>{}); });
    sfor<32>([&](auto M) {
      constexpr int m = M, t0 = m >> 2, n = 8 - t0;
      wait_lgkm<(m <= 2 ? 10 : (m == 3 ? 4 : (m == 4 ? 6 : (2 * (31 - m) < 8 ? 2 * (31 - m) : 8))))>();
      sfor<n>([&](auto I) {
        constexpr int i = I, t = t0 + i;
        a_mfma_h<m == 0>(y[t], cat4(al[m], ah[m]), bq[4 * t - m + 3]);
        if constexpr (m < 2) {
          rdB(ic<m + 2>{}, I);
          if constexpr (i >= 6) rdA(ic<m + 2>{}, ic<i - 6>{});
        } else if constexpr (m <= 4) {
          if constexpr (i < 4) rdA(ic<2 * m + (i >> 1)>{}, ic<i & 1>{});
        } else if constexpr (m + 5 < 32) {
          if constexpr (n == 1) { rdA(ic<m + 5>{}, ic<0>{}); rdA(ic<m + 5>{}, ic<1>{}); }
          else if constexpr (i < 2) rdA(ic<m + 5>{}, I);
        }
      });
    });
    mfma_settle4(y[0], y[1], y[2], y[3]); mfma_settle4(y[4], y[5], y[6], y[7]);
    bar();
    f32x4 ys[4], yk[4];
    if (par) { ys[0] = y[0]; ys[1] = y[1]; ys[2] = y[2]; ys[3] = y[3]; yk[0] = y[4]; yk[1] = y[5]; yk[2] = y[6]; yk[3] = y[7]; }
    else { ys[0] = y[4]; ys[1] = y[5]; ys[2] = y[6]; ys[3] = y[7]; yk[0] = y[0]; yk[1] = y[1]; yk[2] = y[2]; yk[3] = y[3]; }
#pragma unroll
    for (int i = 0; i < 4; ++i) lds_st<f32x4>(smem, S_EX + w * 4096 + i * 1024 + lane * 16, ys[i]);
    bar();
    const float dsk = bf1(p.dsk[kd]);
#pragma unroll
    for (int i = 0; i < 4; ++i) {
      const int t = 4 * par + i;
      const f32x4 v = (yk[i] + lds_ld<f32x4>(smem, S_EX + (w ^ 1) * 4096 + i * 1024 + lane * 16)) * inv_sk;
      const int l0 = 256 * t + 16 * c15 + 4 * g;
      const u32x2 uu = lds_ld<u32x2>(smem, S_U + kch * U_CH + (240 + l0) * 2);
      const f16x2 u01 = ash2(uu.x), u23 = ash2(uu.y);
      const u32 yb0 = pk_bf16(v[0], v[1]), yb1 = pk_bf16(v[2], v[3]);
      const u32 d0 = pk_bf16((float)u01.x * dsk, (float)u01.y * dsk), d1 = pk_bf16((float)u23.x * dsk, (float)u23.y * dsk);
      const u32x2 o = {pk_bf16(bflo(yb0) + bflo(d0), bfhi(yb0) + bfhi(d0)), pk_bf16(bflo(yb1) + bflo(d1), bfhi(yb1) + bfhi(d1))};
      st64c(rWS, o, WS_A + ((4 * t + (c15 >> 2)) * 512 + kd) * 128 + ((c15 & 3) * 16 + 4 * g) * 2);
    }
    wait_vm<0>();
    bar();
    st32c(rWS, epoch, tid == 0 ? WS_AF + cp * 4 : OOB);
  }
  STAMP(7);

  {
    const int mtp = mt2 >> 1, wp = 2 * (mt2 & 1) + mh;
    {
      const u32 fo = WS_HF + ((mtp * 4 + wp) * 16 + c15) * 4;
      u32 hf = ld32c(sWS, fo, 0);
      asm volatile("s_waitcnt vmcnt(0)" : "+v"(hf)::"memory");
      spin_flag(sWS, fo, epoch, hf, kh ? c15 >= 8 : c15 <= 8);
    }
    const u32 hb = WS_H + ((mt2 * 4 + 2 * mh) * NUNIT + kh * 22) * 1024;
    u32x4 hf[22][2];
#pragma unroll
    for (int j = 0; j < 22; ++j)
#pragma unroll
      for (int tt = 0; tt < 2; ++tt) hf[j][tt] = ld128c(sWS, (j == 21 && kh) ? OOB : lane * 16, hb + (tt * NUNIT + j) * 1024);
    STAMP(8);
    const int q4 = (lane & 15) >> 2, p4 = lane & 3, m4 = (2 * g + (q4 >> 1)) & 3;
    u32 wa[4];
#pragma unroll
    for (int n = 0; n < 4; ++n) wa[n] = lb + W2R + kh * W2_HALF + (4 * g + q4) * 128 + ((n ^ m4) * 32) + 8 * p4;
    f32x4 acc[4][2];
    u32x2 wl[2][4], wh[2][4];
    auto rd = [&](auto J, auto R) {
      constexpr int j = J, n = R >> 1, off = (j % W2_NS) * 4096 + (R & 1) * 2048;
      if constexpr ((R & 1) == 0) wl[j & 1][n] = a_dstr<off>(wa[n]); else wh[j & 1][n] = a_dstr<off>(wa[n]);
    };
    auto unit = [&](auto J, auto LAST) {
      constexpr int j = J;
      sfor<8>([&](auto I) {
        constexpr int i = I, n = i >> 1, tt = i & 1;
        if constexpr (tt == 0) wait_lgkm<(LAST ? 6 - 2 * n : 6)>();
        a_mfma_bf<j == 0>(acc[n][tt], cat4(wl[j & 1][n], wh[j & 1][n]), hf[j][tt]);
        if constexpr (!LAST) rd(ic<j + 1>{}, I);
      });
    };
    wait_vm<0>();
#pragma unroll
    for (int j = 0; j < 22; ++j) { tie(hf[j][0]); tie(hf[j][1]); }
    bar();
    sfor<8>([&](auto R) { rd(ic<0>{}, R); });
    sfor<8>([&](auto J) { unit(J, ic<0>{}); });
    bar();
#pragma unroll
    for (int j = 14; j < 22; ++j) stage2(j);
    sfor<5>([&](auto J) { unit(ic<8 + J>{}, ic<0>{}); });
    u32 af = ld32c(sWS, WS_AF + (32 * nt2 + (lane & 31)) * 4, 0);
    asm volatile("s_waitcnt vmcnt(0)" : "+v"(af)::"memory");
    bar();
    sfor<8>([&](auto J) { unit(ic<13 + J>{}, ic<0>{}); });
    unit(ic<21>{}, ic<1>{});
    mfma_settle4(acc[0][0], acc[0][1], acc[1][0], acc[1][1]); mfma_settle4(acc[2][0], acc[2][1], acc[3][0], acc[3][1]);
    spin_flag(sWS, WS_AF + (32 * nt2 + (lane & 31)) * 4, epoch, af, true);
    u32x4 at0 = ld128c(sWS, WS_A + (mt2 * 512 + nt2 * 64) * 128 + tid * 16, 0), at1 = ld128c(sWS, WS_A + (mt2 * 512 + nt2 * 64) * 128 + 4096 + tid * 16, 0);
    const int tok = 64 * mt2 + 32 * mh + 16 * kh + c15;
    u32x2 xv[4], b2v[4];
    const u32x4 sB2 = mk_srd(p.b2, DM * 2);
#pragma unroll
    for (int n = 0; n < 4; ++n) {
      xv[n] = ld64(sX, tok * 1024 + (64 * nt2 + 16 * n + 4 * g) * 2, 0);
      b2v[n] = ld64(sB2, (64 * nt2 + 16 * n + 4 * g) * 2, 0);
    }
    bar();
    f32x4 as[4], ak[4];
    if (kh) { as[0] = acc[0][0]; as[1] = acc[1][0]; as[2] = acc[2][0]; as[3] = acc[3][0]; ak[0] = acc[0][1]; ak[1] = acc[1][1]; ak[2] = acc[2][1]; ak[3] = acc[3][1]; }
    else { as[0] = acc[0][1]; as[1] = acc[1][1]; as[2] = acc[2][1]; as[3] = acc[3][1]; ak[0] = acc[0][0]; ak[1] = acc[1][0]; ak[2] = acc[2][0]; ak[3] = acc[3][0]; }
#pragma unroll
    for (int n = 0; n < 4; ++n) lds_st<f32x4>(smem, S_EX + w * 4096 + n * 1024 + lane * 16, as[n]);
    asm volatile("s_waitcnt vmcnt(0)" : "+v"(at0), "+v"(at1), "+v"(xv[0]), "+v"(xv[1]), "+v"(xv[2]), "+v"(xv[3]), "+v"(b2v[0]), "+v"(b2v[1]), "+v"(b2v[2]), "+v"(b2v[3])::"memory");
    {
      const int row0 = tid >> 3, row1 = 32 + row0, sl = tid & 7;
      lds_st<u32x4>(smem, S_AT + row0 * 128 + (((sl >> 1) ^ ((row0 >> 1) & 3)) * 32) + (sl & 1) * 16, at0);
      lds_st<u32x4>(smem, S_AT + row1 * 128 + (((sl >> 1) ^ ((row1 >> 1) & 3)) * 32) + (sl & 1) * 16, at1);
    }
    bar();
    const int tb = 2 * mh + kh;
#pragma unroll
    for (int n = 0; n < 4; ++n) {
      const f32x4 v = ak[n] + lds_ld<f32x4>(smem, S_EX + (w ^ 2) * 4096 + n * 1024 + lane * 16);
      const int row = 16 * n + 4 * g + q4;
      const u32x2 av = __builtin_bit_cast(u32x2, lds_tr(smem, S_AT + row * 128 + ((tb ^ ((row >> 1) & 3)) * 32) + 8 * p4));
      const u32 m0 = pk_bf16(v[0], v[1]), m1 = pk_bf16(v[2], v[3]);
      const u32 B0 = pk_bf16(bflo(m0) + bflo(b2v[n].x), bfhi(m0) + bfhi(b2v[n].x)), B1 = pk_bf16(bflo(m1) + bflo(b2v[n].y), bfhi(m1) + bfhi(b2v[n].y));
      const u32 r0 = pk_bf16(bflo(xv[n].x) + bflo(av.x), bfhi(xv[n].x) + bfhi(av.x)), r1 = pk_bf16(bflo(xv[n].y) + bflo(av.y), bfhi(xv[n].y) + bfhi(av.y));
      const u32x2 o = {pk_bf16(bflo(r0) + bflo(B0), bfhi(r0) + bfhi(B0)), pk_bf16(bflo(r1) + bflo(B1), bfhi(r1) + bfhi(B1))};
      st64(rOUT, o, tok * 1024 + (64 * nt2 + 16 * n + 4 * g) * 2);
    }
  }
  STAMP(9);
}

extern "C" void kernel_launch(void* const* d_in, const int* in_sizes, int n_in, void* d_out, int out_size, void* d_ws,
                              size_t ws_size, hipStream_t stream) {
  Params p;
  p.x = (const u16*)d_in[0]; p.lar = (const u16*)d_in[1]; p.aim = (const u16*)d_in[2]; p.cre = (const u16*)d_in[3];
  p.cim = (const u16*)d_in[4]; p.ldt = (const u16*)d_in[5]; p.dsk = (const u16*)d_in[6];
  p.w1 = (const u16*)d_in[8]; p.b1 = (const u16*)d_in[9]; p.w2 = (const u16*)d_in[10]; p.b2 = (const u16*)d_in[11];
  p.out = (u16*)d_out; p.ws = (char*)d_ws;
  s4d_mixer_fused<<<256, 256, 0, stream>>>(p);
}
```

```cpp
#include <hip/hip_runtime.h>
#include <stdint.h>
#include <utility>

namespace {
typedef unsigned short u16;
typedef unsigned u32;
typedef unsigned long long u64;
typedef float f32x2 __attribute__((ext_vector_type(2)));
typedef float f32x4 __attribute__((ext_vector_type(4)));
typedef u32 u32x2 __attribute__((ext_vector_type(2)));
typedef u32 u32x4 __attribute__((ext_vector_type(4)));
typedef short s16x4 __attribute__((ext_vector_type(4)));
typedef short s16x8 __attribute__((ext_vector_type(8)));
typedef __bf16 bf16x2 __attribute__((ext_vector_type(2)));
typedef __bf16 bf16x8 __attribute__((ext_vector_type(8)));
typedef _Float16 f16x2 __attribute__((ext_vector_type(2)));
typedef _Float16 f16x8 __attribute__((ext_vector_type(8)));
#define LAS __attribute__((address_space(3)))
#define DEV __device__ __forceinline__
typedef __amdgpu_buffer_rsrc_t rsrc_t;

constexpr int SEQ = 2048, DM = 512, NMODE = 128, FF = 1376, NUNIT = 43;
constexpr u32 WS_E = 0, WS_LNF = 0x1000, WS_HF = 0x2000, WS_AF = 0x4000, WS_STATS = 0x8000, WS_XN = 0x10000,
              WS_A = 0x210000, WS_H = 0x410000, WS_END = WS_H + 128 * NUNIT * 1024, WS_STAMP = 0xA00000;
constexpr u32 SLOT = 28672, SLOT_W = 16384;
constexpr u32 W2R = 0, W2_NS = 14, W2_HALF = W2_NS * 4096;
constexpr u32 S0 = 114688;
constexpr u32 S_LN = S0, LN_ROW = 1088;
constexpr u32 S_TAB = S0, TAB_ROW = 288, TAB_WAVE = 32 * TAB_ROW;
constexpr u32 S_KP = S0 + 36864;
constexpr u32 S_KC = S0 + 43008, KC_CH = 1296;
constexpr u32 S_CS = S0 + 45600;
constexpr u32 S_MISC = S0 + 47520;
constexpr u32 S_G = S0, G_PAR = 4160, G_CH = 8320;
constexpr u32 S_U = S0 + 16640, U_CH = 4608;
constexpr u32 S_EX = S0;
constexpr u32 S_AT = S0 + 25856;
constexpr u32 LDS_BYTES = 163840;
constexpr u32 OOB = 0x80000000u;

#define STAMP(k) do {} while (0)

struct Params {
  const u16 *x, *lar, *aim, *cre, *cim, *ldt, *dsk, *w1, *b1, *w2, *b2;
  u16* out;
  char* ws;
};

DEV float asf(u32 v) { return __builtin_bit_cast(float, v); }
DEV u32 asu(float v) { return __builtin_bit_cast(u32, v); }
DEV f16x2 ash2(u32 v) { return __builtin_bit_cast(f16x2, v); }
DEV float bflo(u32 v) { return asf(v << 16); }
DEV float bfhi(u32 v) { return asf(v & 0xffff0000u); }
DEV u32 pk_bf16(float a, float b) { f32x2 v = {a, b}; return __builtin_bit_cast(u32, __builtin_convertvector(v, bf16x2)); }
DEV u32 pk_f16(float a, float b) { f32x2 v = {a, b}; return __builtin_bit_cast(u32, __builtin_convertvector(v, f16x2)); }
DEV float rbf(float a) { return bflo(pk_bf16(a, a)); }
DEV float bf1(u16 v) { return asf((u32)v << 16); }

template <int N> DEV void wait_vm() { asm volatile("s_waitcnt vmcnt(%0)" ::"n"(N) : "memory"); }
DEV void bar() { asm volatile("s_waitcnt lgkmcnt(0)\n\ts_barrier" ::: "memory"); }
DEV u32x4 mk_srd(const void* base, u32 bytes) {
  u64 b = (u64)base;
  u32x4 s;
  s.x = __builtin_amdgcn_readfirstlane((u32)b);
  s.y = __builtin_amdgcn_readfirstlane((u32)(b >> 32)) & 0xffffu;
  s.z = bytes;
  s.w = 0x00020000u;
  return s;
}
DEV rsrc_t mk_rsrc(const void* base, u32 bytes) { return __builtin_amdgcn_make_buffer_rsrc((void*)base, 0, bytes, 0x00020000); }

#define BLOAD(INS, TY, NAME, FLAGS)                                                                          \
  DEV TY NAME(u32x4 srd, u32 voff, u32 soff) {                                                               \
    TY r;                                                                                                    \
    asm volatile(INS " %0, %1, %2, %3 offen" FLAGS : "=v"(r) : "v"(voff), "s"(srd), "s"(soff) : "memory");    \
    return r;                                                                                                \
  }
BLOAD("buffer_load_dwordx4", u32x4, ld128, "")
BLOAD("buffer_load_dwordx4", u32x4, ld128c, " sc1")
BLOAD("buffer_load_dwordx2", u32x2, ld64, "")
BLOAD("buffer_load_dword", u32, ld32c, " sc1")
BLOAD("buffer_load_ushort", u32, ld16, "")
template <class T> DEV void tie(T& v) { asm volatile("" : "+v"(v)); }

DEV void dma_m0(u32 lds_addr) { asm volatile("s_mov_b32 m0, %0" ::"s"(lds_addr) : "memory"); }
DEV void dma_go(u32x4 srd, u32 voff, u32 soff) { asm volatile("buffer_load_dwordx4 %0, %1, %2 offen lds" ::"v"(voff), "s"(srd), "s"(soff) : "memory"); }
DEV void dma16(u32x4 srd, u32 lds_addr, u32 voff, u32 soff) {
  asm volatile("s_mov_b32 m0, %0\n\ts_nop 0\n\tbuffer_load_dwordx4 %1, %2, %3 offen lds" ::"s"(lds_addr), "v"(voff), "s"(srd), "s"(soff) : "memory");
}
template <int I> using ic = std::integral_constant<int, I>;
template <class F, int... Is> DEV void sfor_impl(F&& f, std::integer_sequence<int, Is...>) { (f(ic<Is>{}), ...); }
template <int N, class F> DEV void sfor(F&& f) { sfor_impl(f, std::make_integer_sequence<int, N>{}); }
template <int OFF> DEV u32x4 a_ds128(u32 addr) { u32x4 r; asm volatile("ds_read_b128 %0, %1 offset:%2" : "=v"(r) : "v"(addr), "n"(OFF)); return r; }
template <int OFF> DEV u32x2 a_dstr(u32 addr) { u32x2 r; asm volatile("ds_read_b64_tr_b16 %0, %1 offset:%2" : "=v"(r) : "v"(addr), "n"(OFF)); return r; }
template <int O0, int O1> DEV u32x2 a_ds2x32(u32 addr) { u32x2 r; asm volatile("ds_read2_b32 %0, %1 offset0:%2 offset1:%3" : "=v"(r) : "v"(addr), "n"(O0), "n"(O1)); return r; }
template <int N> DEV void wait_lgkm() { asm volatile("s_waitcnt lgkmcnt(%0)" ::"n"(N)); }
DEV u32x4 cat4(u32x2 a, u32x2 b) { return __builtin_shufflevector(a, b, 0, 1, 2, 3); }
template <bool INIT> DEV void a_mfma_bf(f32x4& c, u32x4 a, u32x4 b) {
  if constexpr (INIT) asm volatile("v_mfma_f32_16x16x32_bf16 %0, %1, %2, 0" : "=&v"(c) : "v"(a), "v"(b));
  else asm volatile("v_mfma_f32_16x16x32_bf16 %0, %1, %2, %0" : "+v"(c) : "v"(a), "v"(b));
}
template <bool INIT> DEV void a_mfma_h(f32x4& c, u32x4 a, u32x4 b) {
  if constexpr (INIT) asm volatile("v_mfma_f32_16x16x32_f16 %0, %1, %2, 0" : "=&v"(c) : "v"(a), "v"(b));
  else asm volatile("v_mfma_f32_16x16x32_f16 %0, %1, %2, %0" : "+v"(c) : "v"(a), "v"(b));
}
DEV void a_dot2_ones(float& acc, u32 v) { asm volatile("v_dot2c_f32_bf16 %0, 0x3f803f80, %1" : "+v"(acc) : "v"(v)); }
DEV void mfma_settle4(f32x4& a, f32x4& b, f32x4& c, f32x4& d) { asm volatile("s_nop 7\n\ts_nop 1" : "+v"(a), "+v"(b), "+v"(c), "+v"(d)); }
DEV void st128c(rsrc_t r, u32x4 v, u32 voff) { __builtin_amdgcn_raw_buffer_store_b128(v, r, voff, 0, 16); }
DEV void st64c(rsrc_t r, u32x2 v, u32 voff) { __builtin_amdgcn_raw_buffer_store_b64(v, r, voff, 0, 16); }
DEV void st32c(rsrc_t r, u32 v, u32 voff) { __builtin_amdgcn_raw_buffer_store_b32(v, r, voff, 0, 16); }
DEV void st64(rsrc_t r, u32x2 v, u32 voff) { __builtin_amdgcn_raw_buffer_store_b64(v, r, voff, 0, 0); }

template <class T> DEV T lds_ld(const char* smem, u32 off) { return *(const LAS T*)(smem + off); }
template <class T> DEV void lds_st(char* smem, u32 off, T v) { *(LAS T*)(smem + off) = v; }
DEV s16x4 lds_tr(const char* smem, u32 off) { return __builtin_amdgcn_ds_read_tr16_b64_v4i16((LAS s16x4*)(smem + off)); }
DEV s16x8 cat8(s16x4 a, s16x4 b) { return __builtin_shufflevector(a, b, 0, 1, 2, 3, 4, 5, 6, 7); }
DEV f32x4 mfma_bf(s16x8 a, u32x4 b, f32x4 c) {
  return __builtin_amdgcn_mfma_f32_16x16x32_bf16(__builtin_bit_cast(bf16x8, a), __builtin_bit_cast(bf16x8, b), c, 0, 0, 0);
}
DEV f32x4 mfma_h(u32x4 a, u32x4 b, f32x4 c) {
  return __builtin_amdgcn_mfma_f32_16x16x32_f16(__builtin_bit_cast(f16x8, a), __builtin_bit_cast(f16x8, b), c, 0, 0, 0);
}

template <int CTRL, int RM> DEV float dpp_add(float v) {
  return v + asf(__builtin_amdgcn_update_dpp(0, asu(v), CTRL, RM, 0xf, true));
}
DEV float wave_sum(float v) {
  v = dpp_add<0x111, 0xf>(v); v = dpp_add<0x112, 0xf>(v); v = dpp_add<0x114, 0xf>(v); v = dpp_add<0x118, 0xf>(v);
  v = dpp_add<0x142, 0xa>(v); v = dpp_add<0x143, 0xc>(v);
  return asf(__builtin_amdgcn_readlane(asu(v), 63));
}
DEV float sum_rows(float v) {
  auto a = __builtin_amdgcn_permlane16_swap(asu(v), asu(v), false, false);
  v = asf(a[0]) + asf(a[1]);
  auto b = __builtin_amdgcn_permlane32_swap(asu(v), asu(v), false, false);
  return asf(b[0]) + asf(b[1]);
}
DEV f32x2 cmul(f32x2 a, f32x2 b) {
  f32x2 t, r;
  asm("v_pk_mul_f32 %0, %2, %3 op_sel_hi:[1,0]\n\tv_pk_fma_f32 %1, %2, %3, %0 op_sel:[1,1,0] op_sel_hi:[0,1,1] neg_lo:[1,0,0]"
      : "=&v"(t), "=v"(r) : "v"(a), "v"(b));
  return r;
}
DEV float gelu_tanh(float v) {
  const float c1 = -2.3022081986f, c2 = -0.10294323970f;
  float s = v * __builtin_fmaf(v * v, c2, c1);
  return v * __builtin_amdgcn_rcpf(1.0f + __builtin_amdgcn_exp2f(s));
}

DEV void spin_flag(u32x4 srd, u32 voff, u32 epoch, u32 val, bool need) {
  for (int it = 0; it < (1 << 16); ++it) {
    if (__all(!need || val == epoch)) break;
    __builtin_amdgcn_s_sleep(1);
    val = ld32c(srd, voff, 0);
    asm volatile("s_waitcnt vmcnt(0)" : "+v"(val)::"memory");
  }
}

}

__global__ __launch_bounds__(256, 1) void s4d_mixer_fused(Params p) {
  __shared__ __attribute__((aligned(16))) char smem[LDS_BYTES];
  const int tid = threadIdx.x, lane = tid & 63;
  const int w = __builtin_amdgcn_readfirstlane(tid >> 6);
  const int b = blockIdx.x, g8 = b & 7, r = b >> 3;
  const int c15 = lane & 15, g = lane >> 4;
  const int mt = 4 * (g8 >> 1) + (r >> 3), nt = 8 * (g8 & 1) + (r & 7);
  const int ustart = (nt * NUNIT) >> 4, unext = ((nt + 1) * NUNIT) >> 4;
  const int tg = 16 * mt + nt;
  const int cp = 32 * g8 + r;
  const int mt2 = 4 * g8 + (r >> 3), nt2 = r & 7;

  const u32x4 sX = mk_srd(p.x, SEQ * DM * 2), sWS = mk_srd(p.ws, WS_END), sW1 = mk_srd(p.w1, DM * FF * 2), sW2 = mk_srd(p.w2, FF * DM * 2);
  const rsrc_t rWS = mk_rsrc(p.ws, WS_END), rOUT = mk_rsrc(p.out, SEQ * DM * 2);
  const u32 lb = (u32)(uintptr_t)(LAS char*)smem;
  STAMP(0);

  u32x4 pf0;
  {
    u64 pc;
    asm volatile("s_getpc_b64 %0" : "=s"(pc));
    const u64 a = (pc & ~(u64)63) + (u32)((r % 7) * 4096 + tid * 16);
    asm volatile("global_load_dwordx4 %0, %1, off" : "=&v"(pf0) : "v"(a) : "memory");
  }
  u32 eword = ld32c(sWS, WS_E, 0);
  u32x4 xr0 = ld128(sX, lane * 16, (8 * tg + 2 * w) * 1024), xr1 = ld128(sX, lane * 16, (8 * tg + 2 * w + 1) * 1024);
  u32 vx[2], vw[3];
  {
    const int rr = lane >> 3;
    vx[0] = rr * 1024 + (((lane & 7) ^ ((lane >> 4) & 7)) * 16);
    vx[1] = rr * 1024 + (((lane & 7) ^ ((4 + (lane >> 4)) & 7)) * 16);
#pragma unroll
    for (int j = 0; j < 3; ++j) {
      const int P = (3 * w + j) * 64 + lane, k = P / 12, sl = P - 12 * k;
      const int c = (sl >> 1) ^ ((k >> 3) & 1);
      vw[j] = k * (FF * 2) + (16 * c + 8 * (sl & 1)) * 2;
    }
  }
  const u32 x_so = (mt * 128 + 32 * w) * 1024, x_lo = lb + 32 * w * 128, w_so = ustart * 64, w_lo = lb + SLOT_W + 3 * w * 1024;
  auto st1_lds = [&](int kc, int j) -> u32 { return (kc & 3) * SLOT + (j < 4 ? x_lo + j * 1024 : w_lo + (j - 4) * 1024); };
  auto st1_go = [&](int kc, int j) {
    if (j < 4) dma_go(sX, vx[j & 1], x_so + j * 8192 + kc * 128);
    else dma_go(sW1, vw[j - 4], w_so + kc * 64 * (FF * 2));
  };
  sfor<4>([&](auto KC) { sfor<7>([&](auto J) {
    dma_m0(st1_lds(KC, J)); asm volatile("s_nop 0"); st1_go(KC, J); }); });
  asm volatile("s_waitcnt vmcnt(28)" : "+v"(eword), "+v"(xr0), "+v"(xr1), "+v"(pf0)::"memory");
  const u32 epoch = __builtin_amdgcn_readfirstlane(eword) * 0x9E3779B1u + 0x7F4A7C15u;
  STAMP(1);

  {
    u32x4 xr[2] = {xr0, xr1};
    float al[2], be[2];
#pragma unroll
    for (int t = 0; t < 2; ++t) {
      float x[8];
#pragma unroll
      for (int i = 0; i < 4; ++i) { x[2 * i] = bflo(xr[t][i]); x[2 * i + 1] = bfhi(xr[t][i]); }
      const float x0 = asf(__builtin_amdgcn_readfirstlane(asu(x[0])));
      float s = 0.f, q = 0.f;
#pragma unroll
      for (int i = 0; i < 8; ++i) { const float d = x[i] - x0; s += d; q = __builtin_fmaf(d, d, q); }
      const float md = wave_sum(s) * (1.0f / DM);
      const float var = fmaxf(__builtin_fmaf(-md, md, wave_sum(q) * (1.0f / DM)), 0.f);
      const float m1 = rbf(x0 + md);
      const float s1 = rbf(__builtin_amdgcn_sqrtf(rbf(rbf(var) + 1e-5f)));
      const float r1 = __builtin_amdgcn_rcpf(s1);
      float s2 = 0.f, q2 = 0.f;
      u32x4 hv;
#pragma unroll
      for (int i = 0; i < 4; ++i) {
        const u32 a = pk_bf16(x[2 * i] - m1, x[2 * i + 1] - m1);
        const u32 n = pk_bf16(bflo(a) * r1, bfhi(a) * r1);
        const float n0 = bflo(n), n1 = bfhi(n);
        s2 += n0 + n1; q2 = __builtin_fmaf(n0, n0, q2); q2 = __builtin_fmaf(n1, n1, q2);
        hv[i] = pk_f16(n0, n1);
      }
      lds_st<u32x4>(smem, S_LN + (2 * w + t) * LN_ROW + lane * 16, hv);
      const float mean2 = wave_sum(s2) * (1.0f / DM);
      const float var2 = fmaxf(__builtin_fmaf(-mean2, mean2, wave_sum(q2) * (1.0f / DM)), 0.f);
      const float m2 = rbf(mean2);
      const float r2 = __builtin_amdgcn_rcpf(rbf(__builtin_amdgcn_sqrtf(rbf(rbf(var2) + 1e-5f))));
      al[t] = r1 * r2;
      be[t] = __builtin_fmaf(m1, r1, m2) * r2;
    }
    {
      u32x4 sv = {asu(lane ? al[1] : al[0]), epoch, asu(lane ? be[1] : be[0]), epoch};
      st128c(rWS, sv, lane < 2 ? WS_STATS + (8 * tg + 2 * w + lane) * 16 : OOB);
    }
    bar();
#pragma unroll
    for (int i = 0; i < 2; ++i) {
      const int ch0 = (2 * w + i) * 64 + 16 * g;
      const u32 a = S_LN + ((lane & 15) >> 2) * LN_ROW + (ch0 + 4 * (lane & 3)) * 2;
      const s16x8 v = cat8(lds_tr(smem, a), lds_tr(smem, a + 4 * LN_ROW));
      st128c(rWS, __builtin_bit_cast(u32x4, v), WS_XN + tg * 8192 + ((2 * w + i) * 64 + lane) * 16);
    }
  }
  STAMP(2);
  const u32x4 sB1 = mk_srd(p.b1, FF * 2);
  u32x2 b1v[6];
#pragma unroll
  for (int T = 0; T < 6; ++T) b1v[T] = ld64(sB1, (16 * T + 4 * g) * 2, ustart * 64);
  const int kch = w >> 1, kd = 2 * cp + kch;
  u32 q_lar, q_aim, q_cre, q_cim;
  {
    const u32 vo = (64 * (w & 1) + lane) * 2, so = kd * NMODE * 2;
    q_lar = ld16(mk_srd(p.lar, DM * NMODE * 2), vo, so); q_aim = ld16(mk_srd(p.aim, DM * NMODE * 2), vo, so);
    q_cre = ld16(mk_srd(p.cre, DM * NMODE * 2), vo, so); q_cim = ld16(mk_srd(p.cim, DM * NMODE * 2), vo, so);
  }

  f32x4 acc1[6][2];
  float csp[3] = {0.f, 0.f, 0.f};
  {
    const int q4 = (lane & 15) >> 2, p4 = lane & 3;
    const u32 xa0 = lb + (32 * w + c15) * 128 + ((g ^ (c15 >> 1)) * 16);
    const u32 wrow = lb + SLOT_W + (8 * g + q4) * 192 + 8 * p4;
    const u32 xa[2][2] = {{xa0, xa0 ^ 64}, {xa0 + 2 * SLOT, (xa0 ^ 64) + 2 * SLOT}};
    const u32 wa[2][2] = {{wrow + (g & 1) * 32, wrow - (g & 1) * 32}, {wrow + (g & 1) * 32 + 2 * SLOT, wrow - (g & 1) * 32 + 2 * SLOT}};
    u32 wc[2][3];
#pragma unroll
    for (int i = 0; i < 3; ++i) { wc[0][i] = wrow + (((3 * (w & 1) + i) ^ (g & 1)) * 32) + (w >> 1) * 32 * 192; wc[1][i] = wc[0][i] + 2 * SLOT; }
    u32x4 xf[2][2];
    u32x2 wl[2][6], wh[2][6], cl[3], chh[3];
    auto rd = [&](auto S, auto R) {
      constexpr int s = S, r = R, buf = s & 1, sl = (s >> 1) & 3, so = (sl & 1) * SLOT, sp = s & 1;
      if constexpr (r < 2) xf[buf][r] = a_ds128<so + r * 2048>(xa[sl >> 1][sp]);
      else {
        constexpr int T = (r - 2) >> 1, hf = (r - 2) & 1, off = so + T * 32 + (sp * 32 + hf * 4) * 192;
        if constexpr (hf == 0) wl[buf][T] = a_dstr<off>(wa[sl >> 1][T & 1]); else wh[buf][T] = a_dstr<off>(wa[sl >> 1][T & 1]);
      }
    };
    wait_vm<34>();
    bar();
    sfor<14>([&](auto R) { rd(ic<0>{}, R); });
    sfor<16>([&](auto S) {
      constexpr int s = S, buf = s & 1, kc = (s + 1) >> 1;
      if constexpr ((s & 1) && kc < 8) {
        wait_vm<(kc <= 3 ? 27 : (kc <= 5 ? 14 : (kc == 6 ? 7 : 0)))>();
        bar();
        if constexpr (kc == 4) st32c(rWS, epoch, tid == 0 ? WS_LNF + tg * 4 : OOB);
      } else wait_lgkm<0>();
      sfor<12>([&](auto I) {
        constexpr int i = I, T = i >> 1, tt = i & 1;
        constexpr int sc = (s & 1) ? kc + 3 : (s >> 1) + 3;
        constexpr int pj = (s & 1) ? ((i % 3 == 0) ? i / 3 : -1) : ((i % 4 == 1) ? 4 + i / 4 : -1);
        constexpr bool st = pj >= 0 && pj < 7 && sc >= 4 && sc < 8 && s >= 1;
        if constexpr (st) dma_m0(st1_lds(sc, pj));
        a_mfma_bf<s == 0>(acc1[T][tt], cat4(wl[buf][T], wh[buf][T]), xf[buf][tt]);
        if constexpr (st) st1_go(sc, pj);
        if constexpr (s + 1 < 16 && i < 7) { rd(ic<s + 1>{}, ic<2 * i>{}); rd(ic<s + 1>{}, ic<2 * i + 1>{}); }
        if constexpr (!(s & 1) && i >= 7 && i < 10) {
          constexpr int sl = (s >> 1) & 3, so = (sl & 1) * SLOT;
          cl[i - 7] = a_dstr<so>(wc[sl >> 1][i - 7]); chh[i - 7] = a_dstr<so + 4 * 192>(wc[sl >> 1][i - 7]);
        }
        if constexpr ((s & 1) && i >= 7 && i < 10) {
          a_dot2_ones(csp[i - 7], cl[i - 7].x); a_dot2_ones(csp[i - 7], cl[i - 7].y);
          a_dot2_ones(csp[i - 7], chh[i - 7].x); a_dot2_ones(csp[i - 7], chh[i - 7].y);
        }
      });
    });
    mfma_settle4(acc1[0][0], acc1[0][1], acc1[1][0], acc1[1][1]); mfma_settle4(acc1[2][0], acc1[2][1], acc1[3][0], acc1[3][1]);
    mfma_settle4(acc1[4][0], acc1[4][1], acc1[5][0], acc1[5][1]);
    asm volatile("" : "+v"(csp[0]), "+v"(csp[1]), "+v"(csp[2]));
  }
  STAMP(3);
#pragma unroll
  for (int i = 0; i < 3; ++i) {
    const float v = sum_rows(csp[i]);
    if (lane < 16) lds_st<float>(smem, S_CS + (w * 48 + i * 16 + lane) * 4, v);
  }
  u32x4 st0 = ld128c(sWS, WS_STATS + (128 * mt + 32 * w + c15) * 16, 0), st1 = ld128c(sWS, WS_STATS + (128 * mt + 32 * w + 16 + c15) * 16, 0);
  u32 lnf = ld32c(sWS, WS_LNF + tid * 4, 0);
  asm volatile("s_waitcnt vmcnt(0)" : "+v"(st0), "+v"(st1), "+v"(lnf), "+v"(b1v[0]), "+v"(b1v[1]), "+v"(b1v[2]), "+v"(b1v[3]), "+v"(b1v[4]), "+v"(b1v[5]),
               "+v"(q_lar), "+v"(q_aim), "+v"(q_cre), "+v"(q_cim)::"memory");
  for (int it = 0; it < (1 << 16); ++it) {
    if (__all(st0.y == epoch && st0.w == epoch && st1.y == epoch && st1.w == epoch)) break;
    __builtin_amdgcn_s_sleep(1);
    st0 = ld128c(sWS, WS_STATS + (128 * mt + 32 * w + c15) * 16, 0);
    st1 = ld128c(sWS, WS_STATS + (128 * mt + 32 * w + 16 + c15) * 16, 0);
    asm volatile("s_waitcnt vmcnt(0)" : "+v"(st0), "+v"(st1)::"memory");
  }
  spin_flag(sWS, WS_LNF + tid * 4, epoch, lnf, true);
  u32x4 ug0 = ld128c(sWS, WS_XN + tid * 8192 + cp * 32, 0), ug1 = ld128c(sWS, WS_XN + tid * 8192 + cp * 32 + 16, 0);
  bar();
  st32c(rWS, epoch, (b == 0 && tid == 0) ? WS_E : OOB);
  STAMP(4);

  {
    const float al[2] = {asf(st0.x), asf(st1.x)}, be[2] = {asf(st0.z), asf(st1.z)};
#pragma unroll
    for (int uu = 0; uu < 3; ++uu) {
      float bias[8], cs[8];
#pragma unroll
      for (int hh = 0; hh < 2; ++hh) {
        const int T = 2 * uu + hh;
        const u32 o = S_CS + ((T / 3) * 48 + (T % 3) * 16 + 4 * g) * 4;
        const f32x4 c0 = lds_ld<f32x4>(smem, o), c1 = lds_ld<f32x4>(smem, o + 2 * 48 * 4);
#pragma unroll
        for (int i = 0; i < 4; ++i) cs[4 * hh + i] = c0[i] + c1[i];
        bias[4 * hh + 0] = bflo(b1v[T].x); bias[4 * hh + 1] = bfhi(b1v[T].x);
        bias[4 * hh + 2] = bflo(b1v[T].y); bias[4 * hh + 3] = bfhi(b1v[T].y);
      }
#pragma unroll
      for (int tt = 0; tt < 2; ++tt) {
        float hv[8];
#pragma unroll
        for (int j = 0; j < 8; ++j) {
          const float pre = __builtin_fmaf(al[tt], acc1[2 * uu + (j >> 2)][tt][j & 3], __builtin_fmaf(-be[tt], cs[j], bias[j]));
          hv[j] = gelu_tanh(pre);
        }
        const u32x4 o = {pk_bf16(hv[0], hv[1]), pk_bf16(hv[2], hv[3]), pk_bf16(hv[4], hv[5]), pk_bf16(hv[6], hv[7])};
        const u32 off = WS_H + ((mt * 8 + 2 * w + tt) * NUNIT + ustart + uu) * 1024 + lane * 16;
        st128c(rWS, o, ustart + uu < unext ? off : OOB);
      }
    }
  }
  const int kh = w >> 1, mh = w & 1;
  const u32 vw2 = (lane >> 3) * 1024 + ((((lane & 7) >> 1) ^ ((lane >> 4) & 3)) * 32) + (lane & 1) * 16;
  auto stage2 = [&](int j) {
    const int u = kh * 22 + j;
#pragma unroll
    for (int i = 0; i < 2; ++i)
      dma16(sW2, lb + W2R + kh * W2_HALF + (j % W2_NS) * 4096 + (2 * mh + i) * 1024, u < NUNIT ? vw2 : OOB, (u * 32 + (2 * mh + i) * 8) * 1024 + nt2 * 128);
  };
#pragma unroll
  for (int j = 0; j < 14; ++j) stage2(j);
  wait_vm<28>();
  st32c(rWS, epoch, lane == 0 ? WS_HF + ((mt * 4 + w) * 16 + nt) * 4 : OOB);
  STAMP(5);

  float inv_sk;
  {
    const int mh2 = w & 1;
    const float dt = rbf(expf(bf1(p.ldt[kd])));
    const float zr = -rbf(expf(bf1((u16)q_lar))) * dt, zi = bf1((u16)q_aim) * dt;
    f32x2 C = {2.0f * bf1((u16)q_cre) * dt, 2.0f * bf1((u16)q_cim) * dt};
    const float bw = wave_sum(__builtin_fabsf(C.x) + __builtin_fabsf(C.y));
    int se = 267 - (int)((asu(bw) >> 23) & 0xff);
    se = se < 1 ? 1 : (se > 253 ? 253 : se);
    C *= asf((u32)se << 23);
    const float inv_sx = asf((u32)(254 - se) << 23);
    const float e1 = zr * 1.44269504089f;
    const float ph = zi * 0.15915494309f;
    const float pl = __builtin_fmaf(zi, 0.15915494309f, -ph) + zi * 6.4206383e-9f;
    auto cexp = [&](float pw) {
      const float m = __builtin_amdgcn_exp2f(e1 * pw);
      const float a = __builtin_amdgcn_fractf(ph * pw) + pl * pw;
      return f32x2{m * __builtin_amdgcn_cosf(a), m * __builtin_amdgcn_sinf(a)};
    };
    f32x2 P[8], Y[16], E[5], CE[17];
    P[1] = cexp(1.f); Y[1] = cexp(8.f); E[0] = cexp(128.f);
    P[2] = cmul(P[1], P[1]); P[3] = cmul(P[2], P[1]); P[4] = cmul(P[2], P[2]);
    P[5] = cmul(P[4], P[1]); P[6] = cmul(P[4], P[2]); P[7] = cmul(P[4], P[3]);
    Y[0] = f32x2{1.f, 0.f};
    Y[2] = cmul(Y[1], Y[1]); Y[3] = cmul(Y[2], Y[1]); Y[4] = cmul(Y[2], Y[2]);
    Y[5] = cmul(Y[4], Y[1]); Y[6] = cmul(Y[4], Y[2]); Y[7] = cmul(Y[4], Y[3]); Y[8] = cmul(Y[4], Y[4]);
#pragma unroll
    for (int i = 1; i < 8; ++i) Y[8 + i] = cmul(Y[8], Y[i]);
#pragma unroll
    for (int i = 1; i < 5; ++i) E[i] = cmul(E[i - 1], E[i - 1]);
    CE[0] = C; CE[1] = cmul(C, E[0]);
    CE[2] = cmul(CE[0], E[1]); CE[3] = cmul(CE[1], E[1]);
#pragma unroll
    for (int i = 0; i < 4; ++i) CE[4 + i] = cmul(CE[i], E[2]);
#pragma unroll
    for (int i = 0; i < 8; ++i) CE[8 + i] = cmul(CE[i], E[3]);
    CE[16] = cmul(C, E[4]);
    const u32 tab = S_TAB + w * TAB_WAVE, tl = tab + lane * 4;
#pragma unroll
    for (int i = 0; i < 16; ++i) lds_st<u32>(smem, tl + i * TAB_ROW, pk_f16(Y[i].x, -Y[i].y));
    const u32 rd = tab + c15 * TAB_ROW + g * 16;
    u32x4 yf[4];
#pragma unroll
    for (int s = 0; s < 4; ++s) yf[s] = lds_ld<u32x4>(smem, rd + s * 64);
    f32x4 kd3[3];
#pragma unroll
    for (int tl3 = 0; tl3 < 3; ++tl3) {
#pragma unroll
      for (int i = 0; i < 16; ++i) {
        f32x2 v;
        if (tl3 == 0) v = (i & 7) ? cmul(CE[i >> 3], P[i & 7]) : CE[i >> 3];
        else if (tl3 == 1) {
          if (i < 8) v = (i & 3) ? cmul(CE[2 + (i >> 2)], P[2 * (i & 3)]) : CE[2 + (i >> 2)];
          else v = (i & 1) ? cmul(CE[4 + ((i - 8) >> 1)], P[4]) : CE[4 + ((i - 8) >> 1)];
        } else v = i < 8 ? CE[8 + i] : (i == 8 ? CE[16] : f32x2{0.f, 0.f});
        lds_st<u32>(smem, tl + (16 + i) * TAB_ROW, pk_f16(v.x, v.y));
      }
      f32x4 a = {0, 0, 0, 0};
#pragma unroll
      for (int s = 0; s < 4; ++s) a = mfma_h(lds_ld<u32x4>(smem, rd + 16 * TAB_ROW + s * 64), yf[s], a);
      kd3[tl3] = a * inv_sx;
    }
    if (mh2) {
#pragma unroll
      for (int t = 0; t < 3; ++t) lds_st<f32x4>(smem, S_KP + kch * 3072 + t * 1024 + lane * 16, kd3[t]);
      if (lane == 0) lds_st<float>(smem, S_MISC + kch * 4, bw);
    }
    bar();
    if (!mh2) {
      const float bd = bw + lds_ld<float>(smem, S_MISC + kch * 4);
      int sk = 267 - (int)((asu(bd) >> 23) & 0xff);
      sk = sk < 1 ? 1 : (sk > 253 ? 253 : sk);
      const float scale = asf((u32)sk << 23);
      if (lane == 0) lds_st<float>(smem, S_MISC + 8 + kch * 4, asf((u32)(254 - sk) << 23));
      const u32 kc = S_KC + kch * KC_CH;
#pragma unroll
      for (int t = 0; t < 3; ++t) {
        const f32x4 v = (kd3[t] + lds_ld<f32x4>(smem, S_KP + kch * 3072 + t * 1024 + lane * 16)) * scale;
#pragma unroll
        for (int i = 0; i < 4; ++i) {
          int idx;
          if (t == 0) idx = 128 * (g >> 1) + 8 * c15 + 4 * (g & 1) + i;
          else if (t == 1) idx = g < 2 ? 256 + 64 * g + 4 * c15 + i : 384 + 64 * (g - 2) + 32 * (i >> 1) + (i & 1) + 2 * c15;
          else idx = g < 2 ? 512 + 64 * g + 16 * i + c15 : ((g == 2 && i == 0 && c15 == 0) ? 640 : 644);
          lds_st<_Float16>(smem, kc + idx * 2, (_Float16)v[i]);
        }
      }
      if (lane == 0) lds_st<u16>(smem, kc + 641 * 2, (u16)0);
    }
    bar();
    u32 ix[9];
#pragma unroll
    for (int e = 0; e < 9; ++e) {
      const int l = 2047 - (8 * tid + e);
      const u32 fb = asu((float)l);
      const u32 rb = (fb + 0x7fffu + ((fb >> 16) & 1u)) >> 16;
      ix[e] = (l < 0 ? 641u : (l < 128 ? (u32)l : rb - 17024u)) * 2;
    }
#pragma unroll
    for (int ch = 0; ch < 2; ++ch) {
      u32 e[9];
#pragma unroll
      for (int i = 0; i < 9; ++i) e[i] = lds_ld<u16>(smem, S_KC + ch * KC_CH + ix[i]);
      const u32x4 a0 = {e[0] | (e[1] << 16), e[2] | (e[3] << 16), e[4] | (e[5] << 16), e[6] | (e[7] << 16)};
      const u32x4 a1 = {e[1] | (e[2] << 16), e[3] | (e[4] << 16), e[5] | (e[6] << 16), e[7] | (e[8] << 16)};
      lds_st<u32x4>(smem, S_G + ch * G_CH + tid * 16, a0);
      lds_st<u32x4>(smem, S_G + ch * G_CH + G_PAR + tid * 16, a1);
    }
    const u32x4 z4 = {0, 0, 0, 0};
    if (tid < 8) lds_st<u32x4>(smem, S_G + (tid >> 2) * G_CH + ((tid >> 1) & 1) * G_PAR + 4096 + (tid & 1) * 16, z4);
    if (tid < 60) lds_st<u32x4>(smem, S_U + (tid / 30) * U_CH + (tid % 30) * 16, z4);
    tie(ug0); tie(ug1);
    lds_st<u32x4>(smem, S_U + 480 + tid * 16, ug0);
    lds_st<u32x4>(smem, S_U + U_CH + 480 + tid * 16, ug1);
    bar();
    inv_sk = lds_ld<float>(smem, S_MISC + 8 + kch * 4);
  }
  STAMP(6);

  {
    const int par = w & 1;
    const u32 ub = lb + S_U + kch * U_CH + 64 - 64 * par + 32 * c15 + 16 * g;
    const u32 gb0 = lb + S_G + kch * G_CH + ((c15 & 1) ? 0 : G_PAR) + 4 * (1015 - (c15 >> 1) - 16 * par + 4 * g - 32 * 31);
    const u32 gb[4] = {gb0, gb0 + 1024, gb0 + 2048, gb0 + 3072};
    u32x4 bq[32];
    u32x2 al[32], ah[32];
    f32x4 y[8];
    auto rdB = [&](auto M, auto T) { constexpr int q = 4 * T - M + 3; bq[q] = a_ds128<128 * q>(ub); };
    auto rdA = [&](auto M, auto H) {
      constexpr int k = (31 - M) >> 3, o = 32 * ((31 - M) & 7);
      if constexpr (H == 0) al[M] = a_ds2x32<o, o + 1>(gb[k]); else ah[M] = a_ds2x32<o + 2, o + 3>(gb[k]);
    };
    sfor<2>([&](auto M) { sfor<8>([&](auto T) { rdB(M, T); }); rdA(M, ic<0>{}); rdA(M, ic<1>{}); });
    sfor<32>([&](auto M) {
      constexpr int m = M, t0 = m >> 2, n = 8 - t0;
      wait_lgkm<(m <= 2 ? 10 : (m == 3 ? 4 : (m == 4 ? 6 : (2 * (31 - m) < 8 ? 2 * (31 - m) : 8))))>();
      sfor<n>([&](auto I) {
        constexpr int i = I, t = t0 + i;
        a_mfma_h<m == 0>(y[t], cat4(al[m], ah[m]), bq[4 * t - m + 3]);
        if constexpr (m < 2) {
          rdB(ic<m + 2>{}, I);
          if constexpr (i >= 6) rdA(ic<m + 2>{}, ic<i - 6>{});
        } else if constexpr (m <= 4) {
          if constexpr (i < 4) rdA(ic<2 * m + (i >> 1)>{}, ic<i & 1>{});
        } else if constexpr (m + 5 < 32) {
          if constexpr (n == 1) { rdA(ic<m + 5>{}, ic<0>{}); rdA(ic<m + 5>{}, ic<1>{}); }
          else if constexpr (i < 2) rdA(ic<m + 5>{}, I);
        }
      });
    });
    mfma_settle4(y[0], y[1], y[2], y[3]); mfma_settle4(y[4], y[5], y[6], y[7]);
    bar();
    f32x4 ys[4], yk[4];
    if (par) { ys[0] = y[0]; ys[1] = y[1]; ys[2] = y[2]; ys[3] = y[3]; yk[0] = y[4]; yk[1] = y[5]; yk[2] = y[6]; yk[3] = y[7]; }
    else { ys[0] = y[4]; ys[1] = y[5]; ys[2] = y[6]; ys[3] = y[7]; yk[0] = y[0]; yk[1] = y[1]; yk[2] = y[2]; yk[3] = y[3]; }
#pragma unroll
    for (int i = 0; i < 4; ++i) lds_st<f32x4>(smem, S_EX + w * 4096 + i * 1024 + lane * 16, ys[i]);
    bar();
    const float dsk = bf1(p.dsk[kd]);
#pragma unroll
    for (int i = 0; i < 4; ++i) {
      const int t = 4 * par + i;
      const f32x4 v = (yk[i] + lds_ld<f32x4>(smem, S_EX + (w ^ 1) * 4096 + i * 1024 + lane * 16)) * inv_sk;
      const int l0 = 256 * t + 16 * c15 + 4 * g;
      const u32x2 uu = lds_ld<u32x2>(smem, S_U + kch * U_CH + (240 + l0) * 2);
      const f16x2 u01 = ash2(uu.x), u23 = ash2(uu.y);
      const u32 yb0 = pk_bf16(v[0], v[1]), yb1 = pk_bf16(v[2], v[3]);
      const u32 d0 = pk_bf16((float)u01.x * dsk, (float)u01.y * dsk), d1 = pk_bf16((float)u23.x * dsk, (float)u23.y * dsk);
      const u32x2 o = {pk_bf16(bflo(yb0) + bflo(d0), bfhi(yb0) + bfhi(d0)), pk_bf16(bflo(yb1) + bflo(d1), bfhi(yb1) + bfhi(d1))};
      st64c(rWS, o, WS_A + ((4 * t + (c15 >> 2)) * 512 + kd) * 128 + ((c15 & 3) * 16 + 4 * g) * 2);
    }
    wait_vm<0>();
    bar();
    st32c(rWS, epoch, tid == 0 ? WS_AF + cp * 4 : OOB);
  }
  STAMP(7);

  {
    const int mtp = mt2 >> 1, wp = 2 * (mt2 & 1) + mh;
    {
      const u32 fo = WS_HF + ((mtp * 4 + wp) * 16 + c15) * 4;
      u32 hf = ld32c(sWS, fo, 0);
      asm volatile("s_waitcnt vmcnt(0)" : "+v"(hf)::"memory");
      spin_flag(sWS, fo, epoch, hf, kh ? c15 >= 8 : c15 <= 8);
    }
    const u32 hb = WS_H + ((mt2 * 4 + 2 * mh) * NUNIT + kh * 22) * 1024;
    u32x4 hf[22][2];
#pragma unroll
    for (int j = 0; j < 22; ++j)
#pragma unroll
      for (int tt = 0; tt < 2; ++tt) hf[j][tt] = ld128c(sWS, (j == 21 && kh) ? OOB : lane * 16, hb + (tt * NUNIT + j) * 1024);
    STAMP(8);
    const int q4 = (lane & 15) >> 2, p4 = lane & 3, m4 = (2 * g + (q4 >> 1)) & 3;
    u32 wa[4];
#pragma unroll
    for (int n = 0; n < 4; ++n) wa[n] = lb + W2R + kh * W2_HALF + (4 * g + q4) * 128 + ((n ^ m4) * 32) + 8 * p4;
    f32x4 acc[4][2];
    u32x2 wl[2][4], wh[2][4];
    auto rd = [&](auto J, auto R) {
      constexpr int j = J, n = R >> 1, off = (j % W2_NS) * 4096 + (R & 1) * 2048;
      if constexpr ((R & 1) == 0) wl[j & 1][n] = a_dstr<off>(wa[n]); else wh[j & 1][n] = a_dstr<off>(wa[n]);
    };
    auto unit = [&](auto J, auto LAST) {
      constexpr int j = J;
      sfor<8>([&](auto I) {
        constexpr int i = I, n = i >> 1, tt = i & 1;
        if constexpr (tt == 0) wait_lgkm<(LAST ? 6 - 2 * n : 6)>();
        a_mfma_bf<j == 0>(acc[n][tt], cat4(wl[j & 1][n], wh[j & 1][n]), hf[j][tt]);
        if constexpr (!LAST) rd(ic<j + 1>{}, I);
      });
    };
    wait_vm<0>();
#pragma unroll
    for (int j = 0; j < 22; ++j) { tie(hf[j][0]); tie(hf[j][1]); }
    bar();
    sfor<8>([&](auto R) { rd(ic<0>{}, R); });
    sfor<8>([&](auto J) { unit(J, ic<0>{}); });
    bar();
#pragma unroll
    for (int j = 14; j < 22; ++j) stage2(j);
    sfor<5>([&](auto J) { unit(ic<8 + J>{}, ic<0>{}); });
    u32 af = ld32c(sWS, WS_AF + (32 * nt2 + (lane & 31)) * 4, 0);
    asm volatile("s_waitcnt vmcnt(0)" : "+v"(af)::"memory");
    bar();
    sfor<8>([&](auto J) { unit(ic<13 + J>{}, ic<0>{}); });
    unit(ic<21>{}, ic<1>{});
    mfma_settle4(acc[0][0], acc[0][1], acc[1][0], acc[1][1]); mfma_settle4(acc[2][0], acc[2][1], acc[3][0], acc[3][1]);
    spin_flag(sWS, WS_AF + (32 * nt2 + (lane & 31)) * 4, epoch, af, true);
    u32x4 at0 = ld128c(sWS, WS_A + (mt2 * 512 + nt2 * 64) * 128 + tid * 16, 0), at1 = ld128c(sWS, WS_A + (mt2 * 512 + nt2 * 64) * 128 + 4096 + tid * 16, 0);
    const int tok = 64 * mt2 + 32 * mh + 16 * kh + c15;
    u32x2 xv[4], b2v[4];
    const u32x4 sB2 = mk_srd(p.b2, DM * 2);
#pragma unroll
    for (int n = 0; n < 4; ++n) {
      xv[n] = ld64(sX, tok * 1024 + (64 * nt2 + 16 * n + 4 * g) * 2, 0);
      b2v[n] = ld64(sB2, (64 * nt2 + 16 * n + 4 * g) * 2, 0);
    }
    bar();
    f32x4 as[4], ak[4];
    if (kh) { as[0] = acc[0][0]; as[1] = acc[1][0]; as[2] = acc[2][0]; as[3] = acc[3][0]; ak[0] = acc[0][1]; ak[1] = acc[1][1]; ak[2] = acc[2][1]; ak[3] = acc[3][1]; }
    else { as[0] = acc[0][1]; as[1] = acc[1][1]; as[2] = acc[2][1]; as[3] = acc[3][1]; ak[0] = acc[0][0]; ak[1] = acc[1][0]; ak[2] = acc[2][0]; ak[3] = acc[3][0]; }
#pragma unroll
    for (int n = 0; n < 4; ++n) lds_st<f32x4>(smem, S_EX + w * 4096 + n * 1024 + lane * 16, as[n]);
    asm volatile("s_waitcnt vmcnt(0)" : "+v"(at0), "+v"(at1), "+v"(xv[0]), "+v"(xv[1]), "+v"(xv[2]), "+v"(xv[3]), "+v"(b2v[0]), "+v"(b2v[1]), "+v"(b2v[2]), "+v"(b2v[3])::"memory");
    {
      const int row0 = tid >> 3, row1 = 32 + row0, sl = tid & 7;
      lds_st<u32x4>(smem, S_AT + row0 * 128 + (((sl >> 1) ^ ((row0 >> 1) & 3)) * 32) + (sl & 1) * 16, at0);
      lds_st<u32x4>(smem, S_AT + row1 * 128 + (((sl >> 1) ^ ((row1 >> 1) & 3)) * 32) + (sl & 1) * 16, at1);
    }
    bar();
    const int tb = 2 * mh + kh;
#pragma unroll
    for (int n = 0; n < 4; ++n) {
      const f32x4 v = ak[n] + lds_ld<f32x4>(smem, S_EX + (w ^ 2) * 4096 + n * 1024 + lane * 16);
      const int row = 16 * n + 4 * g + q4;
      const u32x2 av = __builtin_bit_cast(u32x2, lds_tr(smem, S_AT + row * 128 + ((tb ^ ((row >> 1) & 3)) * 32) + 8 * p4));
      const u32 m0 = pk_bf16(v[0], v[1]), m1 = pk_bf16(v[2], v[3]);
      const u32 B0 = pk_bf16(bflo(m0) + bflo(b2v[n].x), bfhi(m0) + bfhi(b2v[n].x)), B1 = pk_bf16(bflo(m1) + bflo(b2v[n].y), bfhi(m1) + bfhi(b2v[n].y));
      const u32 r0 = pk_bf16(bflo(xv[n].x) + bflo(av.x), bfhi(xv[n].x) + bfhi(av.x)), r1 = pk_bf16(bflo(xv[n].y) + bflo(av.y), bfhi(xv[n].y) + bfhi(av.y));
      const u32x2 o = {pk_bf16(bflo(r0) + bflo(B0), bfhi(r0) + bfhi(B0)), pk_bf16(bflo(r1) + bflo(B1), bfhi(r1) + bfhi(B1))};
      st64(rOUT, o, tok * 1024 + (64 * nt2 + 16 * n + 4 * g) * 2);
    }
  }
  STAMP(9);
}

extern "C" void kernel_launch(void* const* d_in, const int* in_sizes, int n_in, void* d_out, int out_size, void* d_ws,
                              size_t ws_size, hipStream_t stream) {
  Params p;
  p.x = (const u16*)d_in[0]; p.lar = (const u16*)d_in[1]; p.aim = (const u16*)d_in[2]; p.cre = (const u16*)d_in[3];
  p.cim = (const u16*)d_in[4]; p.ldt = (const u16*)d_in[5]; p.dsk = (const u16*)d_in[6];
  p.w1 = (const u16*)d_in[8]; p.b1 = (const u16*)d_in[9]; p.w2 = (const u16*)d_in[10]; p.b2 = (const u16*)d_in[11];
  p.out = (u16*)d_out; p.ws = (char*)d_ws;
  s4d_mixer_fused<<<256, 256, 0, stream>>>(p);
}
```

```cpp
#include <hip/hip_runtime.h>
#include <stdint.h>
#include <utility>

namespace {
typedef unsigned short u16;
typedef unsigned u32;
typedef unsigned long long u64;
typedef float f32x2 __attribute__((ext_vector_type(2)));
typedef float f32x4 __attribute__((ext_vector_type(4)));
typedef u32 u32x2 __attribute__((ext_vector_type(2)));
typedef u32 u32x4 __attribute__((ext_vector_type(4)));
typedef short s16x4 __attribute__((ext_vector_type(4)));
typedef short s16x8 __attribute__((ext_vector_type(8)));
typedef __bf16 bf16x2 __attribute__((ext_vector_type(2)));
typedef __bf16 bf16x8 __attribute__((ext_vector_type(8)));
typedef _Float16 f16x2 __attribute__((ext_vector_type(2)));
typedef _Float16 f16x8 __attribute__((ext_vector_type(8)));
#define LAS __attribute__((address_space(3)))
#define DEV __device__ __forceinline__
typedef __amdgpu_buffer_rsrc_t rsrc_t;

constexpr int SEQ = 2048, DM = 512, NMODE = 128, FF = 1376, NUNIT = 43;
constexpr u32 WS_E = 0, WS_LNF = 0x1000, WS_HF = 0x2000, WS_AF = 0x4000, WS_STATS = 0x8000, WS_XN = 0x10000,
              WS_A = 0x210000, WS_H = 0x410000, WS_END = WS_H + 128 * NUNIT * 1024, WS_STAMP = 0xA00000;
constexpr u32 SLOT = 28672, SLOT_W = 16384;
constexpr u32 W2R = 0, W2_NS = 14, W2_HALF = W2_NS * 4096;
constexpr u32 S0 = 114688;
constexpr u32 S_LN = S0, LN_ROW = 1088;
constexpr u32 S_TAB = S0, TAB_ROW = 288, TAB_WAVE = 32 * TAB_ROW;
constexpr u32 S_KP = S0 + 36864;
constexpr u32 S_KC = S0 + 43008, KC_CH = 1296;
constexpr u32 S_CS = S0 + 45600;
constexpr u32 S_MISC = S0 + 47520;
constexpr u32 S_G = S0, G_PAR = 4160, G_CH = 8320;
constexpr u32 S_U = S0 + 16640, U_CH = 4608;
constexpr u32 S_EX = S0;
constexpr u32 S_AT = S0 + 25856;
constexpr u32 LDS_BYTES = 163840;
constexpr u32 OOB = 0x80000000u;

#define STAMP(kk) do { if ((kk) == 0) amp_t0 = __builtin_amdgcn_s_memrealtime(); if ((kk) == 1) amp_tk = __builtin_amdgcn_s_memrealtime(); \
  if ((kk) == AMP_LAST) { const u64 te = __builtin_amdgcn_s_memrealtime(); const u64 until = te + 20 * (amp_tk - amp_t0); \
    while (__builtin_amdgcn_s_memrealtime() < until) __builtin_amdgcn_s_sleep(2); } } while (0)
#define AMP_DECL u64 amp_t0 = 0, amp_tk = 0;
constexpr int AMP_LAST = 9;

struct Params {
  const u16 *x, *lar, *aim, *cre, *cim, *ldt, *dsk, *w1, *b1, *w2, *b2;
  u16* out;
  char* ws;
};

DEV float asf(u32 v) { return __builtin_bit_cast(float, v); }
DEV u32 asu(float v) { return __builtin_bit_cast(u32, v); }
DEV f16x2 ash2(u32 v) { return __builtin_bit_cast(f16x2, v); }
DEV float bflo(u32 v) { return asf(v << 16); }
DEV float bfhi(u32 v) { return asf(v & 0xffff0000u); }
DEV u32 pk_bf16(float a, float b) { f32x2 v = {a, b}; return __builtin_bit_cast(u32, __builtin_convertvector(v, bf16x2)); }
DEV u32 pk_f16(float a, float b) { f32x2 v = {a, b}; return __builtin_bit_cast(u32, __builtin_convertvector(v, f16x2)); }
DEV float rbf(float a) { return bflo(pk_bf16(a, a)); }
DEV float bf1(u16 v) { return asf((u32)v << 16); }

template <int N> DEV void wait_vm() { asm volatile("s_waitcnt vmcnt(%0)" ::"n"(N) : "memory"); }
DEV void bar() { asm volatile("s_waitcnt lgkmcnt(0)\n\ts_barrier" ::: "memory"); }
DEV u32x4 mk_srd(const void* base, u32 bytes) {
  u64 b = (u64)base;
  u32x4 s;
  s.x = __builtin_amdgcn_readfirstlane((u32)b);
  s.y = __builtin_amdgcn_readfirstlane((u32)(b >> 32)) & 0xffffu;
  s.z = bytes;
  s.w = 0x00020000u;
  return s;
}
DEV rsrc_t mk_rsrc(const void* base, u32 bytes) { return __builtin_amdgcn_make_buffer_rsrc((void*)base, 0, bytes, 0x00020000); }

#define BLOAD(INS, TY, NAME, FLAGS)                                                                          \
  DEV TY NAME(u32x4 srd, u32 voff, u32 soff) {                                                               \
    TY r;                                                                                                    \
    asm volatile(INS " %0, %1, %2, %3 offen" FLAGS : "=v"(r) : "v"(voff), "s"(srd), "s"(soff) : "memory");    \
    return r;                                                                                                \
  }
BLOAD("buffer_load_dwordx4", u32x4, ld128, "")
BLOAD("buffer_load_dwordx4", u32x4, ld128c, " sc1")
BLOAD("buffer_load_dwordx2", u32x2, ld64, "")
BLOAD("buffer_load_dword", u32, ld32c, " sc1")
BLOAD("buffer_load_ushort", u32, ld16, "")
template <class T> DEV void tie(T& v) { asm volatile("" : "+v"(v)); }

DEV void dma_m0(u32 lds_addr) { asm volatile("s_mov_b32 m0, %0" ::"s"(lds_addr) : "memory"); }
DEV void dma_go(u32x4 srd, u32 voff, u32 soff) { asm volatile("buffer_load_dwordx4 %0, %1, %2 offen lds" ::"v"(voff), "s"(srd), "s"(soff) : "memory"); }
DEV void dma16(u32x4 srd, u32 lds_addr, u32 voff, u32 soff) {
  asm volatile("s_mov_b32 m0, %0\n\ts_nop 0\n\tbuffer_load_dwordx4 %1, %2, %3 offen lds" ::"s"(lds_addr), "v"(voff), "s"(srd), "s"(soff) : "memory");
}
template <int I> using ic = std::integral_constant<int, I>;
template <class F, int... Is> DEV void sfor_impl(F&& f, std::integer_sequence<int, Is...>) { (f(ic<Is>{}), ...); }
template <int N, class F> DEV void sfor(F&& f) { sfor_impl(f, std::make_integer_sequence<int, N>{}); }
template <int OFF> DEV u32x4 a_ds128(u32 addr) { u32x4 r; asm volatile("ds_read_b128 %0, %1 offset:%2" : "=v"(r) : "v"(addr), "n"(OFF)); return r; }
template <int OFF> DEV u32x2 a_dstr(u32 addr) { u32x2 r; asm volatile("ds_read_b64_tr_b16 %0, %1 offset:%2" : "=v"(r) : "v"(addr), "n"(OFF)); return r; }
template <int O0, int O1> DEV u32x2 a_ds2x32(u32 addr) { u32x2 r; asm volatile("ds_read2_b32 %0, %1 offset0:%2 offset1:%3" : "=v"(r) : "v"(addr), "n"(O0), "n"(O1)); return r; }
template <int N> DEV void wait_lgkm() { asm volatile("s_waitcnt lgkmcnt(%0)" ::"n"(N)); }
DEV u32x4 cat4(u32x2 a, u32x2 b) { return __builtin_shufflevector(a, b, 0, 1, 2, 3); }
template <bool INIT> DEV void a_mfma_bf(f32x4& c, u32x4 a, u32x4 b) {
  if constexpr (INIT) asm volatile("v_mfma_f32_16x16x32_bf16 %0, %1, %2, 0" : "=&v"(c) : "v"(a), "v"(b));
  else asm volatile("v_mfma_f32_16x16x32_bf16 %0, %1, %2, %0" : "+v"(c) : "v"(a), "v"(b));
}
template <bool INIT> DEV void a_mfma_h(f32x4& c, u32x4 a, u32x4 b) {
  if constexpr (INIT) asm volatile("v_mfma_f32_16x16x32_f16 %0, %1, %2, 0" : "=&v"(c) : "v"(a), "v"(b));
  else asm volatile("v_mfma_f32_16x16x32_f16 %0, %1, %2, %0" : "+v"(c) : "v"(a), "v"(b));
}
DEV void a_dot2_ones(float& acc, u32 v) { asm volatile("v_dot2c_f32_bf16 %0, 0x3f803f80, %1" : "+v"(acc) : "v"(v)); }
DEV void mfma_settle4(f32x4& a, f32x4& b, f32x4& c, f32x4& d) { asm volatile("s_nop 7\n\ts_nop 1" : "+v"(a), "+v"(b), "+v"(c), "+v"(d)); }
DEV void st128c(rsrc_t r, u32x4 v, u32 voff) { __builtin_amdgcn_raw_buffer_store_b128(v, r, voff, 0, 16); }
DEV void st64c(rsrc_t r, u32x2 v, u32 voff) { __builtin_amdgcn_raw_buffer_store_b64(v, r, voff, 0, 16); }
DEV void st32c(rsrc_t r, u32 v, u32 voff) { __builtin_amdgcn_raw_buffer_store_b32(v, r, voff, 0, 16); }
DEV void st64(rsrc_t r, u32x2 v, u32 voff) { __builtin_amdgcn_raw_buffer_store_b64(v, r, voff, 0, 0); }

template <class T> DEV T lds_ld(const char* smem, u32 off) { return *(const LAS T*)(smem + off); }
template <class T> DEV void lds_st(char* smem, u32 off, T v) { *(LAS T*)(smem + off) = v; }
DEV s16x4 lds_tr(const char* smem, u32 off) { return __builtin_amdgcn_ds_read_tr16_b64_v4i16((LAS s16x4*)(smem + off)); }
DEV s16x8 cat8(s16x4 a, s16x4 b) { return __builtin_shufflevector(a, b, 0, 1, 2, 3, 4, 5, 6, 7); }
DEV f32x4 mfma_bf(s16x8 a, u32x4 b, f32x4 c) {
  return __builtin_amdgcn_mfma_f32_16x16x32_bf16(__builtin_bit_cast(bf16x8, a), __builtin_bit_cast(bf16x8, b), c, 0, 0, 0);
}
DEV f32x4 mfma_h(u32x4 a, u32x4 b, f32x4 c) {
  return __builtin_amdgcn_mfma_f32_16x16x32_f16(__builtin_bit_cast(f16x8, a), __builtin_bit_cast(f16x8, b), c, 0, 0, 0);
}

template <int CTRL, int RM> DEV float dpp_add(float v) {
  return v + asf(__builtin_amdgcn_update_dpp(0, asu(v), CTRL, RM, 0xf, true));
}
DEV float wave_sum(float v) {
  v = dpp_add<0x111, 0xf>(v); v = dpp_add<0x112, 0xf>(v); v = dpp_add<0x114, 0xf>(v); v = dpp_add<0x118, 0xf>(v);
  v = dpp_add<0x142, 0xa>(v); v = dpp_add<0x143, 0xc>(v);
  return asf(__builtin_amdgcn_readlane(asu(v), 63));
}
DEV float sum_rows(float v) {
  auto a = __builtin_amdgcn_permlane16_swap(asu(v), asu(v), false, false);
  v = asf(a[0]) + asf(a[1]);
  auto b = __builtin_amdgcn_permlane32_swap(asu(v), asu(v), false, false);
  return asf(b[0]) + asf(b[1]);
}
DEV f32x2 cmul(f32x2 a, f32x2 b) {
  f32x2 t, r;
  asm("v_pk_mul_f32 %0, %2, %3 op_sel_hi:[1,0]\n\tv_pk_fma_f32 %1, %2, %3, %0 op_sel:[1,1,0] op_sel_hi:[0,1,1] neg_lo:[1,0,0]"
      : "=&v"(t), "=v"(r) : "v"(a), "v"(b));
  return r;
}
DEV float gelu_tanh(float v) {
  const float c1 = -2.3022081986f, c2 = -0.10294323970f;
  float s = v * __builtin_fmaf(v * v, c2, c1);
  return v * __builtin_amdgcn_rcpf(1.0f + __builtin_amdgcn_exp2f(s));
}

DEV void spin_flag(u32x4 srd, u32 voff, u32 epoch, u32 val, bool need) {
  for (int it = 0; it < (1 << 16); ++it) {
    if (__all(!need || val == epoch)) break;
    __builtin_amdgcn_s_sleep(1);
    val = ld32c(srd, voff, 0);
    asm volatile("s_waitcnt vmcnt(0)" : "+v"(val)::"memory");
  }
}

}

__global__ __launch_bounds__(256, 1) void s4d_mixer_fused(Params p) {
  __shared__ __attribute__((aligned(16))) char smem[LDS_BYTES];
  const int tid = threadIdx.x, lane = tid & 63;
  const int w = __builtin_amdgcn_readfirstlane(tid >> 6);
  const int b = blockIdx.x, g8 = b & 7, r = b >> 3;
  const int c15 = lane & 15, g = lane >> 4;
  const int mt = 4 * (g8 >> 1) + (r >> 3), nt = 8 * (g8 & 1) + (r & 7);
  const int ustart = (nt * NUNIT) >> 4, unext = ((nt + 1) * NUNIT) >> 4;
  const int tg = 16 * mt + nt;
  const int cp = 32 * g8 + r;
  const int mt2 = 4 * g8 + (r >> 3), nt2 = r & 7;

  const u32x4 sX = mk_srd(p.x, SEQ * DM * 2), sWS = mk_srd(p.ws, WS_END), sW1 = mk_srd(p.w1, DM * FF * 2), sW2 = mk_srd(p.w2, FF * DM * 2);
  const rsrc_t rWS = mk_rsrc(p.ws, WS_END), rOUT = mk_rsrc(p.out, SEQ * DM * 2);
  const u32 lb = (u32)(uintptr_t)(LAS char*)smem;
  AMP_DECL
  STAMP(0);

  u32x4 pf0;
  {
    u64 pc;
    asm volatile("s_getpc_b64 %0" : "=s"(pc));
    const u64 a = (pc & ~(u64)63) + (u32)((r % 7) * 4096 + tid * 16);
    asm volatile("global_load_dwordx4 %0, %1, off" : "=&v"(pf0) : "v"(a) : "memory");
  }
  u32 eword = ld32c(sWS, WS_E, 0);
  u32x4 xr0 = ld128(sX, lane * 16, (8 * tg + 2 * w) * 1024), xr1 = ld128(sX, lane * 16, (8 * tg + 2 * w + 1) * 1024);
  u32 vx[2], vw[3];
  {
    const int rr = lane >> 3;
    vx[0] = rr * 1024 + (((lane & 7) ^ ((lane >> 4) & 7)) * 16);
    vx[1] = rr * 1024 + (((lane & 7) ^ ((4 + (lane >> 4)) & 7)) * 16);
#pragma unroll
    for (int j = 0; j < 3; ++j) {
      const int P = (3 * w + j) * 64 + lane, k = P / 12, sl = P - 12 * k;
      const int c = (sl >> 1) ^ ((k >> 3) & 1);
      vw[j] = k * (FF * 2) + (16 * c + 8 * (sl & 1)) * 2;
    }
  }
  const u32 x_so = (mt * 128 + 32 * w) * 1024, x_lo = lb + 32 * w * 128, w_so = ustart * 64, w_lo = lb + SLOT_W + 3 * w * 1024;
  auto st1_lds = [&](int kc, int j) -> u32 { return (kc & 3) * SLOT + (j < 4 ? x_lo + j * 1024 : w_lo + (j - 4) * 1024); };
  auto st1_go = [&](int kc, int j) {
    if (j < 4) dma_go(sX, vx[j & 1], x_so + j * 8192 + kc * 128);
    else dma_go(sW1, vw[j - 4], w_so + kc * 64 * (FF * 2));
  };
  sfor<4>([&](auto KC) { sfor<7>([&](auto J) {
    dma_m0(st1_lds(KC, J)); asm volatile("s_nop 0"); st1_go(KC, J); }); });
  asm volatile("s_waitcnt vmcnt(28)" : "+v"(eword), "+v"(xr0), "+v"(xr1), "+v"(pf0)::"memory");
  const u32 epoch = __builtin_amdgcn_readfirstlane(eword) * 0x9E3779B1u + 0x7F4A7C15u;
  STAMP(1);

  {
    u32x4 xr[2] = {xr0, xr1};
    float al[2], be[2];
#pragma unroll
    for (int t = 0; t < 2; ++t) {
      float x[8];
#pragma unroll
      for (int i = 0; i < 4; ++i) { x[2 * i] = bflo(xr[t][i]); x[2 * i + 1] = bfhi(xr[t][i]); }
      const float x0 = asf(__builtin_amdgcn_readfirstlane(asu(x[0])));
      float s = 0.f, q = 0.f;
#pragma unroll
      for (int i = 0; i < 8; ++i) { const float d = x[i] - x0; s += d; q = __builtin_fmaf(d, d, q); }
      const float md = wave_sum(s) * (1.0f / DM);
      const float var = fmaxf(__builtin_fmaf(-md, md, wave_sum(q) * (1.0f / DM)), 0.f);
      const float m1 = rbf(x0 + md);
      const float s1 = rbf(__builtin_amdgcn_sqrtf(rbf(rbf(var) + 1e-5f)));
      const float r1 = __builtin_amdgcn_rcpf(s1);
      float s2 = 0.f, q2 = 0.f;
      u32x4 hv;
#pragma unroll
      for (int i = 0; i < 4; ++i) {
        const u32 a = pk_bf16(x[2 * i] - m1, x[2 * i + 1] - m1);
        const u32 n = pk_bf16(bflo(a) * r1, bfhi(a) * r1);
        const float n0 = bflo(n), n1 = bfhi(n);
        s2 += n0 + n1; q2 = __builtin_fmaf(n0, n0, q2); q2 = __builtin_fmaf(n1, n1, q2);
        hv[i] = pk_f16(n0, n1);
      }
      lds_st<u32x4>(smem, S_LN + (2 * w + t) * LN_ROW + lane * 16, hv);
      const float mean2 = wave_sum(s2) * (1.0f / DM);
      const float var2 = fmaxf(__builtin_fmaf(-mean2, mean2, wave_sum(q2) * (1.0f / DM)), 0.f);
      const float m2 = rbf(mean2);
      const float r2 = __builtin_amdgcn_rcpf(rbf(__builtin_amdgcn_sqrtf(rbf(rbf(var2) + 1e-5f))));
      al[t] = r1 * r2;
      be[t] = __builtin_fmaf(m1, r1, m2) * r2;
    }
    {
      u32x4 sv = {asu(lane ? al[1] : al[0]), epoch, asu(lane ? be[1] : be[0]), epoch};
      st128c(rWS, sv, lane < 2 ? WS_STATS + (8 * tg + 2 * w + lane) * 16 : OOB);
    }
    bar();
#pragma unroll
    for (int i = 0; i < 2; ++i) {
      const int ch0 = (2 * w + i) * 64 + 16 * g;
      const u32 a = S_LN + ((lane & 15) >> 2) * LN_ROW + (ch0 + 4 * (lane & 3)) * 2;
      const s16x8 v = cat8(lds_tr(smem, a), lds_tr(smem, a + 4 * LN_ROW));
      st128c(rWS, __builtin_bit_cast(u32x4, v), WS_XN + tg * 8192 + ((2 * w + i) * 64 + lane) * 16);
    }
  }
  STAMP(2);
  const u32x4 sB1 = mk_srd(p.b1, FF * 2);
  u32x2 b1v[6];
#pragma unroll
  for (int T = 0; T < 6; ++T) b1v[T] = ld64(sB1, (16 * T + 4 * g) * 2, ustart * 64);
  const int kch = w >> 1, kd = 2 * cp + kch;
  u32 q_lar, q_aim, q_cre, q_cim;
  {
    const u32 vo = (64 * (w & 1) + lane) * 2, so = kd * NMODE * 2;
    q_lar = ld16(mk_srd(p.lar, DM * NMODE * 2), vo, so); q_aim = ld16(mk_srd(p.aim, DM * NMODE * 2), vo, so);
    q_cre = ld16(mk_srd(p.cre, DM * NMODE * 2), vo, so); q_cim = ld16(mk_srd(p.cim, DM * NMODE * 2), vo, so);
  }

  f32x4 acc1[6][2];
  float csp[3] = {0.f, 0.f, 0.f};
  {
    const int q4 = (lane & 15) >> 2, p4 = lane & 3;
    const u32 xa0 = lb + (32 * w + c15) * 128 + ((g ^ (c15 >> 1)) * 16);
    const u32 wrow = lb + SLOT_W + (8 * g + q4) * 192 + 8 * p4;
    const u32 xa[2][2] = {{xa0, xa0 ^ 64}, {xa0 + 2 * SLOT, (xa0 ^ 64) + 2 * SLOT}};
    const u32 wa[2][2] = {{wrow + (g & 1) * 32, wrow - (g & 1) * 32}, {wrow + (g & 1) * 32 + 2 * SLOT, wrow - (g & 1) * 32 + 2 * SLOT}};
    u32 wc[2][3];
#pragma unroll
    for (int i = 0; i < 3; ++i) { wc[0][i] = wrow + (((3 * (w & 1) + i) ^ (g & 1)) * 32) + (w >> 1) * 32 * 192; wc[1][i] = wc[0][i] + 2 * SLOT; }
    u32x4 xf[2][2];
    u32x2 wl[2][6], wh[2][6], cl[3], chh[3];
    auto rd = [&](auto S, auto R) {
      constexpr int s = S, r = R, buf = s & 1, sl = (s >> 1) & 3, so = (sl & 1) * SLOT, sp = s & 1;
      if constexpr (r < 2) xf[buf][r] = a_ds128<so + r * 2048>(xa[sl >> 1][sp]);
      else {
        constexpr int T = (r - 2) >> 1, hf = (r - 2) & 1, off = so + T * 32 + (sp * 32 + hf * 4) * 192;
        if constexpr (hf == 0) wl[buf][T] = a_dstr<off>(wa[sl >> 1][T & 1]); else wh[buf][T] = a_dstr<off>(wa[sl >> 1][T & 1]);
      }
    };
    wait_vm<34>();
    bar();
    sfor<14>([&](auto R) { rd(ic<0>{}, R); });
    sfor<16>([&](auto S) {
      constexpr int s = S, buf = s & 1, kc = (s + 1) >> 1;
      if constexpr ((s & 1) && kc < 8) {
        wait_vm<(kc <= 3 ? 27 : (kc <= 5 ? 14 : (kc == 6 ? 7 : 0)))>();
        bar();
        if constexpr (kc == 4) st32c(rWS, epoch, tid == 0 ? WS_LNF + tg * 4 : OOB);
      } else wait_lgkm<0>();
      sfor<12>([&](auto I) {
        constexpr int i = I, T = i >> 1, tt = i & 1;
        constexpr int sc = (s & 1) ? kc + 3 : (s >> 1) + 3;
        constexpr int pj = (s & 1) ? ((i % 3 == 0) ? i / 3 : -1) : ((i % 4 == 1) ? 4 + i / 4 : -1);
        constexpr bool st = pj >= 0 && pj < 7 && sc >= 4 && sc < 8 && s >= 1;
        if constexpr (st) dma_m0(st1_lds(sc, pj));
        a_mfma_bf<s == 0>(acc1[T][tt], cat4(wl[buf][T], wh[buf][T]), xf[buf][tt]);
        if constexpr (st) st1_go(sc, pj);
        if constexpr (s + 1 < 16 && i < 7) { rd(ic<s + 1>{}, ic<2 * i>{}); rd(ic<s + 1>{}, ic<2 * i + 1>{}); }
        if constexpr (!(s & 1) && i >= 7 && i < 10) {
          constexpr int sl = (s >> 1) & 3, so = (sl & 1) * SLOT;
          cl[i - 7] = a_dstr<so>(wc[sl >> 1][i - 7]); chh[i - 7] = a_dstr<so + 4 * 192>(wc[sl >> 1][i - 7]);
        }
        if constexpr ((s & 1) && i >= 7 && i < 10) {
          a_dot2_ones(csp[i - 7], cl[i - 7].x); a_dot2_ones(csp[i - 7], cl[i - 7].y);
          a_dot2_ones(csp[i - 7], chh[i - 7].x); a_dot2_ones(csp[i - 7], chh[i - 7].y);
        }
      });
    });
    mfma_settle4(acc1[0][0], acc1[0][1], acc1[1][0], acc1[1][1]); mfma_settle4(acc1[2][0], acc1[2][1], acc1[3][0], acc1[3][1]);
    mfma_settle4(acc1[4][0], acc1[4][1], acc1[5][0], acc1[5][1]);
    asm volatile("" : "+v"(csp[0]), "+v"(csp[1]), "+v"(csp[2]));
  }
  STAMP(3);
#pragma unroll
  for (int i = 0; i < 3; ++i) {
    const float v = sum_rows(csp[i]);
    if (lane < 16) lds_st<float>(smem, S_CS + (w * 48 + i * 16 + lane) * 4, v);
  }
  u32x4 st0 = ld128c(sWS, WS_STATS + (128 * mt + 32 * w + c15) * 16, 0), st1 = ld128c(sWS, WS_STATS + (128 * mt + 32 * w + 16 + c15) * 16, 0);
  u32 lnf = ld32c(sWS, WS_LNF + tid * 4, 0);
  asm volatile("s_waitcnt vmcnt(0)" : "+v"(st0), "+v"(st1), "+v"(lnf), "+v"(b1v[0]), "+v"(b1v[1]), "+v"(b1v[2]), "+v"(b1v[3]), "+v"(b1v[4]), "+v"(b1v[5]),
               "+v"(q_lar), "+v"(q_aim), "+v"(q_cre), "+v"(q_cim)::"memory");
  for (int it = 0; it < (1 << 16); ++it) {
    if (__all(st0.y == epoch && st0.w == epoch && st1.y == epoch && st1.w == epoch)) break;
    __builtin_amdgcn_s_sleep(1);
    st0 = ld128c(sWS, WS_STATS + (128 * mt + 32 * w + c15) * 16, 0);
    st1 = ld128c(sWS, WS_STATS + (128 * mt + 32 * w + 16 + c15) * 16, 0);
    asm volatile("s_waitcnt vmcnt(0)" : "+v"(st0), "+v"(st1)::"memory");
  }
  spin_flag(sWS, WS_LNF + tid * 4, epoch, lnf, true);
  u32x4 ug0 = ld128c(sWS, WS_XN + tid * 8192 + cp * 32, 0), ug1 = ld128c(sWS, WS_XN + tid * 8192 + cp * 32 + 16, 0);
  bar();
  st32c(rWS, epoch, (b == 0 && tid == 0) ? WS_E : OOB);
  STAMP(4);

  {
    const float al[2] = {asf(st0.x), asf(st1.x)}, be[2] = {asf(st0.z), asf(st1.z)};
#pragma unroll
    for (int uu = 0; uu < 3; ++uu) {
      float bias[8], cs[8];
#pragma unroll
      for (int hh = 0; hh < 2; ++hh) {
        const int T = 2 * uu + hh;
        const u32 o = S_CS + ((T / 3) * 48 + (T % 3) * 16 + 4 * g) * 4;
        const f32x4 c0 = lds_ld<f32x4>(smem, o), c1 = lds_ld<f32x4>(smem, o + 2 * 48 * 4);
#pragma unroll
        for (int i = 0; i < 4; ++i) cs[4 * hh + i] = c0[i] + c1[i];
        bias[4 * hh + 0] = bflo(b1v[T].x); bias[4 * hh + 1] = bfhi(b1v[T].x);
        bias[4 * hh + 2] = bflo(b1v[T].y); bias[4 * hh + 3] = bfhi(b1v[T].y);
      }
#pragma unroll
      for (int tt = 0; tt < 2; ++tt) {
        float hv[8];
#pragma unroll
        for (int j = 0; j < 8; ++j) {
          const float pre = __builtin_fmaf(al[tt], acc1[2 * uu + (j >> 2)][tt][j & 3], __builtin_fmaf(-be[tt], cs[j], bias[j]));
          hv[j] = gelu_tanh(pre);
        }
        const u32x4 o = {pk_bf16(hv[0], hv[1]), pk_bf16(hv[2], hv[3]), pk_bf16(hv[4], hv[5]), pk_bf16(hv[6], hv[7])};
        const u32 off = WS_H + ((mt * 8 + 2 * w + tt) * NUNIT + ustart + uu) * 1024 + lane * 16;
        st128c(rWS, o, ustart + uu < unext ? off : OOB);
      }
    }
  }
  const int kh = w >> 1, mh = w & 1;
  const u32 vw2 = (lane >> 3) * 1024 + ((((lane & 7) >> 1) ^ ((lane >> 4) & 3)) * 32) + (lane & 1) * 16;
  auto stage2 = [&](int j) {
    const int u = kh * 22 + j;
#pragma unroll
    for (int i = 0; i < 2; ++i)
      dma16(sW2, lb + W2R + kh * W2_HALF + (j % W2_NS) * 4096 + (2 * mh + i) * 1024, u < NUNIT ? vw2 : OOB, (u * 32 + (2 * mh + i) * 8) * 1024 + nt2 * 128);
  };
#pragma unroll
  for (int j = 0; j < 14; ++j) stage2(j);
  wait_vm<28>();
  st32c(rWS, epoch, lane == 0 ? WS_HF + ((mt * 4 + w) * 16 + nt) * 4 : OOB);
  STAMP(5);

  float inv_sk;
  {
    const int mh2 = w & 1;
    const float dt = rbf(expf(bf1(p.ldt[kd])));
    const float zr = -rbf(expf(bf1((u16)q_lar))) * dt, zi = bf1((u16)q_aim) * dt;
    f32x2 C = {2.0f * bf1((u16)q_cre) * dt, 2.0f * bf1((u16)q_cim) * dt};
    const float bw = wave_sum(__builtin_fabsf(C.x) + __builtin_fabsf(C.y));
    int se = 267 - (int)((asu(bw) >> 23) & 0xff);
    se = se < 1 ? 1 : (se > 253 ? 253 : se);
    C *= asf((u32)se << 23);
    const float inv_sx = asf((u32)(254 - se) << 23);
    const float e1 = zr * 1.44269504089f;
    const float ph = zi * 0.15915494309f;
    const float pl = __builtin_fmaf(zi, 0.15915494309f, -ph) + zi * 6.4206383e-9f;
    auto cexp = [&](float pw) {
      const float m = __builtin_amdgcn_exp2f(e1 * pw);
      const float a = __builtin_amdgcn_fractf(ph * pw) + pl * pw;
      return f32x2{m * __builtin_amdgcn_cosf(a), m * __builtin_amdgcn_sinf(a)};
    };
    f32x2 P[8], Y[16], E[5], CE[17];
    P[1] = cexp(1.f); Y[1] = cexp(8.f); E[0] = cexp(128.f);
    P[2] = cmul(P[1], P[1]); P[3] = cmul(P[2], P[1]); P[4] = cmul(P[2], P[2]);
    P[5] = cmul(P[4], P[1]); P[6] = cmul(P[4], P[2]); P[7] = cmul(P[4], P[3]);
    Y[0] = f32x2{1.f, 0.f};
    Y[2] = cmul(Y[1], Y[1]); Y[3] = cmul(Y[2], Y[1]); Y[4] = cmul(Y[2], Y[2]);
    Y[5] = cmul(Y[4], Y[1]); Y[6] = cmul(Y[4], Y[2]); Y[7] = cmul(Y[4], Y[3]); Y[8] = cmul(Y[4], Y[4]);
#pragma unroll
    for (int i = 1; i < 8; ++i) Y[8 + i] = cmul(Y[8], Y[i]);
#pragma unroll
    for (int i = 1; i < 5; ++i) E[i] = cmul(E[i - 1], E[i - 1]);
    CE[0] = C; CE[1] = cmul(C, E[0]);
    CE[2] = cmul(CE[0], E[1]); CE[3] = cmul(CE[1], E[1]);
#pragma unroll
    for (int i = 0; i < 4; ++i) CE[4 + i] = cmul(CE[i], E[2]);
#pragma unroll
    for (int i = 0; i < 8; ++i) CE[8 + i] = cmul(CE[i], E[3]);
    CE[16] = cmul(C, E[4]);
    const u32 tab = S_TAB + w * TAB_WAVE, tl = tab + lane * 4;
#pragma unroll
    for (int i = 0; i < 16; ++i) lds_st<u32>(smem, tl + i * TAB_ROW, pk_f16(Y[i].x, -Y[i].y));
    const u32 rd = tab + c15 * TAB_ROW + g * 16;
    u32x4 yf[4];
#pragma unroll
    for (int s = 0; s < 4; ++s) yf[s] = lds_ld<u32x4>(smem, rd + s * 64);
    f32x4 kd3[3];
#pragma unroll
    for (int tl3 = 0; tl3 < 3; ++tl3) {
#pragma unroll
      for (int i = 0; i < 16; ++i) {
        f32x2 v;
        if (tl3 == 0) v = (i & 7) ? cmul(CE[i >> 3], P[i & 7]) : CE[i >> 3];
        else if (tl3 == 1) {
          if (i < 8) v = (i & 3) ? cmul(CE[2 + (i >> 2)], P[2 * (i & 3)]) : CE[2 + (i >> 2)];
          else v = (i & 1) ? cmul(CE[4 + ((i - 8) >> 1)], P[4]) : CE[4 + ((i - 8) >> 1)];
        } else v = i < 8 ? CE[8 + i] : (i == 8 ? CE[16] : f32x2{0.f, 0.f});
        lds_st<u32>(smem, tl + (16 + i) * TAB_ROW, pk_f16(v.x, v.y));
      }
      f32x4 a = {0, 0, 0, 0};
#pragma unroll
      for (int s = 0; s < 4; ++s) a = mfma_h(lds_ld<u32x4>(smem, rd + 16 * TAB_ROW + s * 64), yf[s], a);
      kd3[tl3] = a * inv_sx;
    }
    if (mh2) {
#pragma unroll
      for (int t = 0; t < 3; ++t) lds_st<f32x4>(smem, S_KP + kch * 3072 + t * 1024 + lane * 16, kd3[t]);
      if (lane == 0) lds_st<float>(smem, S_MISC + kch * 4, bw);
    }
    bar();
    if (!mh2) {
      const float bd = bw + lds_ld<float>(smem, S_MISC + kch * 4);
      int sk = 267 - (int)((asu(bd) >> 23) & 0xff);
      sk = sk < 1 ? 1 : (sk > 253 ? 253 : sk);
      const float scale = asf((u32)sk << 23);
      if (lane == 0) lds_st<float>(smem, S_MISC + 8 + kch * 4, asf((u32)(254 - sk) << 23));
      const u32 kc = S_KC + kch * KC_CH;
#pragma unroll
      for (int t = 0; t < 3; ++t) {
        const f32x4 v = (kd3[t] + lds_ld<f32x4>(smem, S_KP + kch * 3072 + t * 1024 + lane * 16)) * scale;
#pragma unroll
        for (int i = 0; i < 4; ++i) {
          int idx;
          if (t == 0) idx = 128 * (g >> 1) + 8 * c15 + 4 * (g & 1) + i;
          else if (t == 1) idx = g < 2 ? 256 + 64 * g + 4 * c15 + i : 384 + 64 * (g - 2) + 32 * (i >> 1) + (i & 1) + 2 * c15;
          else idx = g < 2 ? 512 + 64 * g + 16 * i + c15 : ((g == 2 && i == 0 && c15 == 0) ? 640 : 644);
          lds_st<_Float16>(smem, kc + idx * 2, (_Float16)v[i]);
        }
      }
      if (lane == 0) lds_st<u16>(smem, kc + 641 * 2, (u16)0);
    }
    bar();
    u32 ix[9];
#pragma unroll
    for (int e = 0; e < 9; ++e) {
      const int l = 2047 - (8 * tid + e);
      const u32 fb = asu((float)l);
      const u32 rb = (fb + 0x7fffu + ((fb >> 16) & 1u)) >> 16;
      ix[e] = (l < 0 ? 641u : (l < 128 ? (u32)l : rb - 17024u)) * 2;
    }
#pragma unroll
    for (int ch = 0; ch < 2; ++ch) {
      u32 e[9];
#pragma unroll
      for (int i = 0; i < 9; ++i) e[i] = lds_ld<u16>(smem, S_KC + ch * KC_CH + ix[i]);
      const u32x4 a0 = {e[0] | (e[1] << 16), e[2] | (e[3] << 16), e[4] | (e[5] << 16), e[6] | (e[7] << 16)};
      const u32x4 a1 = {e[1] | (e[2] << 16), e[3] | (e[4] << 16), e[5] | (e[6] << 16), e[7] | (e[8] << 16)};
      lds_st<u32x4>(smem, S_G + ch * G_CH + tid * 16, a0);
      lds_st<u32x4>(smem, S_G + ch * G_CH + G_PAR + tid * 16, a1);
    }
    const u32x4 z4 = {0, 0, 0, 0};
    if (tid < 8) lds_st<u32x4>(smem, S_G + (tid >> 2) * G_CH + ((tid >> 1) & 1) * G_PAR + 4096 + (tid & 1) * 16, z4);
    if (tid < 60) lds_st<u32x4>(smem, S_U + (tid / 30) * U_CH + (tid % 30) * 16, z4);
    tie(ug0); tie(ug1);
    lds_st<u32x4>(smem, S_U + 480 + tid * 16, ug0);
    lds_st<u32x4>(smem, S_U + U_CH + 480 + tid * 16, ug1);
    bar();
    inv_sk = lds_ld<float>(smem, S_MISC + 8 + kch * 4);
  }
  STAMP(6);

  {
    const int par = w & 1;
    const u32 ub = lb + S_U + kch * U_CH + 64 - 64 * par + 32 * c15 + 16 * g;
    const u32 gb0 = lb + S_G + kch * G_CH + ((c15 & 1) ? 0 : G_PAR) + 4 * (1015 - (c15 >> 1) - 16 * par + 4 * g - 32 * 31);
    const u32 gb[4] = {gb0, gb0 + 1024, gb0 + 2048, gb0 + 3072};
    u32x4 bq[32];
    u32x2 al[32], ah[32];
    f32x4 y[8];
    auto rdB = [&](auto M, auto T) { constexpr int q = 4 * T - M + 3; bq[q] = a_ds128<128 * q>(ub); };
    auto rdA = [&](auto M, auto H) {
      constexpr int k = (31 - M) >> 3, o = 32 * ((31 - M) & 7);
      if constexpr (H == 0) al[M] = a_ds2x32<o, o + 1>(gb[k]); else ah[M] = a_ds2x32<o + 2, o + 3>(gb[k]);
    };
    sfor<2>([&](auto M) { sfor<8>([&](auto T) { rdB(M, T); }); rdA(M, ic<0>{}); rdA(M, ic<1>{}); });
    sfor<32>([&](auto M) {
      constexpr int m = M, t0 = m >> 2, n = 8 - t0;
      wait_lgkm<(m <= 2 ? 10 : (m == 3 ? 4 : (m == 4 ? 6 : (2 * (31 - m) < 8 ? 2 * (31 - m) : 8))))>();
      sfor<n>([&](auto I) {
        constexpr int i = I, t = t0 + i;
        a_mfma_h<m == 0>(y[t], cat4(al[m], ah[m]), bq[4 * t - m + 3]);
        if constexpr (m < 2) {
          rdB(ic<m + 2>{}, I);
          if constexpr (i >= 6) rdA(ic<m + 2>{}, ic<i - 6>{});
        } else if constexpr (m <= 4) {
          if constexpr (i < 4) rdA(ic<2 * m + (i >> 1)>{}, ic<i & 1>{});
        } else if constexpr (m + 5 < 32) {
          if constexpr (n == 1) { rdA(ic<m + 5>{}, ic<0>{}); rdA(ic<m + 5>{}, ic<1>{}); }
          else if constexpr (i < 2) rdA(ic<m + 5>{}, I);
        }
      });
    });
    mfma_settle4(y[0], y[1], y[2], y[3]); mfma_settle4(y[4], y[5], y[6], y[7]);
    bar();
    f32x4 ys[4], yk[4];
    if (par) { ys[0] = y[0]; ys[1] = y[1]; ys[2] = y[2]; ys[3] = y[3]; yk[0] = y[4]; yk[1] = y[5]; yk[2] = y[6]; yk[3] = y[7]; }
    else { ys[0] = y[4]; ys[1] = y[5]; ys[2] = y[6]; ys[3] = y[7]; yk[0] = y[0]; yk[1] = y[1]; yk[2] = y[2]; yk[3] = y[3]; }
#pragma unroll
    for (int i = 0; i < 4; ++i) lds_st<f32x4>(smem, S_EX + w * 4096 + i * 1024 + lane * 16, ys[i]);
    bar();
    const float dsk = bf1(p.dsk[kd]);
#pragma unroll
    for (int i = 0; i < 4; ++i) {
      const int t = 4 * par + i;
      const f32x4 v = (yk[i] + lds_ld<f32x4>(smem, S_EX + (w ^ 1) * 4096 + i * 1024 + lane * 16)) * inv_sk;
      const int l0 = 256 * t + 16 * c15 + 4 * g;
      const u32x2 uu = lds_ld<u32x2>(smem, S_U + kch * U_CH + (240 + l0) * 2);
      const f16x2 u01 = ash2(uu.x), u23 = ash2(uu.y);
      const u32 yb0 = pk_bf16(v[0], v[1]), yb1 = pk_bf16(v[2], v[3]);
      const u32 d0 = pk_bf16((float)u01.x * dsk, (float)u01.y * dsk), d1 = pk_bf16((float)u23.x * dsk, (float)u23.y * dsk);
      const u32x2 o = {pk_bf16(bflo(yb0) + bflo(d0), bfhi(yb0) + bfhi(d0)), pk_bf16(bflo(yb1) + bflo(d1), bfhi(yb1) + bfhi(d1))};
      st64c(rWS, o, WS_A + ((4 * t + (c15 >> 2)) * 512 + kd) * 128 + ((c15 & 3) * 16 + 4 * g) * 2);
    }
    wait_vm<0>();
    bar();
    st32c(rWS, epoch, tid == 0 ? WS_AF + cp * 4 : OOB);
  }
  STAMP(7);

  {
    const int mtp = mt2 >> 1, wp = 2 * (mt2 & 1) + mh;
    {
      const u32 fo = WS_HF + ((mtp * 4 + wp) * 16 + c15) * 4;
      u32 hf = ld32c(sWS, fo, 0);
      asm volatile("s_waitcnt vmcnt(0)" : "+v"(hf)::"memory");
      spin_flag(sWS, fo, epoch, hf, kh ? c15 >= 8 : c15 <= 8);
    }
    const u32 hb = WS_H + ((mt2 * 4 + 2 * mh) * NUNIT + kh * 22) * 1024;
    u32x4 hf[22][2];
#pragma unroll
    for (int j = 0; j < 22; ++j)
#pragma unroll
      for (int tt = 0; tt < 2; ++tt) hf[j][tt] = ld128c(sWS, (j == 21 && kh) ? OOB : lane * 16, hb + (tt * NUNIT + j) * 1024);
    STAMP(8);
    const int q4 = (lane & 15) >> 2, p4 = lane & 3, m4 = (2 * g + (q4 >> 1)) & 3;
    u32 wa[4];
#pragma unroll
    for (int n = 0; n < 4; ++n) wa[n] = lb + W2R + kh * W2_HALF + (4 * g + q4) * 128 + ((n ^ m4) * 32) + 8 * p4;
    f32x4 acc[4][2];
    u32x2 wl[2][4], wh[2][4];
    auto rd = [&](auto J, auto R) {
      constexpr int j = J, n = R >> 1, off = (j % W2_NS) * 4096 + (R & 1) * 2048;
      if constexpr ((R & 1) == 0) wl[j & 1][n] = a_dstr<off>(wa[n]); else wh[j & 1][n] = a_dstr<off>(wa[n]);
    };
    auto unit = [&](auto J, auto LAST) {
      constexpr int j = J;
      sfor<8>([&](auto I) {
        constexpr int i = I, n = i >> 1, tt = i & 1;
        if constexpr (tt == 0) wait_lgkm<(LAST ? 6 - 2 * n : 6)>();
        a_mfma_bf<j == 0>(acc[n][tt], cat4(wl[j & 1][n], wh[j & 1][n]), hf[j][tt]);
        if constexpr (!LAST) rd(ic<j + 1>{}, I);
      });
    };
    wait_vm<0>();
#pragma unroll
    for (int j = 0; j < 22; ++j) { tie(hf[j][0]); tie(hf[j][1]); }
    bar();
    sfor<8>([&](auto R) { rd(ic<0>{}, R); });
    sfor<8>([&](auto J) { unit(J, ic<0>{}); });
    bar();
#pragma unroll
    for (int j = 14; j < 22; ++j) stage2(j);
    sfor<5>([&](auto J) { unit(ic<8 + J>{}, ic<0>{}); });
    u32 af = ld32c(sWS, WS_AF + (32 * nt2 + (lane & 31)) * 4, 0);
    asm volatile("s_waitcnt vmcnt(0)" : "+v"(af)::"memory");
    bar();
    sfor<8>([&](auto J) { unit(ic<13 + J>{}, ic<0>{}); });
    unit(ic<21>{}, ic<1>{});
    mfma_settle4(acc[0][0], acc[0][1], acc[1][0], acc[1][1]); mfma_settle4(acc[2][0], acc[2][1], acc[3][0], acc[3][1]);
    spin_flag(sWS, WS_AF + (32 * nt2 + (lane & 31)) * 4, epoch, af, true);
    u32x4 at0 = ld128c(sWS, WS_A + (mt2 * 512 + nt2 * 64) * 128 + tid * 16, 0), at1 = ld128c(sWS, WS_A + (mt2 * 512 + nt2 * 64) * 128 + 4096 + tid * 16, 0);
    const int tok = 64 * mt2 + 32 * mh + 16 * kh + c15;
    u32x2 xv[4], b2v[4];
    const u32x4 sB2 = mk_srd(p.b2, DM * 2);
#pragma unroll
    for (int n = 0; n < 4; ++n) {
      xv[n] = ld64(sX, tok * 1024 + (64 * nt2 + 16 * n + 4 * g) * 2, 0);
      b2v[n] = ld64(sB2, (64 * nt2 + 16 * n + 4 * g) * 2, 0);
    }
    bar();
    f32x4 as[4], ak[4];
    if (kh) { as[0] = acc[0][0]; as[1] = acc[1][0]; as[2] = acc[2][0]; as[3] = acc[3][0]; ak[0] = acc[0][1]; ak[1] = acc[1][1]; ak[2] = acc[2][1]; ak[3] = acc[3][1]; }
    else { as[0] = acc[0][1]; as[1] = acc[1][1]; as[2] = acc[2][1]; as[3] = acc[3][1]; ak[0] = acc[0][0]; ak[1] = acc[1][0]; ak[2] = acc[2][0]; ak[3] = acc[3][0]; }
#pragma unroll
    for (int n = 0; n < 4; ++n) lds_st<f32x4>(smem, S_EX + w * 4096 + n * 1024 + lane * 16, as[n]);
    asm volatile("s_waitcnt vmcnt(0)" : "+v"(at0), "+v"(at1), "+v"(xv[0]), "+v"(xv[1]), "+v"(xv[2]), "+v"(xv[3]), "+v"(b2v[0]), "+v"(b2v[1]), "+v"(b2v[2]), "+v"(b2v[3])::"memory");
    {
      const int row0 = tid >> 3, row1 = 32 + row0, sl = tid & 7;
      lds_st<u32x4>(smem, S_AT + row0 * 128 + (((sl >> 1) ^ ((row0 >> 1) & 3)) * 32) + (sl & 1) * 16, at0);
      lds_st<u32x4>(smem, S_AT + row1 * 128 + (((sl >> 1) ^ ((row1 >> 1) & 3)) * 32) + (sl & 1) * 16, at1);
    }
    bar();
    const int tb = 2 * mh + kh;
#pragma unroll
    for (int n = 0; n < 4; ++n) {
      const f32x4 v = ak[n] + lds_ld<f32x4>(smem, S_EX + (w ^ 2) * 4096 + n * 1024 + lane * 16);
      const int row = 16 * n + 4 * g + q4;
      const u32x2 av = __builtin_bit_cast(u32x2, lds_tr(smem, S_AT + row * 128 + ((tb ^ ((row >> 1) & 3)) * 32) + 8 * p4));
      const u32 m0 = pk_bf16(v[0], v[1]), m1 = pk_bf16(v[2], v[3]);
      const u32 B0 = pk_bf16(bflo(m0) + bflo(b2v[n].x), bfhi(m0) + bfhi(b2v[n].x)), B1 = pk_bf16(bflo(m1) + bflo(b2v[n].y), bfhi(m1) + bfhi(b2v[n].y));
      const u32 r0 = pk_bf16(bflo(xv[n].x) + bflo(av.x), bfhi(xv[n].x) + bfhi(av.x)), r1 = pk_bf16(bflo(xv[n].y) + bflo(av.y), bfhi(xv[n].y) + bfhi(av.y));
      const u32x2 o = {pk_bf16(bflo(r0) + bflo(B0), bfhi(r0) + bfhi(B0)), pk_bf16(bflo(r1) + bflo(B1), bfhi(r1) + bfhi(B1))};
      st64(rOUT, o, tok * 1024 + (64 * nt2 + 16 * n + 4 * g) * 2);
    }
  }
  STAMP(9);
}

extern "C" void kernel_launch(void* const* d_in, const int* in_sizes, int n_in, void* d_out, int out_size, void* d_ws,
                              size_t ws_size, hipStream_t stream) {
  Params p;
  p.x = (const u16*)d_in[0]; p.lar = (const u16*)d_in[1]; p.aim = (const u16*)d_in[2]; p.cre = (const u16*)d_in[3];
  p.cim = (const u16*)d_in[4]; p.ldt = (const u16*)d_in[5]; p.dsk = (const u16*)d_in[6];
  p.w1 = (const u16*)d_in[8]; p.b1 = (const u16*)d_in[9]; p.w2 = (const u16*)d_in[10]; p.b2 = (const u16*)d_in[11];
  p.out = (u16*)d_out; p.ws = (char*)d_ws;
  s4d_mixer_fused<<<256, 256, 0, stream>>>(p);
}
```

```cpp
#include <hip/hip_runtime.h>
#include <stdint.h>
#include <utility>

namespace {
typedef unsigned short u16;
typedef unsigned u32;
typedef unsigned long long u64;
typedef float f32x2 __attribute__((ext_vector_type(2)));
typedef float f32x4 __attribute__((ext_vector_type(4)));
typedef float f32x16 __attribute__((ext_vector_type(16)));
typedef u32 u32x2 __attribute__((ext_vector_type(2)));
typedef u32 u32x4 __attribute__((ext_vector_type(4)));
typedef short s16x4 __attribute__((ext_vector_type(4)));
typedef __bf16 bf16x2 __attribute__((ext_vector_type(2)));
typedef _Float16 f16x2 __attribute__((ext_vector_type(2)));
typedef _Float16 f16x8 __attribute__((ext_vector_type(8)));
#define LAS __attribute__((address_space(3)))
#define DEV __device__ __forceinline__
typedef __amdgpu_buffer_rsrc_t rsrc_t;

constexpr int SEQ = 2048, DM = 512, NMODE = 128, FF = 1376, NUNIT = 43;
constexpr u32 WS_E = 0, WS_LNF = 0x1000, WS_HF = 0x2000, WS_STATS = 0x8000, WS_XN = 0x10000, WS_A = 0x210000, WS_H = 0x410000,
              WS_END = WS_H + 64 * NUNIT * 2048, WS_STAMP = 0xA00000;
constexpr u32 SLOT = 28672, SLOT_W = 16384;
constexpr u32 W2_NS = 14, W2_HALF = W2_NS * 4096;
constexpr u32 LDS_BYTES_ = 163840;
constexpr u32 S0 = 114688;
constexpr u32 S_LN = S0, LN_ROW = 1088;
constexpr u32 TAB_ROW = 544, TAB_CH = 57 * TAB_ROW;
constexpr u32 S_TAB = LDS_BYTES_ - 2 * TAB_CH - 1808;
constexpr u32 S_KC = S_TAB - 2592, KC_CH = 1296;
constexpr u32 S_CSP = LDS_BYTES_ - 1808;
constexpr u32 S_B1 = S_CSP + 768;
constexpr u32 S_MISC = S_B1 + 384;
constexpr u32 S_CS = S_MISC + 64;
constexpr u32 S_G = S0, G_PAR = 4160, G_CH = 8320;
constexpr u32 S_U = S0 + 16640, U_CH = 4608;
constexpr u32 S_EX = S0;
constexpr u32 S_AT = S0 + 16384, S_XT = S0 + 24576, S_OT = S0 + 32768;
constexpr u32 LDS_BYTES = 163840;
constexpr u32 OOB = 0x80000000u;
constexpr u32 POISON = 0xffffffffu;

#define STAMP(k) do {} while (0)
#define CSTAMP(i) do {} while (0)
#define CDUMP(n, base) do {} while (0)

struct Params {
  const u16 *x, *lar, *aim, *cre, *cim, *ldt, *dsk, *w1, *b1, *w2, *b2;
  u16* out;
  char* ws;
};

DEV float asf(u32 v) { return __builtin_bit_cast(float, v); }
DEV u32 asu(float v) { return __builtin_bit_cast(u32, v); }
DEV f16x2 ash2(u32 v) { return __builtin_bit_cast(f16x2, v); }
DEV float bflo(u32 v) { return asf(v << 16); }
DEV float bfhi(u32 v) { return asf(v & 0xffff0000u); }
DEV f32x2 bf2(u32 v) { return f32x2{bflo(v), bfhi(v)}; }
DEV u32 pk_bf16(f32x2 v) { return __builtin_bit_cast(u32, __builtin_convertvector(v, bf16x2)); }
DEV u32 pk_f16(f32x2 v) { return __builtin_bit_cast(u32, __builtin_convertvector(v, f16x2)); }
DEV float rbf(float a) { return bflo(pk_bf16(f32x2{a, a})); }
DEV float bf1(u32 v) { return asf(v << 16); }
DEV f32x2 sp2(float a) { return f32x2{a, a}; }
DEV f32x2 fma2(f32x2 a, f32x2 b, f32x2 c) { return __builtin_elementwise_fma(a, b, c); }

template <int N> DEV void wait_vm() { asm volatile("s_waitcnt vmcnt(%0)" ::"n"(N) : "memory"); }
template <int N> DEV void wait_lgkm() { asm volatile("s_waitcnt lgkmcnt(%0)" ::"n"(N)); }
DEV void bar() { asm volatile("s_waitcnt lgkmcnt(0)\n\ts_barrier" ::: "memory"); }
DEV u32x4 mk_srd(const void* base, u32 bytes) {
  const u64 b = (u64)base;
  u32x4 s;
  s.x = __builtin_amdgcn_readfirstlane((u32)b);
  s.y = __builtin_amdgcn_readfirstlane((u32)(b >> 32)) & 0xffffu;
  s.z = bytes;
  s.w = 0x00020000u;
  return s;
}
DEV rsrc_t mk_rsrc(const void* base, u32 bytes) { return __builtin_amdgcn_make_buffer_rsrc((void*)base, 0, bytes, 0x00020000); }

#define BLOAD(INS, TY, NAME, FLAGS, CON)                                                                     \
  DEV TY NAME(u32x4 srd, u32 voff, u32 soff) {                                                               \
    TY r;                                                                                                    \
    asm volatile(INS " %0, %1, %2, %3 offen" FLAGS : CON(r) : "v"(voff), "s"(srd), "s"(soff) : "memory");     \
    return r;                                                                                                \
  }
BLOAD("buffer_load_dwordx4", u32x4, ld128, "", "=v")
BLOAD("buffer_load_dwordx4", u32x4, ld128c, " sc1", "=v")
BLOAD("buffer_load_dwordx4", u32x4, ld128c_a, " sc1", "=a")
BLOAD("buffer_load_dwordx2", u32x2, ld64, "", "=v")
BLOAD("buffer_load_dword", u32, ld32c, " sc1", "=v")
BLOAD("buffer_load_ushort", u32, ld16, "", "=v")
DEV u32 touch(const void* a) { u32 r; asm volatile("global_load_dword %0, %1, off" : "=v"(r) : "v"(a) : "memory"); return r; }
template <class T> DEV void tie(T& v) { asm volatile("" : "+v"(v)); }

DEV void dma_m0(u32 lds_addr) { asm volatile("s_mov_b32 m0, %0" ::"s"(lds_addr) : "memory"); }
template <int IOFF> DEV void dma_go(u32x4 srd, u32 voff, u32 soff) {
  asm volatile("buffer_load_dwordx4 %0, %1, %2 offen offset:%3 lds" ::"v"(voff), "s"(srd), "s"(soff), "n"(IOFF) : "memory");
}
DEV void st128c(rsrc_t r, u32x4 v, u32 voff) { __builtin_amdgcn_raw_buffer_store_b128(v, r, voff, 0, 16); }
DEV void st64c(rsrc_t r, u32x2 v, u32 voff) { __builtin_amdgcn_raw_buffer_store_b64(v, r, voff, 0, 16); }
DEV void st32c(rsrc_t r, u32 v, u32 voff) { __builtin_amdgcn_raw_buffer_store_b32(v, r, voff, 0, 16); }
DEV void st64(rsrc_t r, u32x2 v, u32 voff) { __builtin_amdgcn_raw_buffer_store_b64(v, r, voff, 0, 0); }

template <class T> DEV T lds_ld(const char* smem, u32 off) { return *(const LAS T*)(smem + off); }
template <class T> DEV void lds_st(char* smem, u32 off, T v) { *(LAS T*)(smem + off) = v; }
DEV u32x2 lds_tr(const char* smem, u32 off) { return __builtin_bit_cast(u32x2, __builtin_amdgcn_ds_read_tr16_b64_v4i16((LAS s16x4*)(smem + off))); }

template <int I> using ic = std::integral_constant<int, I>;
template <class F, int... Is> DEV void sfor_impl(F&& f, std::integer_sequence<int, Is...>) { (f(ic<Is>{}), ...); }
template <int N, class F> DEV void sfor(F&& f) { sfor_impl(f, std::make_integer_sequence<int, N>{}); }
template <int OFF> DEV u32x4 a_ds128(u32 addr) { u32x4 r; asm volatile("ds_read_b128 %0, %1 offset:%2" : "=v"(r) : "v"(addr), "n"(OFF)); return r; }
template <int OFF> DEV u32x2 a_dstr(u32 addr) { u32x2 r; asm volatile("ds_read_b64_tr_b16 %0, %1 offset:%2" : "=v"(r) : "v"(addr), "n"(OFF)); return r; }
template <int O0, int O1> DEV u32x2 a_ds2x32(u32 addr) { u32x2 r; asm volatile("ds_read2_b32 %0, %1 offset0:%2 offset1:%3" : "=v"(r) : "v"(addr), "n"(O0), "n"(O1)); return r; }
DEV u32x4 cat4(u32x2 a, u32x2 b) { return __builtin_shufflevector(a, b, 0, 1, 2, 3); }
template <bool INIT> DEV void mfma32_vv(f32x16& c, u32x4 a, u32x4 b) {
  if constexpr (INIT) asm volatile("v_mfma_f32_32x32x16_bf16 %0, %1, %2, 0" : "=&v"(c) : "v"(a), "v"(b));
  else asm volatile("v_mfma_f32_32x32x16_bf16 %0, %1, %2, %0" : "+v"(c) : "v"(a), "v"(b));
}
template <bool INIT> DEV void mfma32_va(f32x16& c, u32x4 a, u32x4 b) {
  if constexpr (INIT) asm volatile("v_mfma_f32_32x32x16_bf16 %0, %1, %2, 0" : "=&v"(c) : "v"(a), "a"(b));
  else asm volatile("v_mfma_f32_32x32x16_bf16 %0, %1, %2, %0" : "+v"(c) : "v"(a), "a"(b));
}
template <bool INIT> DEV void mfma16_h(f32x4& c, u32x4 a, u32x4 b) {
  if constexpr (INIT) asm volatile("v_mfma_f32_16x16x32_f16 %0, %1, %2, 0" : "=&v"(c) : "v"(a), "v"(b));
  else asm volatile("v_mfma_f32_16x16x32_f16 %0, %1, %2, %0" : "+v"(c) : "v"(a), "v"(b));
}
DEV f32x4 mfma_h(u32x4 a, u32x4 b, f32x4 c) {
  return __builtin_amdgcn_mfma_f32_16x16x32_f16(__builtin_bit_cast(f16x8, a), __builtin_bit_cast(f16x8, b), c, 0, 0, 0);
}
template <bool INIT> DEV void mfma16_bf(f32x4& c, u32x4 a, u32x4 b) {
  if constexpr (INIT) asm volatile("v_mfma_f32_16x16x32_bf16 %0, %1, %2, 0" : "=&v"(c) : "v"(a), "v"(b));
  else asm volatile("v_mfma_f32_16x16x32_bf16 %0, %1, %2, %0" : "+v"(c) : "v"(a), "v"(b));
}
#define FENCE() __builtin_amdgcn_sched_barrier(0)

DEV void wave_sum4(float& a, float& b, float& c, float& d) {
#define S4(CTRL) "v_add_f32_dpp %0, %0, %0 " CTRL "\n\tv_add_f32_dpp %1, %1, %1 " CTRL "\n\tv_add_f32_dpp %2, %2, %2 " CTRL "\n\tv_add_f32_dpp %3, %3, %3 " CTRL "\n\t"
  asm volatile("s_nop 1\n\t" S4("row_shr:1 row_mask:0xf bank_mask:0xf bound_ctrl:1") S4("row_shr:2 row_mask:0xf bank_mask:0xf bound_ctrl:1")
               S4("row_shr:4 row_mask:0xf bank_mask:0xf bound_ctrl:1") S4("row_shr:8 row_mask:0xf bank_mask:0xf bound_ctrl:1")
               S4("row_bcast:15 row_mask:0xa bank_mask:0xf") S4("row_bcast:31 row_mask:0xc bank_mask:0xf") "s_nop 0"
               : "+v"(a), "+v"(b), "+v"(c), "+v"(d));
#undef S4
  a = asf(__builtin_amdgcn_readlane(asu(a), 63)); b = asf(__builtin_amdgcn_readlane(asu(b), 63));
  c = asf(__builtin_amdgcn_readlane(asu(c), 63)); d = asf(__builtin_amdgcn_readlane(asu(d), 63));
}
template <int CTRL, int RM> DEV float dpp_add(float v) { return v + asf(__builtin_amdgcn_update_dpp(0, asu(v), CTRL, RM, 0xf, true)); }
DEV float wave_sum(float v) {
  v = dpp_add<0x111, 0xf>(v); v = dpp_add<0x112, 0xf>(v); v = dpp_add<0x114, 0xf>(v); v = dpp_add<0x118, 0xf>(v);
  v = dpp_add<0x142, 0xa>(v); v = dpp_add<0x143, 0xc>(v);
  return asf(__builtin_amdgcn_readlane(asu(v), 63));
}
DEV void cmul2(f32x2& r0, f32x2 a0, f32x2 b0, f32x2& r1, f32x2 a1, f32x2 b1) {
  f32x2 t0, t1;
  asm("v_pk_mul_f32 %0, %4, %5 op_sel_hi:[1,0]\n\tv_pk_mul_f32 %1, %6, %7 op_sel_hi:[1,0]\n\t"
      "v_pk_fma_f32 %2, %4, %5, %0 op_sel:[1,1,0] op_sel_hi:[0,1,1] neg_lo:[1,0,0]\n\t"
      "v_pk_fma_f32 %3, %6, %7, %1 op_sel:[1,1,0] op_sel_hi:[0,1,1] neg_lo:[1,0,0]"
      : "=&v"(t0), "=&v"(t1), "=&v"(r0), "=&v"(r1) : "v"(a0), "v"(b0), "v"(a1), "v"(b1));
}
DEV f32x2 cmul(f32x2 a, f32x2 b) {
  f32x2 t, r;
  asm("v_pk_mul_f32 %0, %2, %3 op_sel_hi:[1,0]\n\tv_pk_fma_f32 %1, %2, %3, %0 op_sel:[1,1,0] op_sel_hi:[0,1,1] neg_lo:[1,0,0]"
      : "=&v"(t), "=v"(r) : "v"(a), "v"(b));
  return r;
}
DEV f32x2 gelu2(f32x2 v) {
  const f32x2 s = v * fma2(v * v, sp2(-0.10294323970f), sp2(-2.3022081986f));
  const f32x2 d = f32x2{__builtin_amdgcn_exp2f(s.x), __builtin_amdgcn_exp2f(s.y)} + sp2(1.0f);
  return v * f32x2{__builtin_amdgcn_rcpf(d.x), __builtin_amdgcn_rcpf(d.y)};
}
DEV void spin_flag(u32x4 srd, u32 voff, u32 epoch, u32 val, bool need) {
#pragma unroll 1
  for (int it = 0; it < (1 << 16); ++it) {
    if (__all(!need || val == epoch)) break;
    __builtin_amdgcn_s_sleep(1);
    val = ld32c(srd, voff, 0);
    asm volatile("s_waitcnt vmcnt(0)" : "+v"(val)::"memory");
  }
}

constexpr int G2_LEAD = 8;
constexpr int g2_unit_ops(int j) { return (j + G2_LEAD <= 21 ? 2 : 0) + ((j >= 4 && j <= 11) ? 2 : 0) + (j == 15 ? 6 : 0) + (j == 18 ? 2 : 0); }
constexpr int g2_ops_before(int j) { int n = 16 + 4; for (int i = 0; i < j; ++i) n += g2_unit_ops(i); return n; }
constexpr int g2_pos_h(int j) {
  if (j <= 4) return 2 * j + 2;
  if (j < G2_LEAD) return 2 * j + 2 + 4;
  const int i = j - G2_LEAD;
  return g2_ops_before(i) + 2 + ((i >= 4 && i <= 11) ? 1 : 0);
}
constexpr int g2_wait_h(int j) { return g2_ops_before(j) - g2_pos_h(j); }

}

__global__ __launch_bounds__(256, 1) void s4d_mixer_fused(Params p) {
  __shared__ __attribute__((aligned(16))) char smem[LDS_BYTES];
  const int tid = threadIdx.x, lane = tid & 63;
  const int w = __builtin_amdgcn_readfirstlane(tid >> 6);
  const int b = blockIdx.x, g8 = b & 7, r = b >> 3;
  const int mt = 4 * (g8 >> 1) + (r >> 3), nt = 8 * (g8 & 1) + (r & 7);
  const int ustart = (nt * NUNIT) >> 4, unext = ((nt + 1) * NUNIT) >> 4;
  const int tg = 16 * mt + nt;
  const int cp = 32 * g8 + r;
  const int mt2 = 4 * g8 + (r >> 3), nt2 = r & 7;
  const int c15 = lane & 15, g = lane >> 4;
  const int c31 = lane & 31, hh = lane >> 5, b16 = (lane >> 4) & 1;
  const int q4 = (lane & 15) >> 2, p4 = lane & 3;

  const u32x4 sX = mk_srd(p.x, SEQ * DM * 2), sWS = mk_srd(p.ws, WS_END);
  STAMP(0);
  u32x4 xr0 = ld128(sX, lane * 16, (8 * tg + 2 * w) * 1024), xr1 = ld128(sX, lane * 16, (8 * tg + 2 * w + 1) * 1024);
  u32 eword = ld32c(sWS, WS_E, 0);
  u32 tch;
  {
    u64 pc;
    asm volatile("s_getpc_b64 %0" : "=s"(pc));
    const char* a;
    if (tid < 96) a = (const char*)p.x + (512 * (g8 >> 1) + 16 * r + tid / 6) * 1024 + (2 + tid % 6) * 128;
    else if (tid < 192) a = (const char*)p.w1 + (256 + 8 * r + (tid - 96) / 12) * (FF * 2) + (g8 & 1) * 1280 + ((tid - 96) % 12) * 120;
    else a = (const char*)pc + ((r * 64 + lane) % 200) * 128;
    tch = touch(a);
  }
  const int kch = w >> 1, kd = 2 * cp + kch;
  u32 s_ldt, s_dsk;
  asm volatile("s_load_dword %0, %2, %4\n\ts_load_dword %1, %3, %4" : "=&s"(s_ldt), "=&s"(s_dsk) : "s"(p.ldt), "s"(p.dsk), "s"(cp * 4) : "memory");
  const u32x4 sW1 = mk_srd(p.w1, DM * FF * 2), sW2 = mk_srd(p.w2, FF * DM * 2);
  const rsrc_t rWS = mk_rsrc(p.ws, WS_END), rOUT = mk_rsrc(p.out, SEQ * DM * 2);
  const u32 lb = (u32)(uintptr_t)(LAS char*)smem;
  {
    const u32x4 ps = {POISON, POISON, POISON, POISON};
#pragma unroll
    for (int i = 0; i < 2; ++i) {
      const int ln = (2 * w + i) * 8 + (lane >> 3);
      st128c(rWS, ps, WS_A + ((ln >> 1) * 512 + 2 * cp + (ln & 1)) * 128 + (lane & 7) * 16);
    }
  }
  u32 vx[4], vw[3];
#pragma unroll
  for (int j = 0; j < 4; ++j) vx[j] = (8 * j + (lane >> 3)) * 1024 - j * 1024 + (((lane & 7) ^ ((4 * j + (lane >> 4)) & 7)) * 16);
#pragma unroll
  for (int j = 0; j < 3; ++j) {
    const int P = (3 * w + j) * 64 + lane, k = P / 12, sl = P - 12 * k;
    vw[j] = k * (FF * 2) + sl * 16 - j * 1024;
  }
  const u32 x_so = (mt * 128 + 32 * w) * 1024, x_lo = lb + 32 * w * 128, w_so = ustart * 64, w_lo = lb + SLOT_W + 3 * w * 1024;
  auto st1_m0 = [&](auto KC, auto J) {
    if constexpr (J == 0) dma_m0(x_lo + (KC & 3) * SLOT);
    if constexpr (J == 4) dma_m0(w_lo + (KC & 3) * SLOT);
  };
  auto st1_go = [&](auto KC, auto J) {
    if constexpr (J < 4) dma_go<J * 1024>(sX, vx[J], x_so + KC * 128); else dma_go<(J - 4) * 1024>(sW1, vw[J - 4], w_so + KC * 64 * (FF * 2));
  };
  sfor<4>([&](auto KC) { sfor<7>([&](auto J) {
    if constexpr (J == 0 || J == 4) { st1_m0(KC, J); asm volatile("s_nop 0"); }
    st1_go(KC, J); }); });
  asm volatile("s_waitcnt vmcnt(31) lgkmcnt(0)" : "+v"(eword), "+v"(xr0), "+v"(xr1), "+s"(s_ldt), "+s"(s_dsk)::"memory");
  s_ldt = (s_ldt >> (16 * kch)) & 0xffffu; s_dsk = (s_dsk >> (16 * kch)) & 0xffffu;
  const u32 epoch = __builtin_amdgcn_readfirstlane(eword) * 0x9E3779B1u + 0x7F4A7C15u;
  STAMP(1);

  {
    const u32x4 xr[2] = {xr0, xr1};
    f32x2 x[2][4];
    float x0[2], sm[2], sq[2];
#pragma unroll
    for (int t = 0; t < 2; ++t) {
#pragma unroll
      for (int i = 0; i < 4; ++i) x[t][i] = bf2(xr[t][i]);
      x0[t] = asf(__builtin_amdgcn_readfirstlane(asu(x[t][0].x)));
      f32x2 s = {0.f, 0.f}, q = {0.f, 0.f};
#pragma unroll
      for (int i = 0; i < 4; ++i) { const f32x2 d = x[t][i] - sp2(x0[t]); s += d; q = fma2(d, d, q); }
      sm[t] = s.x + s.y; sq[t] = q.x + q.y;
    }
    float m1[2], r1[2], s2[2], q2[2];
#pragma unroll
    for (int t = 0; t < 1; ++t) wave_sum4(sm[0], sq[0], sm[1], sq[1]);
#pragma unroll
    for (int t = 0; t < 2; ++t) { sm[t] *= 1.0f / DM; sq[t] *= 1.0f / DM; }
#pragma unroll
    for (int t = 0; t < 2; ++t) {
      const float var = fmaxf(__builtin_fmaf(-sm[t], sm[t], sq[t]), 0.f);
      m1[t] = rbf(x0[t] + sm[t]);
      r1[t] = __builtin_amdgcn_rcpf(rbf(__builtin_amdgcn_sqrtf(rbf(rbf(var) + 1e-5f))));
      f32x2 s = {0.f, 0.f}, q = {0.f, 0.f};
      u32x4 hv;
#pragma unroll
      for (int i = 0; i < 4; ++i) {
        const f32x2 n = bf2(pk_bf16(bf2(pk_bf16(x[t][i] - sp2(m1[t]))) * sp2(r1[t])));
        s += n; q = fma2(n, n, q);
        hv[i] = pk_f16(n);
      }
      lds_st<u32x4>(smem, S_LN + (2 * w + t) * LN_ROW + lane * 16, hv);
      s2[t] = s.x + s.y; q2[t] = q.x + q.y;
    }
    float al[2], be[2];
#pragma unroll
    for (int t = 0; t < 1; ++t) wave_sum4(s2[0], q2[0], s2[1], q2[1]);
#pragma unroll
    for (int t = 0; t < 2; ++t) { s2[t] *= 1.0f / DM; q2[t] *= 1.0f / DM; }
#pragma unroll
    for (int t = 0; t < 2; ++t) {
      const float var2 = fmaxf(__builtin_fmaf(-s2[t], s2[t], q2[t]), 0.f);
      const float r2 = __builtin_amdgcn_rcpf(rbf(__builtin_amdgcn_sqrtf(rbf(rbf(var2) + 1e-5f))));
      al[t] = r1[t] * r2;
      be[t] = __builtin_fmaf(m1[t], r1[t], rbf(s2[t])) * r2;
    }
    const u32x4 sv = {asu(lane ? al[1] : al[0]), epoch, asu(lane ? be[1] : be[0]), epoch};
    st128c(rWS, sv, lane < 2 ? WS_STATS + (8 * tg + 2 * w + lane) * 16 : OOB);
    bar();
#pragma unroll
    for (int i = 0; i < 2; ++i) {
      const u32 a = S_LN + q4 * LN_ROW + ((2 * w + i) * 64 + 16 * g + 4 * p4) * 2;
      st128c(rWS, cat4(lds_tr(smem, a), lds_tr(smem, a + 4 * LN_ROW)), WS_XN + tg * 8192 + ((2 * w + i) * 64 + lane) * 16);
    }
  }
  STAMP(2);
  u32x2 b1v = ld64(mk_srd(p.b1, FF * 2), lane < 24 ? lane * 8 : OOB, ustart * 64);
  u32 q_lar, q_aim, q_cre, q_cim, q_cre2, q_cim2;
  {
    const u32 vo = (64 * (w & 1) + lane) * 2, so = kd * NMODE * 2;
    q_lar = ld16(mk_srd(p.lar, DM * NMODE * 2), vo, so); q_aim = ld16(mk_srd(p.aim, DM * NMODE * 2), vo, so);
    const u32x4 sCr = mk_srd(p.cre, DM * NMODE * 2), sCi = mk_srd(p.cim, DM * NMODE * 2);
    q_cre = ld16(sCr, vo, so); q_cim = ld16(sCi, vo, so);
    q_cre2 = ld16(sCr, vo ^ 128, so); q_cim2 = ld16(sCi, vo ^ 128, so);
  }

  f32x16 acc1[3];
  f32x4 csp[3];
  u32x4 stv;
  u32 lnf, tch2;
  {
    const u32 xa0 = lb + (32 * w + c31) * 128 + ((hh ^ ((c31 >> 1) & 7)) * 16);
    const u32 wa0 = lb + SLOT_W + (8 * hh + q4) * 192 + b16 * 32 + 8 * p4;
    u32 xa[2][4], wa[2], wc[2];
    u32x4 ones = {0x3f803f80u, 0x3f803f80u, 0x3f803f80u, 0x3f803f80u};
    tie(ones);
#pragma unroll
    for (int s = 0; s < 4; ++s) { xa[0][s] = xa0 ^ (s * 32); xa[1][s] = xa[0][s] + 2 * SLOT; }
    wa[0] = wa0; wa[1] = wa0 + 2 * SLOT;
    wc[0] = lb + SLOT_W + (32 * (w >> 1) + 8 * g + q4) * 192 + 3 * (w & 1) * 32 + 8 * p4; wc[1] = wc[0] + 2 * SLOT;
    u32x4 xf[2];
    u32x2 wl[2][3], wh[2][3], cl[2], chh[2];
    auto rd = [&](auto S, auto R) {
      constexpr int s = S, rr = R, buf = s & 1, kc = s >> 2, sl = kc & 3, so = (sl & 1) * SLOT, sp = s & 3;
      if constexpr (rr == 0) xf[buf] = a_ds128<so>(xa[sl >> 1][sp]);
      else {
        constexpr int u = (rr - 1) >> 1, hf = (rr - 1) & 1, off = so + u * 64 + (sp * 16 + hf * 4) * 192;
        if constexpr (hf == 0) wl[buf][u] = a_dstr<off>(wa[sl >> 1]); else wh[buf][u] = a_dstr<off>(wa[sl >> 1]);
      }
    };
    wait_vm<31>();
    tie(tch);
    bar();
    CSTAMP(0);
    sfor<7>([&](auto R) { rd(ic<0>{}, R); });
    sfor<32>([&](auto S) {
      constexpr int s = S, buf = s & 1, sp = s & 3, kc = s >> 2;
      constexpr bool last = s == 31, brk = sp == 2 && kc < 7;
      constexpr bool csr = sp < 3, cprev = sp >= 1;
      sfor<3>([&](auto U) {
        constexpr int u = U;
        constexpr int gi = sp == 2 ? (u == 2 ? 0 : -1) : (u == 0 ? -1 : (sp == 3 ? u : 2 * sp + 2 + u));
        constexpr int kcs = sp >= 2 ? kc + 3 : kc + 2;
        constexpr bool st = gi >= 0 && gi < 7 && kcs >= 4 && kcs < 8;
        if constexpr (u == 0) wait_lgkm<(s == 0 || sp == 0) ? 4 : 6>();
        else if constexpr (u == 1) {
          if constexpr (last) wait_lgkm<0>();
          else wait_lgkm<(csr ? 5 : 3)>();
          if constexpr (brk) {
            constexpr int n = kc + 1;
            wait_vm<(n == 1 ? 24 : (n <= 3 ? 17 : (n == 4 ? 7 : (n <= 6 ? 8 : 1))))>();
            asm volatile("s_barrier" ::: "memory");
            CSTAMP(n);
            if constexpr (n == 4) {
              st32c(rWS, epoch, tid == 0 ? WS_LNF + tg * 4 : OOB);
              tie(b1v);
              lds_st<f32x4>(smem, lane < 24 ? S_B1 + lane * 16 : S_MISC, f32x4{bflo(b1v.x), bfhi(b1v.x), bflo(b1v.y), bfhi(b1v.y)});
            }
            if constexpr (n == 5) stv = ld128c(sWS, WS_STATS + (128 * mt + 32 * w + c31) * 16, 0);
            if constexpr (n == 6) lnf = ld32c(sWS, WS_LNF + tid * 4, 0);
            if constexpr (n == 7) {
              const int ix = tid < 120 ? tid : tid - 120 - (tid >= 240 ? 120 : 0), ri = 15 * r + (ix >> 3);
              tch2 = touch((const char*)p.w2 + (ri < 256 ? 448 + ri : 896 + ri) * 1024 + (ix & 7) * 128);
            }
          }
        }
        if constexpr (st && (gi == 0 || gi == 4)) st1_m0(ic<kcs>{}, ic<gi>{});
        mfma32_vv<s == 0>(acc1[u], cat4(wl[buf][u], wh[buf][u]), xf[buf]);
        if constexpr (st) st1_go(ic<kcs>{}, ic<gi>{});
        if constexpr (!last) {
          if constexpr (u == 0) sfor<3>([&](auto R) { rd(ic<s + 1>{}, R); });
          if constexpr (u == 1) sfor<2>([&](auto R) { rd(ic<s + 1>{}, ic<3 + R>{}); });
          if constexpr (u == 2) sfor<2>([&](auto R) { rd(ic<s + 1>{}, ic<5 + R>{}); });
        }
        if constexpr (csr && u == 0) {
          constexpr int sl = kc & 3, so = (sl & 1) * SLOT + sp * 32;
          cl[sp & 1] = a_dstr<so>(wc[sl >> 1]); chh[sp & 1] = a_dstr<so + 4 * 192>(wc[sl >> 1]);
        }
        if constexpr (cprev && u == 2) mfma16_bf<kc == 0>(csp[sp - 1], cat4(cl[(sp - 1) & 1], chh[(sp - 1) & 1]), ones);
      });
    });
    asm volatile("s_nop 7\n\ts_nop 3" : "+v"(acc1[0]), "+v"(acc1[1]), "+v"(acc1[2]), "+v"(csp[0]), "+v"(csp[1]), "+v"(csp[2]));
    CSTAMP(8);
    CDUMP(9, 0);
  }
  STAMP(3);
#pragma unroll
  for (int i = 0; i < 3; ++i) lds_st<f32x4>(smem, c15 == 0 ? S_CSP + ((w >> 1) * 96 + (3 * (w & 1) + i) * 16 + 4 * g) * 4 : S_MISC, csp[i]);
  bar();
  {
    const int f = tid < 96 ? tid : 0;
    lds_st<float>(smem, tid < 96 ? S_CS + tid * 4 : S_MISC, lds_ld<float>(smem, S_CSP + f * 4) + lds_ld<float>(smem, S_CSP + 384 + f * 4));
  }
  asm volatile("s_waitcnt vmcnt(1)" : "+v"(stv), "+v"(lnf), "+v"(q_lar), "+v"(q_aim), "+v"(q_cre), "+v"(q_cim), "+v"(q_cre2), "+v"(q_cim2)::"memory");
#pragma unroll 1
  for (int it = 0; it < (1 << 16); ++it) {
    if (__all(stv.y == epoch && stv.w == epoch)) break;
    __builtin_amdgcn_s_sleep(1);
    stv = ld128c(sWS, WS_STATS + (128 * mt + 32 * w + c31) * 16, 0);
    asm volatile("s_waitcnt vmcnt(0)" : "+v"(stv)::"memory");
  }
  spin_flag(sWS, WS_LNF + tid * 4, epoch, lnf, true);
  u32x4 ug0 = ld128c(sWS, WS_XN + tid * 8192 + cp * 32, 0), ug1 = ld128c(sWS, WS_XN + tid * 8192 + cp * 32 + 16, 0);
  st32c(rWS, epoch, (b == 0 && tid == 0) ? WS_E : OOB);
  bar();
  STAMP(4);

  {
    const f32x2 al = sp2(asf(stv.x)), nb = sp2(-asf(stv.z));
    const u32 hso = WS_H + ((mt * 4 + w) * NUNIT + ustart) * 2048 + lane * 16;
#pragma unroll
    for (int u = 0; u < 3; ++u) {
#pragma unroll
      for (int s = 0; s < 2; ++s) {
        u32x4 o;
#pragma unroll
        for (int q = 0; q < 2; ++q) {
          const int fo = 32 * u + 16 * s + 8 * q;
          const f32x4 bi = lds_ld<f32x4>(smem, S_B1 + hh * 16 + fo * 4), cs = lds_ld<f32x4>(smem, S_CS + hh * 16 + fo * 4);
          const int rg = 8 * s + 4 * q;
          const f32x2 p0 = fma2(al, f32x2{acc1[u][rg], acc1[u][rg + 1]}, fma2(nb, f32x2{cs.x, cs.y}, f32x2{bi.x, bi.y}));
          const f32x2 p1 = fma2(al, f32x2{acc1[u][rg + 2], acc1[u][rg + 3]}, fma2(nb, f32x2{cs.z, cs.w}, f32x2{bi.z, bi.w}));
          o[2 * q] = pk_bf16(gelu2(p0));
          o[2 * q + 1] = pk_bf16(gelu2(p1));
        }
        st128c(rWS, o, ustart + u < unext ? hso + (u * 2 + s) * 1024 : OOB);
      }
    }
  }
  const int kh = w >> 1, mh = w & 1;
  const u32 vw2 = (lane >> 3) * 1024 + ((((lane & 7) >> 1) ^ ((lane >> 3) & 2)) * 32) + (lane & 1) * 16;
  const u32 vw2p[2] = {vw2, vw2 + 7168};
  const u32 vw2z[2] = {kh ? OOB : vw2p[0], kh ? OOB : vw2p[1]};
  const u32 w2_lo = lb + kh * W2_HALF + 2 * mh * 1024, w2_so = (kh * 22 * 32 + 16 * mh) * 1024 + nt2 * 128;
  auto st2_m0 = [&](auto J) { dma_m0(w2_lo + (J % W2_NS) * 4096); };
  auto st2_go = [&](auto J, auto I) { dma_go<I * 1024>(sW2, J == 21 ? vw2z[I] : vw2p[I], w2_so + J * 32 * 1024); };
  auto st2_piece = [&](auto N) {
    FENCE();
    if constexpr ((N & 1) == 0) { st2_m0(ic<(N >> 1)>{}); asm volatile("s_nop 0"); }
    st2_go(ic<(N >> 1)>{}, ic<(N & 1)>{});
    FENCE();
  };
  STAMP(5);

  float inv_sk;
  {
    CSTAMP(0);
    const int mh2 = w & 1;
    const float dt = rbf(expf(bf1(s_ldt)));
    const float zr = -rbf(expf(bf1(q_lar))) * dt, zi = bf1(q_aim) * dt;
    f32x2 C = f32x2{bf1(q_cre), bf1(q_cim)} * sp2(2.0f * dt);
    st2_piece(ic<0>{});
    const float bd = wave_sum((__builtin_fabsf(C.x) + __builtin_fabsf(C.y)) + (__builtin_fabsf(bf1(q_cre2)) + __builtin_fabsf(bf1(q_cim2))) * (2.0f * dt));
    int se = 267 - (int)((asu(bd) >> 23) & 0xff);
    se = se < 1 ? 1 : (se > 253 ? 253 : se);
    C *= sp2(asf((u32)se << 23));
    inv_sk = asf((u32)(254 - se) << 23);
    st2_piece(ic<1>{});
    const float e1 = zr * 1.44269504089f;
    const float ph = zi * 0.15915494309f;
    const float pl = __builtin_fmaf(zi, 0.15915494309f, -ph) + zi * 6.4206383e-9f;
    auto cexp = [&](float pw) {
      const float m = __builtin_amdgcn_exp2f(e1 * pw);
      const float a = __builtin_amdgcn_fractf(ph * pw) + pl * pw;
      return f32x2{__builtin_amdgcn_cosf(a), __builtin_amdgcn_sinf(a)} * sp2(m);
    };
    f32x2 P[8], Y[16], E[5], CE[17];
    P[1] = cexp(1.f); Y[1] = cexp(8.f);
    st2_piece(ic<2>{});
    E[0] = cexp(128.f);
    Y[0] = f32x2{1.f, 0.f}; CE[0] = C;
    const u32 tl = S_TAB + kch * TAB_CH + (64 * mh2 + lane) * 4;
    auto putY = [&](int i) { lds_st<u32>(smem, tl + i * TAB_ROW, pk_f16(f32x2{Y[i].x, -Y[i].y})); };
    auto putX = [&](int row, f32x2 v) { lds_st<u32>(smem, tl + (16 + row) * TAB_ROW, pk_f16(v)); };
    cmul2(P[2], P[1], P[1], Y[2], Y[1], Y[1]); cmul2(E[1], E[0], E[0], CE[1], C, E[0]);
    st2_piece(ic<3>{});
    cmul2(P[3], P[2], P[1], P[4], P[2], P[2]); cmul2(Y[3], Y[2], Y[1], Y[4], Y[2], Y[2]);
    cmul2(E[2], E[1], E[1], CE[2], CE[0], E[1]); cmul2(CE[3], CE[1], E[1], P[5], P[4], P[1]);
    putY(0); putY(1); putY(2); putY(3);
    st2_piece(ic<4>{});
    cmul2(P[6], P[4], P[2], P[7], P[4], P[3]); cmul2(Y[5], Y[4], Y[1], Y[6], Y[4], Y[2]);
    cmul2(Y[7], Y[4], Y[3], Y[8], Y[4], Y[4]); cmul2(E[3], E[2], E[2], CE[4], CE[0], E[2]);
    putY(4); putY(5); putY(6); putY(7);
    st2_piece(ic<5>{});
    cmul2(CE[5], CE[1], E[2], CE[6], CE[2], E[2]); cmul2(CE[7], CE[3], E[2], E[4], E[3], E[3]);
    cmul2(Y[9], Y[8], Y[1], Y[10], Y[8], Y[2]); cmul2(Y[11], Y[8], Y[3], Y[12], Y[8], Y[4]);
    putY(8); putY(9); putY(10); putY(11);
    st2_piece(ic<6>{});
    cmul2(Y[13], Y[8], Y[5], Y[14], Y[8], Y[6]); cmul2(Y[15], Y[8], Y[7], CE[16], C, E[4]);
    putY(12); putY(13); putY(14); putY(15);
    st2_piece(ic<7>{});
    CSTAMP(1);
    putX(0, CE[0]); putX(8, CE[1]);
    sfor<7>([&](auto I) {
      constexpr int i = I + 1;
      f32x2 a, c;
      cmul2(a, CE[0], P[i], c, CE[1], P[i]);
      putX(i, a); putX(8 + i, c);
      if constexpr (i == 2 || i == 4 || i == 6) st2_piece(ic<7 + i / 2>{});
    });
    putX(16, CE[2]); putX(20, CE[3]);
    sfor<3>([&](auto I) {
      constexpr int i = I + 1;
      f32x2 a, c;
      cmul2(a, CE[2], P[2 * i], c, CE[3], P[2 * i]);
      putX(16 + i, a); putX(20 + i, c);
    });
    st2_piece(ic<11>{});
    putX(24, CE[4]); putX(26, CE[5]); putX(28, CE[6]); putX(30, CE[7]);
    {
      f32x2 a, c;
      cmul2(a, CE[4], P[4], c, CE[5], P[4]); putX(25, a); putX(27, c);
      cmul2(a, CE[6], P[4], c, CE[7], P[4]); putX(29, a); putX(31, c);
    }
    st2_piece(ic<12>{});
    cmul2(CE[8], CE[0], E[3], CE[9], CE[1], E[3]); cmul2(CE[10], CE[2], E[3], CE[11], CE[3], E[3]);
    putX(32, CE[8]); putX(33, CE[9]); putX(34, CE[10]); putX(35, CE[11]);
    st2_piece(ic<13>{});
    cmul2(CE[12], CE[4], E[3], CE[13], CE[5], E[3]); cmul2(CE[14], CE[6], E[3], CE[15], CE[7], E[3]);
    putX(36, CE[12]); putX(37, CE[13]); putX(38, CE[14]); putX(39, CE[15]); putX(40, CE[16]);
    st2_piece(ic<14>{});
    CSTAMP(2);
    u32 ix[9];
#pragma unroll
    for (int e = 0; e < 9; ++e) {
      const int l = 2047 - (8 * tid + e);
      const u32 rb = pk_bf16(sp2((float)l)) & 0xffffu;
      ix[e] = (l < 0 ? 641u : (l < 128 ? (u32)l : rb - 17024u)) * 2;
    }
    st2_piece(ic<15>{});
    bar();
    CSTAMP(3);
    {
      const u32 rd = S_TAB + kch * TAB_CH + c15 * TAB_ROW + g * 16;
      const u32 kc = S_KC + kch * KC_CH;
      u32x4 yf[8];
#pragma unroll
      for (int s = 0; s < 8; ++s) yf[s] = lds_ld<u32x4>(smem, rd + s * 64);
      auto tile = [&](int t) {
        f32x4 a = {0, 0, 0, 0};
        const u32 ra = S_TAB + kch * TAB_CH + (16 + min(16 * t + c15, 40)) * TAB_ROW + g * 16;
#pragma unroll
        for (int s = 0; s < 8; ++s) a = mfma_h(lds_ld<u32x4>(smem, ra + s * 64), yf[s], a);
        return a;
      };
      if (mh2 == 0) {
        const f32x4 v0 = tile(0), v2 = tile(2);
        lds_st<u32x2>(smem, kc + (128 * (g >> 1) + 8 * c15 + 4 * (g & 1)) * 2, u32x2{pk_f16(f32x2{v0[0], v0[1]}), pk_f16(f32x2{v0[2], v0[3]})});
        const u32 h01 = pk_f16(f32x2{v2[0], v2[1]}), h23 = pk_f16(f32x2{v2[2], v2[3]});
        const u32 t2 = g < 2 ? 512 + 64 * g + c15 : 644, st2 = g < 2 ? 16 : 0;
        lds_st<u16>(smem, kc + ((g == 2 && c15 == 0) ? 640 : t2) * 2, (u16)h01);
        lds_st<u16>(smem, kc + (t2 + st2) * 2, (u16)(h01 >> 16));
        lds_st<u16>(smem, kc + (t2 + 2 * st2) * 2, (u16)h23);
        lds_st<u16>(smem, kc + (t2 + 3 * st2) * 2, (u16)(h23 >> 16));
      } else {
        const f32x4 v1 = tile(1);
        const u32 t1a = g < 2 ? 256 + 64 * g + 4 * c15 : 384 + 64 * (g - 2) + 2 * c15;
        lds_st<u32>(smem, kc + t1a * 2, pk_f16(f32x2{v1[0], v1[1]}));
        lds_st<u32>(smem, kc + (t1a + (g < 2 ? 2 : 32)) * 2, pk_f16(f32x2{v1[2], v1[3]}));
        if (lane == 0) lds_st<u16>(smem, kc + 641 * 2, (u16)0);
      }
    }
    CSTAMP(4);
    asm volatile("s_waitcnt vmcnt(16)" : "+v"(ug0), "+v"(ug1), "+v"(tch2)::"memory");
    st32c(rWS, epoch, lane == 0 ? WS_HF + ((mt * 4 + w) * 16 + nt) * 4 : OOB);
    st2_piece(ic<16>{});
    CSTAMP(5);
    bar();
    CSTAMP(6);
#pragma unroll
    for (int ch = 0; ch < 2; ++ch) {
      u32 e[9];
#pragma unroll
      for (int i = 0; i < 9; ++i) e[i] = lds_ld<u16>(smem, S_KC + ch * KC_CH + ix[i]);
      const u32x4 a0 = {e[0] | (e[1] << 16), e[2] | (e[3] << 16), e[4] | (e[5] << 16), e[6] | (e[7] << 16)};
      const u32x4 a1 = {e[1] | (e[2] << 16), e[3] | (e[4] << 16), e[5] | (e[6] << 16), e[7] | (e[8] << 16)};
      lds_st<u32x4>(smem, S_G + ch * G_CH + tid * 16, a0);
      lds_st<u32x4>(smem, S_G + ch * G_CH + G_PAR + tid * 16, a1);
      if (ch == 0) st2_piece(ic<17>{});
    }
    const u32x4 z4 = {0, 0, 0, 0};
    if (tid < 8) lds_st<u32x4>(smem, S_G + (tid >> 2) * G_CH + ((tid >> 1) & 1) * G_PAR + 4096 + (tid & 1) * 16, z4);
    if (tid < 60) lds_st<u32x4>(smem, S_U + (tid / 30) * U_CH + (tid % 30) * 16, z4);
    lds_st<u32x4>(smem, S_U + 480 + tid * 16, ug0);
    lds_st<u32x4>(smem, S_U + U_CH + 480 + tid * 16, ug1);
    CSTAMP(7);
    bar();
    CSTAMP(8);
    CDUMP(9, 10);
  }
  STAMP(6);

  u32x4 hf[22][2];
  const u32 hb = WS_H + ((mt2 * 2 + mh) * NUNIT + kh * 22) * 2048;
  const u32 hvo = lane * 16, hvz = kh ? OOB : hvo;
  auto ldh = [&](auto J, auto S) { hf[J][S] = ld128c_a(sWS, J == 21 ? hvz : hvo, hb + (J * 2 + S) * 1024); };
  {
    const int par = w & 1;
    const u32 ub = lb + S_U + kch * U_CH + 64 - 64 * par + 32 * c15 + 16 * g;
    const u32 gb0 = lb + S_G + kch * G_CH + ((c15 & 1) ? 0 : G_PAR) + 4 * (1015 - (c15 >> 1) - 16 * par + 4 * g - 32 * 31);
    const u32 gb[4] = {gb0, gb0 + 1024, gb0 + 2048, gb0 + 3072};
    const u32 hfo = WS_HF + (((mt2 >> 1) * 4 + 2 * (mt2 & 1) + mh) * 16 + c15) * 4;
    const bool hneed = kh ? c15 >= 8 : c15 <= 8;
    u32 hfl;
    u32x4 bq[32];
    u32x2 al[32], ah[32];
    f32x4 y[8];
    auto rdB = [&](auto M, auto T) { constexpr int q = 4 * T - M + 3; bq[q] = a_ds128<128 * q>(ub); };
    auto rdA = [&](auto M, auto H) {
      constexpr int k = (31 - M) >> 3, o = 32 * ((31 - M) & 7);
      if constexpr (H == 0) al[M] = a_ds2x32<o, o + 1>(gb[k]); else ah[M] = a_ds2x32<o + 2, o + 3>(gb[k]);
    };
    CSTAMP(0);
    sfor<2>([&](auto M) { sfor<8>([&](auto T) { rdB(M, T); }); rdA(M, ic<0>{}); rdA(M, ic<1>{}); });
    sfor<32>([&](auto M) {
      constexpr int m = M, t0 = m >> 2, n = 8 - t0;
      if constexpr (m == 11) hfl = ld32c(sWS, hfo, 0);
      if constexpr (m == 20) {
        asm volatile("s_waitcnt vmcnt(0)" : "+v"(hfl)::"memory");
        spin_flag(sWS, hfo, epoch, hfl, hneed);
      }
      wait_lgkm<(m <= 2 ? 10 : (m == 3 ? 4 : (m == 4 ? 6 : (2 * (31 - m) < 8 ? 2 * (31 - m) : 8))))>();
      sfor<n>([&](auto I) {
        constexpr int i = I, t = t0 + i;
        mfma16_h<m == 0>(y[t], cat4(al[m], ah[m]), bq[4 * t - m + 3]);
        if constexpr (m < 2) {
          rdB(ic<m + 2>{}, I);
          if constexpr (i >= 6) rdA(ic<m + 2>{}, ic<i - 6>{});
        } else if constexpr (m <= 4) {
          if constexpr (i < 4) rdA(ic<2 * m + (i >> 1)>{}, ic<i & 1>{});
        } else if constexpr (m + 5 < 32) {
          if constexpr (i < 2) rdA(ic<m + 5>{}, I);
        }
        if constexpr (m >= 1 && m <= 10 && i == 3) {
          constexpr int n = 17 + m;
          if constexpr ((n & 1) == 0) { st2_m0(ic<(n >> 1)>{}); asm volatile("s_nop 0"); }
          st2_go(ic<(n >> 1)>{}, ic<(n & 1)>{});
        }
        if constexpr (i == 0 && m >= 20 && (m & 1) == 0) ldh(ic<(m >= 20 ? (m - 20) / 4 : 0)>{}, ic<(m >= 20 ? ((m - 20) >> 1) & 1 : 0)>{});
      });
    });
    asm volatile("s_nop 7\n\ts_nop 1" : "+v"(y[0]), "+v"(y[1]), "+v"(y[2]), "+v"(y[3]), "+v"(y[4]), "+v"(y[5]), "+v"(y[6]), "+v"(y[7]));
    CSTAMP(1);
    bar();
    CSTAMP(2);
    auto send = [&](auto PAR) {
      FENCE();
#pragma unroll
      for (int i = 0; i < 4; ++i) lds_st<f32x4>(smem, S_EX + w * 4096 + i * 1024 + lane * 16, y[4 * (1 - PAR) + i]);
      FENCE();
    };
    if (par) send(ic<1>{}); else send(ic<0>{});
    ldh(ic<3>{}, ic<0>{}); ldh(ic<3>{}, ic<1>{});
    CSTAMP(3);
    bar();
    CSTAMP(4);
    ldh(ic<4>{}, ic<0>{}); ldh(ic<4>{}, ic<1>{});
    const float dsk = bf1(s_dsk);
    u32x2 ao[4];
    auto finish = [&](auto PAR) {
      FENCE();
#pragma unroll
      for (int i = 0; i < 4; ++i) {
        const f32x4 v = (y[4 * PAR + i] + lds_ld<f32x4>(smem, S_EX + (w ^ 1) * 4096 + i * 1024 + lane * 16)) * inv_sk;
        const u32x2 uu = lds_ld<u32x2>(smem, S_U + kch * U_CH + (240 + 256 * (4 * PAR + i) + 16 * c15 + 4 * g) * 2);
        const f16x2 u01 = ash2(uu.x), u23 = ash2(uu.y);
        ao[i] = u32x2{pk_bf16(fma2(f32x2{(float)u01.x, (float)u01.y}, sp2(dsk), f32x2{v[0], v[1]})),
                      pk_bf16(fma2(f32x2{(float)u23.x, (float)u23.y}, sp2(dsk), f32x2{v[2], v[3]}))};
      }
      FENCE();
    };
    if (par) finish(ic<1>{}); else finish(ic<0>{});
#pragma unroll
    for (int i = 0; i < 4; ++i) {
      u32x2 o = ao[i];
      o.x = o.x == POISON ? 0xffc0ffc0u : o.x;
      o.y = o.y == POISON ? 0xffc0ffc0u : o.y;
      st64c(rWS, o, WS_A + ((4 * (4 * par + i) + (c15 >> 2)) * 512 + kd) * 128 + ((c15 & 3) * 16 + 4 * g) * 2);
    }
    CSTAMP(5);
    sfor<3>([&](auto J) { ldh(ic<5 + J>{}, ic<0>{}); ldh(ic<5 + J>{}, ic<1>{}); });
    CSTAMP(6);
    CDUMP(7, 21);
  }
  STAMP(7);

  {
    u32 wa[2];
#pragma unroll
    for (int n = 0; n < 2; ++n) wa[n] = lb + kh * W2_HALF + (4 * hh + q4) * 128 + (((2 * n + b16) ^ (q4 & 2)) * 32) + 8 * p4;
    f32x16 acc[2];
    u32x2 wl[2][4], wh[2][4];
    u32x4 at0, at1;
    u32x4 xt[2];
    u32x2 b2v[4];
    const int ch0 = 64 * nt2 + 32 * kh + 4 * hh;
    const u32 ato = WS_A + (mt2 * 512 + nt2 * 64) * 128 + tid * 16;
    const u32 xto = (64 * mt2 + (tid >> 3)) * 1024 + nt2 * 128 + (tid & 7) * 16;
    auto rd = [&](auto J, auto I, auto H) {
      constexpr int j = J, i = I, off = (j % W2_NS) * 4096 + ((i >> 1) * 16 + H * 8) * 128;
      if constexpr (H == 0) wl[j & 1][i] = a_dstr<off>(wa[i & 1]); else wh[j & 1][i] = a_dstr<off>(wa[i & 1]);
    };
    bar();
    sfor<4>([&](auto I) { rd(ic<0>{}, I, ic<0>{}); rd(ic<0>{}, I, ic<1>{}); });
    sfor<22>([&](auto J) {
      constexpr int j = J;
      constexpr bool last = j == 21;
      if constexpr (j == 4 || j == 8) bar();
      if constexpr (j == 13 || j == 17) {
        wait_vm<g2_ops_before(j) - g2_ops_before(j == 13 ? 8 : 12)>();
        bar();
      }
      wait_vm<g2_wait_h(j)>();
      asm volatile("" : "+a"(hf[j][0]), "+a"(hf[j][1]));
      sfor<4>([&](auto I) {
        constexpr int i = I;
        constexpr bool stg = j >= 4 && j <= 11 && (i & 1);
        constexpr int ju = 14 + (j - 4) , pi = i >> 1;
        if constexpr (j == 4 || j == 8 || j == 13 || j == 17) { if constexpr (i == 0) wait_lgkm<0>(); }
        else wait_lgkm<(last ? 6 - 2 * i : 6)>();
        if constexpr (stg && pi == 0) st2_m0(ic<ju>{});
        mfma32_va<j == 0 && i < 2>(acc[i & 1], cat4(wl[j & 1][i], wh[j & 1][i]), hf[j][i >> 1]);
        if constexpr (stg) st2_go(ic<ju>{}, ic<pi>{});
        if constexpr ((i & 1) == 0 && j + G2_LEAD <= 21) ldh(ic<j + G2_LEAD>{}, ic<(i >> 1)>{});
        if constexpr (!last) { rd(ic<j + 1>{}, I, ic<0>{}); rd(ic<j + 1>{}, I, ic<1>{}); }
        if constexpr (j == 15 && i < 2) xt[i] = ld128(sX, xto + i * 32 * 1024, 0);
        if constexpr (j == 15 && i == 2) {
          const u32x4 sB2 = mk_srd(p.b2, DM * 2);
#pragma unroll
          for (int q = 0; q < 4; ++q) b2v[q] = ld64(sB2, (ch0 + 8 * q) * 2, 0);
        }
        if constexpr (j == 18 && i == 0) { at0 = ld128c(sWS, ato, 0); at1 = ld128c(sWS, ato + 4096, 0); }
      });
    });
    asm volatile("s_nop 7\n\ts_nop 3" : "+v"(acc[0]), "+v"(acc[1]));
    STAMP(8);
    auto send2 = [&](auto KH) {
      FENCE();
#pragma unroll
      for (int q = 0; q < 4; ++q) {
        const f32x16& as = acc[1 - KH];
        lds_st<f32x4>(smem, S_EX + w * 4096 + q * 1024 + lane * 16, f32x4{as[4 * q], as[4 * q + 1], as[4 * q + 2], as[4 * q + 3]});
      }
      FENCE();
    };
    if (kh) send2(ic<1>{}); else send2(ic<0>{});
    asm volatile("s_waitcnt vmcnt(0)" : "+v"(at0), "+v"(at1), "+v"(xt[0]), "+v"(xt[1]), "+v"(b2v[0]), "+v"(b2v[1]), "+v"(b2v[2]), "+v"(b2v[3])::"memory");
    const int row0 = tid >> 3, sl = tid & 7;
    {
      const u32 o = S_XT + row0 * 128 + ((sl ^ (row0 & 7)) * 16);
      lds_st<u32x4>(smem, o, xt[0]);
      lds_st<u32x4>(smem, o + 32 * 128, xt[1]);
    }
#pragma unroll 1
    for (int it = 0; it < (1 << 16); ++it) {
      const u32 m0 = max(max(at0.x, at0.y), max(at0.z, at0.w)), m1 = max(max(at1.x, at1.y), max(at1.z, at1.w));
      if (!__any(max(m0, m1) == POISON)) break;
      at0 = ld128c(sWS, ato, 0); at1 = ld128c(sWS, ato + 4096, 0);
      asm volatile("s_waitcnt vmcnt(0)" : "+v"(at0), "+v"(at1)::"memory");
    }
    {
      const u32 o = S_AT + row0 * 128 + (((sl >> 1) ^ (row0 & 2)) * 32) + (sl & 1) * 16;
      lds_st<u32x4>(smem, o, at0);
      lds_st<u32x4>(smem, o + 32 * 128, at1);
    }
    bar();
    const int tl = 32 * mh + c31;
    auto fin2 = [&](auto KH) {
      FENCE();
#pragma unroll
      for (int q = 0; q < 4; ++q) {
        const f32x16& ak = acc[KH];
        const f32x4 v = f32x4{ak[4 * q], ak[4 * q + 1], ak[4 * q + 2], ak[4 * q + 3]} + lds_ld<f32x4>(smem, S_EX + (w ^ 2) * 4096 + q * 1024 + lane * 16);
        const int row = 32 * KH + 8 * q + 4 * hh + q4;
        const u32x2 av = lds_tr(smem, S_AT + row * 128 + (((2 * mh + b16) ^ (row & 2)) * 32) + 8 * p4);
        const u32 po = tl * 128 + (((4 * KH + q) ^ (tl & 7)) * 16) + hh * 8;
        const u32x2 xv = lds_ld<u32x2>(smem, S_XT + po);
        const f32x2 o0 = (bf2(xv.x) + bf2(av.x)) + (f32x2{v[0], v[1]} + bf2(b2v[q].x)), o1 = (bf2(xv.y) + bf2(av.y)) + (f32x2{v[2], v[3]} + bf2(b2v[q].y));
        lds_st<u32x2>(smem, S_OT + po, u32x2{pk_bf16(o0), pk_bf16(o1)});
      }
      FENCE();
    };
    if (kh) fin2(ic<1>{}); else fin2(ic<0>{});
    bar();
    {
      const u32 o = S_OT + row0 * 128 + ((sl ^ (row0 & 7)) * 16);
      const u32x4 o0 = lds_ld<u32x4>(smem, o), o1 = lds_ld<u32x4>(smem, o + 32 * 128);
      __builtin_amdgcn_raw_buffer_store_b128(o0, rOUT, xto, 0, 0);
      __builtin_amdgcn_raw_buffer_store_b128(o1, rOUT, xto + 32 * 1024, 0, 0);
    }
  }
  STAMP(9);
}

extern "C" void kernel_launch(void* const* d_in, const int* in_sizes, int n_in, void* d_out, int out_size, void* d_ws,
                              size_t ws_size, hipStream_t stream) {
  Params p;
  p.x = (const u16*)d_in[0]; p.lar = (const u16*)d_in[1]; p.aim = (const u16*)d_in[2]; p.cre = (const u16*)d_in[3];
  p.cim = (const u16*)d_in[4]; p.ldt = (const u16*)d_in[5]; p.dsk = (const u16*)d_in[6];
  p.w1 = (const u16*)d_in[8]; p.b1 = (const u16*)d_in[9]; p.w2 = (const u16*)d_in[10]; p.b2 = (const u16*)d_in[11];
  p.out = (u16*)d_out; p.ws = (char*)d_ws;
  s4d_mixer_fused<<<256, 256, 0, stream>>>(p);
}
```

```cpp
#include <hip/hip_runtime.h>
#include <stdint.h>
#include <utility>

namespace {
typedef unsigned short u16;
typedef unsigned u32;
typedef unsigned long long u64;
typedef float f32x2 __attribute__((ext_vector_type(2)));
typedef float f32x4 __attribute__((ext_vector_type(4)));
typedef float f32x16 __attribute__((ext_vector_type(16)));
typedef u32 u32x2 __attribute__((ext_vector_type(2)));
typedef u32 u32x4 __attribute__((ext_vector_type(4)));
typedef short s16x4 __attribute__((ext_vector_type(4)));
typedef __bf16 bf16x2 __attribute__((ext_vector_type(2)));
typedef _Float16 f16x2 __attribute__((ext_vector_type(2)));
typedef _Float16 f16x8 __attribute__((ext_vector_type(8)));
#define LAS __attribute__((address_space(3)))
#define DEV __device__ __forceinline__
typedef __amdgpu_buffer_rsrc_t rsrc_t;

constexpr int SEQ = 2048, DM = 512, NMODE = 128, FF = 1376, NUNIT = 43;
constexpr u32 WS_E = 0, WS_LNF = 0x1000, WS_HF = 0x2000, WS_STATS = 0x8000, WS_XN = 0x10000, WS_A = 0x210000, WS_H = 0x410000,
              WS_END = WS_H + 64 * NUNIT * 2048, WS_STAMP = 0xA00000;
constexpr u32 SLOT = 28672, SLOT_W = 16384;
constexpr u32 W2_NS = 14, W2_HALF = W2_NS * 4096;
constexpr u32 LDS_BYTES_ = 163840;
constexpr u32 S0 = 114688;
constexpr u32 S_LN = S0, LN_ROW = 1088;
constexpr u32 TAB_ROW = 544, TAB_CH = 57 * TAB_ROW;
constexpr u32 S_TAB = LDS_BYTES_ - 2 * TAB_CH - 1808;
constexpr u32 S_KC = S_TAB - 2592, KC_CH = 1296;
constexpr u32 S_CSP = LDS_BYTES_ - 1808;
constexpr u32 S_B1 = S_CSP + 768;
constexpr u32 S_MISC = S_B1 + 384;
constexpr u32 S_CS = S_MISC + 64;
constexpr u32 S_G = S0, G_PAR = 4160, G_CH = 8320;
constexpr u32 S_U = S0 + 16640, U_CH = 4608;
constexpr u32 S_EX = S0;
constexpr u32 S_AT = S0 + 16384, S_XT = S0 + 24576, S_OT = S0 + 32768;
constexpr u32 LDS_BYTES = 163840;
constexpr u32 OOB = 0x80000000u;
constexpr u32 POISON = 0xffffffffu;

#define STAMP(k) do {} while (0)
#define CSTAMP(i) do {} while (0)
#define CDUMP(n, base) do {} while (0)

struct Params {
  const u16 *x, *lar, *aim, *cre, *cim, *ldt, *dsk, *w1, *b1, *w2, *b2;
  u16* out;
  char* ws;
};

DEV float asf(u32 v) { return __builtin_bit_cast(float, v); }
DEV u32 asu(float v) { return __builtin_bit_cast(u32, v); }
DEV f16x2 ash2(u32 v) { return __builtin_bit_cast(f16x2, v); }
DEV float bflo(u32 v) { return asf(v << 16); }
DEV float bfhi(u32 v) { return asf(v & 0xffff0000u); }
DEV f32x2 bf2(u32 v) { return f32x2{bflo(v), bfhi(v)}; }
DEV u32 pk_bf16(f32x2 v) { return __builtin_bit_cast(u32, __builtin_convertvector(v, bf16x2)); }
DEV u32 pk_f16(f32x2 v) { return __builtin_bit_cast(u32, __builtin_convertvector(v, f16x2)); }
DEV float rbf(float a) { return bflo(pk_bf16(f32x2{a, a})); }
DEV float bf1(u32 v) { return asf(v << 16); }
DEV f32x2 sp2(float a) { return f32x2{a, a}; }
DEV f32x2 fma2(f32x2 a, f32x2 b, f32x2 c) { return __builtin_elementwise_fma(a, b, c); }

template <int N> DEV void wait_vm() { asm volatile("s_waitcnt vmcnt(%0)" ::"n"(N) : "memory"); }
template <int N> DEV void wait_lgkm() { asm volatile("s_waitcnt lgkmcnt(%0)" ::"n"(N)); }
DEV void bar() { asm volatile("s_waitcnt lgkmcnt(0)\n\ts_barrier" ::: "memory"); }
DEV u32x4 mk_srd(const void* base, u32 bytes) {
  const u64 b = (u64)base;
  u32x4 s;
  s.x = __builtin_amdgcn_readfirstlane((u32)b);
  s.y = __builtin_amdgcn_readfirstlane((u32)(b >> 32)) & 0xffffu;
  s.z = bytes;
  s.w = 0x00020000u;
  return s;
}
DEV rsrc_t mk_rsrc(const void* base, u32 bytes) { return __builtin_amdgcn_make_buffer_rsrc((void*)base, 0, bytes, 0x00020000); }

#define BLOAD(INS, TY, NAME, FLAGS, CON)                                                                     \
  DEV TY NAME(u32x4 srd, u32 voff, u32 soff) {                                                               \
    TY r;                                                                                                    \
    asm volatile(INS " %0, %1, %2, %3 offen" FLAGS : CON(r) : "v"(voff), "s"(srd), "s"(soff) : "memory");     \
    return r;                                                                                                \
  }
BLOAD("buffer_load_dwordx4", u32x4, ld128, "", "=v")
BLOAD("buffer_load_dwordx4", u32x4, ld128c, " sc1", "=v")
BLOAD("buffer_load_dwordx4", u32x4, ld128c_a, " sc1", "=a")
BLOAD("buffer_load_dwordx2", u32x2, ld64, "", "=v")
BLOAD("buffer_load_dword", u32, ld32c, " sc1", "=v")
BLOAD("buffer_load_ushort", u32, ld16, "", "=v")
DEV u32 touch(const void* a) { u32 r; asm volatile("global_load_dword %0, %1, off" : "=v"(r) : "v"(a) : "memory"); return r; }
template <class T> DEV void tie(T& v) { asm volatile("" : "+v"(v)); }

DEV void dma_m0(u32 lds_addr) { asm volatile("s_mov_b32 m0, %0" ::"s"(lds_addr) : "memory"); }
template <int IOFF> DEV void dma_go(u32x4 srd, u32 voff, u32 soff) {
  asm volatile("buffer_load_dwordx4 %0, %1, %2 offen offset:%3 lds" ::"v"(voff), "s"(srd), "s"(soff), "n"(IOFF) : "memory");
}
DEV void st128c(rsrc_t r, u32x4 v, u32 voff) { __builtin_amdgcn_raw_buffer_store_b128(v, r, voff, 0, 16); }
DEV void st64c(rsrc_t r, u32x2 v, u32 voff) { __builtin_amdgcn_raw_buffer_store_b64(v, r, voff, 0, 16); }
DEV void st32c(rsrc_t r, u32 v, u32 voff) { __builtin_amdgcn_raw_buffer_store_b32(v, r, voff, 0, 16); }
DEV void st64(rsrc_t r, u32x2 v, u32 voff) { __builtin_amdgcn_raw_buffer_store_b64(v, r, voff, 0, 0); }

template <class T> DEV T lds_ld(const char* smem, u32 off) { return *(const LAS T*)(smem + off); }
template <class T> DEV void lds_st(char* smem, u32 off, T v) { *(LAS T*)(smem + off) = v; }
DEV u32x2 lds_tr(const char* smem, u32 off) { return __builtin_bit_cast(u32x2, __builtin_amdgcn_ds_read_tr16_b64_v4i16((LAS s16x4*)(smem + off))); }

template <int I> using ic = std::integral_constant<int, I>;
template <class F, int... Is> DEV void sfor_impl(F&& f, std::integer_sequence<int, Is...>) { (f(ic<Is>{}), ...); }
template <int N, class F> DEV void sfor(F&& f) { sfor_impl(f, std::make_integer_sequence<int, N>{}); }
template <int OFF> DEV u32x4 a_ds128(u32 addr) { u32x4 r; asm volatile("ds_read_b128 %0, %1 offset:%2" : "=v"(r) : "v"(addr), "n"(OFF)); return r; }
template <int OFF> DEV u32x2 a_dstr(u32 addr) { u32x2 r; asm volatile("ds_read_b64_tr_b16 %0, %1 offset:%2" : "=v"(r) : "v"(addr), "n"(OFF)); return r; }
template <int O0, int O1> DEV u32x2 a_ds2x32(u32 addr) { u32x2 r; asm volatile("ds_read2_b32 %0, %1 offset0:%2 offset1:%3" : "=v"(r) : "v"(addr), "n"(O0), "n"(O1)); return r; }
DEV u32x4 cat4(u32x2 a, u32x2 b) { return __builtin_shufflevector(a, b, 0, 1, 2, 3); }
template <bool INIT> DEV void mfma32_vv(f32x16& c, u32x4 a, u32x4 b) {
  if constexpr (INIT) asm volatile("v_mfma_f32_32x32x16_bf16 %0, %1, %2, 0" : "=&v"(c) : "v"(a), "v"(b));
  else asm volatile("v_mfma_f32_32x32x16_bf16 %0, %1, %2, %0" : "+v"(c) : "v"(a), "v"(b));
}
template <bool INIT> DEV void mfma32_va(f32x16& c, u32x4 a, u32x4 b) {
  if constexpr (INIT) asm volatile("v_mfma_f32_32x32x16_bf16 %0, %1, %2, 0" : "=&v"(c) : "v"(a), "a"(b));
  else asm volatile("v_mfma_f32_32x32x16_bf16 %0, %1, %2, %0" : "+v"(c) : "v"(a), "a"(b));
}
template <bool INIT> DEV void mfma16_h(f32x4& c, u32x4 a, u32x4 b) {
  if constexpr (INIT) asm volatile("v_mfma_f32_16x16x32_f16 %0, %1, %2, 0" : "=&v"(c) : "v"(a), "v"(b));
  else asm volatile("v_mfma_f32_16x16x32_f16 %0, %1, %2, %0" : "+v"(c) : "v"(a), "v"(b));
}
DEV f32x4 mfma_h(u32x4 a, u32x4 b, f32x4 c) {
  return __builtin_amdgcn_mfma_f32_16x16x32_f16(__builtin_bit_cast(f16x8, a), __builtin_bit_cast(f16x8, b), c, 0, 0, 0);
}
template <bool INIT> DEV void mfma16_bf(f32x4& c, u32x4 a, u32x4 b) {
  if constexpr (INIT) asm volatile("v_mfma_f32_16x16x32_bf16 %0, %1, %2, 0" : "=&v"(c) : "v"(a), "v"(b));
  else asm volatile("v_mfma_f32_16x16x32_bf16 %0, %1, %2, %0" : "+v"(c) : "v"(a), "v"(b));
}
#define FENCE() __builtin_amdgcn_sched_barrier(0)

DEV void wave_sum4(float& a, float& b, float& c, float& d) {
#define S4(CTRL) "v_add_f32_dpp %0, %0, %0 " CTRL "\n\tv_add_f32_dpp %1, %1, %1 " CTRL "\n\tv_add_f32_dpp %2, %2, %2 " CTRL "\n\tv_add_f32_dpp %3, %3, %3 " CTRL "\n\t"
  asm volatile("s_nop 1\n\t" S4("row_shr:1 row_mask:0xf bank_mask:0xf bound_ctrl:1") S4("row_shr:2 row_mask:0xf bank_mask:0xf bound_ctrl:1")
               S4("row_shr:4 row_mask:0xf bank_mask:0xf bound_ctrl:1") S4("row_shr:8 row_mask:0xf bank_mask:0xf bound_ctrl:1")
               S4("row_bcast:15 row_mask:0xa bank_mask:0xf") S4("row_bcast:31 row_mask:0xc bank_mask:0xf") "s_nop 0"
               : "+v"(a), "+v"(b), "+v"(c), "+v"(d));
#undef S4
  a = asf(__builtin_amdgcn_readlane(asu(a), 63)); b = asf(__builtin_amdgcn_readlane(asu(b), 63));
  c = asf(__builtin_amdgcn_readlane(asu(c), 63)); d = asf(__builtin_amdgcn_readlane(asu(d), 63));
}
template <int CTRL, int RM> DEV float dpp_add(float v) { return v + asf(__builtin_amdgcn_update_dpp(0, asu(v), CTRL, RM, 0xf, true)); }
DEV float wave_sum(float v) {
  v = dpp_add<0x111, 0xf>(v); v = dpp_add<0x112, 0xf>(v); v = dpp_add<0x114, 0xf>(v); v = dpp_add<0x118, 0xf>(v);
  v = dpp_add<0x142, 0xa>(v); v = dpp_add<0x143, 0xc>(v);
  return asf(__builtin_amdgcn_readlane(asu(v), 63));
}
DEV void cmul2(f32x2& r0, f32x2 a0, f32x2 b0, f32x2& r1, f32x2 a1, f32x2 b1) {
  f32x2 t0, t1;
  asm("v_pk_mul_f32 %0, %4, %5 op_sel_hi:[1,0]\n\tv_pk_mul_f32 %1, %6, %7 op_sel_hi:[1,0]\n\t"
      "v_pk_fma_f32 %2, %4, %5, %0 op_sel:[1,1,0] op_sel_hi:[0,1,1] neg_lo:[1,0,0]\n\t"
      "v_pk_fma_f32 %3, %6, %7, %1 op_sel:[1,1,0] op_sel_hi:[0,1,1] neg_lo:[1,0,0]"
      : "=&v"(t0), "=&v"(t1), "=&v"(r0), "=&v"(r1) : "v"(a0), "v"(b0), "v"(a1), "v"(b1));
}
DEV f32x2 cmul(f32x2 a, f32x2 b) {
  f32x2 t, r;
  asm("v_pk_mul_f32 %0, %2, %3 op_sel_hi:[1,0]\n\tv_pk_fma_f32 %1, %2, %3, %0 op_sel:[1,1,0] op_sel_hi:[0,1,1] neg_lo:[1,0,0]"
      : "=&v"(t), "=v"(r) : "v"(a), "v"(b));
  return r;
}
DEV f32x2 gelu2(f32x2 v) {
  const f32x2 s = v * fma2(v * v, sp2(-0.10294323970f), sp2(-2.3022081986f));
  const f32x2 d = f32x2{__builtin_amdgcn_exp2f(s.x), __builtin_amdgcn_exp2f(s.y)} + sp2(1.0f);
  return v * f32x2{__builtin_amdgcn_rcpf(d.x), __builtin_amdgcn_rcpf(d.y)};
}
DEV void spin_flag(u32x4 srd, u32 voff, u32 epoch, u32 val, bool need) {
#pragma unroll 1
  for (int it = 0; it < (1 << 16); ++it) {
    if (__all(!need || val == epoch)) break;
    __builtin_amdgcn_s_sleep(1);
    val = ld32c(srd, voff, 0);
    asm volatile("s_waitcnt vmcnt(0)" : "+v"(val)::"memory");
  }
}

constexpr int G2_LEAD = 8;
constexpr int g2_unit_ops(int j) { return (j + G2_LEAD <= 21 ? 2 : 0) + ((j >= 4 && j <= 11) ? 2 : 0) + (j == 15 ? 6 : 0) + (j == 18 ? 2 : 0); }
constexpr int g2_ops_before(int j) { int n = 16 + 4; for (int i = 0; i < j; ++i) n += g2_unit_ops(i); return n; }
constexpr int g2_pos_h(int j) {
  if (j <= 4) return 2 * j + 2;
  if (j < G2_LEAD) return 2 * j + 2 + 4;
  const int i = j - G2_LEAD;
  return g2_ops_before(i) + 2 + ((i >= 4 && i <= 11) ? 1 : 0);
}
constexpr int g2_wait_h(int j) { return g2_ops_before(j) - g2_pos_h(j); }

}

__global__ __launch_bounds__(256, 1) void s4d_mixer_fused(Params p) {
  __shared__ __attribute__((aligned(16))) char smem[LDS_BYTES];
  const int tid = threadIdx.x, lane = tid & 63;
  const int w = __builtin_amdgcn_readfirstlane(tid >> 6);
  const int b = blockIdx.x, g8 = b & 7, r = b >> 3;
  const int mt = 4 * (g8 >> 1) + (r >> 3), nt = 8 * (g8 & 1) + (r & 7);
  const int ustart = (nt * NUNIT) >> 4, unext = ((nt + 1) * NUNIT) >> 4;
  const int tg = 16 * mt + nt;
  const int cp = 32 * g8 + r;
  const int mt2 = 4 * g8 + (r >> 3), nt2 = r & 7;
  const int c15 = lane & 15, g = lane >> 4;
  const int c31 = lane & 31, hh = lane >> 5, b16 = (lane >> 4) & 1;
  const int q4 = (lane & 15) >> 2, p4 = lane & 3;

  const u32x4 sX = mk_srd(p.x, SEQ * DM * 2), sWS = mk_srd(p.ws, WS_END);
  const u64 amp0 = __builtin_amdgcn_s_memrealtime();
  if (threadIdx.x == 0) __hip_atomic_store((u64*)(p.ws + 0xA80000) + blockIdx.x, amp0, __ATOMIC_RELAXED, __HIP_MEMORY_SCOPE_AGENT);
  u64 ampk = amp0;
  u32x4 xr0 = ld128(sX, lane * 16, (8 * tg + 2 * w) * 1024), xr1 = ld128(sX, lane * 16, (8 * tg + 2 * w + 1) * 1024);
  u32 eword = ld32c(sWS, WS_E, 0);
  u32 tch;
  {
    u64 pc;
    asm volatile("s_getpc_b64 %0" : "=s"(pc));
    const char* a;
    if (tid < 96) a = (const char*)p.x + (512 * (g8 >> 1) + 16 * r + tid / 6) * 1024 + (2 + tid % 6) * 128;
    else if (tid < 192) a = (const char*)p.w1 + (256 + 8 * r + (tid - 96) / 12) * (FF * 2) + (g8 & 1) * 1280 + ((tid - 96) % 12) * 120;
    else a = (const char*)pc + ((r * 64 + lane) % 200) * 128;
    tch = touch(a);
  }
  const int kch = w >> 1, kd = 2 * cp + kch;
  u32 s_ldt, s_dsk;
  asm volatile("s_load_dword %0, %2, %4\n\ts_load_dword %1, %3, %4" : "=&s"(s_ldt), "=&s"(s_dsk) : "s"(p.ldt), "s"(p.dsk), "s"(cp * 4) : "memory");
  const u32x4 sW1 = mk_srd(p.w1, DM * FF * 2), sW2 = mk_srd(p.w2, FF * DM * 2);
  const rsrc_t rWS = mk_rsrc(p.ws, WS_END), rOUT = mk_rsrc(p.out, SEQ * DM * 2);
  const u32 lb = (u32)(uintptr_t)(LAS char*)smem;
  {
    const u32x4 ps = {POISON, POISON, POISON, POISON};
#pragma unroll
    for (int i = 0; i < 2; ++i) {
      const int ln = (2 * w + i) * 8 + (lane >> 3);
      st128c(rWS, ps, WS_A + ((ln >> 1) * 512 + 2 * cp + (ln & 1)) * 128 + (lane & 7) * 16);
    }
  }
  u32 vx[4], vw[3];
#pragma unroll
  for (int j = 0; j < 4; ++j) vx[j] = (8 * j + (lane >> 3)) * 1024 - j * 1024 + (((lane & 7) ^ ((4 * j + (lane >> 4)) & 7)) * 16);
#pragma unroll
  for (int j = 0; j < 3; ++j) {
    const int P = (3 * w + j) * 64 + lane, k = P / 12, sl = P - 12 * k;
    vw[j] = k * (FF * 2) + sl * 16 - j * 1024;
  }
  const u32 x_so = (mt * 128 + 32 * w) * 1024, x_lo = lb + 32 * w * 128, w_so = ustart * 64, w_lo = lb + SLOT_W + 3 * w * 1024;
  auto st1_m0 = [&](auto KC, auto J) {
    if constexpr (J == 0) dma_m0(x_lo + (KC & 3) * SLOT);
    if constexpr (J == 4) dma_m0(w_lo + (KC & 3) * SLOT);
  };
  auto st1_go = [&](auto KC, auto J) {
    if constexpr (J < 4) dma_go<J * 1024>(sX, vx[J], x_so + KC * 128); else dma_go<(J - 4) * 1024>(sW1, vw[J - 4], w_so + KC * 64 * (FF * 2));
  };
  sfor<4>([&](auto KC) { sfor<7>([&](auto J) {
    if constexpr (J == 0 || J == 4) { st1_m0(KC, J); asm volatile("s_nop 0"); }
    st1_go(KC, J); }); });
  asm volatile("s_waitcnt vmcnt(31) lgkmcnt(0)" : "+v"(eword), "+v"(xr0), "+v"(xr1), "+s"(s_ldt), "+s"(s_dsk)::"memory");
  s_ldt = (s_ldt >> (16 * kch)) & 0xffffu; s_dsk = (s_dsk >> (16 * kch)) & 0xffffu;
  const u32 epoch = __builtin_amdgcn_readfirstlane(eword) * 0x9E3779B1u + 0x7F4A7C15u;
  STAMP(1);

  {
    const u32x4 xr[2] = {xr0, xr1};
    f32x2 x[2][4];
    float x0[2], sm[2], sq[2];
#pragma unroll
    for (int t = 0; t < 2; ++t) {
#pragma unroll
      for (int i = 0; i < 4; ++i) x[t][i] = bf2(xr[t][i]);
      x0[t] = asf(__builtin_amdgcn_readfirstlane(asu(x[t][0].x)));
      f32x2 s = {0.f, 0.f}, q = {0.f, 0.f};
#pragma unroll
      for (int i = 0; i < 4; ++i) { const f32x2 d = x[t][i] - sp2(x0[t]); s += d; q = fma2(d, d, q); }
      sm[t] = s.x + s.y; sq[t] = q.x + q.y;
    }
    float m1[2], r1[2], s2[2], q2[2];
#pragma unroll
    for (int t = 0; t < 1; ++t) wave_sum4(sm[0], sq[0], sm[1], sq[1]);
#pragma unroll
    for (int t = 0; t < 2; ++t) { sm[t] *= 1.0f / DM; sq[t] *= 1.0f / DM; }
#pragma unroll
    for (int t = 0; t < 2; ++t) {
      const float var = fmaxf(__builtin_fmaf(-sm[t], sm[t], sq[t]), 0.f);
      m1[t] = rbf(x0[t] + sm[t]);
      r1[t] = __builtin_amdgcn_rcpf(rbf(__builtin_amdgcn_sqrtf(rbf(rbf(var) + 1e-5f))));
      f32x2 s = {0.f, 0.f}, q = {0.f, 0.f};
      u32x4 hv;
#pragma unroll
      for (int i = 0; i < 4; ++i) {
        const f32x2 n = bf2(pk_bf16(bf2(pk_bf16(x[t][i] - sp2(m1[t]))) * sp2(r1[t])));
        s += n; q = fma2(n, n, q);
        hv[i] = pk_f16(n);
      }
      lds_st<u32x4>(smem, S_LN + (2 * w + t) * LN_ROW + lane * 16, hv);
      s2[t] = s.x + s.y; q2[t] = q.x + q.y;
    }
    float al[2], be[2];
#pragma unroll
    for (int t = 0; t < 1; ++t) wave_sum4(s2[0], q2[0], s2[1], q2[1]);
#pragma unroll
    for (int t = 0; t < 2; ++t) { s2[t] *= 1.0f / DM; q2[t] *= 1.0f / DM; }
#pragma unroll
    for (int t = 0; t < 2; ++t) {
      const float var2 = fmaxf(__builtin_fmaf(-s2[t], s2[t], q2[t]), 0.f);
      const float r2 = __builtin_amdgcn_rcpf(rbf(__builtin_amdgcn_sqrtf(rbf(rbf(var2) + 1e-5f))));
      al[t] = r1[t] * r2;
      be[t] = __builtin_fmaf(m1[t], r1[t], rbf(s2[t])) * r2;
    }
    const u32x4 sv = {asu(lane ? al[1] : al[0]), epoch, asu(lane ? be[1] : be[0]), epoch};
    st128c(rWS, sv, lane < 2 ? WS_STATS + (8 * tg + 2 * w + lane) * 16 : OOB);
    bar();
#pragma unroll
    for (int i = 0; i < 2; ++i) {
      const u32 a = S_LN + q4 * LN_ROW + ((2 * w + i) * 64 + 16 * g + 4 * p4) * 2;
      st128c(rWS, cat4(lds_tr(smem, a), lds_tr(smem, a + 4 * LN_ROW)), WS_XN + tg * 8192 + ((2 * w + i) * 64 + lane) * 16);
    }
  }
  STAMP(2);
  u32x2 b1v = ld64(mk_srd(p.b1, FF * 2), lane < 24 ? lane * 8 : OOB, ustart * 64);
  u32 q_lar, q_aim, q_cre, q_cim, q_cre2, q_cim2;
  {
    const u32 vo = (64 * (w & 1) + lane) * 2, so = kd * NMODE * 2;
    q_lar = ld16(mk_srd(p.lar, DM * NMODE * 2), vo, so); q_aim = ld16(mk_srd(p.aim, DM * NMODE * 2), vo, so);
    const u32x4 sCr = mk_srd(p.cre, DM * NMODE * 2), sCi = mk_srd(p.cim, DM * NMODE * 2);
    q_cre = ld16(sCr, vo, so); q_cim = ld16(sCi, vo, so);
    q_cre2 = ld16(sCr, vo ^ 128, so); q_cim2 = ld16(sCi, vo ^ 128, so);
  }

  f32x16 acc1[3];
  f32x4 csp[3];
  u32x4 stv;
  u32 lnf, tch2;
  {
    const u32 xa0 = lb + (32 * w + c31) * 128 + ((hh ^ ((c31 >> 1) & 7)) * 16);
    const u32 wa0 = lb + SLOT_W + (8 * hh + q4) * 192 + b16 * 32 + 8 * p4;
    u32 xa[2][4], wa[2], wc[2];
    u32x4 ones = {0x3f803f80u, 0x3f803f80u, 0x3f803f80u, 0x3f803f80u};
    tie(ones);
#pragma unroll
    for (int s = 0; s < 4; ++s) { xa[0][s] = xa0 ^ (s * 32); xa[1][s] = xa[0][s] + 2 * SLOT; }
    wa[0] = wa0; wa[1] = wa0 + 2 * SLOT;
    wc[0] = lb + SLOT_W + (32 * (w >> 1) + 8 * g + q4) * 192 + 3 * (w & 1) * 32 + 8 * p4; wc[1] = wc[0] + 2 * SLOT;
    u32x4 xf[2];
    u32x2 wl[2][3], wh[2][3], cl[2], chh[2];
    auto rd = [&](auto S, auto R) {
      constexpr int s = S, rr = R, buf = s & 1, kc = s >> 2, sl = kc & 3, so = (sl & 1) * SLOT, sp = s & 3;
      if constexpr (rr == 0) xf[buf] = a_ds128<so>(xa[sl >> 1][sp]);
      else {
        constexpr int u = (rr - 1) >> 1, hf = (rr - 1) & 1, off = so + u * 64 + (sp * 16 + hf * 4) * 192;
        if constexpr (hf == 0) wl[buf][u] = a_dstr<off>(wa[sl >> 1]); else wh[buf][u] = a_dstr<off>(wa[sl >> 1]);
      }
    };
    wait_vm<31>();
    tie(tch);
    bar();
    CSTAMP(0);
    sfor<7>([&](auto R) { rd(ic<0>{}, R); });
    sfor<32>([&](auto S) {
      constexpr int s = S, buf = s & 1, sp = s & 3, kc = s >> 2;
      constexpr bool last = s == 31, brk = sp == 2 && kc < 7;
      constexpr bool csr = sp < 3, cprev = sp >= 1;
      sfor<3>([&](auto U) {
        constexpr int u = U;
        constexpr int gi = sp == 2 ? (u == 2 ? 0 : -1) : (u == 0 ? -1 : (sp == 3 ? u : 2 * sp + 2 + u));
        constexpr int kcs = sp >= 2 ? kc + 3 : kc + 2;
        constexpr bool st = gi >= 0 && gi < 7 && kcs >= 4 && kcs < 8;
        if constexpr (u == 0) wait_lgkm<(s == 0 || sp == 0) ? 4 : 6>();
        else if constexpr (u == 1) {
          if constexpr (last) wait_lgkm<0>();
          else wait_lgkm<(csr ? 5 : 3)>();
          if constexpr (brk) {
            constexpr int n = kc + 1;
            wait_vm<(n == 1 ? 24 : (n <= 3 ? 17 : (n == 4 ? 7 : (n <= 6 ? 8 : 1))))>();
            asm volatile("s_barrier" ::: "memory");
            CSTAMP(n);
            if constexpr (n == 4) {
              st32c(rWS, epoch, tid == 0 ? WS_LNF + tg * 4 : OOB);
              tie(b1v);
              lds_st<f32x4>(smem, lane < 24 ? S_B1 + lane * 16 : S_MISC, f32x4{bflo(b1v.x), bfhi(b1v.x), bflo(b1v.y), bfhi(b1v.y)});
            }
            if constexpr (n == 5) stv = ld128c(sWS, WS_STATS + (128 * mt + 32 * w + c31) * 16, 0);
            if constexpr (n == 6) lnf = ld32c(sWS, WS_LNF + tid * 4, 0);
            if constexpr (n == 7) {
              const int ix = tid < 120 ? tid : tid - 120 - (tid >= 240 ? 120 : 0), ri = 15 * r + (ix >> 3);
              tch2 = touch((const char*)p.w2 + (ri < 256 ? 448 + ri : 896 + ri) * 1024 + (ix & 7) * 128);
            }
          }
        }
        if constexpr (st && (gi == 0 || gi == 4)) st1_m0(ic<kcs>{}, ic<gi>{});
        mfma32_vv<s == 0>(acc1[u], cat4(wl[buf][u], wh[buf][u]), xf[buf]);
        if constexpr (st) st1_go(ic<kcs>{}, ic<gi>{});
        if constexpr (!last) {
          if constexpr (u == 0) sfor<3>([&](auto R) { rd(ic<s + 1>{}, R); });
          if constexpr (u == 1) sfor<2>([&](auto R) { rd(ic<s + 1>{}, ic<3 + R>{}); });
          if constexpr (u == 2) sfor<2>([&](auto R) { rd(ic<s + 1>{}, ic<5 + R>{}); });
        }
        if constexpr (csr && u == 0) {
          constexpr int sl = kc & 3, so = (sl & 1) * SLOT + sp * 32;
          cl[sp & 1] = a_dstr<so>(wc[sl >> 1]); chh[sp & 1] = a_dstr<so + 4 * 192>(wc[sl >> 1]);
        }
        if constexpr (cprev && u == 2) mfma16_bf<kc == 0>(csp[sp - 1], cat4(cl[(sp - 1) & 1], chh[(sp - 1) & 1]), ones);
      });
    });
    asm volatile("s_nop 7\n\ts_nop 3" : "+v"(acc1[0]), "+v"(acc1[1]), "+v"(acc1[2]), "+v"(csp[0]), "+v"(csp[1]), "+v"(csp[2]));
    CSTAMP(8);
    CDUMP(9, 0);
  }
  STAMP(3);
#pragma unroll
  for (int i = 0; i < 3; ++i) lds_st<f32x4>(smem, c15 == 0 ? S_CSP + ((w >> 1) * 96 + (3 * (w & 1) + i) * 16 + 4 * g) * 4 : S_MISC, csp[i]);
  bar();
  {
    const int f = tid < 96 ? tid : 0;
    lds_st<float>(smem, tid < 96 ? S_CS + tid * 4 : S_MISC, lds_ld<float>(smem, S_CSP + f * 4) + lds_ld<float>(smem, S_CSP + 384 + f * 4));
  }
  asm volatile("s_waitcnt vmcnt(1)" : "+v"(stv), "+v"(lnf), "+v"(q_lar), "+v"(q_aim), "+v"(q_cre), "+v"(q_cim), "+v"(q_cre2), "+v"(q_cim2)::"memory");
#pragma unroll 1
  for (int it = 0; it < (1 << 16); ++it) {
    if (__all(stv.y == epoch && stv.w == epoch)) break;
    __builtin_amdgcn_s_sleep(1);
    stv = ld128c(sWS, WS_STATS + (128 * mt + 32 * w + c31) * 16, 0);
    asm volatile("s_waitcnt vmcnt(0)" : "+v"(stv)::"memory");
  }
  spin_flag(sWS, WS_LNF + tid * 4, epoch, lnf, true);
  u32x4 ug0 = ld128c(sWS, WS_XN + tid * 8192 + cp * 32, 0), ug1 = ld128c(sWS, WS_XN + tid * 8192 + cp * 32 + 16, 0);
  st32c(rWS, epoch, (b == 0 && tid == 0) ? WS_E : OOB);
  bar();
  STAMP(4);

  {
    const f32x2 al = sp2(asf(stv.x)), nb = sp2(-asf(stv.z));
    const u32 hso = WS_H + ((mt * 4 + w) * NUNIT + ustart) * 2048 + lane * 16;
#pragma unroll
    for (int u = 0; u < 3; ++u) {
#pragma unroll
      for (int s = 0; s < 2; ++s) {
        u32x4 o;
#pragma unroll
        for (int q = 0; q < 2; ++q) {
          const int fo = 32 * u + 16 * s + 8 * q;
          const f32x4 bi = lds_ld<f32x4>(smem, S_B1 + hh * 16 + fo * 4), cs = lds_ld<f32x4>(smem, S_CS + hh * 16 + fo * 4);
          const int rg = 8 * s + 4 * q;
          const f32x2 p0 = fma2(al, f32x2{acc1[u][rg], acc1[u][rg + 1]}, fma2(nb, f32x2{cs.x, cs.y}, f32x2{bi.x, bi.y}));
          const f32x2 p1 = fma2(al, f32x2{acc1[u][rg + 2], acc1[u][rg + 3]}, fma2(nb, f32x2{cs.z, cs.w}, f32x2{bi.z, bi.w}));
          o[2 * q] = pk_bf16(gelu2(p0));
          o[2 * q + 1] = pk_bf16(gelu2(p1));
        }
        st128c(rWS, o, ustart + u < unext ? hso + (u * 2 + s) * 1024 : OOB);
      }
    }
  }
  const int kh = w >> 1, mh = w & 1;
  const u32 vw2 = (lane >> 3) * 1024 + ((((lane & 7) >> 1) ^ ((lane >> 3) & 2)) * 32) + (lane & 1) * 16;
  const u32 vw2p[2] = {vw2, vw2 + 7168};
  const u32 vw2z[2] = {kh ? OOB : vw2p[0], kh ? OOB : vw2p[1]};
  const u32 w2_lo = lb + kh * W2_HALF + 2 * mh * 1024, w2_so = (kh * 22 * 32 + 16 * mh) * 1024 + nt2 * 128;
  auto st2_m0 = [&](auto J) { dma_m0(w2_lo + (J % W2_NS) * 4096); };
  auto st2_go = [&](auto J, auto I) { dma_go<I * 1024>(sW2, J == 21 ? vw2z[I] : vw2p[I], w2_so + J * 32 * 1024); };
  auto st2_piece = [&](auto N) {
    FENCE();
    if constexpr ((N & 1) == 0) { st2_m0(ic<(N >> 1)>{}); asm volatile("s_nop 0"); }
    st2_go(ic<(N >> 1)>{}, ic<(N & 1)>{});
    FENCE();
  };
  STAMP(5);

  float inv_sk;
  {
    CSTAMP(0);
    const int mh2 = w & 1;
    const float dt = rbf(expf(bf1(s_ldt)));
    const float zr = -rbf(expf(bf1(q_lar))) * dt, zi = bf1(q_aim) * dt;
    f32x2 C = f32x2{bf1(q_cre), bf1(q_cim)} * sp2(2.0f * dt);
    st2_piece(ic<0>{});
    const float bd = wave_sum((__builtin_fabsf(C.x) + __builtin_fabsf(C.y)) + (__builtin_fabsf(bf1(q_cre2)) + __builtin_fabsf(bf1(q_cim2))) * (2.0f * dt));
    int se = 267 - (int)((asu(bd) >> 23) & 0xff);
    se = se < 1 ? 1 : (se > 253 ? 253 : se);
    C *= sp2(asf((u32)se << 23));
    inv_sk = asf((u32)(254 - se) << 23);
    st2_piece(ic<1>{});
    const float e1 = zr * 1.44269504089f;
    const float ph = zi * 0.15915494309f;
    const float pl = __builtin_fmaf(zi, 0.15915494309f, -ph) + zi * 6.4206383e-9f;
    auto cexp = [&](float pw) {
      const float m = __builtin_amdgcn_exp2f(e1 * pw);
      const float a = __builtin_amdgcn_fractf(ph * pw) + pl * pw;
      return f32x2{__builtin_amdgcn_cosf(a), __builtin_amdgcn_sinf(a)} * sp2(m);
    };
    f32x2 P[8], Y[16], E[5], CE[17];
    P[1] = cexp(1.f); Y[1] = cexp(8.f);
    st2_piece(ic<2>{});
    E[0] = cexp(128.f);
    Y[0] = f32x2{1.f, 0.f}; CE[0] = C;
    const u32 tl = S_TAB + kch * TAB_CH + (64 * mh2 + lane) * 4;
    auto putY = [&](int i) { lds_st<u32>(smem, tl + i * TAB_ROW, pk_f16(f32x2{Y[i].x, -Y[i].y})); };
    auto putX = [&](int row, f32x2 v) { lds_st<u32>(smem, tl + (16 + row) * TAB_ROW, pk_f16(v)); };
    cmul2(P[2], P[1], P[1], Y[2], Y[1], Y[1]); cmul2(E[1], E[0], E[0], CE[1], C, E[0]);
    st2_piece(ic<3>{});
    cmul2(P[3], P[2], P[1], P[4], P[2], P[2]); cmul2(Y[3], Y[2], Y[1], Y[4], Y[2], Y[2]);
    cmul2(E[2], E[1], E[1], CE[2], CE[0], E[1]); cmul2(CE[3], CE[1], E[1], P[5], P[4], P[1]);
    putY(0); putY(1); putY(2); putY(3);
    st2_piece(ic<4>{});
    cmul2(P[6], P[4], P[2], P[7], P[4], P[3]); cmul2(Y[5], Y[4], Y[1], Y[6], Y[4], Y[2]);
    cmul2(Y[7], Y[4], Y[3], Y[8], Y[4], Y[4]); cmul2(E[3], E[2], E[2], CE[4], CE[0], E[2]);
    putY(4); putY(5); putY(6); putY(7);
    st2_piece(ic<5>{});
    cmul2(CE[5], CE[1], E[2], CE[6], CE[2], E[2]); cmul2(CE[7], CE[3], E[2], E[4], E[3], E[3]);
    cmul2(Y[9], Y[8], Y[1], Y[10], Y[8], Y[2]); cmul2(Y[11], Y[8], Y[3], Y[12], Y[8], Y[4]);
    putY(8); putY(9); putY(10); putY(11);
    st2_piece(ic<6>{});
    cmul2(Y[13], Y[8], Y[5], Y[14], Y[8], Y[6]); cmul2(Y[15], Y[8], Y[7], CE[16], C, E[4]);
    putY(12); putY(13); putY(14); putY(15);
    st2_piece(ic<7>{});
    CSTAMP(1);
    putX(0, CE[0]); putX(8, CE[1]);
    sfor<7>([&](auto I) {
      constexpr int i = I + 1;
      f32x2 a, c;
      cmul2(a, CE[0], P[i], c, CE[1], P[i]);
      putX(i, a); putX(8 + i, c);
      if constexpr (i == 2 || i == 4 || i == 6) st2_piece(ic<7 + i / 2>{});
    });
    putX(16, CE[2]); putX(20, CE[3]);
    sfor<3>([&](auto I) {
      constexpr int i = I + 1;
      f32x2 a, c;
      cmul2(a, CE[2], P[2 * i], c, CE[3], P[2 * i]);
      putX(16 + i, a); putX(20 + i, c);
    });
    st2_piece(ic<11>{});
    putX(24, CE[4]); putX(26, CE[5]); putX(28, CE[6]); putX(30, CE[7]);
    {
      f32x2 a, c;
      cmul2(a, CE[4], P[4], c, CE[5], P[4]); putX(25, a); putX(27, c);
      cmul2(a, CE[6], P[4], c, CE[7], P[4]); putX(29, a); putX(31, c);
    }
    st2_piece(ic<12>{});
    cmul2(CE[8], CE[0], E[3], CE[9], CE[1], E[3]); cmul2(CE[10], CE[2], E[3], CE[11], CE[3], E[3]);
    putX(32, CE[8]); putX(33, CE[9]); putX(34, CE[10]); putX(35, CE[11]);
    st2_piece(ic<13>{});
    cmul2(CE[12], CE[4], E[3], CE[13], CE[5], E[3]); cmul2(CE[14], CE[6], E[3], CE[15], CE[7], E[3]);
    putX(36, CE[12]); putX(37, CE[13]); putX(38, CE[14]); putX(39, CE[15]); putX(40, CE[16]);
    st2_piece(ic<14>{});
    CSTAMP(2);
    u32 ix[9];
#pragma unroll
    for (int e = 0; e < 9; ++e) {
      const int l = 2047 - (8 * tid + e);
      const u32 rb = pk_bf16(sp2((float)l)) & 0xffffu;
      ix[e] = (l < 0 ? 641u : (l < 128 ? (u32)l : rb - 17024u)) * 2;
    }
    st2_piece(ic<15>{});
    bar();
    CSTAMP(3);
    {
      const u32 rd = S_TAB + kch * TAB_CH + c15 * TAB_ROW + g * 16;
      const u32 kc = S_KC + kch * KC_CH;
      u32x4 yf[8];
#pragma unroll
      for (int s = 0; s < 8; ++s) yf[s] = lds_ld<u32x4>(smem, rd + s * 64);
      auto tile = [&](int t) {
        f32x4 a = {0, 0, 0, 0};
        const u32 ra = S_TAB + kch * TAB_CH + (16 + min(16 * t + c15, 40)) * TAB_ROW + g * 16;
#pragma unroll
        for (int s = 0; s < 8; ++s) a = mfma_h(lds_ld<u32x4>(smem, ra + s * 64), yf[s], a);
        return a;
      };
      if (mh2 == 0) {
        const f32x4 v0 = tile(0), v2 = tile(2);
        lds_st<u32x2>(smem, kc + (128 * (g >> 1) + 8 * c15 + 4 * (g & 1)) * 2, u32x2{pk_f16(f32x2{v0[0], v0[1]}), pk_f16(f32x2{v0[2], v0[3]})});
        const u32 h01 = pk_f16(f32x2{v2[0], v2[1]}), h23 = pk_f16(f32x2{v2[2], v2[3]});
        const u32 t2 = g < 2 ? 512 + 64 * g + c15 : 644, st2 = g < 2 ? 16 : 0;
        lds_st<u16>(smem, kc + ((g == 2 && c15 == 0) ? 640 : t2) * 2, (u16)h01);
        lds_st<u16>(smem, kc + (t2 + st2) * 2, (u16)(h01 >> 16));
        lds_st<u16>(smem, kc + (t2 + 2 * st2) * 2, (u16)h23);
        lds_st<u16>(smem, kc + (t2 + 3 * st2) * 2, (u16)(h23 >> 16));
      } else {
        const f32x4 v1 = tile(1);
        const u32 t1a = g < 2 ? 256 + 64 * g + 4 * c15 : 384 + 64 * (g - 2) + 2 * c15;
        lds_st<u32>(smem, kc + t1a * 2, pk_f16(f32x2{v1[0], v1[1]}));
        lds_st<u32>(smem, kc + (t1a + (g < 2 ? 2 : 32)) * 2, pk_f16(f32x2{v1[2], v1[3]}));
        if (lane == 0) lds_st<u16>(smem, kc + 641 * 2, (u16)0);
      }
    }
    CSTAMP(4);
    asm volatile("s_waitcnt vmcnt(16)" : "+v"(ug0), "+v"(ug1), "+v"(tch2)::"memory");
    st32c(rWS, epoch, lane == 0 ? WS_HF + ((mt * 4 + w) * 16 + nt) * 4 : OOB);
    st2_piece(ic<16>{});
    CSTAMP(5);
    bar();
    CSTAMP(6);
#pragma unroll
    for (int ch = 0; ch < 2; ++ch) {
      u32 e[9];
#pragma unroll
      for (int i = 0; i < 9; ++i) e[i] = lds_ld<u16>(smem, S_KC + ch * KC_CH + ix[i]);
      const u32x4 a0 = {e[0] | (e[1] << 16), e[2] | (e[3] << 16), e[4] | (e[5] << 16), e[6] | (e[7] << 16)};
      const u32x4 a1 = {e[1] | (e[2] << 16), e[3] | (e[4] << 16), e[5] | (e[6] << 16), e[7] | (e[8] << 16)};
      lds_st<u32x4>(smem, S_G + ch * G_CH + tid * 16, a0);
      lds_st<u32x4>(smem, S_G + ch * G_CH + G_PAR + tid * 16, a1);
      if (ch == 0) st2_piece(ic<17>{});
    }
    const u32x4 z4 = {0, 0, 0, 0};
    if (tid < 8) lds_st<u32x4>(smem, S_G + (tid >> 2) * G_CH + ((tid >> 1) & 1) * G_PAR + 4096 + (tid & 1) * 16, z4);
    if (tid < 60) lds_st<u32x4>(smem, S_U + (tid / 30) * U_CH + (tid % 30) * 16, z4);
    lds_st<u32x4>(smem, S_U + 480 + tid * 16, ug0);
    lds_st<u32x4>(smem, S_U + U_CH + 480 + tid * 16, ug1);
    CSTAMP(7);
    bar();
    CSTAMP(8);
    CDUMP(9, 10);
  }
  STAMP(6);

  u32x4 hf[22][2];
  const u32 hb = WS_H + ((mt2 * 2 + mh) * NUNIT + kh * 22) * 2048;
  const u32 hvo = lane * 16, hvz = kh ? OOB : hvo;
  auto ldh = [&](auto J, auto S) { hf[J][S] = ld128c_a(sWS, J == 21 ? hvz : hvo, hb + (J * 2 + S) * 1024); };
  {
    const int par = w & 1;
    const u32 ub = lb + S_U + kch * U_CH + 64 - 64 * par + 32 * c15 + 16 * g;
    const u32 gb0 = lb + S_G + kch * G_CH + ((c15 & 1) ? 0 : G_PAR) + 4 * (1015 - (c15 >> 1) - 16 * par + 4 * g - 32 * 31);
    const u32 gb[4] = {gb0, gb0 + 1024, gb0 + 2048, gb0 + 3072};
    const u32 hfo = WS_HF + (((mt2 >> 1) * 4 + 2 * (mt2 & 1) + mh) * 16 + c15) * 4;
    const bool hneed = kh ? c15 >= 8 : c15 <= 8;
    u32 hfl;
    u32x4 bq[32];
    u32x2 al[32], ah[32];
    f32x4 y[8];
    auto rdB = [&](auto M, auto T) { constexpr int q = 4 * T - M + 3; bq[q] = a_ds128<128 * q>(ub); };
    auto rdA = [&](auto M, auto H) {
      constexpr int k = (31 - M) >> 3, o = 32 * ((31 - M) & 7);
      if constexpr (H == 0) al[M] = a_ds2x32<o, o + 1>(gb[k]); else ah[M] = a_ds2x32<o + 2, o + 3>(gb[k]);
    };
    CSTAMP(0);
    sfor<2>([&](auto M) { sfor<8>([&](auto T) { rdB(M, T); }); rdA(M, ic<0>{}); rdA(M, ic<1>{}); });
    sfor<32>([&](auto M) {
      constexpr int m = M, t0 = m >> 2, n = 8 - t0;
      if constexpr (m == 11) hfl = ld32c(sWS, hfo, 0);
      if constexpr (m == 20) {
        asm volatile("s_waitcnt vmcnt(0)" : "+v"(hfl)::"memory");
        spin_flag(sWS, hfo, epoch, hfl, hneed);
      }
      wait_lgkm<(m <= 2 ? 10 : (m == 3 ? 4 : (m == 4 ? 6 : (2 * (31 - m) < 8 ? 2 * (31 - m) : 8))))>();
      sfor<n>([&](auto I) {
        constexpr int i = I, t = t0 + i;
        mfma16_h<m == 0>(y[t], cat4(al[m], ah[m]), bq[4 * t - m + 3]);
        if constexpr (m < 2) {
          rdB(ic<m + 2>{}, I);
          if constexpr (i >= 6) rdA(ic<m + 2>{}, ic<i - 6>{});
        } else if constexpr (m <= 4) {
          if constexpr (i < 4) rdA(ic<2 * m + (i >> 1)>{}, ic<i & 1>{});
        } else if constexpr (m + 5 < 32) {
          if constexpr (i < 2) rdA(ic<m + 5>{}, I);
        }
        if constexpr (m >= 1 && m <= 10 && i == 3) {
          constexpr int n = 17 + m;
          if constexpr ((n & 1) == 0) { st2_m0(ic<(n >> 1)>{}); asm volatile("s_nop 0"); }
          st2_go(ic<(n >> 1)>{}, ic<(n & 1)>{});
        }
        if constexpr (i == 0 && m >= 20 && (m & 1) == 0) ldh(ic<(m >= 20 ? (m - 20) / 4 : 0)>{}, ic<(m >= 20 ? ((m - 20) >> 1) & 1 : 0)>{});
      });
    });
    asm volatile("s_nop 7\n\ts_nop 1" : "+v"(y[0]), "+v"(y[1]), "+v"(y[2]), "+v"(y[3]), "+v"(y[4]), "+v"(y[5]), "+v"(y[6]), "+v"(y[7]));
    CSTAMP(1);
    bar();
    CSTAMP(2);
    auto send = [&](auto PAR) {
      FENCE();
#pragma unroll
      for (int i = 0; i < 4; ++i) lds_st<f32x4>(smem, S_EX + w * 4096 + i * 1024 + lane * 16, y[4 * (1 - PAR) + i]);
      FENCE();
    };
    if (par) send(ic<1>{}); else send(ic<0>{});
    ldh(ic<3>{}, ic<0>{}); ldh(ic<3>{}, ic<1>{});
    CSTAMP(3);
    bar();
    CSTAMP(4);
    ldh(ic<4>{}, ic<0>{}); ldh(ic<4>{}, ic<1>{});
    const float dsk = bf1(s_dsk);
    u32x2 ao[4];
    auto finish = [&](auto PAR) {
      FENCE();
#pragma unroll
      for (int i = 0; i < 4; ++i) {
        const f32x4 v = (y[4 * PAR + i] + lds_ld<f32x4>(smem, S_EX + (w ^ 1) * 4096 + i * 1024 + lane * 16)) * inv_sk;
        const u32x2 uu = lds_ld<u32x2>(smem, S_U + kch * U_CH + (240 + 256 * (4 * PAR + i) + 16 * c15 + 4 * g) * 2);
        const f16x2 u01 = ash2(uu.x), u23 = ash2(uu.y);
        ao[i] = u32x2{pk_bf16(fma2(f32x2{(float)u01.x, (float)u01.y}, sp2(dsk), f32x2{v[0], v[1]})),
                      pk_bf16(fma2(f32x2{(float)u23.x, (float)u23.y}, sp2(dsk), f32x2{v[2], v[3]}))};
      }
      FENCE();
    };
    if (par) finish(ic<1>{}); else finish(ic<0>{});
#pragma unroll
    for (int i = 0; i < 4; ++i) {
      u32x2 o = ao[i];
      o.x = o.x == POISON ? 0xffc0ffc0u : o.x;
      o.y = o.y == POISON ? 0xffc0ffc0u : o.y;
      st64c(rWS, o, WS_A + ((4 * (4 * par + i) + (c15 >> 2)) * 512 + kd) * 128 + ((c15 & 3) * 16 + 4 * g) * 2);
    }
    CSTAMP(5);
    sfor<3>([&](auto J) { ldh(ic<5 + J>{}, ic<0>{}); ldh(ic<5 + J>{}, ic<1>{}); });
    CSTAMP(6);
    CDUMP(7, 21);
  }
  STAMP(7);

  {
    u32 wa[2];
#pragma unroll
    for (int n = 0; n < 2; ++n) wa[n] = lb + kh * W2_HALF + (4 * hh + q4) * 128 + (((2 * n + b16) ^ (q4 & 2)) * 32) + 8 * p4;
    f32x16 acc[2];
    u32x2 wl[2][4], wh[2][4];
    u32x4 at0, at1;
    u32x4 xt[2];
    u32x2 b2v[4];
    const int ch0 = 64 * nt2 + 32 * kh + 4 * hh;
    const u32 ato = WS_A + (mt2 * 512 + nt2 * 64) * 128 + tid * 16;
    const u32 xto = (64 * mt2 + (tid >> 3)) * 1024 + nt2 * 128 + (tid & 7) * 16;
    auto rd = [&](auto J, auto I, auto H) {
      constexpr int j = J, i = I, off = (j % W2_NS) * 4096 + ((i >> 1) * 16 + H * 8) * 128;
      if constexpr (H == 0) wl[j & 1][i] = a_dstr<off>(wa[i & 1]); else wh[j & 1][i] = a_dstr<off>(wa[i & 1]);
    };
    bar();
    sfor<4>([&](auto I) { rd(ic<0>{}, I, ic<0>{}); rd(ic<0>{}, I, ic<1>{}); });
    sfor<22>([&](auto J) {
      constexpr int j = J;
      constexpr bool last = j == 21;
      if constexpr (j == 4 || j == 8) bar();
      if constexpr (j == 13 || j == 17) {
        wait_vm<g2_ops_before(j) - g2_ops_before(j == 13 ? 8 : 12)>();
        bar();
      }
      wait_vm<g2_wait_h(j)>();
      asm volatile("" : "+a"(hf[j][0]), "+a"(hf[j][1]));
      sfor<4>([&](auto I) {
        constexpr int i = I;
        constexpr bool stg = j >= 4 && j <= 11 && (i & 1);
        constexpr int ju = 14 + (j - 4) , pi = i >> 1;
        if constexpr (j == 4 || j == 8 || j == 13 || j == 17) { if constexpr (i == 0) wait_lgkm<0>(); }
        else wait_lgkm<(last ? 6 - 2 * i : 6)>();
        if constexpr (stg && pi == 0) st2_m0(ic<ju>{});
        mfma32_va<j == 0 && i < 2>(acc[i & 1], cat4(wl[j & 1][i], wh[j & 1][i]), hf[j][i >> 1]);
        if constexpr (stg) st2_go(ic<ju>{}, ic<pi>{});
        if constexpr ((i & 1) == 0 && j + G2_LEAD <= 21) ldh(ic<j + G2_LEAD>{}, ic<(i >> 1)>{});
        if constexpr (!last) { rd(ic<j + 1>{}, I, ic<0>{}); rd(ic<j + 1>{}, I, ic<1>{}); }
        if constexpr (j == 15 && i < 2) xt[i] = ld128(sX, xto + i * 32 * 1024, 0);
        if constexpr (j == 15 && i == 2) {
          const u32x4 sB2 = mk_srd(p.b2, DM * 2);
#pragma unroll
          for (int q = 0; q < 4; ++q) b2v[q] = ld64(sB2, (ch0 + 8 * q) * 2, 0);
        }
        if constexpr (j == 18 && i == 0) { at0 = ld128c(sWS, ato, 0); at1 = ld128c(sWS, ato + 4096, 0); }
      });
    });
    asm volatile("s_nop 7\n\ts_nop 3" : "+v"(acc[0]), "+v"(acc[1]));
    STAMP(8);
    auto send2 = [&](auto KH) {
      FENCE();
#pragma unroll
      for (int q = 0; q < 4; ++q) {
        const f32x16& as = acc[1 - KH];
        lds_st<f32x4>(smem, S_EX + w * 4096 + q * 1024 + lane * 16, f32x4{as[4 * q], as[4 * q + 1], as[4 * q + 2], as[4 * q + 3]});
      }
      FENCE();
    };
    if (kh) send2(ic<1>{}); else send2(ic<0>{});
    asm volatile("s_waitcnt vmcnt(0)" : "+v"(at0), "+v"(at1), "+v"(xt[0]), "+v"(xt[1]), "+v"(b2v[0]), "+v"(b2v[1]), "+v"(b2v[2]), "+v"(b2v[3])::"memory");
    const int row0 = tid >> 3, sl = tid & 7;
    {
      const u32 o = S_XT + row0 * 128 + ((sl ^ (row0 & 7)) * 16);
      lds_st<u32x4>(smem, o, xt[0]);
      lds_st<u32x4>(smem, o + 32 * 128, xt[1]);
    }
#pragma unroll 1
    for (int it = 0; it < (1 << 16); ++it) {
      const u32 m0 = max(max(at0.x, at0.y), max(at0.z, at0.w)), m1 = max(max(at1.x, at1.y), max(at1.z, at1.w));
      if (!__any(max(m0, m1) == POISON)) break;
      at0 = ld128c(sWS, ato, 0); at1 = ld128c(sWS, ato + 4096, 0);
      asm volatile("s_waitcnt vmcnt(0)" : "+v"(at0), "+v"(at1)::"memory");
    }
    {
      const u32 o = S_AT + row0 * 128 + (((sl >> 1) ^ (row0 & 2)) * 32) + (sl & 1) * 16;
      lds_st<u32x4>(smem, o, at0);
      lds_st<u32x4>(smem, o + 32 * 128, at1);
    }
    bar();
    const int tl = 32 * mh + c31;
    auto fin2 = [&](auto KH) {
      FENCE();
#pragma unroll
      for (int q = 0; q < 4; ++q) {
        const f32x16& ak = acc[KH];
        const f32x4 v = f32x4{ak[4 * q], ak[4 * q + 1], ak[4 * q + 2], ak[4 * q + 3]} + lds_ld<f32x4>(smem, S_EX + (w ^ 2) * 4096 + q * 1024 + lane * 16);
        const int row = 32 * KH + 8 * q + 4 * hh + q4;
        const u32x2 av = lds_tr(smem, S_AT + row * 128 + (((2 * mh + b16) ^ (row & 2)) * 32) + 8 * p4);
        const u32 po = tl * 128 + (((4 * KH + q) ^ (tl & 7)) * 16) + hh * 8;
        const u32x2 xv = lds_ld<u32x2>(smem, S_XT + po);
        const f32x2 o0 = (bf2(xv.x) + bf2(av.x)) + (f32x2{v[0], v[1]} + bf2(b2v[q].x)), o1 = (bf2(xv.y) + bf2(av.y)) + (f32x2{v[2], v[3]} + bf2(b2v[q].y));
        lds_st<u32x2>(smem, S_OT + po, u32x2{pk_bf16(o0), pk_bf16(o1)});
      }
      FENCE();
    };
    if (kh) fin2(ic<1>{}); else fin2(ic<0>{});
    bar();
    {
      const u32 o = S_OT + row0 * 128 + ((sl ^ (row0 & 7)) * 16);
      const u32x4 o0 = lds_ld<u32x4>(smem, o), o1 = lds_ld<u32x4>(smem, o + 32 * 128);
      __builtin_amdgcn_raw_buffer_store_b128(o0, rOUT, xto, 0, 0);
      __builtin_amdgcn_raw_buffer_store_b128(o1, rOUT, xto + 32 * 1024, 0, 0);
    }
  }
  {
    const u64 te = __builtin_amdgcn_s_memrealtime();
    u64 tf = __hip_atomic_load((const u64*)(p.ws + 0xA80000) + (threadIdx.x & 255), __ATOMIC_RELAXED, __HIP_MEMORY_SCOPE_AGENT);
    for (int o = 32; o; o >>= 1) { u64 v = __shfl_xor(tf, o); tf = v < tf ? v : tf; }
    const u64 until = te + 20 * (te - tf);
    while (__builtin_amdgcn_s_memrealtime() < until) __builtin_amdgcn_s_sleep(2);
  }
}

extern "C" void kernel_launch(void* const* d_in, const int* in_sizes, int n_in, void* d_out, int out_size, void* d_ws,
                              size_t ws_size, hipStream_t stream) {
  Params p;
  p.x = (const u16*)d_in[0]; p.lar = (const u16*)d_in[1]; p.aim = (const u16*)d_in[2]; p.cre = (const u16*)d_in[3];
  p.cim = (const u16*)d_in[4]; p.ldt = (const u16*)d_in[5]; p.dsk = (const u16*)d_in[6];
  p.w1 = (const u16*)d_in[8]; p.b1 = (const u16*)d_in[9]; p.w2 = (const u16*)d_in[10]; p.b2 = (const u16*)d_in[11];
  p.out = (u16*)d_out; p.ws = (char*)d_ws;
  s4d_mixer_fused<<<256, 256, 0, stream>>>(p);
}
```

```cpp
#include <hip/hip_runtime.h>
#include <stdint.h>
#include <utility>

namespace {
typedef unsigned short u16;
typedef unsigned u32;
typedef unsigned long long u64;
typedef float f32x2 __attribute__((ext_vector_type(2)));
typedef float f32x4 __attribute__((ext_vector_type(4)));
typedef float f32x16 __attribute__((ext_vector_type(16)));
typedef u32 u32x2 __attribute__((ext_vector_type(2)));
typedef u32 u32x4 __attribute__((ext_vector_type(4)));
typedef short s16x4 __attribute__((ext_vector_type(4)));
typedef __bf16 bf16x2 __attribute__((ext_vector_type(2)));
typedef _Float16 f16x2 __attribute__((ext_vector_type(2)));
typedef _Float16 f16x8 __attribute__((ext_vector_type(8)));
#define LAS __attribute__((address_space(3)))
#define DEV __device__ __forceinline__
typedef __amdgpu_buffer_rsrc_t rsrc_t;

constexpr int SEQ = 2048, DM = 512, NMODE = 128, FF = 1376, NUNIT = 43;
constexpr u32 WS_E = 0, WS_LNF = 0x1000, WS_HF = 0x2000  , WS_STATS = 0x8000, WS_XN = 0x10000, WS_A = 0x210000, WS_H = 0x410000,
              WS_END = WS_H + 64 * NUNIT * 2048, WS_STAMP = 0xA00000;
constexpr u32 SLOT = 28672, SLOT_W = 16384;
constexpr u32 W2_NS = 14, W2_HALF = W2_NS * 4096;
constexpr u32 LDS_BYTES_ = 163840;
constexpr u32 S0 = 114688;
constexpr u32 S_LN = S0, LN_ROW = 1088;
constexpr u32 TAB_ROW = 544, TAB_CH = 57 * TAB_ROW;
constexpr u32 S_TAB = LDS_BYTES_ - 2 * TAB_CH - 1808;
constexpr u32 S_TB = 32768;
constexpr u32 S_KC = S_TAB - 2592, KC_CH = 1296;
constexpr u32 S_CSP = LDS_BYTES_ - 1808;
constexpr u32 S_B1 = S_CSP + 768;
constexpr u32 S_MISC = S_B1 + 384;
constexpr u32 S_CS = S_MISC + 64;
constexpr u32 S_G = S0, G_PAR = 4160, G_CH = 8320;
constexpr u32 S_U = S0 + 16640, U_CH = 4608;
constexpr u32 S_EX = S0;
constexpr u32 S_AT = S0 + 16384, S_XT = S0 + 24576, S_OT = S0 + 32768, S_B2 = S0 + 40960;
constexpr u32 LDS_BYTES = 163840;
constexpr u32 OOB = 0x80000000u;
constexpr u32 POISON = 0xffffffffu;

#define STAMP(k) do {} while (0)
#define CSTAMP(i) do {} while (0)
#define CDUMP(n, base) do {} while (0)

struct Params {
  const u16 *x, *lar, *aim, *cre, *cim, *ldt, *dsk, *w1, *b1, *w2, *b2;
  u16* out;
  char* ws;
};

DEV float asf(u32 v) { return __builtin_bit_cast(float, v); }
DEV u32 asu(float v) { return __builtin_bit_cast(u32, v); }
DEV f16x2 ash2(u32 v) { return __builtin_bit_cast(f16x2, v); }
DEV float bflo(u32 v) { return asf(v << 16); }
DEV float bfhi(u32 v) { return asf(v & 0xffff0000u); }
DEV f32x2 bf2(u32 v) { return f32x2{bflo(v), bfhi(v)}; }
DEV u32 pk_bf16(f32x2 v) { return __builtin_bit_cast(u32, __builtin_convertvector(v, bf16x2)); }
DEV u32 pk_f16(f32x2 v) { return __builtin_bit_cast(u32, __builtin_convertvector(v, f16x2)); }
DEV float rbf(float a) { return bflo(pk_bf16(f32x2{a, a})); }
DEV float bf1(u32 v) { return asf(v << 16); }
DEV f32x2 sp2(float a) { return f32x2{a, a}; }
DEV f32x2 fma2(f32x2 a, f32x2 b, f32x2 c) { return __builtin_elementwise_fma(a, b, c); }

template <int N> DEV void wait_vm() { asm volatile("s_waitcnt vmcnt(%0)" ::"n"(N) : "memory"); }
template <int N> DEV void wait_lgkm() { asm volatile("s_waitcnt lgkmcnt(%0)" ::"n"(N)); }
DEV void bar() { asm volatile("s_waitcnt lgkmcnt(0)\n\ts_barrier" ::: "memory"); }
DEV u32x4 mk_srd(const void* base, u32 bytes) {
  const u64 b = (u64)base;
  u32x4 s;
  s.x = __builtin_amdgcn_readfirstlane((u32)b);
  s.y = __builtin_amdgcn_readfirstlane((u32)(b >> 32)) & 0xffffu;
  s.z = bytes;
  s.w = 0x00020000u;
  return s;
}
DEV rsrc_t mk_rsrc(const void* base, u32 bytes) { return __builtin_amdgcn_make_buffer_rsrc((void*)base, 0, bytes, 0x00020000); }

#define BLOAD(INS, TY, NAME, FLAGS, CON)                                                                     \
  DEV TY NAME(u32x4 srd, u32 voff, u32 soff) {                                                               \
    TY r;                                                                                                    \
    asm volatile(INS " %0, %1, %2, %3 offen" FLAGS : CON(r) : "v"(voff), "s"(srd), "s"(soff) : "memory");     \
    return r;                                                                                                \
  }
BLOAD("buffer_load_dwordx4", u32x4, ld128, "", "=v")
BLOAD("buffer_load_dwordx4", u32x4, ld128c, " sc1", "=v")
BLOAD("buffer_load_dwordx4", u32x4, ld128c_a, " sc1", "=a")
BLOAD("buffer_load_dwordx2", u32x2, ld64, "", "=v")
BLOAD("buffer_load_dword", u32, ld32c, " sc1", "=v")
BLOAD("buffer_load_ushort", u32, ld16, "", "=v")
DEV u32 touch(const void* a) { u32 r; asm volatile("global_load_dword %0, %1, off" : "=v"(r) : "v"(a) : "memory"); return r; }
template <class T> DEV void tie(T& v) { asm volatile("" : "+v"(v)); }

DEV void dma_m0(u32 lds_addr) { asm volatile("s_mov_b32 m0, %0" ::"s"(lds_addr) : "memory"); }
template <int IOFF> DEV void dma_go(u32x4 srd, u32 voff, u32 soff) {
  asm volatile("buffer_load_dwordx4 %0, %1, %2 offen offset:%3 lds" ::"v"(voff), "s"(srd), "s"(soff), "n"(IOFF) : "memory");
}
DEV void st128c(rsrc_t r, u32x4 v, u32 voff) { __builtin_amdgcn_raw_buffer_store_b128(v, r, voff, 0, 16); }
DEV void st64c(rsrc_t r, u32x2 v, u32 voff) { __builtin_amdgcn_raw_buffer_store_b64(v, r, voff, 0, 16); }
DEV void st32c(rsrc_t r, u32 v, u32 voff) { __builtin_amdgcn_raw_buffer_store_b32(v, r, voff, 0, 16); }
DEV void st64(rsrc_t r, u32x2 v, u32 voff) { __builtin_amdgcn_raw_buffer_store_b64(v, r, voff, 0, 0); }

template <class T> DEV T lds_ld(const char* smem, u32 off) { return *(const LAS T*)(smem + off); }
template <class T> DEV void lds_st(char* smem, u32 off, T v) { *(LAS T*)(smem + off) = v; }
DEV u32x2 lds_tr(const char* smem, u32 off) { return __builtin_bit_cast(u32x2, __builtin_amdgcn_ds_read_tr16_b64_v4i16((LAS s16x4*)(smem + off))); }

template <int I> using ic = std::integral_constant<int, I>;
template <class F, int... Is> DEV void sfor_impl(F&& f, std::integer_sequence<int, Is...>) { (f(ic<Is>{}), ...); }
template <int N, class F> DEV void sfor(F&& f) { sfor_impl(f, std::make_integer_sequence<int, N>{}); }
template <int OFF> DEV u32x4 a_ds128(u32 addr) { u32x4 r; asm volatile("ds_read_b128 %0, %1 offset:%2" : "=v"(r) : "v"(addr), "n"(OFF)); return r; }
template <int OFF> DEV u32x2 a_dstr(u32 addr) { u32x2 r; asm volatile("ds_read_b64_tr_b16 %0, %1 offset:%2" : "=v"(r) : "v"(addr), "n"(OFF)); return r; }
template <int O0, int O1> DEV u32x2 a_ds2x32(u32 addr) { u32x2 r; asm volatile("ds_read2_b32 %0, %1 offset0:%2 offset1:%3" : "=v"(r) : "v"(addr), "n"(O0), "n"(O1)); return r; }
DEV u32x4 cat4(u32x2 a, u32x2 b) { return __builtin_shufflevector(a, b, 0, 1, 2, 3); }
template <bool INIT> DEV void mfma32_vv(f32x16& c, u32x4 a, u32x4 b) {
  if constexpr (INIT) asm volatile("v_mfma_f32_32x32x16_bf16 %0, %1, %2, 0" : "=&v"(c) : "v"(a), "v"(b));
  else asm volatile("v_mfma_f32_32x32x16_bf16 %0, %1, %2, %0" : "+v"(c) : "v"(a), "v"(b));
}
template <bool INIT> DEV void mfma32_va(f32x16& c, u32x4 a, u32x4 b) {
  if constexpr (INIT) asm volatile("v_mfma_f32_32x32x16_bf16 %0, %1, %2, 0" : "=&v"(c) : "v"(a), "a"(b));
  else asm volatile("v_mfma_f32_32x32x16_bf16 %0, %1, %2, %0" : "+v"(c) : "v"(a), "a"(b));
}
template <bool INIT> DEV void mfma16_h(f32x4& c, u32x4 a, u32x4 b) {
  if constexpr (INIT) asm volatile("v_mfma_f32_16x16x32_f16 %0, %1, %2, 0" : "=&v"(c) : "v"(a), "v"(b));
  else asm volatile("v_mfma_f32_16x16x32_f16 %0, %1, %2, %0" : "+v"(c) : "v"(a), "v"(b));
}
DEV f32x4 mfma_h(u32x4 a, u32x4 b, f32x4 c) {
  return __builtin_amdgcn_mfma_f32_16x16x32_f16(__builtin_bit_cast(f16x8, a), __builtin_bit_cast(f16x8, b), c, 0, 0, 0);
}
template <bool INIT> DEV void mfma16_bf(f32x4& c, u32x4 a, u32x4 b) {
  if constexpr (INIT) asm volatile("v_mfma_f32_16x16x32_bf16 %0, %1, %2, 0" : "=&v"(c) : "v"(a), "v"(b));
  else asm volatile("v_mfma_f32_16x16x32_bf16 %0, %1, %2, %0" : "+v"(c) : "v"(a), "v"(b));
}
#define FENCE() __builtin_amdgcn_sched_barrier(0)

DEV void wave_sum4(float& a, float& b, float& c, float& d) {
#define S4(CTRL) "v_add_f32_dpp %0, %0, %0 " CTRL "\n\tv_add_f32_dpp %1, %1, %1 " CTRL "\n\tv_add_f32_dpp %2, %2, %2 " CTRL "\n\tv_add_f32_dpp %3, %3, %3 " CTRL "\n\t"
  asm volatile("s_nop 1\n\t" S4("row_shr:1 row_mask:0xf bank_mask:0xf bound_ctrl:1") S4("row_shr:2 row_mask:0xf bank_mask:0xf bound_ctrl:1")
               S4("row_shr:4 row_mask:0xf bank_mask:0xf bound_ctrl:1") S4("row_shr:8 row_mask:0xf bank_mask:0xf bound_ctrl:1")
               S4("row_bcast:15 row_mask:0xa bank_mask:0xf") S4("row_bcast:31 row_mask:0xc bank_mask:0xf") "s_nop 0"
               : "+v"(a), "+v"(b), "+v"(c), "+v"(d));
#undef S4
  a = asf(__builtin_amdgcn_readlane(asu(a), 63)); b = asf(__builtin_amdgcn_readlane(asu(b), 63));
  c = asf(__builtin_amdgcn_readlane(asu(c), 63)); d = asf(__builtin_amdgcn_readlane(asu(d), 63));
}
DEV void wave_sum2(float& a, float& b) {
#define S2(CTRL) "v_add_f32_dpp %0, %0, %0 " CTRL "\n\tv_add_f32_dpp %1, %1, %1 " CTRL "\n\ts_nop 0\n\t"
  asm volatile("s_nop 1\n\t" S2("row_shr:1 row_mask:0xf bank_mask:0xf bound_ctrl:1") S2("row_shr:2 row_mask:0xf bank_mask:0xf bound_ctrl:1")
               S2("row_shr:4 row_mask:0xf bank_mask:0xf bound_ctrl:1") S2("row_shr:8 row_mask:0xf bank_mask:0xf bound_ctrl:1")
               S2("row_bcast:15 row_mask:0xa bank_mask:0xf") S2("row_bcast:31 row_mask:0xc bank_mask:0xf")
               : "+v"(a), "+v"(b));
#undef S2
  a = asf(__builtin_amdgcn_readlane(asu(a), 63)); b = asf(__builtin_amdgcn_readlane(asu(b), 63));
}
template <int CTRL, int RM> DEV float dpp_add(float v) { return v + asf(__builtin_amdgcn_update_dpp(0, asu(v), CTRL, RM, 0xf, true)); }
DEV float wave_sum(float v) {
  v = dpp_add<0x111, 0xf>(v); v = dpp_add<0x112, 0xf>(v); v = dpp_add<0x114, 0xf>(v); v = dpp_add<0x118, 0xf>(v);
  v = dpp_add<0x142, 0xa>(v); v = dpp_add<0x143, 0xc>(v);
  return asf(__builtin_amdgcn_readlane(asu(v), 63));
}
DEV void cmul2(f32x2& r0, f32x2 a0, f32x2 b0, f32x2& r1, f32x2 a1, f32x2 b1) {
  f32x2 t0, t1;
  asm("v_pk_mul_f32 %0, %4, %5 op_sel_hi:[1,0]\n\tv_pk_mul_f32 %1, %6, %7 op_sel_hi:[1,0]\n\t"
      "v_pk_fma_f32 %2, %4, %5, %0 op_sel:[1,1,0] op_sel_hi:[0,1,1] neg_lo:[1,0,0]\n\t"
      "v_pk_fma_f32 %3, %6, %7, %1 op_sel:[1,1,0] op_sel_hi:[0,1,1] neg_lo:[1,0,0]"
      : "=&v"(t0), "=&v"(t1), "=&v"(r0), "=&v"(r1) : "v"(a0), "v"(b0), "v"(a1), "v"(b1));
}
DEV f32x2 cmul(f32x2 a, f32x2 b) {
  f32x2 t, r;
  asm("v_pk_mul_f32 %0, %2, %3 op_sel_hi:[1,0]\n\tv_pk_fma_f32 %1, %2, %3, %0 op_sel:[1,1,0] op_sel_hi:[0,1,1] neg_lo:[1,0,0]"
      : "=&v"(t), "=v"(r) : "v"(a), "v"(b));
  return r;
}
DEV u32 cvt_bf2(float a, float b) { u32 r; asm("v_cvt_pk_bf16_f32 %0, %1, %2" : "=v"(r) : "v"(a), "v"(b)); return r; }
DEV u32 cvt_h2(float a, float b) { u32 r; asm("v_cvt_pk_f16_f32 %0, %1, %2" : "=v"(r) : "v"(a), "v"(b)); return r; }
DEV float fadd1(float a, float b) { float r; asm("v_add_f32 %0, %1, %2" : "=v"(r) : "v"(a), "v"(b)); return r; }
DEV float fsub1(float a, float b) { float r; asm("v_sub_f32 %0, %1, %2" : "=v"(r) : "v"(a), "v"(b)); return r; }
DEV float fmul1(float a, float b) { float r; asm("v_mul_f32 %0, %1, %2" : "=v"(r) : "v"(a), "v"(b)); return r; }
DEV float ffma1(float a, float b, float c) { float r; asm("v_fma_f32 %0, %1, %2, %3" : "=v"(r) : "v"(a), "v"(b), "v"(c)); return r; }
DEV float rbf1(float a) { return bflo(cvt_bf2(a, a)); }
DEV u32 pk_f16_conj(f32x2 v) { u32 r; asm("v_cvt_pk_f16_f32 %0, %1, -%2" : "=v"(r) : "v"(v.x), "v"(v.y)); return r; }
DEV f32x2 gelu2(f32x2 v) {
  const f32x2 s = v * fma2(v * v, sp2(-0.10294323970f), sp2(-2.3022081986f));
  const f32x2 d = f32x2{__builtin_amdgcn_exp2f(s.x), __builtin_amdgcn_exp2f(s.y)} + sp2(1.0f);
  return v * f32x2{__builtin_amdgcn_rcpf(d.x), __builtin_amdgcn_rcpf(d.y)};
}
DEV void spin_flag(u32x4 srd, u32 voff, u32 epoch, u32 val, bool need) {
#pragma unroll 1
  for (int it = 0; it < (1 << 16); ++it) {
    if (__all(!need || val == epoch)) break;
    __builtin_amdgcn_s_sleep(1);
    val = ld32c(srd, voff, 0);
    asm volatile("s_waitcnt vmcnt(0)" : "+v"(val)::"memory");
  }
}

constexpr int G2_LEAD = 10;
constexpr int g2_wait_h(int j) { return 2 * ((j + G2_LEAD - 1 < 21 ? j + G2_LEAD - 1 : 21) - j); }

}

__global__ __launch_bounds__(512, 2) void s4d_mixer_fused(Params p) {
  __shared__ __attribute__((aligned(16))) char smem[LDS_BYTES];
  const int tid = threadIdx.x, lane = tid & 63, t8 = tid & 255;
  const int wid = __builtin_amdgcn_readfirstlane(tid >> 6);
  const int side = wid >> 2, w = wid & 3;
  const int b = blockIdx.x, g8 = b & 7, r = b >> 3;
  const int mt = 4 * (g8 >> 1) + (r >> 3), nt = 8 * (g8 & 1) + (r & 7);
  const int ustart = (nt * NUNIT) >> 4, unext = ((nt + 1) * NUNIT) >> 4;
  const int tg = 16 * mt + nt;
  const int cp = 32 * g8 + r;
  const int mt2 = 4 * g8 + (r >> 3), nt2 = r & 7;
  const int c15 = lane & 15, g = lane >> 4;
  const int c31 = lane & 31, hh = lane >> 5, b16 = (lane >> 4) & 1;
  const int q4 = (lane & 15) >> 2, p4 = lane & 3;
  const int kch = w >> 1, kd = 2 * cp + kch, kh = w >> 1, mh = w & 1;

  const u32x4 sX = mk_srd(p.x, SEQ * DM * 2), sWS = mk_srd(p.ws, WS_END);
  const rsrc_t rWS = mk_rsrc(p.ws, WS_END), rOUT = mk_rsrc(p.out, SEQ * DM * 2);
  const u32x4 sW1 = mk_srd(p.w1, DM * FF * 2), sW2 = mk_srd(p.w2, FF * DM * 2);
  const u32 lb = (u32)(uintptr_t)(LAS char*)smem;
  const u64 amp0 = __builtin_amdgcn_s_memrealtime();
  if (threadIdx.x == 0) __hip_atomic_store((u64*)(p.ws + 0xA80000) + blockIdx.x, amp0, __ATOMIC_RELAXED, __HIP_MEMORY_SCOPE_AGENT);
  u64 ampk = amp0;

  u32x4 xr = ld128(sX, lane * 16, (8 * tg + 2 * w + side) * 1024);
  u32 vx[4], vw[3];
#pragma unroll
  for (int j = 0; j < 4; ++j) vx[j] = (8 * j + (lane >> 3)) * 1024 - j * 1024 + (((lane & 7) ^ ((4 * j + (lane >> 4)) & 7)) * 16);
#pragma unroll
  for (int j = 0; j < 3; ++j) {
    const int P = (3 * w + j) * 64 + lane, k = P / 12, sl = P - 12 * k;
    vw[j] = k * (FF * 2) + sl * 16 - j * 1024;
  }
  const u32 x_so = (mt * 128 + 32 * w) * 1024, x_lo = lb + 32 * w * 128, w_so = ustart * 64, w_lo = lb + SLOT_W + 3 * w * 1024;
  auto st1_m0 = [&](auto KC, auto J) {
    if constexpr (J == 0) dma_m0(x_lo + (KC & 3) * SLOT);
    if constexpr (J == 4) dma_m0(w_lo + (KC & 3) * SLOT);
  };
  auto st1_go = [&](auto KC, auto J) {
    if constexpr (J < 4) dma_go<J * 1024>(sX, vx[J], x_so + KC * 128); else dma_go<(J - 4) * 1024>(sW1, vw[J - 4], w_so + KC * 64 * (FF * 2));
  };
  auto stage_rt = [&](int kc) {
    dma_m0(x_lo + kc * SLOT); asm volatile("s_nop 0");
    sfor<4>([&](auto J) { dma_go<J * 1024>(sX, vx[J], x_so + kc * 128); });
    dma_m0(w_lo + kc * SLOT); asm volatile("s_nop 0");
    sfor<3>([&](auto J) { dma_go<J * 1024>(sW1, vw[J], w_so + kc * 64 * (FF * 2)); });
  };
  stage_rt(side);
  u32 eword = ld32c(sWS, WS_E, 0);
  u32 q_lar, q_aim, q_cre, q_cim, q_cre2, q_cim2;
  {
    const u32 vo = (64 * (w & 1) + lane) * 2, so = kd * NMODE * 2;
    const u32x4 sCr = mk_srd(p.cre, DM * NMODE * 2), sCi = mk_srd(p.cim, DM * NMODE * 2);
    q_lar = ld16(mk_srd(p.lar, DM * NMODE * 2), vo, so); q_aim = ld16(mk_srd(p.aim, DM * NMODE * 2), vo, so);
    q_cre = ld16(sCr, vo, so); q_cim = ld16(sCi, vo, so);
    q_cre2 = ld16(sCr, vo ^ 128, so); q_cim2 = ld16(sCi, vo ^ 128, so);
  }
  u32x2 b1v = ld64(mk_srd(p.b1, FF * 2), lane < 24 ? lane * 8 : OOB, ustart * 64);
  u32 tch;
  {
    u64 pc;
    asm volatile("s_getpc_b64 %0" : "=s"(pc));
    const char* a;
    if (tid < 96) a = (const char*)p.x + (512 * (g8 >> 1) + 16 * r + tid / 6) * 1024 + (2 + tid % 6) * 128;
    else if (tid < 192) a = (const char*)p.w1 + (256 + 8 * r + (tid - 96) / 12) * (FF * 2) + (g8 & 1) * 1280 + ((tid - 96) % 12) * 120;
    else a = (const char*)pc + ((r * 64 + tid) % 208) * 128;
    tch = touch(a);
  }
  {
    const int ln = wid * 8 + (lane >> 3);
    st128c(rWS, u32x4{POISON, POISON, POISON, POISON}, WS_A + ((ln >> 1) * 512 + 2 * cp + (ln & 1)) * 128 + (lane & 7) * 16);
  }
  stage_rt(2 + side);
  u32 s_ldt, s_dsk;
  asm volatile("s_load_dword %0, %2, %4\n\ts_load_dword %1, %3, %4\n\ts_waitcnt lgkmcnt(0)" : "=&s"(s_ldt), "=&s"(s_dsk) : "s"(p.ldt), "s"(p.dsk), "s"(cp * 4) : "memory");
  s_ldt = (s_ldt >> (16 * kch)) & 0xffffu; s_dsk = (s_dsk >> (16 * kch)) & 0xffffu;
  u32 ixp[5];
#pragma unroll
  for (int e = 0; e < 9; ++e) {
    const int l = 2047 - (8 * t8 + e);
    const u32 rb = pk_bf16(sp2((float)l)) & 0xffffu;
    const u32 ix = (l < 0 ? 641u : (l < 128 ? (u32)l : rb - 17024u)) * 2;
    if (e & 1) ixp[e >> 1] |= ix << 16; else ixp[e >> 1] = ix;
  }
#pragma unroll
  for (int e = 0; e < 5; ++e) tie(ixp[e]);
  asm volatile("s_waitcnt vmcnt(24)" : "+v"(xr)::"memory");
  ampk = __builtin_amdgcn_s_memrealtime();

  u32 epoch;
  {
    f32x2 x[4];
#pragma unroll
    for (int i = 0; i < 4; ++i) x[i] = bf2(xr[i]);
    const float x0 = asf(__builtin_amdgcn_readfirstlane(asu(x[0].x)));
    f32x2 s = {0.f, 0.f}, q = {0.f, 0.f};
#pragma unroll
    for (int i = 0; i < 4; ++i) { const f32x2 d = x[i] - sp2(x0); s += d; q = fma2(d, d, q); }
    float sm = s.x + s.y, sq = q.x + q.y;
    wave_sum2(sm, sq);
    const float md = sm * (1.0f / DM);
    const float var = fmaxf(__builtin_fmaf(-md, md, sq * (1.0f / DM)), 0.f);
    const float m1 = rbf(x0 + md);
    const float r1 = __builtin_amdgcn_rcpf(rbf(__builtin_amdgcn_sqrtf(rbf(rbf(var) + 1e-5f))));
    s = f32x2{0.f, 0.f}; q = f32x2{0.f, 0.f};
    u32x4 hv;
#pragma unroll
    for (int i = 0; i < 4; ++i) {
      const f32x2 n = bf2(pk_bf16(bf2(pk_bf16(x[i] - sp2(m1))) * sp2(r1)));
      s += n; q = fma2(n, n, q);
      hv[i] = pk_f16(n);
    }
    lds_st<u32x4>(smem, S_LN + (2 * w + side) * LN_ROW + lane * 16, hv);
    float s2 = s.x + s.y, q2 = q.x + q.y;
    wave_sum2(s2, q2);
    const float mean2 = s2 * (1.0f / DM);
    const float var2 = fmaxf(__builtin_fmaf(-mean2, mean2, q2 * (1.0f / DM)), 0.f);
    const float r2 = __builtin_amdgcn_rcpf(rbf(__builtin_amdgcn_sqrtf(rbf(rbf(var2) + 1e-5f))));
    asm volatile("s_waitcnt vmcnt(8)" : "+v"(eword), "+v"(q_lar), "+v"(q_aim), "+v"(q_cre), "+v"(q_cim), "+v"(q_cre2), "+v"(q_cim2), "+v"(b1v), "+v"(tch)::"memory");
    epoch = __builtin_amdgcn_readfirstlane(eword) * 0x9E3779B1u + 0x7F4A7C15u;
    st128c(rWS, u32x4{asu(r1 * r2), epoch, asu(__builtin_fmaf(m1, r1, rbf(mean2)) * r2), epoch}, lane == 0 ? WS_STATS + (8 * tg + 2 * w + side) * 16 : OOB);
  }
  f32x16 acc1[3];
  f32x4 csp[3];
  u32x4 stv;
  if (side == 0) {
    const u32 xa0 = lb + (32 * w + c31) * 128 + ((hh ^ ((c31 >> 1) & 7)) * 16);
    const u32 wa0 = lb + SLOT_W + (8 * hh + q4) * 192 + b16 * 32 + 8 * p4;
    u32 xa[2][4], wa[2];
#pragma unroll
    for (int s = 0; s < 4; ++s) { xa[0][s] = xa0 ^ (s * 32); xa[1][s] = xa[0][s] + 2 * SLOT; }
    wa[0] = wa0; wa[1] = wa0 + 2 * SLOT;
    u32x4 xf[2];
    u32x2 wl[2][3], wh[2][3];
    auto rd = [&](auto S, auto R) {
      constexpr int s = S, rr = R, buf = s & 1, kc = s >> 2, sl = kc & 3, so = (sl & 1) * SLOT, sp = s & 3;
      if constexpr (rr == 0) xf[buf] = a_ds128<so>(xa[sl >> 1][sp]);
      else {
        constexpr int u = (rr - 1) >> 1, hf = (rr - 1) & 1, off = so + u * 64 + (sp * 16 + hf * 4) * 192;
        if constexpr (hf == 0) wl[buf][u] = a_dstr<off>(wa[sl >> 1]); else wh[buf][u] = a_dstr<off>(wa[sl >> 1]);
      }
    };
    bar();
    CSTAMP(0);
    sfor<7>([&](auto R) { rd(ic<0>{}, R); });
    sfor<32>([&](auto S) {
      constexpr int s = S, buf = s & 1, sp = s & 3, kc = s >> 2;
      constexpr bool last = s == 31, brk = sp == 2 && kc < 7;
      sfor<3>([&](auto U) {
        constexpr int u = U;
        constexpr int gi = sp == 2 ? (u == 2 ? 0 : -1) : (u == 0 ? -1 : (sp == 3 ? u : 2 * sp + 2 + u));
        constexpr int kcs = sp >= 2 ? kc + 3 : kc + 2;
        constexpr bool st = gi >= 0 && gi < 7 && kcs >= 4 && kcs < 8;
        if constexpr (u == 0) wait_lgkm<4>();
        else if constexpr (u == 1) {
          wait_lgkm<(last ? 0 : 3)>();
          if constexpr (brk) {
            constexpr int n = kc + 1;
            if constexpr (n == 2) wait_vm<1>();
            else if constexpr (n >= 4 && n < 7) wait_vm<7>();
            else if constexpr (n == 7) wait_vm<0>();
            CSTAMP(2 * n - 1);
            asm volatile("s_barrier" ::: "memory");
            CSTAMP(2 * n);
            if constexpr (n == 7) stv = ld128c(sWS, WS_STATS + (128 * mt + 32 * w + c31) * 16, 0);
          }
        }
        if constexpr (st && (gi == 0 || gi == 4)) st1_m0(ic<kcs>{}, ic<gi>{});
        mfma32_vv<s == 0>(acc1[u], cat4(wl[buf][u], wh[buf][u]), xf[buf]);
        if constexpr (st) st1_go(ic<kcs>{}, ic<gi>{});
        if constexpr (!last) {
          if constexpr (u == 0) sfor<3>([&](auto R) { rd(ic<s + 1>{}, R); });
          if constexpr (u == 1) sfor<2>([&](auto R) { rd(ic<s + 1>{}, ic<3 + R>{}); });
          if constexpr (u == 2) sfor<2>([&](auto R) { rd(ic<s + 1>{}, ic<5 + R>{}); });
        }
      });
    });
    asm volatile("s_nop 7\n\ts_nop 3" : "+v"(acc1[0]), "+v"(acc1[1]), "+v"(acc1[2]));
    CSTAMP(15); CDUMP(16, 0);
    STAMP(2);
    bar();
#pragma unroll
    for (int q = 0; q < 6; ++q) {
      const f32x16& a = acc1[q < 2 ? 1 : 2];
      const int o = q < 2 ? 8 + 4 * q : 4 * (q - 2);
      lds_st<f32x4>(smem, S_TB + w * 6144 + q * 1024 + lane * 16, f32x4{a[o], a[o + 1], a[o + 2], a[o + 3]});
    }
    asm volatile("s_waitcnt vmcnt(0)" : "+v"(stv)::"memory");
  } else {
    const u32 wc0 = lb + SLOT_W + (32 * (w >> 1) + 8 * g + q4) * 192 + 3 * (w & 1) * 32 + 8 * p4;
    u32x4 ones = {0x3f803f80u, 0x3f803f80u, 0x3f803f80u, 0x3f803f80u};
    tie(ones);
    u32x2 cl[3], ch[3];
    auto cs_rd = [&](auto KC) {
      const u32 a = wc0 + (KC & 3) * SLOT;
      sfor<3>([&](auto I) { cl[I] = a_dstr<I * 32>(a); ch[I] = a_dstr<I * 32 + 4 * 192>(a); });
      FENCE();
    };
    auto cs_sum = [&](auto KC) {
      FENCE();
      wait_lgkm<0>();
      sfor<3>([&](auto I) { mfma16_bf<KC == 0>(csp[I], cat4(cl[I], ch[I]), ones); });
    };
    bar();
    CSTAMP(0);
    cs_rd(ic<0>{});
#pragma unroll
    for (int i = 0; i < 2; ++i) {
      const u32 a = S_LN + q4 * LN_ROW + ((2 * w + i) * 64 + 16 * g + 4 * p4) * 2;
      st128c(rWS, cat4(lds_tr(smem, a), lds_tr(smem, a + 4 * LN_ROW)), WS_XN + tg * 8192 + ((2 * w + i) * 64 + lane) * 16);
    }
    lds_st<f32x4>(smem, lane < 24 ? S_B1 + lane * 16 : S_MISC, f32x4{bflo(b1v.x), bfhi(b1v.x), bflo(b1v.y), bfhi(b1v.y)});
    cs_sum(ic<0>{});
    CSTAMP(1);
    bar();
    CSTAMP(2);
    cs_rd(ic<1>{}); cs_sum(ic<1>{});
    CSTAMP(3);
    bar();
    CSTAMP(4);
    cs_rd(ic<2>{}); cs_sum(ic<2>{});
    wait_vm<3>();
    CSTAMP(5);
    bar();
    CSTAMP(6);
    cs_rd(ic<3>{}); cs_sum(ic<3>{});
    CSTAMP(7);
    bar();
    CSTAMP(8);
    cs_rd(ic<4>{}); cs_sum(ic<4>{});
    CSTAMP(9);
    wait_vm<0>();
    bar();
    CSTAMP(10);
    st32c(rWS, epoch, t8 == 0 ? WS_LNF + tg * 4 : OOB);
    cs_rd(ic<5>{}); cs_sum(ic<5>{});
    CSTAMP(11);
    bar();
    CSTAMP(12);
    cs_rd(ic<6>{}); cs_sum(ic<6>{});
    CSTAMP(13);
    bar();
    CSTAMP(14);
    stv = ld128c(sWS, WS_STATS + (128 * mt + 32 * w + c31) * 16, 0);
    cs_rd(ic<7>{}); cs_sum(ic<7>{});
    asm volatile("s_nop 7\n\ts_nop 1" : "+v"(csp[0]), "+v"(csp[1]), "+v"(csp[2]));
#pragma unroll
    for (int i = 0; i < 3; ++i) lds_st<f32x4>(smem, c15 == 0 ? S_CSP + ((w >> 1) * 96 + (3 * (w & 1) + i) * 16 + 4 * g) * 4 : S_MISC, csp[i]);
    CSTAMP(15); CDUMP(16, 20);
    STAMP(2);
    bar();
    const int f = t8 < 96 ? t8 : 0;
    lds_st<float>(smem, t8 < 96 ? S_CS + t8 * 4 : S_MISC, lds_ld<float>(smem, S_CSP + f * 4) + lds_ld<float>(smem, S_CSP + 384 + f * 4));
    asm volatile("s_waitcnt vmcnt(0)" : "+v"(stv)::"memory");
  }
#pragma unroll 1
  for (int it = 0; it < (1 << 16); ++it) {
    if (__all(stv.y == epoch && stv.w == epoch)) break;
    __builtin_amdgcn_s_sleep(1);
    stv = ld128c(sWS, WS_STATS + (128 * mt + 32 * w + c31) * 16, 0);
    asm volatile("s_waitcnt vmcnt(0)" : "+v"(stv)::"memory");
  }
  bar();
  CSTAMP(0);
  STAMP(3);

  {
    f32x4 av[6];
    if (side) {
#pragma unroll
      for (int q = 0; q < 6; ++q) av[q] = lds_ld<f32x4>(smem, S_TB + w * 6144 + q * 1024 + lane * 16);
    } else {
#pragma unroll
      for (int q = 0; q < 6; ++q) {
        const f32x16& a = acc1[q >> 2];
        const int o = 4 * (q & 3);
        av[q] = f32x4{a[o], a[o + 1], a[o + 2], a[o + 3]};
      }
    }
    const f32x2 al = sp2(asf(stv.x)), nb = sp2(-asf(stv.z));
    const u32 hso = WS_H + ((mt * 4 + w) * NUNIT + ustart) * 2048 + 3 * side * 1024 + lane * 16;
    const u32 cb = hh * 16 + 3 * side * 64;
#pragma unroll
    for (int i = 0; i < 3; ++i) {
      u32x4 o;
#pragma unroll
      for (int q = 0; q < 2; ++q) {
        const f32x4 bi = lds_ld<f32x4>(smem, S_B1 + cb + (16 * i + 8 * q) * 4), cs = lds_ld<f32x4>(smem, S_CS + cb + (16 * i + 8 * q) * 4);
        const f32x4 a = av[2 * i + q];
        const f32x2 p0 = fma2(al, f32x2{a.x, a.y}, fma2(nb, f32x2{cs.x, cs.y}, f32x2{bi.x, bi.y}));
        const f32x2 p1 = fma2(al, f32x2{a.z, a.w}, fma2(nb, f32x2{cs.z, cs.w}, f32x2{bi.z, bi.w}));
        o[2 * q] = pk_bf16(gelu2(p0));
        o[2 * q + 1] = pk_bf16(gelu2(p1));
      }
      st128c(rWS, o, ustart + ((3 * side + i) >> 1) < unext ? hso + i * 1024 : OOB);
    }
  }
  CSTAMP(1);
  u32 lnf = ld32c(sWS, WS_LNF + t8 * 4, 0);
  u32 tch2;
  {
    const int ix = tid < 480 ? tid : tid - 480, ri = 15 * r + (ix >> 5);
    tch2 = touch((const char*)p.w2 + (ri < 256 ? 448 + ri : 896 + ri) * 1024 + (ix & 31) * 32);
  }
  st32c(rWS, epoch, (b == 0 && tid == 0) ? WS_E : OOB);
  STAMP(4);
  const u32 vw2 = (lane >> 3) * 1024 + ((((lane & 7) >> 1) ^ ((lane >> 3) & 2)) * 32) + (lane & 1) * 16;
  const u32 vw2p[2] = {vw2, vw2 + 7168};
  const u32 vw2z[2] = {kh ? OOB : vw2p[0], kh ? OOB : vw2p[1]};
  const u32 w2_lo = lb + kh * W2_HALF + 2 * mh * 1024, w2_so = (kh * 22 * 32 + 16 * mh) * 1024 + nt2 * 128;
  auto st2_piece = [&](auto N) {
    constexpr int j = N >> 1, i = N & 1;
    FENCE();
    if constexpr (i == 0) { dma_m0(w2_lo + (j % W2_NS) * 4096); asm volatile("s_nop 0"); }
    dma_go<i * 1024>(sW2, j == 21 ? vw2z[i] : vw2p[i], w2_so + j * 32 * 1024);
    FENCE();
  };

  float inv_sk;
  {
    const int mh2 = w & 1;
    const float dt = rbf(expf(bf1(s_ldt)));
    const float zr = -rbf(expf(bf1(q_lar))) * dt, zi = bf1(q_aim) * dt;
    f32x2 C = f32x2{bf1(q_cre), bf1(q_cim)} * sp2(2.0f * dt);
    const float bd = wave_sum((__builtin_fabsf(C.x) + __builtin_fabsf(C.y)) + (__builtin_fabsf(bf1(q_cre2)) + __builtin_fabsf(bf1(q_cim2))) * (2.0f * dt));
    int se = 267 - (int)((asu(bd) >> 23) & 0xff);
    se = se < 1 ? 1 : (se > 253 ? 253 : se);
    C *= sp2(asf((u32)se << 23));
    inv_sk = asf((u32)(254 - se) << 23);
    const float e1 = zr * 1.44269504089f;
    const float ph = zi * 0.15915494309f;
    const float pl = __builtin_fmaf(zi, 0.15915494309f, -ph) + zi * 6.4206383e-9f;
    auto cexp = [&](float pw) {
      const float m = __builtin_amdgcn_exp2f(e1 * pw);
      const float a = __builtin_amdgcn_fractf(ph * pw) + pl * pw;
      return f32x2{__builtin_amdgcn_cosf(a), __builtin_amdgcn_sinf(a)} * sp2(m);
    };
    CSTAMP(2);
    const u32 tl = S_TAB + kch * TAB_CH + (64 * mh2 + lane) * 4;
    auto putX = [&](int row, f32x2 v) { lds_st<u32>(smem, tl + (16 + row) * TAB_ROW, pk_f16(v)); };
    if (side == 0) {
      f32x2 P[8], E[3], CE[8];
      P[1] = cexp(1.f); E[0] = cexp(128.f);
      CE[0] = C;
      cmul2(P[2], P[1], P[1], E[1], E[0], E[0]);
      cmul2(CE[1], C, E[0], P[3], P[2], P[1]);
      cmul2(P[4], P[2], P[2], E[2], E[1], E[1]);
      cmul2(CE[2], CE[0], E[1], CE[3], CE[1], E[1]);
      cmul2(P[5], P[4], P[1], P[6], P[4], P[2]);
      cmul2(CE[4], CE[0], E[2], CE[5], CE[1], E[2]);
      cmul2(CE[6], CE[2], E[2], CE[7], CE[3], E[2]);
      P[7] = cmul(P[4], P[3]);
      putX(0, CE[0]); putX(8, CE[1]);
#pragma unroll
      for (int i = 1; i < 8; ++i) {
        f32x2 a, c;
        cmul2(a, CE[0], P[i], c, CE[1], P[i]);
        putX(i, a); putX(8 + i, c);
      }
      putX(16, CE[2]); putX(20, CE[3]);
#pragma unroll
      for (int i = 1; i < 4; ++i) {
        f32x2 a, c;
        cmul2(a, CE[2], P[2 * i], c, CE[3], P[2 * i]);
        putX(16 + i, a); putX(20 + i, c);
      }
      putX(24, CE[4]); putX(26, CE[5]); putX(28, CE[6]); putX(30, CE[7]);
      f32x2 a, c;
      cmul2(a, CE[4], P[4], c, CE[5], P[4]); putX(25, a); putX(27, c);
      cmul2(a, CE[6], P[4], c, CE[7], P[4]); putX(29, a); putX(31, c);
    } else {
      f32x2 Y[16], E[5], CE[9];
      st2_piece(ic<0>{});
      Y[1] = cexp(8.f); E[0] = cexp(128.f);
      Y[0] = f32x2{1.f, 0.f};
      st2_piece(ic<1>{});
      auto putY = [&](int i) { lds_st<u32>(smem, tl + i * TAB_ROW, pk_f16_conj(Y[i])); };
      cmul2(Y[2], Y[1], Y[1], E[1], E[0], E[0]);
      st2_piece(ic<2>{});
      cmul2(Y[3], Y[2], Y[1], Y[4], Y[2], Y[2]);
      putY(0); putY(1); putY(2);
      st2_piece(ic<3>{});
      cmul2(E[2], E[1], E[1], Y[5], Y[4], Y[1]);
      cmul2(Y[6], Y[4], Y[2], Y[7], Y[4], Y[3]);
      putY(3); putY(4);
      st2_piece(ic<4>{});
      cmul2(Y[8], Y[4], Y[4], E[3], E[2], E[2]);
      putY(5); putY(6); putY(7);
      st2_piece(ic<5>{});
      cmul2(Y[9], Y[8], Y[1], Y[10], Y[8], Y[2]);
      cmul2(E[4], E[3], E[3], CE[0], C, E[3]);
      putY(8);
      st2_piece(ic<6>{});
      cmul2(Y[11], Y[8], Y[3], Y[12], Y[8], Y[4]);
      putY(9); putY(10);
      st2_piece(ic<7>{});
      cmul2(Y[13], Y[8], Y[5], Y[14], Y[8], Y[6]);
      putY(11); putY(12);
      st2_piece(ic<8>{});
      cmul2(Y[15], Y[8], Y[7], CE[1], CE[0], E[0]);
      putY(13); putY(14);
      st2_piece(ic<9>{});
      cmul2(CE[2], CE[0], E[1], CE[8], C, E[4]);
      putY(15); putX(32, CE[0]); putX(33, CE[1]);
      st2_piece(ic<10>{});
      cmul2(CE[3], CE[1], E[1], CE[4], CE[0], E[2]);
      putX(34, CE[2]); putX(40, CE[8]);
      st2_piece(ic<11>{});
      cmul2(CE[5], CE[1], E[2], CE[6], CE[2], E[2]);
      putX(35, CE[3]); putX(36, CE[4]);
      st2_piece(ic<12>{});
      CE[7] = cmul(CE[3], E[2]);
      putX(37, CE[5]); putX(38, CE[6]);
      st2_piece(ic<13>{});
      putX(39, CE[7]);
      st2_piece(ic<14>{});
    }
    CSTAMP(3);
    if (side) wait_vm<17>(); else wait_vm<2>();
    tie(lnf); tie(tch2);
    st32c(rWS, epoch, lane == 0 ? WS_HF + (((mt * 4 + w) * 16 + nt) * 2 + side) * 4 : OOB);
    spin_flag(sWS, WS_LNF + t8 * 4, epoch, lnf, true);
    u32x4 ug = ld128c(sWS, WS_XN + t8 * 8192 + cp * 32 + side * 16, 0);
    if (side) st2_piece(ic<15>{});
    CSTAMP(4);
    bar();
    CSTAMP(5);
    const int tile = side ? 2 : mh2;
    const u32 kc = S_KC + kch * KC_CH;
    if (!(side && mh2)) {
      const u32 rd = S_TAB + kch * TAB_CH + c15 * TAB_ROW + g * 16;
      const u32 ra = S_TAB + kch * TAB_CH + (16 + min(16 * tile + c15, 40)) * TAB_ROW + g * 16;
      u32x4 yf[8], xf[8];
#pragma unroll
      for (int s = 0; s < 8; ++s) { yf[s] = lds_ld<u32x4>(smem, rd + s * 64); xf[s] = lds_ld<u32x4>(smem, ra + s * 64); }
      f32x4 a0 = {0, 0, 0, 0}, a1 = {0, 0, 0, 0};
#pragma unroll
      for (int s = 0; s < 4; ++s) { a0 = mfma_h(xf[2 * s], yf[2 * s], a0); a1 = mfma_h(xf[2 * s + 1], yf[2 * s + 1], a1); }
      const f32x4 v = a0 + a1;
      const u32 h01 = pk_f16(f32x2{v[0], v[1]}), h23 = pk_f16(f32x2{v[2], v[3]});
      if (tile == 0) {
        lds_st<u32x2>(smem, kc + (128 * (g >> 1) + 8 * c15 + 4 * (g & 1)) * 2, u32x2{h01, h23});
      } else if (tile == 1) {
        const u32 t1a = g < 2 ? 256 + 64 * g + 4 * c15 : 384 + 64 * (g - 2) + 2 * c15;
        lds_st<u32>(smem, kc + t1a * 2, h01);
        lds_st<u32>(smem, kc + (t1a + (g < 2 ? 2 : 32)) * 2, h23);
      } else {
        const u32 t2 = g < 2 ? 512 + 64 * g + c15 : 644, st2 = g < 2 ? 16 : 0;
        lds_st<u16>(smem, kc + ((g == 2 && c15 == 0) ? 640 : t2) * 2, (u16)h01);
        lds_st<u16>(smem, kc + (t2 + st2) * 2, (u16)(h01 >> 16));
        lds_st<u16>(smem, kc + (t2 + 2 * st2) * 2, (u16)h23);
        lds_st<u16>(smem, kc + (t2 + 3 * st2) * 2, (u16)(h23 >> 16));
      }
    } else if (lane == 0) lds_st<u16>(smem, kc + 641 * 2, (u16)0);
    CSTAMP(6);
    bar();
    CSTAMP(7);
    if (side) wait_vm<1>(); else wait_vm<0>();
    tie(ug);
    {
      u32 e[9];
#pragma unroll
      for (int i = 0; i < 9; ++i) e[i] = lds_ld<u16>(smem, S_KC + side * KC_CH + ((i & 1) ? ixp[i >> 1] >> 16 : ixp[i >> 1] & 0xffffu));
      const u32x4 a0 = {e[0] | (e[1] << 16), e[2] | (e[3] << 16), e[4] | (e[5] << 16), e[6] | (e[7] << 16)};
      const u32x4 a1 = {e[1] | (e[2] << 16), e[3] | (e[4] << 16), e[5] | (e[6] << 16), e[7] | (e[8] << 16)};
      lds_st<u32x4>(smem, S_G + side * G_CH + t8 * 16, a0);
      lds_st<u32x4>(smem, S_G + side * G_CH + G_PAR + t8 * 16, a1);
      const u32x4 z4 = {0, 0, 0, 0};
      if (t8 < 4) lds_st<u32x4>(smem, S_G + side * G_CH + (t8 >> 1) * G_PAR + 4096 + (t8 & 1) * 16, z4);
      if (t8 < 30) lds_st<u32x4>(smem, S_U + side * U_CH + t8 * 16, z4);
      lds_st<u32x4>(smem, S_U + side * U_CH + 480 + t8 * 16, ug);
    }
    CSTAMP(8);
    bar();
    CSTAMP(9);
    if (side) CDUMP(10, 48); else CDUMP(10, 38);
  }
  STAMP(5);

  const int ch0 = 64 * nt2 + 32 * kh + 4 * hh;
  const u32 xto = (64 * mt2 + (t8 >> 3)) * 1024 + nt2 * 128 + (t8 & 7) * 16;
  f32x16 acc[2];
  if (side == 0) {
    const int par = w & 1;
    const u32 ub = lb + S_U + kch * U_CH + 64 - 64 * par + 32 * c15 + 16 * g;
    const u32 gb0 = lb + S_G + kch * G_CH + ((c15 & 1) ? 0 : G_PAR) + 4 * (1015 - (c15 >> 1) - 16 * par + 4 * g - 32 * 31);
    const u32 gb[4] = {gb0, gb0 + 1024, gb0 + 2048, gb0 + 3072};
    u32x4 bq[32];
    u32x2 al[32], ah[32];
    f32x4 y[8];
    auto rdB = [&](auto M, auto T) { constexpr int q = 4 * T - M + 3; bq[q] = a_ds128<128 * q>(ub); };
    auto rdA = [&](auto M, auto H) {
      constexpr int k = (31 - M) >> 3, o = 32 * ((31 - M) & 7);
      if constexpr (H == 0) al[M] = a_ds2x32<o, o + 1>(gb[k]); else ah[M] = a_ds2x32<o + 2, o + 3>(gb[k]);
    };
    sfor<2>([&](auto M) { sfor<8>([&](auto T) { rdB(M, T); }); rdA(M, ic<0>{}); rdA(M, ic<1>{}); });
    sfor<32>([&](auto M) {
      constexpr int m = M, t0 = m >> 2, n = 8 - t0;
      wait_lgkm<(m <= 2 ? 10 : (m == 3 ? 4 : (m == 4 ? 6 : (2 * (31 - m) < 8 ? 2 * (31 - m) : 8))))>();
      sfor<n>([&](auto I) {
        constexpr int i = I, t = t0 + i;
        mfma16_h<m == 0>(y[t], cat4(al[m], ah[m]), bq[4 * t - m + 3]);
        if constexpr (m < 2) {
          rdB(ic<m + 2>{}, I);
          if constexpr (i >= 6) rdA(ic<m + 2>{}, ic<i - 6>{});
        } else if constexpr (m <= 4) {
          if constexpr (i < 4) rdA(ic<2 * m + (i >> 1)>{}, ic<i & 1>{});
        } else if constexpr (m + 5 < 32) {
          if constexpr (i < 2) rdA(ic<m + 5>{}, I);
        }
      });
    });
    asm volatile("s_nop 7\n\ts_nop 1" : "+v"(y[0]), "+v"(y[1]), "+v"(y[2]), "+v"(y[3]), "+v"(y[4]), "+v"(y[5]), "+v"(y[6]), "+v"(y[7]));
    STAMP(6);
    bar();
    auto send = [&](auto PAR) {
      FENCE();
#pragma unroll
      for (int i = 0; i < 4; ++i) lds_st<f32x4>(smem, S_EX + w * 4096 + i * 1024 + lane * 16, y[4 * (1 - PAR) + i]);
      FENCE();
    };
    if (par) send(ic<1>{}); else send(ic<0>{});
    bar();
    const float dsk = bf1(s_dsk);
    u32x2 ao[4];
    auto finish = [&](auto PAR) {
      FENCE();
#pragma unroll
      for (int i = 0; i < 4; ++i) {
        const f32x4 o = lds_ld<f32x4>(smem, S_EX + (w ^ 1) * 4096 + i * 1024 + lane * 16);
        const u32x2 uu = lds_ld<u32x2>(smem, S_U + kch * U_CH + (240 + 256 * (4 * PAR + i) + 16 * c15 + 4 * g) * 2);
        const f16x2 u01 = ash2(uu.x), u23 = ash2(uu.y);
        const float uf[4] = {(float)u01.x, (float)u01.y, (float)u23.x, (float)u23.y};
        float v[4];
#pragma unroll
        for (int e = 0; e < 4; ++e) v[e] = ffma1(uf[e], dsk, fmul1(fadd1(y[4 * PAR + i][e], o[e]), inv_sk));
        ao[i] = u32x2{cvt_bf2(v[0], v[1]), cvt_bf2(v[2], v[3])};
      }
      FENCE();
    };
    if (par) finish(ic<1>{}); else finish(ic<0>{});
#pragma unroll
    for (int i = 0; i < 4; ++i) {
      u32x2 o = ao[i];
      o.x = o.x == POISON ? 0xffc0ffc0u : o.x;
      o.y = o.y == POISON ? 0xffc0ffc0u : o.y;
      st64c(rWS, o, WS_A + ((4 * (4 * par + i) + (c15 >> 2)) * 512 + kd) * 128 + ((c15 & 3) * 16 + 4 * g) * 2);
    }
    STAMP(7);
    u32x4 xt0 = ld128(sX, xto, 0), xt1 = ld128(sX, xto + 32 * 1024, 0);
    u32 b2w = ld16(mk_srd(p.b2, DM * 2), t8 < 64 ? (64 * nt2 + t8) * 2 : OOB, 0);
    bar();
    sfor<16>([&](auto N) { st2_piece(ic<28 + N>{}); });
    const u32 ato = WS_A + (mt2 * 512 + nt2 * 64) * 128 + t8 * 16;
    u32x4 at0 = ld128c(sWS, ato, 0), at1 = ld128c(sWS, ato + 4096, 0);
    asm volatile("s_waitcnt vmcnt(10)" : "+v"(xt0), "+v"(xt1), "+v"(b2w)::"memory");
    bar();
    const int row0 = t8 >> 3, sl = t8 & 7;
    {
      const u32 o = S_XT + row0 * 128 + ((sl ^ (row0 & 7)) * 16);
      lds_st<u32x4>(smem, o, xt0);
      lds_st<u32x4>(smem, o + 32 * 128, xt1);
      lds_st<float>(smem, t8 < 64 ? S_B2 + t8 * 4 : S_MISC, bf1(b2w));
    }
    wait_vm<2>();
    bar();
    asm volatile("s_waitcnt vmcnt(0)" : "+v"(at0), "+v"(at1)::"memory");
#pragma unroll 1
    for (int it = 0; it < (1 << 16); ++it) {
      const u32 m0 = max(max(at0.x, at0.y), max(at0.z, at0.w)), m1 = max(max(at1.x, at1.y), max(at1.z, at1.w));
      if (!__any(max(m0, m1) == POISON)) break;
      at0 = ld128c(sWS, ato, 0); at1 = ld128c(sWS, ato + 4096, 0);
      asm volatile("s_waitcnt vmcnt(0)" : "+v"(at0), "+v"(at1)::"memory");
    }
    {
      const u32 o = S_AT + row0 * 128 + (((sl >> 1) ^ (row0 & 2)) * 32) + (sl & 1) * 16;
      lds_st<u32x4>(smem, o, at0);
      lds_st<u32x4>(smem, o + 32 * 128, at1);
    }
    bar();
  } else {
    u32x4 hf[22][2];
    const u32 hb = WS_H + ((mt2 * 2 + mh) * NUNIT + kh * 22) * 2048;
    const u32 hvo = lane * 16, hvz = kh ? OOB : hvo;
    auto ldh = [&](auto J, auto S) { hf[J][S] = ld128c(sWS, J == 21 ? hvz : hvo, hb + (J * 2 + S) * 1024); };
    sfor<12>([&](auto N) { st2_piece(ic<16 + N>{}); });
    {
      const u32 hfo = WS_HF + (((mt2 >> 1) * 4 + 2 * (mt2 & 1) + mh) * 32 + c31) * 4;
      u32 hfl = ld32c(sWS, hfo, 0);
      asm volatile("s_waitcnt vmcnt(0)" : "+v"(hfl)::"memory");
      spin_flag(sWS, hfo, epoch, hfl, kh ? c31 >= 16 : c31 <= 17);
    }
    sfor<4>([&](auto J) { ldh(J, ic<0>{}); ldh(J, ic<1>{}); });
    STAMP(6);
    bar();
    sfor<3>([&](auto J) { ldh(ic<4 + J>{}, ic<0>{}); ldh(ic<4 + J>{}, ic<1>{}); });
    bar();
    sfor<G2_LEAD - 7>([&](auto J) { ldh(ic<7 + J>{}, ic<0>{}); ldh(ic<7 + J>{}, ic<1>{}); });
    STAMP(7);
    u32 wa[2];
#pragma unroll
    for (int n = 0; n < 2; ++n) wa[n] = lb + kh * W2_HALF + (4 * hh + q4) * 128 + (((2 * n + b16) ^ (q4 & 2)) * 32) + 8 * p4;
    u32x2 wl[2][4], wh[2][4];
    auto rd = [&](auto J, auto I, auto H) {
      constexpr int j = J, i = I, off = (j % W2_NS) * 4096 + ((i >> 1) * 16 + H * 8) * 128;
      if constexpr (H == 0) wl[j & 1][i] = a_dstr<off>(wa[i & 1]); else wh[j & 1][i] = a_dstr<off>(wa[i & 1]);
    };
    sfor<4>([&](auto I) { rd(ic<0>{}, I, ic<0>{}); rd(ic<0>{}, I, ic<1>{}); });
    sfor<22>([&](auto J) {
      constexpr int j = J;
      constexpr bool last = j == 21, brk = j == 8 || j == 13 || j == 17;
      if constexpr (brk) bar();
      wait_vm<g2_wait_h(j)>();
      tie(hf[j][0]); tie(hf[j][1]);
      sfor<4>([&](auto I) {
        constexpr int i = I;
        if constexpr (!brk) wait_lgkm<(last ? 6 - 2 * i : 6)>();
        mfma32_vv<j == 0 && i < 2>(acc[i & 1], cat4(wl[j & 1][i], wh[j & 1][i]), hf[j][i >> 1]);
        if constexpr ((i & 1) == 0 && j + G2_LEAD <= 21) ldh(ic<j + G2_LEAD>{}, ic<(i >> 1)>{});
        if constexpr (!last) { rd(ic<j + 1>{}, I, ic<0>{}); rd(ic<j + 1>{}, I, ic<1>{}); }
      });
    });
    asm volatile("s_nop 7\n\ts_nop 3" : "+v"(acc[0]), "+v"(acc[1]));
    STAMP(8);
    auto send2 = [&](auto KH) {
      FENCE();
#pragma unroll
      for (int q = 0; q < 4; ++q) {
        const f32x16& as = acc[1 - KH];
        lds_st<f32x4>(smem, S_EX + w * 4096 + q * 1024 + lane * 16, f32x4{as[4 * q], as[4 * q + 1], as[4 * q + 2], as[4 * q + 3]});
      }
      FENCE();
    };
    if (kh) send2(ic<1>{}); else send2(ic<0>{});
    bar();
    const int tl = 32 * mh + c31;
    auto fin2 = [&](auto KH) {
      FENCE();
#pragma unroll
      for (int q = 0; q < 4; ++q) {
        const f32x16& ak = acc[KH];
        const f32x4 v = f32x4{ak[4 * q], ak[4 * q + 1], ak[4 * q + 2], ak[4 * q + 3]} + lds_ld<f32x4>(smem, S_EX + (w ^ 2) * 4096 + q * 1024 + lane * 16);
        const f32x4 bb = lds_ld<f32x4>(smem, S_B2 + (32 * KH + 8 * q + 4 * hh) * 4);
        const int row = 32 * KH + 8 * q + 4 * hh + q4;
        const u32x2 av = lds_tr(smem, S_AT + row * 128 + (((2 * mh + b16) ^ (row & 2)) * 32) + 8 * p4);
        const u32 po = tl * 128 + (((4 * KH + q) ^ (tl & 7)) * 16) + hh * 8;
        const u32x2 xv = lds_ld<u32x2>(smem, S_XT + po);
        const f32x2 o0 = (bf2(xv.x) + bf2(av.x)) + (f32x2{v[0], v[1]} + f32x2{bb.x, bb.y}), o1 = (bf2(xv.y) + bf2(av.y)) + (f32x2{v[2], v[3]} + f32x2{bb.z, bb.w});
        lds_st<u32x2>(smem, S_OT + po, u32x2{pk_bf16(o0), pk_bf16(o1)});
      }
      FENCE();
    };
    if (kh) fin2(ic<1>{}); else fin2(ic<0>{});
  }
  bar();
  {
    const int row = tid >> 3, sl = tid & 7;
    const u32x4 o = lds_ld<u32x4>(smem, S_OT + row * 128 + ((sl ^ (row & 7)) * 16));
    __builtin_amdgcn_raw_buffer_store_b128(o, rOUT, (64 * mt2 + row) * 1024 + nt2 * 128 + sl * 16, 0, 0);
  }
  { const u64 te = __builtin_amdgcn_s_memrealtime(); u64 until = te + 20 * (ampk - amp0);
    while (__builtin_amdgcn_s_memrealtime() < until) __builtin_amdgcn_s_sleep(2); }
}

extern "C" void kernel_launch(void* const* d_in, const int* in_sizes, int n_in, void* d_out, int out_size, void* d_ws,
                              size_t ws_size, hipStream_t stream) {
  Params p;
  p.x = (const u16*)d_in[0]; p.lar = (const u16*)d_in[1]; p.aim = (const u16*)d_in[2]; p.cre = (const u16*)d_in[3];
  p.cim = (const u16*)d_in[4]; p.ldt = (const u16*)d_in[5]; p.dsk = (const u16*)d_in[6];
  p.w1 = (const u16*)d_in[8]; p.b1 = (const u16*)d_in[9]; p.w2 = (const u16*)d_in[10]; p.b2 = (const u16*)d_in[11];
  p.out = (u16*)d_out; p.ws = (char*)d_ws;
  s4d_mixer_fused<<<256, 512, 0, stream>>>(p);
}
```

```cpp
#include <hip/hip_runtime.h>
#include <stdint.h>
#include <utility>

namespace {
typedef unsigned short u16;
typedef unsigned u32;
typedef unsigned long long u64;
typedef float f32x2 __attribute__((ext_vector_type(2)));
typedef float f32x4 __attribute__((ext_vector_type(4)));
typedef float f32x16 __attribute__((ext_vector_type(16)));
typedef u32 u32x2 __attribute__((ext_vector_type(2)));
typedef u32 u32x4 __attribute__((ext_vector_type(4)));
typedef short s16x4 __attribute__((ext_vector_type(4)));
typedef __bf16 bf16x2 __attribute__((ext_vector_type(2)));
typedef _Float16 f16x2 __attribute__((ext_vector_type(2)));
typedef _Float16 f16x8 __attribute__((ext_vector_type(8)));
#define LAS __attribute__((address_space(3)))
#define DEV __device__ __forceinline__
typedef __amdgpu_buffer_rsrc_t rsrc_t;

constexpr int SEQ = 2048, DM = 512, NMODE = 128, FF = 1376, NUNIT = 43;
constexpr u32 WS_E = 0, WS_LNF = 0x1000, WS_HF = 0x2000  , WS_STATS = 0x8000, WS_XN = 0x10000, WS_A = 0x210000, WS_H = 0x410000,
              WS_END = WS_H + 64 * NUNIT * 2048, WS_STAMP = 0xA00000;
constexpr u32 SLOT = 28672, SLOT_W = 16384;
constexpr u32 W2_NS = 14, W2_HALF = W2_NS * 4096;
constexpr u32 LDS_BYTES_ = 163840;
constexpr u32 S0 = 114688;
constexpr u32 S_DUMMY = LDS_BYTES_ - 256;
constexpr u32 S_LN = S0, LN_ROW = 1088;
constexpr u32 TAB_ROW = 544, TAB_CH = 57 * TAB_ROW;
constexpr u32 S_TAB = LDS_BYTES_ - 2 * TAB_CH - 1872;
constexpr u32 S_TB = 32768;
constexpr u32 S_KC = S_TAB - 2592, KC_CH = 1296;
constexpr u32 S_CSP = LDS_BYTES_ - 1872;
constexpr u32 S_B1 = S_CSP + 768;
constexpr u32 S_MISC = S_B1 + 384;
constexpr u32 S_CS = S_MISC + 64;
constexpr u32 S_G = S0, G_PAR = 4160, G_CH = 8320;
constexpr u32 S_U = S0 + 16640, U_CH = 4608;
constexpr u32 S_EX = S0;
constexpr u32 S_AT = S0 + 16384, S_XT = S0 + 24576, S_OT = S0 + 32768, S_B2 = S0 + 40960;
constexpr u32 LDS_BYTES = 163840;
constexpr u32 OOB = 0x80000000u;
constexpr u32 POISON = 0xffffffffu;

#define STAMP(k) do {} while (0)
#define CSTAMP(i) do {} while (0)
#define CDUMP(n, base) do {} while (0)

struct Params {
  const u16 *x, *lar, *aim, *cre, *cim, *ldt, *dsk, *w1, *b1, *w2, *b2;
  u16* out;
  char* ws;
};

DEV float asf(u32 v) { return __builtin_bit_cast(float, v); }
DEV u32 asu(float v) { return __builtin_bit_cast(u32, v); }
DEV f16x2 ash2(u32 v) { return __builtin_bit_cast(f16x2, v); }
DEV float bflo(u32 v) { return asf(v << 16); }
DEV float bfhi(u32 v) { return asf(v & 0xffff0000u); }
DEV f32x2 bf2(u32 v) { return f32x2{bflo(v), bfhi(v)}; }
DEV u32 pk_bf16(f32x2 v) { return __builtin_bit_cast(u32, __builtin_convertvector(v, bf16x2)); }
DEV u32 pk_f16(f32x2 v) { return __builtin_bit_cast(u32, __builtin_convertvector(v, f16x2)); }
DEV float rbf(float a) { return bflo(pk_bf16(f32x2{a, a})); }
DEV float bf1(u32 v) { return asf(v << 16); }
DEV f32x2 sp2(float a) { return f32x2{a, a}; }
DEV f32x2 fma2(f32x2 a, f32x2 b, f32x2 c) { return __builtin_elementwise_fma(a, b, c); }

template <int N> DEV void wait_vm() { asm volatile("s_waitcnt vmcnt(%0)" ::"n"(N) : "memory"); }
template <int N> DEV void wait_lgkm() { asm volatile("s_waitcnt lgkmcnt(%0)" ::"n"(N)); }
DEV void bar() { asm volatile("s_waitcnt lgkmcnt(0)\n\ts_barrier" ::: "memory"); }
DEV u32x4 mk_srd(const void* base, u32 bytes) {
  const u64 b = (u64)base;
  u32x4 s;
  s.x = __builtin_amdgcn_readfirstlane((u32)b);
  s.y = __builtin_amdgcn_readfirstlane((u32)(b >> 32)) & 0xffffu;
  s.z = bytes;
  s.w = 0x00020000u;
  return s;
}
DEV rsrc_t mk_rsrc(const void* base, u32 bytes) { return __builtin_amdgcn_make_buffer_rsrc((void*)base, 0, bytes, 0x00020000); }

#define BLOAD(INS, TY, NAME, FLAGS, CON)                                                                     \
  DEV TY NAME(u32x4 srd, u32 voff, u32 soff) {                                                               \
    TY r;                                                                                                    \
    asm volatile(INS " %0, %1, %2, %3 offen" FLAGS : CON(r) : "v"(voff), "s"(srd), "s"(soff) : "memory");     \
    return r;                                                                                                \
  }
BLOAD("buffer_load_dwordx4", u32x4, ld128, "", "=v")
BLOAD("buffer_load_dwordx4", u32x4, ld128c, " sc1", "=v")
BLOAD("buffer_load_dwordx4", u32x4, ld128c_a, " sc1", "=a")
BLOAD("buffer_load_dwordx2", u32x2, ld64, "", "=v")
BLOAD("buffer_load_dword", u32, ld32c, " sc1", "=v")
BLOAD("buffer_load_ushort", u32, ld16, "", "=v")
DEV void touch(const void* a, u32 lds_dummy) { asm volatile("s_mov_b32 m0, %1\n\ts_nop 0\n\tglobal_load_lds_dword %0, off" ::"v"(a), "s"(lds_dummy) : "memory"); }
template <class T> DEV void tie(T& v) { asm volatile("" : "+v"(v)); }

DEV void dma_m0(u32 lds_addr) { asm volatile("s_mov_b32 m0, %0" ::"s"(lds_addr) : "memory"); }
template <int IOFF> DEV void dma_go(u32x4 srd, u32 voff, u32 soff) {
  asm volatile("buffer_load_dwordx4 %0, %1, %2 offen offset:%3 lds" ::"v"(voff), "s"(srd), "s"(soff), "n"(IOFF) : "memory");
}
DEV void st128c(rsrc_t r, u32x4 v, u32 voff) { __builtin_amdgcn_raw_buffer_store_b128(v, r, voff, 0, 16); }
DEV void st64c(rsrc_t r, u32x2 v, u32 voff) { __builtin_amdgcn_raw_buffer_store_b64(v, r, voff, 0, 16); }
DEV void st32c(rsrc_t r, u32 v, u32 voff) { __builtin_amdgcn_raw_buffer_store_b32(v, r, voff, 0, 16); }
DEV void st64(rsrc_t r, u32x2 v, u32 voff) { __builtin_amdgcn_raw_buffer_store_b64(v, r, voff, 0, 0); }

template <class T> DEV T lds_ld(const char* smem, u32 off) { return *(const LAS T*)(smem + off); }
template <class T> DEV void lds_st(char* smem, u32 off, T v) { *(LAS T*)(smem + off) = v; }
DEV u32x2 lds_tr(const char* smem, u32 off) { return __builtin_bit_cast(u32x2, __builtin_amdgcn_ds_read_tr16_b64_v4i16((LAS s16x4*)(smem + off))); }

template <int I> using ic = std::integral_constant<int, I>;
template <class F, int... Is> DEV void sfor_impl(F&& f, std::integer_sequence<int, Is...>) { (f(ic<Is>{}), ...); }
template <int N, class F> DEV void sfor(F&& f) { sfor_impl(f, std::make_integer_sequence<int, N>{}); }
template <int OFF> DEV u32x4 a_ds128(u32 addr) { u32x4 r; asm volatile("ds_read_b128 %0, %1 offset:%2" : "=v"(r) : "v"(addr), "n"(OFF)); return r; }
template <int OFF> DEV u32x2 a_dstr(u32 addr) { u32x2 r; asm volatile("ds_read_b64_tr_b16 %0, %1 offset:%2" : "=v"(r) : "v"(addr), "n"(OFF)); return r; }
template <int O0, int O1> DEV u32x2 a_ds2x32(u32 addr) { u32x2 r; asm volatile("ds_read2_b32 %0, %1 offset0:%2 offset1:%3" : "=v"(r) : "v"(addr), "n"(O0), "n"(O1)); return r; }
DEV u32x4 cat4(u32x2 a, u32x2 b) { return __builtin_shufflevector(a, b, 0, 1, 2, 3); }
template <bool INIT> DEV void mfma32_vv(f32x16& c, u32x4 a, u32x4 b) {
  if constexpr (INIT) asm volatile("v_mfma_f32_32x32x16_bf16 %0, %1, %2, 0" : "=&v"(c) : "v"(a), "v"(b));
  else asm volatile("v_mfma_f32_32x32x16_bf16 %0, %1, %2, %0" : "+v"(c) : "v"(a), "v"(b));
}
template <bool INIT> DEV void mfma32_va(f32x16& c, u32x4 a, u32x4 b) {
  if constexpr (INIT) asm volatile("v_mfma_f32_32x32x16_bf16 %0, %1, %2, 0" : "=&v"(c) : "v"(a), "a"(b));
  else asm volatile("v_mfma_f32_32x32x16_bf16 %0, %1, %2, %0" : "+v"(c) : "v"(a), "a"(b));
}
template <bool INIT> DEV void mfma16_h(f32x4& c, u32x4 a, u32x4 b) {
  if constexpr (INIT) asm volatile("v_mfma_f32_16x16x32_f16 %0, %1, %2, 0" : "=&v"(c) : "v"(a), "v"(b));
  else asm volatile("v_mfma_f32_16x16x32_f16 %0, %1, %2, %0" : "+v"(c) : "v"(a), "v"(b));
}
DEV f32x4 mfma_h(u32x4 a, u32x4 b, f32x4 c) {
  return __builtin_amdgcn_mfma_f32_16x16x32_f16(__builtin_bit_cast(f16x8, a), __builtin_bit_cast(f16x8, b), c, 0, 0, 0);
}
template <bool INIT> DEV void mfma16_bf(f32x4& c, u32x4 a, u32x4 b) {
  if constexpr (INIT) asm volatile("v_mfma_f32_16x16x32_bf16 %0, %1, %2, 0" : "=&v"(c) : "v"(a), "v"(b));
  else asm volatile("v_mfma_f32_16x16x32_bf16 %0, %1, %2, %0" : "+v"(c) : "v"(a), "v"(b));
}
#define FENCE() __builtin_amdgcn_sched_barrier(0)

DEV void wave_sum4(float& a, float& b, float& c, float& d) {
#define S4(CTRL) "v_add_f32_dpp %0, %0, %0 " CTRL "\n\tv_add_f32_dpp %1, %1, %1 " CTRL "\n\tv_add_f32_dpp %2, %2, %2 " CTRL "\n\tv_add_f32_dpp %3, %3, %3 " CTRL "\n\t"
  asm volatile("s_nop 1\n\t" S4("row_shr:1 row_mask:0xf bank_mask:0xf bound_ctrl:1") S4("row_shr:2 row_mask:0xf bank_mask:0xf bound_ctrl:1")
               S4("row_shr:4 row_mask:0xf bank_mask:0xf bound_ctrl:1") S4("row_shr:8 row_mask:0xf bank_mask:0xf bound_ctrl:1")
               S4("row_bcast:15 row_mask:0xa bank_mask:0xf") S4("row_bcast:31 row_mask:0xc bank_mask:0xf") "s_nop 0"
               : "+v"(a), "+v"(b), "+v"(c), "+v"(d));
#undef S4
  a = asf(__builtin_amdgcn_readlane(asu(a), 63)); b = asf(__builtin_amdgcn_readlane(asu(b), 63));
  c = asf(__builtin_amdgcn_readlane(asu(c), 63)); d = asf(__builtin_amdgcn_readlane(asu(d), 63));
}
DEV void wave_sum2(float& a, float& b) {
#define S2(CTRL) "v_add_f32_dpp %0, %0, %0 " CTRL "\n\tv_add_f32_dpp %1, %1, %1 " CTRL "\n\ts_nop 0\n\t"
  asm volatile("s_nop 1\n\t" S2("row_shr:1 row_mask:0xf bank_mask:0xf bound_ctrl:1") S2("row_shr:2 row_mask:0xf bank_mask:0xf bound_ctrl:1")
               S2("row_shr:4 row_mask:0xf bank_mask:0xf bound_ctrl:1") S2("row_shr:8 row_mask:0xf bank_mask:0xf bound_ctrl:1")
               S2("row_bcast:15 row_mask:0xa bank_mask:0xf") S2("row_bcast:31 row_mask:0xc bank_mask:0xf")
               : "+v"(a), "+v"(b));
#undef S2
  a = asf(__builtin_amdgcn_readlane(asu(a), 63)); b = asf(__builtin_amdgcn_readlane(asu(b), 63));
}
template <int CTRL, int RM> DEV float dpp_add(float v) { return v + asf(__builtin_amdgcn_update_dpp(0, asu(v), CTRL, RM, 0xf, true)); }
DEV float wave_sum(float v) {
  v = dpp_add<0x111, 0xf>(v); v = dpp_add<0x112, 0xf>(v); v = dpp_add<0x114, 0xf>(v); v = dpp_add<0x118, 0xf>(v);
  v = dpp_add<0x142, 0xa>(v); v = dpp_add<0x143, 0xc>(v);
  return asf(__builtin_amdgcn_readlane(asu(v), 63));
}
DEV void cmul2(f32x2& r0, f32x2 a0, f32x2 b0, f32x2& r1, f32x2 a1, f32x2 b1) {
  f32x2 t0, t1;
  asm("v_pk_mul_f32 %0, %4, %5 op_sel_hi:[1,0]\n\tv_pk_mul_f32 %1, %6, %7 op_sel_hi:[1,0]\n\t"
      "v_pk_fma_f32 %2, %4, %5, %0 op_sel:[1,1,0] op_sel_hi:[0,1,1] neg_lo:[1,0,0]\n\t"
      "v_pk_fma_f32 %3, %6, %7, %1 op_sel:[1,1,0] op_sel_hi:[0,1,1] neg_lo:[1,0,0]"
      : "=&v"(t0), "=&v"(t1), "=&v"(r0), "=&v"(r1) : "v"(a0), "v"(b0), "v"(a1), "v"(b1));
}
DEV f32x2 cmul(f32x2 a, f32x2 b) {
  f32x2 t, r;
  asm("v_pk_mul_f32 %0, %2, %3 op_sel_hi:[1,0]\n\tv_pk_fma_f32 %1, %2, %3, %0 op_sel:[1,1,0] op_sel_hi:[0,1,1] neg_lo:[1,0,0]"
      : "=&v"(t), "=v"(r) : "v"(a), "v"(b));
  return r;
}
DEV u32 cvt_bf2(float a, float b) { u32 r; asm("v_cvt_pk_bf16_f32 %0, %1, %2" : "=v"(r) : "v"(a), "v"(b)); return r; }
DEV u32 cvt_h2(float a, float b) { u32 r; asm("v_cvt_pk_f16_f32 %0, %1, %2" : "=v"(r) : "v"(a), "v"(b)); return r; }
DEV float fadd1(float a, float b) { float r; asm("v_add_f32 %0, %1, %2" : "=v"(r) : "v"(a), "v"(b)); return r; }
DEV float fsub1(float a, float b) { float r; asm("v_sub_f32 %0, %1, %2" : "=v"(r) : "v"(a), "v"(b)); return r; }
DEV float fmul1(float a, float b) { float r; asm("v_mul_f32 %0, %1, %2" : "=v"(r) : "v"(a), "v"(b)); return r; }
DEV float ffma1(float a, float b, float c) { float r; asm("v_fma_f32 %0, %1, %2, %3" : "=v"(r) : "v"(a), "v"(b), "v"(c)); return r; }
DEV float rbf1(float a) { return bflo(cvt_bf2(a, a)); }
DEV u32 pk_f16_conj(f32x2 v) { u32 r; asm("v_cvt_pk_f16_f32 %0, %1, -%2" : "=v"(r) : "v"(v.x), "v"(v.y)); return r; }
DEV f32x2 gelu2(f32x2 v) {
  const f32x2 s = v * fma2(v * v, sp2(-0.10294323970f), sp2(-2.3022081986f));
  const f32x2 d = f32x2{__builtin_amdgcn_exp2f(s.x), __builtin_amdgcn_exp2f(s.y)} + sp2(1.0f);
  return v * f32x2{__builtin_amdgcn_rcpf(d.x), __builtin_amdgcn_rcpf(d.y)};
}
DEV void spin_flag(u32x4 srd, u32 voff, u32 epoch, u32 val, bool need) {
#pragma unroll 1
  for (int it = 0; it < (1 << 16); ++it) {
    if (__all(!need || val == epoch)) break;
    __builtin_amdgcn_s_sleep(1);
    val = ld32c(srd, voff, 0);
    asm volatile("s_waitcnt vmcnt(0)" : "+v"(val)::"memory");
  }
}

constexpr int G2_LEAD = 10;
constexpr int g2_wait_h(int j) { return 2 * ((j + G2_LEAD - 1 < 21 ? j + G2_LEAD - 1 : 21) - j); }

}

__global__ __launch_bounds__(512, 2) void s4d_mixer_fused(Params p) {
  __shared__ __attribute__((aligned(16))) char smem[LDS_BYTES];
  const int tid = threadIdx.x, lane = tid & 63, t8 = tid & 255;
  const int wid = __builtin_amdgcn_readfirstlane(tid >> 6);
  const int side = wid >> 2, w = wid & 3;
  const int b = blockIdx.x, g8 = b & 7, r = b >> 3;
  const int mt = 4 * (g8 >> 1) + (r >> 3), nt = 8 * (g8 & 1) + (r & 7);
  const int ustart = (nt * NUNIT) >> 4, unext = ((nt + 1) * NUNIT) >> 4;
  const int tg = 16 * mt + nt;
  const int cp = 32 * g8 + r;
  const int mt2 = 4 * g8 + (r >> 3), nt2 = r & 7;
  const int c15 = lane & 15, g = lane >> 4;
  const int c31 = lane & 31, hh = lane >> 5, b16 = (lane >> 4) & 1;
  const int q4 = (lane & 15) >> 2, p4 = lane & 3;
  const int kch = w >> 1, kd = 2 * cp + kch, kh = w >> 1, mh = w & 1;

  const u32x4 sX = mk_srd(p.x, SEQ * DM * 2), sWS = mk_srd(p.ws, WS_END);
  const rsrc_t rWS = mk_rsrc(p.ws, WS_END), rOUT = mk_rsrc(p.out, SEQ * DM * 2);
  const u32x4 sW1 = mk_srd(p.w1, DM * FF * 2), sW2 = mk_srd(p.w2, FF * DM * 2);
  const u32 lb = (u32)(uintptr_t)(LAS char*)smem;
  STAMP(0);

  u32x4 xr = ld128(sX, lane * 16, (8 * tg + 2 * w + side) * 1024);
  u32 vx[4], vw[3];
#pragma unroll
  for (int j = 0; j < 4; ++j) vx[j] = (8 * j + (lane >> 3)) * 1024 - j * 1024 + (((lane & 7) ^ ((4 * j + (lane >> 4)) & 7)) * 16);
#pragma unroll
  for (int j = 0; j < 3; ++j) {
    const int P = (3 * w + j) * 64 + lane, k = P / 12, sl = P - 12 * k;
    vw[j] = k * (FF * 2) + sl * 16 - j * 1024;
  }
  const u32 x_so = (mt * 128 + 32 * w) * 1024, x_lo = lb + 32 * w * 128, w_so = ustart * 64, w_lo = lb + SLOT_W + 3 * w * 1024;
  auto st1_m0 = [&](auto KC, auto J) {
    if constexpr (J == 0) dma_m0(x_lo + (KC & 3) * SLOT);
    if constexpr (J == 4) dma_m0(w_lo + (KC & 3) * SLOT);
  };
  auto st1_go = [&](auto KC, auto J) {
    if constexpr (J < 4) dma_go<J * 1024>(sX, vx[J], x_so + KC * 128); else dma_go<(J - 4) * 1024>(sW1, vw[J - 4], w_so + KC * 64 * (FF * 2));
  };
  auto stage_rt = [&](int kc) {
    dma_m0(x_lo + kc * SLOT); asm volatile("s_nop 0");
    sfor<4>([&](auto J) { dma_go<J * 1024>(sX, vx[J], x_so + kc * 128); });
    dma_m0(w_lo + kc * SLOT); asm volatile("s_nop 0");
    sfor<3>([&](auto J) { dma_go<J * 1024>(sW1, vw[J], w_so + kc * 64 * (FF * 2)); });
  };
  u32 eword = ld32c(sWS, WS_E, 0);
  stage_rt(side);
  stage_rt(2 + side);
  {
    u64 pc;
    asm volatile("s_getpc_b64 %0" : "=s"(pc));
    const char* a;
    if (tid < 96) a = (const char*)p.x + (512 * (g8 >> 1) + 16 * r + tid / 6) * 1024 + (2 + tid % 6) * 128;
    else if (tid < 192) a = (const char*)p.w1 + (256 + 8 * r + (tid - 96) / 12) * (FF * 2) + (g8 & 1) * 1280 + ((tid - 96) % 12) * 120;
    else a = (const char*)pc + ((r * 64 + tid) % 208) * 128;
    touch(a, lb + S_DUMMY);
  }
  u32 ixp[5];
#pragma unroll
  for (int e = 0; e < 9; ++e) {
    const int l = 2047 - (8 * t8 + e);
    const u32 rb = pk_bf16(sp2((float)l)) & 0xffffu;
    const u32 ix = (l < 0 ? 641u : (l < 128 ? (u32)l : rb - 17024u)) * 2;
    if (e & 1) ixp[e >> 1] |= ix << 16; else ixp[e >> 1] = ix;
  }
#pragma unroll
  for (int e = 0; e < 5; ++e) tie(ixp[e]);
  asm volatile("s_waitcnt vmcnt(16)" : "+v"(xr)::"memory");
  STAMP(1);

  u32 epoch;
  {
    f32x2 x[4];
#pragma unroll
    for (int i = 0; i < 4; ++i) x[i] = bf2(xr[i]);
    const float x0 = asf(__builtin_amdgcn_readfirstlane(asu(x[0].x)));
    f32x2 s = {0.f, 0.f}, q = {0.f, 0.f};
#pragma unroll
    for (int i = 0; i < 4; ++i) { const f32x2 d = x[i] - sp2(x0); s += d; q = fma2(d, d, q); }
    float sm = s.x + s.y, sq = q.x + q.y;
    wave_sum2(sm, sq);
    const float md = sm * (1.0f / DM);
    const float var = fmaxf(__builtin_fmaf(-md, md, sq * (1.0f / DM)), 0.f);
    const float m1 = rbf(x0 + md);
    const float r1 = __builtin_amdgcn_rcpf(rbf(__builtin_amdgcn_sqrtf(rbf(rbf(var) + 1e-5f))));
    s = f32x2{0.f, 0.f}; q = f32x2{0.f, 0.f};
    u32x4 hv;
#pragma unroll
    for (int i = 0; i < 4; ++i) {
      const f32x2 n = bf2(pk_bf16(bf2(pk_bf16(x[i] - sp2(m1))) * sp2(r1)));
      s += n; q = fma2(n, n, q);
      hv[i] = pk_f16(n);
    }
    lds_st<u32x4>(smem, S_LN + (2 * w + side) * LN_ROW + lane * 16, hv);
    float s2 = s.x + s.y, q2 = q.x + q.y;
    wave_sum2(s2, q2);
    const float mean2 = s2 * (1.0f / DM);
    const float var2 = fmaxf(__builtin_fmaf(-mean2, mean2, q2 * (1.0f / DM)), 0.f);
    const float r2 = __builtin_amdgcn_rcpf(rbf(__builtin_amdgcn_sqrtf(rbf(rbf(var2) + 1e-5f))));
    asm volatile("s_waitcnt vmcnt(8)" : "+v"(eword)::"memory");
    epoch = __builtin_amdgcn_readfirstlane(eword) * 0x9E3779B1u + 0x7F4A7C15u;
    st128c(rWS, u32x4{asu(r1 * r2), epoch, asu(__builtin_fmaf(m1, r1, rbf(mean2)) * r2), epoch}, lane == 0 ? WS_STATS + (8 * tg + 2 * w + side) * 16 : OOB);
  }
  {
    const int ln = wid * 8 + (lane >> 3);
    st128c(rWS, u32x4{POISON, POISON, POISON, POISON}, WS_A + ((ln >> 1) * 512 + 2 * cp + (ln & 1)) * 128 + (lane & 7) * 16);
  }
  u32 q_lar, q_aim, q_cre, q_cim, q_cre2, q_cim2, q_ldt, q_dsk;
  u32x2 b1v;
  auto load_small = [&]() {
    const u32 vo = (64 * (w & 1) + lane) * 2, so = kd * NMODE * 2;
    const u32x4 sCr = mk_srd(p.cre, DM * NMODE * 2), sCi = mk_srd(p.cim, DM * NMODE * 2);
    q_lar = ld16(mk_srd(p.lar, DM * NMODE * 2), vo, so); q_aim = ld16(mk_srd(p.aim, DM * NMODE * 2), vo, so);
    q_cre = ld16(sCr, vo, so); q_cim = ld16(sCi, vo, so);
    q_cre2 = ld16(sCr, vo ^ 128, so); q_cim2 = ld16(sCi, vo ^ 128, so);
    q_ldt = ld16(mk_srd(p.ldt, DM * 2), 0, kd * 2); q_dsk = ld16(mk_srd(p.dsk, DM * 2), 0, kd * 2);
    b1v = ld64(mk_srd(p.b1, FF * 2), lane < 24 ? lane * 8 : OOB, ustart * 64);
  };
#define TIE_SMALL "+v"(q_lar), "+v"(q_aim), "+v"(q_cre), "+v"(q_cim), "+v"(q_cre2), "+v"(q_cim2), "+v"(q_ldt), "+v"(q_dsk), "+v"(b1v)
  f32x16 acc1[3];
  f32x4 csp[3];
  u32x4 stv;
  if (side == 0) {
    const u32 xa0 = lb + (32 * w + c31) * 128 + ((hh ^ ((c31 >> 1) & 7)) * 16);
    const u32 wa0 = lb + SLOT_W + (8 * hh + q4) * 192 + b16 * 32 + 8 * p4;
    u32 xa[2][4], wa[2];
#pragma unroll
    for (int s = 0; s < 4; ++s) { xa[0][s] = xa0 ^ (s * 32); xa[1][s] = xa[0][s] + 2 * SLOT; }
    wa[0] = wa0; wa[1] = wa0 + 2 * SLOT;
    u32x4 xf[2];
    u32x2 wl[2][3], wh[2][3];
    auto rd = [&](auto S, auto R) {
      constexpr int s = S, rr = R, buf = s & 1, kc = s >> 2, sl = kc & 3, so = (sl & 1) * SLOT, sp = s & 3;
      if constexpr (rr == 0) xf[buf] = a_ds128<so>(xa[sl >> 1][sp]);
      else {
        constexpr int u = (rr - 1) >> 1, hf = (rr - 1) & 1, off = so + u * 64 + (sp * 16 + hf * 4) * 192;
        if constexpr (hf == 0) wl[buf][u] = a_dstr<off>(wa[sl >> 1]); else wh[buf][u] = a_dstr<off>(wa[sl >> 1]);
      }
    };
    load_small();
    bar();
    CSTAMP(0);
    sfor<7>([&](auto R) { rd(ic<0>{}, R); });
    sfor<32>([&](auto S) {
      constexpr int s = S, buf = s & 1, sp = s & 3, kc = s >> 2;
      constexpr bool last = s == 31, brk = sp == 2 && kc < 7;
      sfor<3>([&](auto U) {
        constexpr int u = U;
        constexpr int gi = sp == 2 ? (u == 2 ? 0 : -1) : (u == 0 ? -1 : (sp == 3 ? u : 2 * sp + 2 + u));
        constexpr int kcs = sp >= 2 ? kc + 3 : kc + 2;
        constexpr bool st = gi >= 0 && gi < 7 && kcs >= 4 && kcs < 8;
        if constexpr (u == 0) wait_lgkm<4>();
        else if constexpr (u == 1) {
          wait_lgkm<(last ? 0 : 3)>();
          if constexpr (brk) {
            constexpr int n = kc + 1;
            if constexpr (n == 2) wait_vm<12>();
            else if constexpr (n == 4) asm volatile("s_waitcnt vmcnt(7)" : TIE_SMALL :: "memory");
            else if constexpr (n > 4 && n < 7) wait_vm<7>();
            else if constexpr (n == 7) wait_vm<0>();
            CSTAMP(2 * n - 1);
            asm volatile("s_barrier" ::: "memory");
            CSTAMP(2 * n);
            if constexpr (n == 7) stv = ld128c(sWS, WS_STATS + (128 * mt + 32 * w + c31) * 16, 0);
          }
        }
        if constexpr (st && (gi == 0 || gi == 4)) st1_m0(ic<kcs>{}, ic<gi>{});
        mfma32_vv<s == 0>(acc1[u], cat4(wl[buf][u], wh[buf][u]), xf[buf]);
        if constexpr (st) st1_go(ic<kcs>{}, ic<gi>{});
        if constexpr (!last) {
          if constexpr (u == 0) sfor<3>([&](auto R) { rd(ic<s + 1>{}, R); });
          if constexpr (u == 1) sfor<2>([&](auto R) { rd(ic<s + 1>{}, ic<3 + R>{}); });
          if constexpr (u == 2) sfor<2>([&](auto R) { rd(ic<s + 1>{}, ic<5 + R>{}); });
        }
      });
    });
    asm volatile("s_nop 7\n\ts_nop 3" : "+v"(acc1[0]), "+v"(acc1[1]), "+v"(acc1[2]));
    CSTAMP(15); CDUMP(16, 0);
    STAMP(2);
    bar();
#pragma unroll
    for (int q = 0; q < 6; ++q) {
      const f32x16& a = acc1[q < 2 ? 1 : 2];
      const int o = q < 2 ? 8 + 4 * q : 4 * (q - 2);
      lds_st<f32x4>(smem, S_TB + w * 6144 + q * 1024 + lane * 16, f32x4{a[o], a[o + 1], a[o + 2], a[o + 3]});
    }
    asm volatile("s_waitcnt vmcnt(0)" : "+v"(stv)::"memory");
  } else {
    const u32 wc0 = lb + SLOT_W + (32 * (w >> 1) + 8 * g + q4) * 192 + 3 * (w & 1) * 32 + 8 * p4;
    u32x4 ones = {0x3f803f80u, 0x3f803f80u, 0x3f803f80u, 0x3f803f80u};
    tie(ones);
    u32x2 cl[3], ch[3];
    auto cs_rd = [&](auto KC) {
      const u32 a = wc0 + (KC & 3) * SLOT;
      sfor<3>([&](auto I) { cl[I] = a_dstr<I * 32>(a); ch[I] = a_dstr<I * 32 + 4 * 192>(a); });
      FENCE();
    };
    auto cs_sum = [&](auto KC) {
      FENCE();
      wait_lgkm<0>();
      sfor<3>([&](auto I) { mfma16_bf<KC == 0>(csp[I], cat4(cl[I], ch[I]), ones); });
    };
    load_small();
    bar();
    CSTAMP(0);
    cs_rd(ic<0>{});
#pragma unroll
    for (int i = 0; i < 2; ++i) {
      const u32 a = S_LN + q4 * LN_ROW + ((2 * w + i) * 64 + 16 * g + 4 * p4) * 2;
      st128c(rWS, cat4(lds_tr(smem, a), lds_tr(smem, a + 4 * LN_ROW)), WS_XN + tg * 8192 + ((2 * w + i) * 64 + lane) * 16);
    }
    cs_sum(ic<0>{});
    CSTAMP(1);
    bar();
    CSTAMP(2);
    cs_rd(ic<1>{}); cs_sum(ic<1>{});
    CSTAMP(3);
    bar();
    CSTAMP(4);
    cs_rd(ic<2>{}); cs_sum(ic<2>{});
    wait_vm<14>();
    CSTAMP(5);
    bar();
    CSTAMP(6);
    cs_rd(ic<3>{}); cs_sum(ic<3>{});
    CSTAMP(7);
    bar();
    CSTAMP(8);
    cs_rd(ic<4>{}); cs_sum(ic<4>{});
    CSTAMP(9);
    asm volatile("s_waitcnt vmcnt(0)" : TIE_SMALL :: "memory");
    lds_st<f32x4>(smem, lane < 24 ? S_B1 + lane * 16 : S_MISC, f32x4{bflo(b1v.x), bfhi(b1v.x), bflo(b1v.y), bfhi(b1v.y)});
    bar();
    CSTAMP(10);
    st32c(rWS, epoch, t8 == 0 ? WS_LNF + tg * 4 : OOB);
    cs_rd(ic<5>{}); cs_sum(ic<5>{});
    CSTAMP(11);
    bar();
    CSTAMP(12);
    cs_rd(ic<6>{}); cs_sum(ic<6>{});
    CSTAMP(13);
    bar();
    CSTAMP(14);
    stv = ld128c(sWS, WS_STATS + (128 * mt + 32 * w + c31) * 16, 0);
    cs_rd(ic<7>{}); cs_sum(ic<7>{});
    asm volatile("s_nop 7\n\ts_nop 1" : "+v"(csp[0]), "+v"(csp[1]), "+v"(csp[2]));
#pragma unroll
    for (int i = 0; i < 3; ++i) lds_st<f32x4>(smem, c15 == 0 ? S_CSP + ((w >> 1) * 96 + (3 * (w & 1) + i) * 16 + 4 * g) * 4 : S_MISC, csp[i]);
    CSTAMP(15); CDUMP(16, 20);
    STAMP(2);
    bar();
    const int f = t8 < 96 ? t8 : 0;
    lds_st<float>(smem, t8 < 96 ? S_CS + t8 * 4 : S_MISC, lds_ld<float>(smem, S_CSP + f * 4) + lds_ld<float>(smem, S_CSP + 384 + f * 4));
    asm volatile("s_waitcnt vmcnt(0)" : "+v"(stv)::"memory");
  }
#pragma unroll 1
  for (int it = 0; it < (1 << 16); ++it) {
    if (__all(stv.y == epoch && stv.w == epoch)) break;
    __builtin_amdgcn_s_sleep(1);
    stv = ld128c(sWS, WS_STATS + (128 * mt + 32 * w + c31) * 16, 0);
    asm volatile("s_waitcnt vmcnt(0)" : "+v"(stv)::"memory");
  }
  bar();
  CSTAMP(0);
  STAMP(3);

  {
    f32x4 av[6];
    if (side) {
#pragma unroll
      for (int q = 0; q < 6; ++q) av[q] = lds_ld<f32x4>(smem, S_TB + w * 6144 + q * 1024 + lane * 16);
    } else {
#pragma unroll
      for (int q = 0; q < 6; ++q) {
        const f32x16& a = acc1[q >> 2];
        const int o = 4 * (q & 3);
        av[q] = f32x4{a[o], a[o + 1], a[o + 2], a[o + 3]};
      }
    }
    const f32x2 al = sp2(asf(stv.x)), nb = sp2(-asf(stv.z));
    const u32 hso = WS_H + ((mt * 4 + w) * NUNIT + ustart) * 2048 + 3 * side * 1024 + lane * 16;
    const u32 cb = hh * 16 + 3 * side * 64;
#pragma unroll
    for (int i = 0; i < 3; ++i) {
      u32x4 o;
#pragma unroll
      for (int q = 0; q < 2; ++q) {
        const f32x4 bi = lds_ld<f32x4>(smem, S_B1 + cb + (16 * i + 8 * q) * 4), cs = lds_ld<f32x4>(smem, S_CS + cb + (16 * i + 8 * q) * 4);
        const f32x4 a = av[2 * i + q];
        const f32x2 p0 = fma2(al, f32x2{a.x, a.y}, fma2(nb, f32x2{cs.x, cs.y}, f32x2{bi.x, bi.y}));
        const f32x2 p1 = fma2(al, f32x2{a.z, a.w}, fma2(nb, f32x2{cs.z, cs.w}, f32x2{bi.z, bi.w}));
        o[2 * q] = pk_bf16(gelu2(p0));
        o[2 * q + 1] = pk_bf16(gelu2(p1));
      }
      st128c(rWS, o, ustart + ((3 * side + i) >> 1) < unext ? hso + i * 1024 : OOB);
    }
  }
  CSTAMP(1);
  u32 lnf = ld32c(sWS, WS_LNF + t8 * 4, 0);
  {
    const int ix = tid < 480 ? tid : tid - 480, ri = 15 * r + (ix >> 5);
    touch((const char*)p.w2 + (ri < 256 ? 448 + ri : 896 + ri) * 1024 + (ix & 31) * 32, lb + S_DUMMY);
  }
  st32c(rWS, epoch, (b == 0 && tid == 0) ? WS_E : OOB);
  STAMP(4);
  const u32 vw2 = (lane >> 3) * 1024 + ((((lane & 7) >> 1) ^ ((lane >> 3) & 2)) * 32) + (lane & 1) * 16;
  const u32 vw2p[2] = {vw2, vw2 + 7168};
  const u32 vw2z[2] = {kh ? OOB : vw2p[0], kh ? OOB : vw2p[1]};
  const u32 w2_lo = lb + kh * W2_HALF + 2 * mh * 1024, w2_so = (kh * 22 * 32 + 16 * mh) * 1024 + nt2 * 128;
  auto st2_piece = [&](auto N) {
    constexpr int j = N >> 1, i = N & 1;
    FENCE();
    if constexpr (i == 0) { dma_m0(w2_lo + (j % W2_NS) * 4096); asm volatile("s_nop 0"); }
    dma_go<i * 1024>(sW2, j == 21 ? vw2z[i] : vw2p[i], w2_so + j * 32 * 1024);
    FENCE();
  };

  float inv_sk;
  {
    const int mh2 = w & 1;
    const float dt = rbf(expf(bf1(q_ldt)));
    const float zr = -rbf(expf(bf1(q_lar))) * dt, zi = bf1(q_aim) * dt;
    f32x2 C = f32x2{bf1(q_cre), bf1(q_cim)} * sp2(2.0f * dt);
    const float bd = wave_sum((__builtin_fabsf(C.x) + __builtin_fabsf(C.y)) + (__builtin_fabsf(bf1(q_cre2)) + __builtin_fabsf(bf1(q_cim2))) * (2.0f * dt));
    int se = 267 - (int)((asu(bd) >> 23) & 0xff);
    se = se < 1 ? 1 : (se > 253 ? 253 : se);
    C *= sp2(asf((u32)se << 23));
    inv_sk = asf((u32)(254 - se) << 23);
    const float e1 = zr * 1.44269504089f;
    const float ph = zi * 0.15915494309f;
    const float pl = __builtin_fmaf(zi, 0.15915494309f, -ph) + zi * 6.4206383e-9f;
    auto cexp = [&](float pw) {
      const float m = __builtin_amdgcn_exp2f(e1 * pw);
      const float a = __builtin_amdgcn_fractf(ph * pw) + pl * pw;
      return f32x2{__builtin_amdgcn_cosf(a), __builtin_amdgcn_sinf(a)} * sp2(m);
    };
    CSTAMP(2);
    const u32 tl = S_TAB + kch * TAB_CH + (64 * mh2 + lane) * 4;
    auto putX = [&](int row, f32x2 v) { lds_st<u32>(smem, tl + (16 + row) * TAB_ROW, pk_f16(v)); };
    if (side == 0) {
      f32x2 P[8], E[3], CE[8];
      P[1] = cexp(1.f); E[0] = cexp(128.f);
      CE[0] = C;
      cmul2(P[2], P[1], P[1], E[1], E[0], E[0]);
      cmul2(CE[1], C, E[0], P[3], P[2], P[1]);
      cmul2(P[4], P[2], P[2], E[2], E[1], E[1]);
      cmul2(CE[2], CE[0], E[1], CE[3], CE[1], E[1]);
      cmul2(P[5], P[4], P[1], P[6], P[4], P[2]);
      cmul2(CE[4], CE[0], E[2], CE[5], CE[1], E[2]);
      cmul2(CE[6], CE[2], E[2], CE[7], CE[3], E[2]);
      P[7] = cmul(P[4], P[3]);
      putX(0, CE[0]); putX(8, CE[1]);
#pragma unroll
      for (int i = 1; i < 8; ++i) {
        f32x2 a, c;
        cmul2(a, CE[0], P[i], c, CE[1], P[i]);
        putX(i, a); putX(8 + i, c);
      }
      putX(16, CE[2]); putX(20, CE[3]);
#pragma unroll
      for (int i = 1; i < 4; ++i) {
        f32x2 a, c;
        cmul2(a, CE[2], P[2 * i], c, CE[3], P[2 * i]);
        putX(16 + i, a); putX(20 + i, c);
      }
      putX(24, CE[4]); putX(26, CE[5]); putX(28, CE[6]); putX(30, CE[7]);
      f32x2 a, c;
      cmul2(a, CE[4], P[4], c, CE[5], P[4]); putX(25, a); putX(27, c);
      cmul2(a, CE[6], P[4], c, CE[7], P[4]); putX(29, a); putX(31, c);
    } else {
      f32x2 Y[16], E[5], CE[9];
      st2_piece(ic<0>{});
      Y[1] = cexp(8.f); E[0] = cexp(128.f);
      Y[0] = f32x2{1.f, 0.f};
      st2_piece(ic<1>{});
      auto putY = [&](int i) { lds_st<u32>(smem, tl + i * TAB_ROW, pk_f16_conj(Y[i])); };
      cmul2(Y[2], Y[1], Y[1], E[1], E[0], E[0]);
      st2_piece(ic<2>{});
      cmul2(Y[3], Y[2], Y[1], Y[4], Y[2], Y[2]);
      putY(0); putY(1); putY(2);
      st2_piece(ic<3>{});
      cmul2(E[2], E[1], E[1], Y[5], Y[4], Y[1]);
      cmul2(Y[6], Y[4], Y[2], Y[7], Y[4], Y[3]);
      putY(3); putY(4);
      st2_piece(ic<4>{});
      cmul2(Y[8], Y[4], Y[4], E[3], E[2], E[2]);
      putY(5); putY(6); putY(7);
      st2_piece(ic<5>{});
      cmul2(Y[9], Y[8], Y[1], Y[10], Y[8], Y[2]);
      cmul2(E[4], E[3], E[3], CE[0], C, E[3]);
      putY(8);
      st2_piece(ic<6>{});
      cmul2(Y[11], Y[8], Y[3], Y[12], Y[8], Y[4]);
      putY(9); putY(10);
      st2_piece(ic<7>{});
      cmul2(Y[13], Y[8], Y[5], Y[14], Y[8], Y[6]);
      putY(11); putY(12);
      st2_piece(ic<8>{});
      cmul2(Y[15], Y[8], Y[7], CE[1], CE[0], E[0]);
      putY(13); putY(14);
      st2_piece(ic<9>{});
      cmul2(CE[2], CE[0], E[1], CE[8], C, E[4]);
      putY(15); putX(32, CE[0]); putX(33, CE[1]);
      st2_piece(ic<10>{});
      cmul2(CE[3], CE[1], E[1], CE[4], CE[0], E[2]);
      putX(34, CE[2]); putX(40, CE[8]);
      st2_piece(ic<11>{});
      cmul2(CE[5], CE[1], E[2], CE[6], CE[2], E[2]);
      putX(35, CE[3]); putX(36, CE[4]);
      st2_piece(ic<12>{});
      CE[7] = cmul(CE[3], E[2]);
      putX(37, CE[5]); putX(38, CE[6]);
      st2_piece(ic<13>{});
      putX(39, CE[7]);
      st2_piece(ic<14>{});
    }
    CSTAMP(3);
    if (side) wait_vm<17>(); else wait_vm<2>();
    tie(lnf);
    st32c(rWS, epoch, lane == 0 ? WS_HF + (((mt * 4 + w) * 16 + nt) * 2 + side) * 4 : OOB);
    spin_flag(sWS, WS_LNF + t8 * 4, epoch, lnf, true);
    u32x4 ug = ld128c(sWS, WS_XN + t8 * 8192 + cp * 32 + side * 16, 0);
    if (side) st2_piece(ic<15>{});
    CSTAMP(4);
    bar();
    CSTAMP(5);
    const int tile = side ? 2 : mh2;
    const u32 kc = S_KC + kch * KC_CH;
    if (!(side && mh2)) {
      const u32 rd = S_TAB + kch * TAB_CH + c15 * TAB_ROW + g * 16;
      const u32 ra = S_TAB + kch * TAB_CH + (16 + min(16 * tile + c15, 40)) * TAB_ROW + g * 16;
      u32x4 yf[8], xf[8];
#pragma unroll
      for (int s = 0; s < 8; ++s) { yf[s] = lds_ld<u32x4>(smem, rd + s * 64); xf[s] = lds_ld<u32x4>(smem, ra + s * 64); }
      f32x4 a0 = {0, 0, 0, 0}, a1 = {0, 0, 0, 0};
#pragma unroll
      for (int s = 0; s < 4; ++s) { a0 = mfma_h(xf[2 * s], yf[2 * s], a0); a1 = mfma_h(xf[2 * s + 1], yf[2 * s + 1], a1); }
      const f32x4 v = a0 + a1;
      const u32 h01 = pk_f16(f32x2{v[0], v[1]}), h23 = pk_f16(f32x2{v[2], v[3]});
      if (tile == 0) {
        lds_st<u32x2>(smem, kc + (128 * (g >> 1) + 8 * c15 + 4 * (g & 1)) * 2, u32x2{h01, h23});
      } else if (tile == 1) {
        const u32 t1a = g < 2 ? 256 + 64 * g + 4 * c15 : 384 + 64 * (g - 2) + 2 * c15;
        lds_st<u32>(smem, kc + t1a * 2, h01);
        lds_st<u32>(smem, kc + (t1a + (g < 2 ? 2 : 32)) * 2, h23);
      } else {
        const u32 t2 = g < 2 ? 512 + 64 * g + c15 : 644, st2 = g < 2 ? 16 : 0;
        lds_st<u16>(smem, kc + ((g == 2 && c15 == 0) ? 640 : t2) * 2, (u16)h01);
        lds_st<u16>(smem, kc + (t2 + st2) * 2, (u16)(h01 >> 16));
        lds_st<u16>(smem, kc + (t2 + 2 * st2) * 2, (u16)h23);
        lds_st<u16>(smem, kc + (t2 + 3 * st2) * 2, (u16)(h23 >> 16));
      }
    } else if (lane == 0) lds_st<u16>(smem, kc + 641 * 2, (u16)0);
    CSTAMP(6);
    bar();
    CSTAMP(7);
    if (side) wait_vm<1>(); else wait_vm<0>();
    tie(ug);
    {
      u32 e[9];
#pragma unroll
      for (int i = 0; i < 9; ++i) e[i] = lds_ld<u16>(smem, S_KC + side * KC_CH + ((i & 1) ? ixp[i >> 1] >> 16 : ixp[i >> 1] & 0xffffu));
      const u32x4 a0 = {e[0] | (e[1] << 16), e[2] | (e[3] << 16), e[4] | (e[5] << 16), e[6] | (e[7] << 16)};
      const u32x4 a1 = {e[1] | (e[2] << 16), e[3] | (e[4] << 16), e[5] | (e[6] << 16), e[7] | (e[8] << 16)};
      lds_st<u32x4>(smem, S_G + side * G_CH + t8 * 16, a0);
      lds_st<u32x4>(smem, S_G + side * G_CH + G_PAR + t8 * 16, a1);
      const u32x4 z4 = {0, 0, 0, 0};
      if (t8 < 4) lds_st<u32x4>(smem, S_G + side * G_CH + (t8 >> 1) * G_PAR + 4096 + (t8 & 1) * 16, z4);
      if (t8 < 30) lds_st<u32x4>(smem, S_U + side * U_CH + t8 * 16, z4);
      lds_st<u32x4>(smem, S_U + side * U_CH + 480 + t8 * 16, ug);
    }
    CSTAMP(8);
    bar();
    CSTAMP(9);
    if (side) CDUMP(10, 48); else CDUMP(10, 38);
  }
  STAMP(5);

  const int ch0 = 64 * nt2 + 32 * kh + 4 * hh;
  const u32 xto = (64 * mt2 + (t8 >> 3)) * 1024 + nt2 * 128 + (t8 & 7) * 16;
  f32x16 acc[2];
  if (side == 0) {
    const int par = w & 1;
    const u32 ub = lb + S_U + kch * U_CH + 64 - 64 * par + 32 * c15 + 16 * g;
    const u32 gb0 = lb + S_G + kch * G_CH + ((c15 & 1) ? 0 : G_PAR) + 4 * (1015 - (c15 >> 1) - 16 * par + 4 * g - 32 * 31);
    const u32 gb[4] = {gb0, gb0 + 1024, gb0 + 2048, gb0 + 3072};
    u32x4 bq[32];
    u32x2 al[32], ah[32];
    f32x4 y[8];
    auto rdB = [&](auto M, auto T) { constexpr int q = 4 * T - M + 3; bq[q] = a_ds128<128 * q>(ub); };
    auto rdA = [&](auto M, auto H) {
      constexpr int k = (31 - M) >> 3, o = 32 * ((31 - M) & 7);
      if constexpr (H == 0) al[M] = a_ds2x32<o, o + 1>(gb[k]); else ah[M] = a_ds2x32<o + 2, o + 3>(gb[k]);
    };
    sfor<2>([&](auto M) { sfor<8>([&](auto T) { rdB(M, T); }); rdA(M, ic<0>{}); rdA(M, ic<1>{}); });
    sfor<32>([&](auto M) {
      constexpr int m = M, t0 = m >> 2, n = 8 - t0;
      wait_lgkm<(m <= 2 ? 10 : (m == 3 ? 4 : (m == 4 ? 6 : (2 * (31 - m) < 8 ? 2 * (31 - m) : 8))))>();
      sfor<n>([&](auto I) {
        constexpr int i = I, t = t0 + i;
        mfma16_h<m == 0>(y[t], cat4(al[m], ah[m]), bq[4 * t - m + 3]);
        if constexpr (m < 2) {
          rdB(ic<m + 2>{}, I);
          if constexpr (i >= 6) rdA(ic<m + 2>{}, ic<i - 6>{});
        } else if constexpr (m <= 4) {
          if constexpr (i < 4) rdA(ic<2 * m + (i >> 1)>{}, ic<i & 1>{});
        } else if constexpr (m + 5 < 32) {
          if constexpr (i < 2) rdA(ic<m + 5>{}, I);
        }
      });
    });
    asm volatile("s_nop 7\n\ts_nop 1" : "+v"(y[0]), "+v"(y[1]), "+v"(y[2]), "+v"(y[3]), "+v"(y[4]), "+v"(y[5]), "+v"(y[6]), "+v"(y[7]));
    STAMP(6);
    bar();
    auto send = [&](auto PAR) {
      FENCE();
#pragma unroll
      for (int i = 0; i < 4; ++i) lds_st<f32x4>(smem, S_EX + w * 4096 + i * 1024 + lane * 16, y[4 * (1 - PAR) + i]);
      FENCE();
    };
    if (par) send(ic<1>{}); else send(ic<0>{});
    bar();
    const float dsk = bf1(q_dsk);
    u32x2 ao[4];
    auto finish = [&](auto PAR) {
      FENCE();
#pragma unroll
      for (int i = 0; i < 4; ++i) {
        const f32x4 o = lds_ld<f32x4>(smem, S_EX + (w ^ 1) * 4096 + i * 1024 + lane * 16);
        const u32x2 uu = lds_ld<u32x2>(smem, S_U + kch * U_CH + (240 + 256 * (4 * PAR + i) + 16 * c15 + 4 * g) * 2);
        const f16x2 u01 = ash2(uu.x), u23 = ash2(uu.y);
        const float uf[4] = {(float)u01.x, (float)u01.y, (float)u23.x, (float)u23.y};
        float v[4];
#pragma unroll
        for (int e = 0; e < 4; ++e) v[e] = ffma1(uf[e], dsk, fmul1(fadd1(y[4 * PAR + i][e], o[e]), inv_sk));
        ao[i] = u32x2{cvt_bf2(v[0], v[1]), cvt_bf2(v[2], v[3])};
      }
      FENCE();
    };
    if (par) finish(ic<1>{}); else finish(ic<0>{});
#pragma unroll
    for (int i = 0; i < 4; ++i) {
      u32x2 o = ao[i];
      o.x = o.x == POISON ? 0xffc0ffc0u : o.x;
      o.y = o.y == POISON ? 0xffc0ffc0u : o.y;
      st64c(rWS, o, WS_A + ((4 * (4 * par + i) + (c15 >> 2)) * 512 + kd) * 128 + ((c15 & 3) * 16 + 4 * g) * 2);
    }
    STAMP(7);
    u32x4 xt0 = ld128(sX, xto, 0), xt1 = ld128(sX, xto + 32 * 1024, 0);
    u32 b2w = ld16(mk_srd(p.b2, DM * 2), t8 < 64 ? (64 * nt2 + t8) * 2 : OOB, 0);
    bar();
    sfor<16>([&](auto N) { st2_piece(ic<28 + N>{}); });
    const u32 ato = WS_A + (mt2 * 512 + nt2 * 64) * 128 + t8 * 16;
    u32x4 at0 = ld128c(sWS, ato, 0), at1 = ld128c(sWS, ato + 4096, 0);
    asm volatile("s_waitcnt vmcnt(10)" : "+v"(xt0), "+v"(xt1), "+v"(b2w)::"memory");
    bar();
    const int row0 = t8 >> 3, sl = t8 & 7;
    {
      const u32 o = S_XT + row0 * 128 + ((sl ^ (row0 & 7)) * 16);
      lds_st<u32x4>(smem, o, xt0);
      lds_st<u32x4>(smem, o + 32 * 128, xt1);
      lds_st<float>(smem, t8 < 64 ? S_B2 + t8 * 4 : S_MISC, bf1(b2w));
    }
    wait_vm<2>();
    bar();
    asm volatile("s_waitcnt vmcnt(0)" : "+v"(at0), "+v"(at1)::"memory");
#pragma unroll 1
    for (int it = 0; it < (1 << 16); ++it) {
      const u32 m0 = max(max(at0.x, at0.y), max(at0.z, at0.w)), m1 = max(max(at1.x, at1.y), max(at1.z, at1.w));
      if (!__any(max(m0, m1) == POISON)) break;
      at0 = ld128c(sWS, ato, 0); at1 = ld128c(sWS, ato + 4096, 0);
      asm volatile("s_waitcnt vmcnt(0)" : "+v"(at0), "+v"(at1)::"memory");
    }
    {
      const u32 o = S_AT + row0 * 128 + (((sl >> 1) ^ (row0 & 2)) * 32) + (sl & 1) * 16;
      lds_st<u32x4>(smem, o, at0);
      lds_st<u32x4>(smem, o + 32 * 128, at1);
    }
    bar();
  } else {
    u32x4 hf[22][2];
    const u32 hb = WS_H + ((mt2 * 2 + mh) * NUNIT + kh * 22) * 2048;
    const u32 hvo = lane * 16, hvz = kh ? OOB : hvo;
    auto ldh = [&](auto J, auto S) { hf[J][S] = ld128c(sWS, J == 21 ? hvz : hvo, hb + (J * 2 + S) * 1024); };
    sfor<12>([&](auto N) { st2_piece(ic<16 + N>{}); });
    {
      const u32 hfo = WS_HF + (((mt2 >> 1) * 4 + 2 * (mt2 & 1) + mh) * 32 + c31) * 4;
      u32 hfl = ld32c(sWS, hfo, 0);
      asm volatile("s_waitcnt vmcnt(0)" : "+v"(hfl)::"memory");
      spin_flag(sWS, hfo, epoch, hfl, kh ? c31 >= 16 : c31 <= 17);
    }
    sfor<4>([&](auto J) { ldh(J, ic<0>{}); ldh(J, ic<1>{}); });
    STAMP(6);
    bar();
    sfor<3>([&](auto J) { ldh(ic<4 + J>{}, ic<0>{}); ldh(ic<4 + J>{}, ic<1>{}); });
    bar();
    sfor<G2_LEAD - 7>([&](auto J) { ldh(ic<7 + J>{}, ic<0>{}); ldh(ic<7 + J>{}, ic<1>{}); });
    STAMP(7);
    u32 wa[2];
#pragma unroll
    for (int n = 0; n < 2; ++n) wa[n] = lb + kh * W2_HALF + (4 * hh + q4) * 128 + (((2 * n + b16) ^ (q4 & 2)) * 32) + 8 * p4;
    u32x2 wl[2][4], wh[2][4];
    auto rd = [&](auto J, auto I, auto H) {
      constexpr int j = J, i = I, off = (j % W2_NS) * 4096 + ((i >> 1) * 16 + H * 8) * 128;
      if constexpr (H == 0) wl[j & 1][i] = a_dstr<off>(wa[i & 1]); else wh[j & 1][i] = a_dstr<off>(wa[i & 1]);
    };
    sfor<4>([&](auto I) { rd(ic<0>{}, I, ic<0>{}); rd(ic<0>{}, I, ic<1>{}); });
    sfor<22>([&](auto J) {
      constexpr int j = J;
      constexpr bool last = j == 21, brk = j == 8 || j == 13 || j == 17;
      if constexpr (brk) bar();
      wait_vm<g2_wait_h(j)>();
      tie(hf[j][0]); tie(hf[j][1]);
      sfor<4>([&](auto I) {
        constexpr int i = I;
        if constexpr (!brk) wait_lgkm<(last ? 6 - 2 * i : 6)>();
        mfma32_vv<j == 0 && i < 2>(acc[i & 1], cat4(wl[j & 1][i], wh[j & 1][i]), hf[j][i >> 1]);
        if constexpr ((i & 1) == 0 && j + G2_LEAD <= 21) ldh(ic<j + G2_LEAD>{}, ic<(i >> 1)>{});
        if constexpr (!last) { rd(ic<j + 1>{}, I, ic<0>{}); rd(ic<j + 1>{}, I, ic<1>{}); }
      });
    });
    asm volatile("s_nop 7\n\ts_nop 3" : "+v"(acc[0]), "+v"(acc[1]));
    STAMP(8);
    auto send2 = [&](auto KH) {
      FENCE();
#pragma unroll
      for (int q = 0; q < 4; ++q) {
        const f32x16& as = acc[1 - KH];
        lds_st<f32x4>(smem, S_EX + w * 4096 + q * 1024 + lane * 16, f32x4{as[4 * q], as[4 * q + 1], as[4 * q + 2], as[4 * q + 3]});
      }
      FENCE();
    };
    if (kh) send2(ic<1>{}); else send2(ic<0>{});
    bar();
    const int tl = 32 * mh + c31;
    auto fin2 = [&](auto KH) {
      FENCE();
#pragma unroll
      for (int q = 0; q < 4; ++q) {
        const f32x16& ak = acc[KH];
        const f32x4 v = f32x4{ak[4 * q], ak[4 * q + 1], ak[4 * q + 2], ak[4 * q + 3]} + lds_ld<f32x4>(smem, S_EX + (w ^ 2) * 4096 + q * 1024 + lane * 16);
        const f32x4 bb = lds_ld<f32x4>(smem, S_B2 + (32 * KH + 8 * q + 4 * hh) * 4);
        const int row = 32 * KH + 8 * q + 4 * hh + q4;
        const u32x2 av = lds_tr(smem, S_AT + row * 128 + (((2 * mh + b16) ^ (row & 2)) * 32) + 8 * p4);
        const u32 po = tl * 128 + (((4 * KH + q) ^ (tl & 7)) * 16) + hh * 8;
        const u32x2 xv = lds_ld<u32x2>(smem, S_XT + po);
        const f32x2 o0 = (bf2(xv.x) + bf2(av.x)) + (f32x2{v[0], v[1]} + f32x2{bb.x, bb.y}), o1 = (bf2(xv.y) + bf2(av.y)) + (f32x2{v[2], v[3]} + f32x2{bb.z, bb.w});
        lds_st<u32x2>(smem, S_OT + po, u32x2{pk_bf16(o0), pk_bf16(o1)});
      }
      FENCE();
    };
    if (kh) fin2(ic<1>{}); else fin2(ic<0>{});
  }
  bar();
  {
    const int row = tid >> 3, sl = tid & 7;
    const u32x4 o = lds_ld<u32x4>(smem, S_OT + row * 128 + ((sl ^ (row & 7)) * 16));
    __builtin_amdgcn_raw_buffer_store_b128(o, rOUT, (64 * mt2 + row) * 1024 + nt2 * 128 + sl * 16, 0, 0);
  }
  STAMP(9);
}

extern "C" void kernel_launch(void* const* d_in, const int* in_sizes, int n_in, void* d_out, int out_size, void* d_ws,
                              size_t ws_size, hipStream_t stream) {
  Params p;
  p.x = (const u16*)d_in[0]; p.lar = (const u16*)d_in[1]; p.aim = (const u16*)d_in[2]; p.cre = (const u16*)d_in[3];
  p.cim = (const u16*)d_in[4]; p.ldt = (const u16*)d_in[5]; p.dsk = (const u16*)d_in[6];
  p.w1 = (const u16*)d_in[8]; p.b1 = (const u16*)d_in[9]; p.w2 = (const u16*)d_in[10]; p.b2 = (const u16*)d_in[11];
  p.out = (u16*)d_out; p.ws = (char*)d_ws;
  s4d_mixer_fused<<<256, 512, 0, stream>>>(p);
}
```

```cpp
#include <hip/hip_runtime.h>
#include <stdint.h>
#include <utility>

namespace {
typedef unsigned short u16;
typedef unsigned u32;
typedef unsigned long long u64;
typedef float f32x2 __attribute__((ext_vector_type(2)));
typedef float f32x4 __attribute__((ext_vector_type(4)));
typedef float f32x16 __attribute__((ext_vector_type(16)));
typedef u32 u32x2 __attribute__((ext_vector_type(2)));
typedef u32 u32x4 __attribute__((ext_vector_type(4)));
typedef short s16x4 __attribute__((ext_vector_type(4)));
typedef __bf16 bf16x2 __attribute__((ext_vector_type(2)));
typedef _Float16 f16x2 __attribute__((ext_vector_type(2)));
typedef _Float16 f16x8 __attribute__((ext_vector_type(8)));
#define LAS __attribute__((address_space(3)))
#define DEV __device__ __forceinline__
typedef __amdgpu_buffer_rsrc_t rsrc_t;

constexpr int SEQ = 2048, DM = 512, NMODE = 128, FF = 1376, NUNIT = 43;
constexpr u32 WS_E = 0, WS_LNF = 0x1000, WS_HF = 0x2000  , WS_STATS = 0x8000, WS_XN = 0x10000, WS_A = 0x210000, WS_H = 0x410000,
              WS_END = WS_H + 64 * NUNIT * 2048, WS_STAMP = 0xA00000;
constexpr u32 SLOT = 28672, SLOT_W = 16384;
constexpr u32 W2_NS = 14, W2_HALF = W2_NS * 4096;
constexpr u32 LDS_BYTES_ = 163840;
constexpr u32 S0 = 114688;
constexpr u32 S_DUMMY = LDS_BYTES_ - 256;
constexpr u32 S_LN = S0, LN_ROW = 1088;
constexpr u32 TAB_ROW = 544, TAB_CH = 57 * TAB_ROW;
constexpr u32 S_TAB = LDS_BYTES_ - 2 * TAB_CH - 1872;
constexpr u32 S_TB = 32768;
constexpr u32 S_KC = S_TAB - 2592, KC_CH = 1296;
constexpr u32 S_CSP = LDS_BYTES_ - 1872;
constexpr u32 S_B1 = S_CSP + 768;
constexpr u32 S_MISC = S_B1 + 384;
constexpr u32 S_CS = S_MISC + 64;
constexpr u32 S_G = S0, G_PAR = 4160, G_CH = 8320;
constexpr u32 S_U = S0 + 16640, U_CH = 4608;
constexpr u32 S_EX = S0;
constexpr u32 S_AT = S0 + 16384, S_XT = S0 + 24576, S_OT = S0 + 32768, S_B2 = S0 + 40960;
constexpr u32 LDS_BYTES = 163840;
constexpr u32 OOB = 0x80000000u;
constexpr u32 POISON = 0xffffffffu;

#define STAMP(k) do {} while (0)
#define CSTAMP(i) do {} while (0)
#define CDUMP(n, base) do {} while (0)

struct Params {
  const u16 *x, *lar, *aim, *cre, *cim, *ldt, *dsk, *w1, *b1, *w2, *b2;
  u16* out;
  char* ws;
};

DEV float asf(u32 v) { return __builtin_bit_cast(float, v); }
DEV u32 asu(float v) { return __builtin_bit_cast(u32, v); }
DEV f16x2 ash2(u32 v) { return __builtin_bit_cast(f16x2, v); }
DEV float bflo(u32 v) { return asf(v << 16); }
DEV float bfhi(u32 v) { return asf(v & 0xffff0000u); }
DEV f32x2 bf2(u32 v) { return f32x2{bflo(v), bfhi(v)}; }
DEV u32 pk_bf16(f32x2 v) { return __builtin_bit_cast(u32, __builtin_convertvector(v, bf16x2)); }
DEV u32 pk_f16(f32x2 v) { return __builtin_bit_cast(u32, __builtin_convertvector(v, f16x2)); }
DEV float rbf(float a) { return bflo(pk_bf16(f32x2{a, a})); }
DEV float bf1(u32 v) { return asf(v << 16); }
DEV f32x2 sp2(float a) { return f32x2{a, a}; }
DEV f32x2 fma2(f32x2 a, f32x2 b, f32x2 c) { return __builtin_elementwise_fma(a, b, c); }

template <int N> DEV void wait_vm() { asm volatile("s_waitcnt vmcnt(%0)" ::"n"(N) : "memory"); }
template <int N> DEV void wait_lgkm() { asm volatile("s_waitcnt lgkmcnt(%0)" ::"n"(N)); }
DEV void bar() { asm volatile("s_waitcnt lgkmcnt(0)\n\ts_barrier" ::: "memory"); }
DEV u32x4 mk_srd(const void* base, u32 bytes) {
  const u64 b = (u64)base;
  u32x4 s;
  s.x = __builtin_amdgcn_readfirstlane((u32)b);
  s.y = __builtin_amdgcn_readfirstlane((u32)(b >> 32)) & 0xffffu;
  s.z = bytes;
  s.w = 0x00020000u;
  return s;
}
DEV rsrc_t mk_rsrc(const void* base, u32 bytes) { return __builtin_amdgcn_make_buffer_rsrc((void*)base, 0, bytes, 0x00020000); }

#define BLOAD(INS, TY, NAME, FLAGS, CON)                                                                     \
  DEV TY NAME(u32x4 srd, u32 voff, u32 soff) {                                                               \
    TY r;                                                                                                    \
    asm volatile(INS " %0, %1, %2, %3 offen" FLAGS : CON(r) : "v"(voff), "s"(srd), "s"(soff) : "memory");     \
    return r;                                                                                                \
  }
BLOAD("buffer_load_dwordx4", u32x4, ld128, "", "=v")
BLOAD("buffer_load_dwordx4", u32x4, ld128c, " sc1", "=v")
BLOAD("buffer_load_dwordx4", u32x4, ld128c_a, " sc1", "=a")
BLOAD("buffer_load_dwordx2", u32x2, ld64, "", "=v")
BLOAD("buffer_load_dword", u32, ld32c, " sc1", "=v")
BLOAD("buffer_load_ushort", u32, ld16, "", "=v")
DEV void touch(const void* a, u32 lds_dummy) { asm volatile("s_mov_b32 m0, %1\n\ts_nop 0\n\tglobal_load_lds_dword %0, off" ::"v"(a), "s"(lds_dummy) : "memory"); }
template <class T> DEV void tie(T& v) { asm volatile("" : "+v"(v)); }

DEV void dma_m0(u32 lds_addr) { asm volatile("s_mov_b32 m0, %0" ::"s"(lds_addr) : "memory"); }
template <int IOFF> DEV void dma_go(u32x4 srd, u32 voff, u32 soff) {
  asm volatile("buffer_load_dwordx4 %0, %1, %2 offen offset:%3 lds" ::"v"(voff), "s"(srd), "s"(soff), "n"(IOFF) : "memory");
}
DEV void st128c(rsrc_t r, u32x4 v, u32 voff) { __builtin_amdgcn_raw_buffer_store_b128(v, r, voff, 0, 16); }
DEV void st64c(rsrc_t r, u32x2 v, u32 voff) { __builtin_amdgcn_raw_buffer_store_b64(v, r, voff, 0, 16); }
DEV void st32c(rsrc_t r, u32 v, u32 voff) { __builtin_amdgcn_raw_buffer_store_b32(v, r, voff, 0, 16); }
DEV void st64(rsrc_t r, u32x2 v, u32 voff) { __builtin_amdgcn_raw_buffer_store_b64(v, r, voff, 0, 0); }

template <class T> DEV T lds_ld(const char* smem, u32 off) { return *(const LAS T*)(smem + off); }
template <class T> DEV void lds_st(char* smem, u32 off, T v) { *(LAS T*)(smem + off) = v; }
DEV u32x2 lds_tr(const char* smem, u32 off) { return __builtin_bit_cast(u32x2, __builtin_amdgcn_ds_read_tr16_b64_v4i16((LAS s16x4*)(smem + off))); }

template <int I> using ic = std::integral_constant<int, I>;
template <class F, int... Is> DEV void sfor_impl(F&& f, std::integer_sequence<int, Is...>) { (f(ic<Is>{}), ...); }
template <int N, class F> DEV void sfor(F&& f) { sfor_impl(f, std::make_integer_sequence<int, N>{}); }
template <int OFF> DEV u32x4 a_ds128(u32 addr) { u32x4 r; asm volatile("ds_read_b128 %0, %1 offset:%2" : "=v"(r) : "v"(addr), "n"(OFF)); return r; }
template <int OFF> DEV u32x2 a_dstr(u32 addr) { u32x2 r; asm volatile("ds_read_b64_tr_b16 %0, %1 offset:%2" : "=v"(r) : "v"(addr), "n"(OFF)); return r; }
template <int O0, int O1> DEV u32x2 a_ds2x32(u32 addr) { u32x2 r; asm volatile("ds_read2_b32 %0, %1 offset0:%2 offset1:%3" : "=v"(r) : "v"(addr), "n"(O0), "n"(O1)); return r; }
DEV u32x4 cat4(u32x2 a, u32x2 b) { return __builtin_shufflevector(a, b, 0, 1, 2, 3); }
template <bool INIT> DEV void mfma32_vv(f32x16& c, u32x4 a, u32x4 b) {
  if constexpr (INIT) asm volatile("v_mfma_f32_32x32x16_bf16 %0, %1, %2, 0" : "=&v"(c) : "v"(a), "v"(b));
  else asm volatile("v_mfma_f32_32x32x16_bf16 %0, %1, %2, %0" : "+v"(c) : "v"(a), "v"(b));
}
template <bool INIT> DEV void mfma32_va(f32x16& c, u32x4 a, u32x4 b) {
  if constexpr (INIT) asm volatile("v_mfma_f32_32x32x16_bf16 %0, %1, %2, 0" : "=&v"(c) : "v"(a), "a"(b));
  else asm volatile("v_mfma_f32_32x32x16_bf16 %0, %1, %2, %0" : "+v"(c) : "v"(a), "a"(b));
}
template <bool INIT> DEV void mfma16_h(f32x4& c, u32x4 a, u32x4 b) {
  if constexpr (INIT) asm volatile("v_mfma_f32_16x16x32_f16 %0, %1, %2, 0" : "=&v"(c) : "v"(a), "v"(b));
  else asm volatile("v_mfma_f32_16x16x32_f16 %0, %1, %2, %0" : "+v"(c) : "v"(a), "v"(b));
}
DEV f32x4 mfma_h(u32x4 a, u32x4 b, f32x4 c) {
  return __builtin_amdgcn_mfma_f32_16x16x32_f16(__builtin_bit_cast(f16x8, a), __builtin_bit_cast(f16x8, b), c, 0, 0, 0);
}
template <bool INIT> DEV void mfma16_bf(f32x4& c, u32x4 a, u32x4 b) {
  if constexpr (INIT) asm volatile("v_mfma_f32_16x16x32_bf16 %0, %1, %2, 0" : "=&v"(c) : "v"(a), "v"(b));
  else asm volatile("v_mfma_f32_16x16x32_bf16 %0, %1, %2, %0" : "+v"(c) : "v"(a), "v"(b));
}
#define FENCE() __builtin_amdgcn_sched_barrier(0)

DEV void wave_sum4(float& a, float& b, float& c, float& d) {
#define S4(CTRL) "v_add_f32_dpp %0, %0, %0 " CTRL "\n\tv_add_f32_dpp %1, %1, %1 " CTRL "\n\tv_add_f32_dpp %2, %2, %2 " CTRL "\n\tv_add_f32_dpp %3, %3, %3 " CTRL "\n\t"
  asm volatile("s_nop 1\n\t" S4("row_shr:1 row_mask:0xf bank_mask:0xf bound_ctrl:1") S4("row_shr:2 row_mask:0xf bank_mask:0xf bound_ctrl:1")
               S4("row_shr:4 row_mask:0xf bank_mask:0xf bound_ctrl:1") S4("row_shr:8 row_mask:0xf bank_mask:0xf bound_ctrl:1")
               S4("row_bcast:15 row_mask:0xa bank_mask:0xf") S4("row_bcast:31 row_mask:0xc bank_mask:0xf") "s_nop 0"
               : "+v"(a), "+v"(b), "+v"(c), "+v"(d));
#undef S4
  a = asf(__builtin_amdgcn_readlane(asu(a), 63)); b = asf(__builtin_amdgcn_readlane(asu(b), 63));
  c = asf(__builtin_amdgcn_readlane(asu(c), 63)); d = asf(__builtin_amdgcn_readlane(asu(d), 63));
}
DEV void wave_sum2(float& a, float& b) {
#define S2(CTRL) "v_add_f32_dpp %0, %0, %0 " CTRL "\n\tv_add_f32_dpp %1, %1, %1 " CTRL "\n\ts_nop 0\n\t"
  asm volatile("s_nop 1\n\t" S2("row_shr:1 row_mask:0xf bank_mask:0xf bound_ctrl:1") S2("row_shr:2 row_mask:0xf bank_mask:0xf bound_ctrl:1")
               S2("row_shr:4 row_mask:0xf bank_mask:0xf bound_ctrl:1") S2("row_shr:8 row_mask:0xf bank_mask:0xf bound_ctrl:1")
               S2("row_bcast:15 row_mask:0xa bank_mask:0xf") S2("row_bcast:31 row_mask:0xc bank_mask:0xf")
               : "+v"(a), "+v"(b));
#undef S2
  a = asf(__builtin_amdgcn_readlane(asu(a), 63)); b = asf(__builtin_amdgcn_readlane(asu(b), 63));
}
template <int CTRL, int RM> DEV float dpp_add(float v) { return v + asf(__builtin_amdgcn_update_dpp(0, asu(v), CTRL, RM, 0xf, true)); }
DEV float wave_sum(float v) {
  v = dpp_add<0x111, 0xf>(v); v = dpp_add<0x112, 0xf>(v); v = dpp_add<0x114, 0xf>(v); v = dpp_add<0x118, 0xf>(v);
  v = dpp_add<0x142, 0xa>(v); v = dpp_add<0x143, 0xc>(v);
  return asf(__builtin_amdgcn_readlane(asu(v), 63));
}
DEV void cmul2(f32x2& r0, f32x2 a0, f32x2 b0, f32x2& r1, f32x2 a1, f32x2 b1) {
  f32x2 t0, t1;
  asm("v_pk_mul_f32 %0, %4, %5 op_sel_hi:[1,0]\n\tv_pk_mul_f32 %1, %6, %7 op_sel_hi:[1,0]\n\t"
      "v_pk_fma_f32 %2, %4, %5, %0 op_sel:[1,1,0] op_sel_hi:[0,1,1] neg_lo:[1,0,0]\n\t"
      "v_pk_fma_f32 %3, %6, %7, %1 op_sel:[1,1,0] op_sel_hi:[0,1,1] neg_lo:[1,0,0]"
      : "=&v"(t0), "=&v"(t1), "=&v"(r0), "=&v"(r1) : "v"(a0), "v"(b0), "v"(a1), "v"(b1));
}
DEV f32x2 cmul(f32x2 a, f32x2 b) {
  f32x2 t, r;
  asm("v_pk_mul_f32 %0, %2, %3 op_sel_hi:[1,0]\n\tv_pk_fma_f32 %1, %2, %3, %0 op_sel:[1,1,0] op_sel_hi:[0,1,1] neg_lo:[1,0,0]"
      : "=&v"(t), "=v"(r) : "v"(a), "v"(b));
  return r;
}
DEV u32 cvt_bf2(float a, float b) { u32 r; asm("v_cvt_pk_bf16_f32 %0, %1, %2" : "=v"(r) : "v"(a), "v"(b)); return r; }
DEV u32 cvt_h2(float a, float b) { u32 r; asm("v_cvt_pk_f16_f32 %0, %1, %2" : "=v"(r) : "v"(a), "v"(b)); return r; }
DEV float fadd1(float a, float b) { float r; asm("v_add_f32 %0, %1, %2" : "=v"(r) : "v"(a), "v"(b)); return r; }
DEV float fsub1(float a, float b) { float r; asm("v_sub_f32 %0, %1, %2" : "=v"(r) : "v"(a), "v"(b)); return r; }
DEV float fmul1(float a, float b) { float r; asm("v_mul_f32 %0, %1, %2" : "=v"(r) : "v"(a), "v"(b)); return r; }
DEV float ffma1(float a, float b, float c) { float r; asm("v_fma_f32 %0, %1, %2, %3" : "=v"(r) : "v"(a), "v"(b), "v"(c)); return r; }
DEV float rbf1(float a) { return bflo(cvt_bf2(a, a)); }
DEV u32 pk_f16_conj(f32x2 v) { u32 r; asm("v_cvt_pk_f16_f32 %0, %1, -%2" : "=v"(r) : "v"(v.x), "v"(v.y)); return r; }
DEV f32x2 gelu2(f32x2 v) {
  const f32x2 s = v * fma2(v * v, sp2(-0.10294323970f), sp2(-2.3022081986f));
  const f32x2 d = f32x2{__builtin_amdgcn_exp2f(s.x), __builtin_amdgcn_exp2f(s.y)} + sp2(1.0f);
  return v * f32x2{__builtin_amdgcn_rcpf(d.x), __builtin_amdgcn_rcpf(d.y)};
}
DEV void spin_flag(u32x4 srd, u32 voff, u32 epoch, u32 val, bool need) {
#pragma unroll 1
  for (int it = 0; it < (1 << 16); ++it) {
    if (__all(!need || val == epoch)) break;
    __builtin_amdgcn_s_sleep(1);
    val = ld32c(srd, voff, 0);
    asm volatile("s_waitcnt vmcnt(0)" : "+v"(val)::"memory");
  }
}

constexpr int G2_LEAD = 10;
constexpr int g2_wait_h(int j) { return 2 * ((j + G2_LEAD - 1 < 21 ? j + G2_LEAD - 1 : 21) - j); }

}

__global__ __launch_bounds__(512, 2) void s4d_mixer_fused(Params p) {
  __shared__ __attribute__((aligned(16))) char smem[LDS_BYTES];
  const int tid = threadIdx.x, lane = tid & 63, t8 = tid & 255;
  const int wid = __builtin_amdgcn_readfirstlane(tid >> 6);
  const int side = wid >> 2, w = wid & 3;
  const int b = blockIdx.x, g8 = b & 7, r = b >> 3;
  const int mt = 4 * (g8 >> 1) + (r >> 3), nt = 8 * (g8 & 1) + (r & 7);
  const int ustart = (nt * NUNIT) >> 4, unext = ((nt + 1) * NUNIT) >> 4;
  const int tg = 16 * mt + nt;
  const int cp = 32 * g8 + r;
  const int mt2 = 4 * g8 + (r >> 3), nt2 = r & 7;
  const int c15 = lane & 15, g = lane >> 4;
  const int c31 = lane & 31, hh = lane >> 5, b16 = (lane >> 4) & 1;
  const int q4 = (lane & 15) >> 2, p4 = lane & 3;
  const int kch = w >> 1, kd = 2 * cp + kch, kh = w >> 1, mh = w & 1;

  const u32x4 sX = mk_srd(p.x, SEQ * DM * 2), sWS = mk_srd(p.ws, WS_END);
  const rsrc_t rWS = mk_rsrc(p.ws, WS_END), rOUT = mk_rsrc(p.out, SEQ * DM * 2);
  const u32x4 sW1 = mk_srd(p.w1, DM * FF * 2), sW2 = mk_srd(p.w2, FF * DM * 2);
  const u32 lb = (u32)(uintptr_t)(LAS char*)smem;
  STAMP(0);

  u32x4 xr = ld128(sX, lane * 16, (8 * tg + 2 * w + side) * 1024);
  u32 vx[4], vw[3];
#pragma unroll
  for (int j = 0; j < 4; ++j) vx[j] = (8 * j + (lane >> 3)) * 1024 - j * 1024 + (((lane & 7) ^ ((4 * j + (lane >> 4)) & 7)) * 16);
#pragma unroll
  for (int j = 0; j < 3; ++j) {
    const int P = (3 * w + j) * 64 + lane, k = P / 12, sl = P - 12 * k;
    vw[j] = k * (FF * 2) + sl * 16 - j * 1024;
  }
  const u32 x_so = (mt * 128 + 32 * w) * 1024, x_lo = lb + 32 * w * 128, w_so = ustart * 64, w_lo = lb + SLOT_W + 3 * w * 1024;
  auto st1_m0 = [&](auto KC, auto J) {
    if constexpr (J == 0) dma_m0(x_lo + (KC & 3) * SLOT);
    if constexpr (J == 4) dma_m0(w_lo + (KC & 3) * SLOT);
  };
  auto st1_go = [&](auto KC, auto J) {
    if constexpr (J < 4) dma_go<J * 1024>(sX, vx[J], x_so + KC * 128); else dma_go<(J - 4) * 1024>(sW1, vw[J - 4], w_so + KC * 64 * (FF * 2));
  };
  auto stage = [&](auto KC) { sfor<7>([&](auto J) { if constexpr (J == 0 || J == 4) { st1_m0(KC, J); asm volatile("s_nop 0"); } st1_go(KC, J); }); };
  u32 eword = ld32c(sWS, WS_E, 0);
  if (side == 0) { stage(ic<0>{}); stage(ic<1>{}); stage(ic<2>{}); stage(ic<3>{}); }
  {
    u64 pc;
    asm volatile("s_getpc_b64 %0" : "=s"(pc));
    const char* a;
    if (tid < 96) a = (const char*)p.x + (512 * (g8 >> 1) + 16 * r + tid / 6) * 1024 + (2 + tid % 6) * 128;
    else if (tid < 192) a = (const char*)p.w1 + (256 + 8 * r + (tid - 96) / 12) * (FF * 2) + (g8 & 1) * 1280 + ((tid - 96) % 12) * 120;
    else a = (const char*)pc + ((r * 64 + tid) % 208) * 128;
    touch(a, lb + S_DUMMY);
  }
  u32 ixp[5];
#pragma unroll
  for (int e = 0; e < 9; ++e) {
    const int l = 2047 - (8 * t8 + e);
    const u32 rb = pk_bf16(sp2((float)l)) & 0xffffu;
    const u32 ix = (l < 0 ? 641u : (l < 128 ? (u32)l : rb - 17024u)) * 2;
    if (e & 1) ixp[e >> 1] |= ix << 16; else ixp[e >> 1] = ix;
  }
#pragma unroll
  for (int e = 0; e < 5; ++e) tie(ixp[e]);
  if (side == 0) wait_vm<30>(); else wait_vm<2>();
  tie(xr);
  STAMP(1);

  u32 epoch;
  {
    f32x2 x[4];
#pragma unroll
    for (int i = 0; i < 4; ++i) x[i] = bf2(xr[i]);
    const float x0 = asf(__builtin_amdgcn_readfirstlane(asu(x[0].x)));
    f32x2 s = {0.f, 0.f}, q = {0.f, 0.f};
#pragma unroll
    for (int i = 0; i < 4; ++i) { const f32x2 d = x[i] - sp2(x0); s += d; q = fma2(d, d, q); }
    float sm = s.x + s.y, sq = q.x + q.y;
    wave_sum2(sm, sq);
    const float md = sm * (1.0f / DM);
    const float var = fmaxf(__builtin_fmaf(-md, md, sq * (1.0f / DM)), 0.f);
    const float m1 = rbf(x0 + md);
    const float r1 = __builtin_amdgcn_rcpf(rbf(__builtin_amdgcn_sqrtf(rbf(rbf(var) + 1e-5f))));
    s = f32x2{0.f, 0.f}; q = f32x2{0.f, 0.f};
    u32x4 hv;
#pragma unroll
    for (int i = 0; i < 4; ++i) {
      const f32x2 n = bf2(pk_bf16(bf2(pk_bf16(x[i] - sp2(m1))) * sp2(r1)));
      s += n; q = fma2(n, n, q);
      hv[i] = pk_f16(n);
    }
    lds_st<u32x4>(smem, S_LN + (2 * w + side) * LN_ROW + lane * 16, hv);
    float s2 = s.x + s.y, q2 = q.x + q.y;
    wave_sum2(s2, q2);
    const float mean2 = s2 * (1.0f / DM);
    const float var2 = fmaxf(__builtin_fmaf(-mean2, mean2, q2 * (1.0f / DM)), 0.f);
    const float r2 = __builtin_amdgcn_rcpf(rbf(__builtin_amdgcn_sqrtf(rbf(rbf(var2) + 1e-5f))));
    if (side == 0) wait_vm<29>(); else wait_vm<1>();
    tie(eword);
    epoch = __builtin_amdgcn_readfirstlane(eword) * 0x9E3779B1u + 0x7F4A7C15u;
    st128c(rWS, u32x4{asu(r1 * r2), epoch, asu(__builtin_fmaf(m1, r1, rbf(mean2)) * r2), epoch}, lane == 0 ? WS_STATS + (8 * tg + 2 * w + side) * 16 : OOB);
  }
  {
    const int ln = wid * 8 + (lane >> 3);
    st128c(rWS, u32x4{POISON, POISON, POISON, POISON}, WS_A + ((ln >> 1) * 512 + 2 * cp + (ln & 1)) * 128 + (lane & 7) * 16);
  }
  u32 q_lar, q_aim, q_cre, q_cim, q_cre2, q_cim2, q_ldt, q_dsk;
  u32x2 b1v;
  auto load_small = [&]() {
    const u32 vo = (64 * (w & 1) + lane) * 2, so = kd * NMODE * 2;
    const u32x4 sCr = mk_srd(p.cre, DM * NMODE * 2), sCi = mk_srd(p.cim, DM * NMODE * 2);
    q_lar = ld16(mk_srd(p.lar, DM * NMODE * 2), vo, so); q_aim = ld16(mk_srd(p.aim, DM * NMODE * 2), vo, so);
    q_cre = ld16(sCr, vo, so); q_cim = ld16(sCi, vo, so);
    q_cre2 = ld16(sCr, vo ^ 128, so); q_cim2 = ld16(sCi, vo ^ 128, so);
    q_ldt = ld16(mk_srd(p.ldt, DM * 2), 0, kd * 2); q_dsk = ld16(mk_srd(p.dsk, DM * 2), 0, kd * 2);
    b1v = ld64(mk_srd(p.b1, FF * 2), lane < 24 ? lane * 8 : OOB, ustart * 64);
  };
#define TIE_SMALL "+v"(q_lar), "+v"(q_aim), "+v"(q_cre), "+v"(q_cim), "+v"(q_cre2), "+v"(q_cim2), "+v"(q_ldt), "+v"(q_dsk), "+v"(b1v)
  f32x16 acc1[3];
  f32x4 csp[3];
  u32x4 stv;
  if (side == 0) {
    const u32 xa0 = lb + (32 * w + c31) * 128 + ((hh ^ ((c31 >> 1) & 7)) * 16);
    const u32 wa0 = lb + SLOT_W + (8 * hh + q4) * 192 + b16 * 32 + 8 * p4;
    u32 xa[2][4], wa[2];
#pragma unroll
    for (int s = 0; s < 4; ++s) { xa[0][s] = xa0 ^ (s * 32); xa[1][s] = xa[0][s] + 2 * SLOT; }
    wa[0] = wa0; wa[1] = wa0 + 2 * SLOT;
    u32x4 xf[2];
    u32x2 wl[2][3], wh[2][3];
    auto rd = [&](auto S, auto R) {
      constexpr int s = S, rr = R, buf = s & 1, kc = s >> 2, sl = kc & 3, so = (sl & 1) * SLOT, sp = s & 3;
      if constexpr (rr == 0) xf[buf] = a_ds128<so>(xa[sl >> 1][sp]);
      else {
        constexpr int u = (rr - 1) >> 1, hf = (rr - 1) & 1, off = so + u * 64 + (sp * 16 + hf * 4) * 192;
        if constexpr (hf == 0) wl[buf][u] = a_dstr<off>(wa[sl >> 1]); else wh[buf][u] = a_dstr<off>(wa[sl >> 1]);
      }
    };
    load_small();
    wait_vm<33>();
    bar();
    CSTAMP(0);
    sfor<7>([&](auto R) { rd(ic<0>{}, R); });
    sfor<32>([&](auto S) {
      constexpr int s = S, buf = s & 1, sp = s & 3, kc = s >> 2;
      constexpr bool last = s == 31, brk = sp == 2 && kc < 7;
      sfor<3>([&](auto U) {
        constexpr int u = U;
        constexpr int gi = sp == 2 ? (u == 2 ? 0 : -1) : (u == 0 ? -1 : (sp == 3 ? u : 2 * sp + 2 + u));
        constexpr int kcs = sp >= 2 ? kc + 3 : kc + 2;
        constexpr bool st = gi >= 0 && gi < 7 && kcs >= 4 && kcs < 8;
        if constexpr (u == 0) wait_lgkm<4>();
        else if constexpr (u == 1) {
          wait_lgkm<(last ? 0 : 3)>();
          if constexpr (brk) {
            constexpr int n = kc + 1;
            if constexpr (n == 1) wait_vm<26>();
            else if constexpr (n == 2 || n == 3) wait_vm<19>();
            else if constexpr (n == 4) asm volatile("s_waitcnt vmcnt(7)" : TIE_SMALL :: "memory");
            else if constexpr (n > 4 && n < 7) wait_vm<7>();
            else if constexpr (n == 7) wait_vm<0>();
            CSTAMP(2 * n - 1);
            asm volatile("s_barrier" ::: "memory");
            CSTAMP(2 * n);
            if constexpr (n == 7) stv = ld128c(sWS, WS_STATS + (128 * mt + 32 * w + c31) * 16, 0);
          }
        }
        if constexpr (st && (gi == 0 || gi == 4)) st1_m0(ic<kcs>{}, ic<gi>{});
        mfma32_vv<s == 0>(acc1[u], cat4(wl[buf][u], wh[buf][u]), xf[buf]);
        if constexpr (st) st1_go(ic<kcs>{}, ic<gi>{});
        if constexpr (!last) {
          if constexpr (u == 0) sfor<3>([&](auto R) { rd(ic<s + 1>{}, R); });
          if constexpr (u == 1) sfor<2>([&](auto R) { rd(ic<s + 1>{}, ic<3 + R>{}); });
          if constexpr (u == 2) sfor<2>([&](auto R) { rd(ic<s + 1>{}, ic<5 + R>{}); });
        }
      });
    });
    asm volatile("s_nop 7\n\ts_nop 3" : "+v"(acc1[0]), "+v"(acc1[1]), "+v"(acc1[2]));
    CSTAMP(15); CDUMP(16, 0);
    STAMP(2);
    bar();
#pragma unroll
    for (int q = 0; q < 6; ++q) {
      const f32x16& a = acc1[q < 2 ? 1 : 2];
      const int o = q < 2 ? 8 + 4 * q : 4 * (q - 2);
      lds_st<f32x4>(smem, S_TB + w * 6144 + q * 1024 + lane * 16, f32x4{a[o], a[o + 1], a[o + 2], a[o + 3]});
    }
    asm volatile("s_waitcnt vmcnt(0)" : "+v"(stv)::"memory");
  } else {
    const u32 wc0 = lb + SLOT_W + (32 * (w >> 1) + 8 * g + q4) * 192 + 3 * (w & 1) * 32 + 8 * p4;
    u32x4 ones = {0x3f803f80u, 0x3f803f80u, 0x3f803f80u, 0x3f803f80u};
    tie(ones);
    u32x2 cl[3], ch[3];
    auto cs_rd = [&](auto KC) {
      const u32 a = wc0 + (KC & 3) * SLOT;
      sfor<3>([&](auto I) { cl[I] = a_dstr<I * 32>(a); ch[I] = a_dstr<I * 32 + 4 * 192>(a); });
      FENCE();
    };
    auto cs_sum = [&](auto KC) {
      FENCE();
      wait_lgkm<0>();
      sfor<3>([&](auto I) { mfma16_bf<KC == 0>(csp[I], cat4(cl[I], ch[I]), ones); });
    };
    load_small();
    bar();
    CSTAMP(0);
    cs_rd(ic<0>{});
#pragma unroll
    for (int i = 0; i < 2; ++i) {
      const u32 a = S_LN + q4 * LN_ROW + ((2 * w + i) * 64 + 16 * g + 4 * p4) * 2;
      st128c(rWS, cat4(lds_tr(smem, a), lds_tr(smem, a + 4 * LN_ROW)), WS_XN + tg * 8192 + ((2 * w + i) * 64 + lane) * 16);
    }
    cs_sum(ic<0>{});
    CSTAMP(1);
    bar();
    CSTAMP(2);
    cs_rd(ic<1>{}); cs_sum(ic<1>{});
    CSTAMP(3);
    bar();
    CSTAMP(4);
    cs_rd(ic<2>{}); cs_sum(ic<2>{});
    CSTAMP(5);
    bar();
    CSTAMP(6);
    cs_rd(ic<3>{}); cs_sum(ic<3>{});
    CSTAMP(7);
    bar();
    CSTAMP(8);
    cs_rd(ic<4>{}); cs_sum(ic<4>{});
    CSTAMP(9);
    asm volatile("s_waitcnt vmcnt(0)" : TIE_SMALL :: "memory");
    lds_st<f32x4>(smem, lane < 24 ? S_B1 + lane * 16 : S_MISC, f32x4{bflo(b1v.x), bfhi(b1v.x), bflo(b1v.y), bfhi(b1v.y)});
    bar();
    CSTAMP(10);
    st32c(rWS, epoch, t8 == 0 ? WS_LNF + tg * 4 : OOB);
    cs_rd(ic<5>{}); cs_sum(ic<5>{});
    CSTAMP(11);
    bar();
    CSTAMP(12);
    cs_rd(ic<6>{}); cs_sum(ic<6>{});
    CSTAMP(13);
    bar();
    CSTAMP(14);
    stv = ld128c(sWS, WS_STATS + (128 * mt + 32 * w + c31) * 16, 0);
    cs_rd(ic<7>{}); cs_sum(ic<7>{});
    asm volatile("s_nop 7\n\ts_nop 1" : "+v"(csp[0]), "+v"(csp[1]), "+v"(csp[2]));
#pragma unroll
    for (int i = 0; i < 3; ++i) lds_st<f32x4>(smem, c15 == 0 ? S_CSP + ((w >> 1) * 96 + (3 * (w & 1) + i) * 16 + 4 * g) * 4 : S_MISC, csp[i]);
    CSTAMP(15); CDUMP(16, 20);
    STAMP(2);
    bar();
    const int f = t8 < 96 ? t8 : 0;
    lds_st<float>(smem, t8 < 96 ? S_CS + t8 * 4 : S_MISC, lds_ld<float>(smem, S_CSP + f * 4) + lds_ld<float>(smem, S_CSP + 384 + f * 4));
    asm volatile("s_waitcnt vmcnt(0)" : "+v"(stv)::"memory");
  }
#pragma unroll 1
  for (int it = 0; it < (1 << 16); ++it) {
    if (__all(stv.y == epoch && stv.w == epoch)) break;
    __builtin_amdgcn_s_sleep(1);
    stv = ld128c(sWS, WS_STATS + (128 * mt + 32 * w + c31) * 16, 0);
    asm volatile("s_waitcnt vmcnt(0)" : "+v"(stv)::"memory");
  }
  bar();
  CSTAMP(0);
  STAMP(3);

  {
    f32x4 av[6];
    if (side) {
#pragma unroll
      for (int q = 0; q < 6; ++q) av[q] = lds_ld<f32x4>(smem, S_TB + w * 6144 + q * 1024 + lane * 16);
    } else {
#pragma unroll
      for (int q = 0; q < 6; ++q) {
        const f32x16& a = acc1[q >> 2];
        const int o = 4 * (q & 3);
        av[q] = f32x4{a[o], a[o + 1], a[o + 2], a[o + 3]};
      }
    }
    const f32x2 al = sp2(asf(stv.x)), nb = sp2(-asf(stv.z));
    const u32 hso = WS_H + ((mt * 4 + w) * NUNIT + ustart) * 2048 + 3 * side * 1024 + lane * 16;
    const u32 cb = hh * 16 + 3 * side * 64;
#pragma unroll
    for (int i = 0; i < 3; ++i) {
      u32x4 o;
#pragma unroll
      for (int q = 0; q < 2; ++q) {
        const f32x4 bi = lds_ld<f32x4>(smem, S_B1 + cb + (16 * i + 8 * q) * 4), cs = lds_ld<f32x4>(smem, S_CS + cb + (16 * i + 8 * q) * 4);
        const f32x4 a = av[2 * i + q];
        const f32x2 p0 = fma2(al, f32x2{a.x, a.y}, fma2(nb, f32x2{cs.x, cs.y}, f32x2{bi.x, bi.y}));
        const f32x2 p1 = fma2(al, f32x2{a.z, a.w}, fma2(nb, f32x2{cs.z, cs.w}, f32x2{bi.z, bi.w}));
        o[2 * q] = pk_bf16(gelu2(p0));
        o[2 * q + 1] = pk_bf16(gelu2(p1));
      }
      st128c(rWS, o, ustart + ((3 * side + i) >> 1) < unext ? hso + i * 1024 : OOB);
    }
  }
  CSTAMP(1);
  u32 lnf = ld32c(sWS, WS_LNF + t8 * 4, 0);
  {
    const int ix = tid < 480 ? tid : tid - 480, ri = 15 * r + (ix >> 5);
    touch((const char*)p.w2 + (ri < 256 ? 448 + ri : 896 + ri) * 1024 + (ix & 31) * 32, lb + S_DUMMY);
  }
  st32c(rWS, epoch, (b == 0 && tid == 0) ? WS_E : OOB);
  STAMP(4);
  const u32 vw2 = (lane >> 3) * 1024 + ((((lane & 7) >> 1) ^ ((lane >> 3) & 2)) * 32) + (lane & 1) * 16;
  const u32 vw2p[2] = {vw2, vw2 + 7168};
  const u32 vw2z[2] = {kh ? OOB : vw2p[0], kh ? OOB : vw2p[1]};
  const u32 w2_lo = lb + kh * W2_HALF + 2 * mh * 1024, w2_so = (kh * 22 * 32 + 16 * mh) * 1024 + nt2 * 128;
  auto st2_piece = [&](auto N) {
    constexpr int j = N >> 1, i = N & 1;
    FENCE();
    if constexpr (i == 0) { dma_m0(w2_lo + (j % W2_NS) * 4096); asm volatile("s_nop 0"); }
    dma_go<i * 1024>(sW2, j == 21 ? vw2z[i] : vw2p[i], w2_so + j * 32 * 1024);
    FENCE();
  };

  float inv_sk;
  {
    const int mh2 = w & 1;
    const float dt = rbf(expf(bf1(q_ldt)));
    const float zr = -rbf(expf(bf1(q_lar))) * dt, zi = bf1(q_aim) * dt;
    f32x2 C = f32x2{bf1(q_cre), bf1(q_cim)} * sp2(2.0f * dt);
    const float bd = wave_sum((__builtin_fabsf(C.x) + __builtin_fabsf(C.y)) + (__builtin_fabsf(bf1(q_cre2)) + __builtin_fabsf(bf1(q_cim2))) * (2.0f * dt));
    int se = 267 - (int)((asu(bd) >> 23) & 0xff);
    se = se < 1 ? 1 : (se > 253 ? 253 : se);
    C *= sp2(asf((u32)se << 23));
    inv_sk = asf((u32)(254 - se) << 23);
    const float e1 = zr * 1.44269504089f;
    const float ph = zi * 0.15915494309f;
    const float pl = __builtin_fmaf(zi, 0.15915494309f, -ph) + zi * 6.4206383e-9f;
    auto cexp = [&](float pw) {
      const float m = __builtin_amdgcn_exp2f(e1 * pw);
      const float a = __builtin_amdgcn_fractf(ph * pw) + pl * pw;
      return f32x2{__builtin_amdgcn_cosf(a), __builtin_amdgcn_sinf(a)} * sp2(m);
    };
    CSTAMP(2);
    const u32 tl = S_TAB + kch * TAB_CH + (64 * mh2 + lane) * 4;
    auto putX = [&](int row, f32x2 v) { lds_st<u32>(smem, tl + (16 + row) * TAB_ROW, pk_f16(v)); };
    if (side == 0) {
      f32x2 P[8], E[3], CE[8];
      P[1] = cexp(1.f); E[0] = cexp(128.f);
      CE[0] = C;
      cmul2(P[2], P[1], P[1], E[1], E[0], E[0]);
      cmul2(CE[1], C, E[0], P[3], P[2], P[1]);
      cmul2(P[4], P[2], P[2], E[2], E[1], E[1]);
      cmul2(CE[2], CE[0], E[1], CE[3], CE[1], E[1]);
      cmul2(P[5], P[4], P[1], P[6], P[4], P[2]);
      cmul2(CE[4], CE[0], E[2], CE[5], CE[1], E[2]);
      cmul2(CE[6], CE[2], E[2], CE[7], CE[3], E[2]);
      P[7] = cmul(P[4], P[3]);
      putX(0, CE[0]); putX(8, CE[1]);
#pragma unroll
      for (int i = 1; i < 8; ++i) {
        f32x2 a, c;
        cmul2(a, CE[0], P[i], c, CE[1], P[i]);
        putX(i, a); putX(8 + i, c);
      }
      putX(16, CE[2]); putX(20, CE[3]);
#pragma unroll
      for (int i = 1; i < 4; ++i) {
        f32x2 a, c;
        cmul2(a, CE[2], P[2 * i], c, CE[3], P[2 * i]);
        putX(16 + i, a); putX(20 + i, c);
      }
      putX(24, CE[4]); putX(26, CE[5]); putX(28, CE[6]); putX(30, CE[7]);
      f32x2 a, c;
      cmul2(a, CE[4], P[4], c, CE[5], P[4]); putX(25, a); putX(27, c);
      cmul2(a, CE[6], P[4], c, CE[7], P[4]); putX(29, a); putX(31, c);
    } else {
      f32x2 Y[16], E[5], CE[9];
      st2_piece(ic<0>{});
      Y[1] = cexp(8.f); E[0] = cexp(128.f);
      Y[0] = f32x2{1.f, 0.f};
      st2_piece(ic<1>{});
      auto putY = [&](int i) { lds_st<u32>(smem, tl + i * TAB_ROW, pk_f16_conj(Y[i])); };
      cmul2(Y[2], Y[1], Y[1], E[1], E[0], E[0]);
      st2_piece(ic<2>{});
      cmul2(Y[3], Y[2], Y[1], Y[4], Y[2], Y[2]);
      putY(0); putY(1); putY(2);
      st2_piece(ic<3>{});
      cmul2(E[2], E[1], E[1], Y[5], Y[4], Y[1]);
      cmul2(Y[6], Y[4], Y[2], Y[7], Y[4], Y[3]);
      putY(3); putY(4);
      st2_piece(ic<4>{});
      cmul2(Y[8], Y[4], Y[4], E[3], E[2], E[2]);
      putY(5); putY(6); putY(7);
      st2_piece(ic<5>{});
      cmul2(Y[9], Y[8], Y[1], Y[10], Y[8], Y[2]);
      cmul2(E[4], E[3], E[3], CE[0], C, E[3]);
      putY(8);
      st2_piece(ic<6>{});
      cmul2(Y[11], Y[8], Y[3], Y[12], Y[8], Y[4]);
      putY(9); putY(10);
      st2_piece(ic<7>{});
      cmul2(Y[13], Y[8], Y[5], Y[14], Y[8], Y[6]);
      putY(11); putY(12);
      st2_piece(ic<8>{});
      cmul2(Y[15], Y[8], Y[7], CE[1], CE[0], E[0]);
      putY(13); putY(14);
      st2_piece(ic<9>{});
      cmul2(CE[2], CE[0], E[1], CE[8], C, E[4]);
      putY(15); putX(32, CE[0]); putX(33, CE[1]);
      st2_piece(ic<10>{});
      cmul2(CE[3], CE[1], E[1], CE[4], CE[0], E[2]);
      putX(34, CE[2]); putX(40, CE[8]);
      st2_piece(ic<11>{});
      cmul2(CE[5], CE[1], E[2], CE[6], CE[2], E[2]);
      putX(35, CE[3]); putX(36, CE[4]);
      st2_piece(ic<12>{});
      CE[7] = cmul(CE[3], E[2]);
      putX(37, CE[5]); putX(38, CE[6]);
      st2_piece(ic<13>{});
      putX(39, CE[7]);
      st2_piece(ic<14>{});
    }
    CSTAMP(3);
    if (side) wait_vm<17>(); else wait_vm<2>();
    tie(lnf);
    st32c(rWS, epoch, lane == 0 ? WS_HF + (((mt * 4 + w) * 16 + nt) * 2 + side) * 4 : OOB);
    spin_flag(sWS, WS_LNF + t8 * 4, epoch, lnf, true);
    u32x4 ug = ld128c(sWS, WS_XN + t8 * 8192 + cp * 32 + side * 16, 0);
    if (side) st2_piece(ic<15>{});
    CSTAMP(4);
    bar();
    CSTAMP(5);
    const int tile = side ? 2 : mh2;
    const u32 kc = S_KC + kch * KC_CH;
    if (!(side && mh2)) {
      const u32 rd = S_TAB + kch * TAB_CH + c15 * TAB_ROW + g * 16;
      const u32 ra = S_TAB + kch * TAB_CH + (16 + min(16 * tile + c15, 40)) * TAB_ROW + g * 16;
      u32x4 yf[8], xf[8];
#pragma unroll
      for (int s = 0; s < 8; ++s) { yf[s] = lds_ld<u32x4>(smem, rd + s * 64); xf[s] = lds_ld<u32x4>(smem, ra + s * 64); }
      f32x4 a0 = {0, 0, 0, 0}, a1 = {0, 0, 0, 0};
#pragma unroll
      for (int s = 0; s < 4; ++s) { a0 = mfma_h(xf[2 * s], yf[2 * s], a0); a1 = mfma_h(xf[2 * s + 1], yf[2 * s + 1], a1); }
      const f32x4 v = a0 + a1;
      const u32 h01 = pk_f16(f32x2{v[0], v[1]}), h23 = pk_f16(f32x2{v[2], v[3]});
      if (tile == 0) {
        lds_st<u32x2>(smem, kc + (128 * (g >> 1) + 8 * c15 + 4 * (g & 1)) * 2, u32x2{h01, h23});
      } else if (tile == 1) {
        const u32 t1a = g < 2 ? 256 + 64 * g + 4 * c15 : 384 + 64 * (g - 2) + 2 * c15;
        lds_st<u32>(smem, kc + t1a * 2, h01);
        lds_st<u32>(smem, kc + (t1a + (g < 2 ? 2 : 32)) * 2, h23);
      } else {
        const u32 t2 = g < 2 ? 512 + 64 * g + c15 : 644, st2 = g < 2 ? 16 : 0;
        lds_st<u16>(smem, kc + ((g == 2 && c15 == 0) ? 640 : t2) * 2, (u16)h01);
        lds_st<u16>(smem, kc + (t2 + st2) * 2, (u16)(h01 >> 16));
        lds_st<u16>(smem, kc + (t2 + 2 * st2) * 2, (u16)h23);
        lds_st<u16>(smem, kc + (t2 + 3 * st2) * 2, (u16)(h23 >> 16));
      }
    } else if (lane == 0) lds_st<u16>(smem, kc + 641 * 2, (u16)0);
    CSTAMP(6);
    bar();
    CSTAMP(7);
    if (side) wait_vm<1>(); else wait_vm<0>();
    tie(ug);
    {
      u32 e[9];
#pragma unroll
      for (int i = 0; i < 9; ++i) e[i] = lds_ld<u16>(smem, S_KC + side * KC_CH + ((i & 1) ? ixp[i >> 1] >> 16 : ixp[i >> 1] & 0xffffu));
      const u32x4 a0 = {e[0] | (e[1] << 16), e[2] | (e[3] << 16), e[4] | (e[5] << 16), e[6] | (e[7] << 16)};
      const u32x4 a1 = {e[1] | (e[2] << 16), e[3] | (e[4] << 16), e[5] | (e[6] << 16), e[7] | (e[8] << 16)};
      lds_st<u32x4>(smem, S_G + side * G_CH + t8 * 16, a0);
      lds_st<u32x4>(smem, S_G + side * G_CH + G_PAR + t8 * 16, a1);
      const u32x4 z4 = {0, 0, 0, 0};
      if (t8 < 4) lds_st<u32x4>(smem, S_G + side * G_CH + (t8 >> 1) * G_PAR + 4096 + (t8 & 1) * 16, z4);
      if (t8 < 30) lds_st<u32x4>(smem, S_U + side * U_CH + t8 * 16, z4);
      lds_st<u32x4>(smem, S_U + side * U_CH + 480 + t8 * 16, ug);
    }
    CSTAMP(8);
    bar();
    CSTAMP(9);
    if (side) CDUMP(10, 48); else CDUMP(10, 38);
  }
  STAMP(5);

  const int ch0 = 64 * nt2 + 32 * kh + 4 * hh;
  const u32 xto = (64 * mt2 + (t8 >> 3)) * 1024 + nt2 * 128 + (t8 & 7) * 16;
  f32x16 acc[2];
  if (side == 0) {
    const int par = w & 1;
    const u32 ub = lb + S_U + kch * U_CH + 64 - 64 * par + 32 * c15 + 16 * g;
    const u32 gb0 = lb + S_G + kch * G_CH + ((c15 & 1) ? 0 : G_PAR) + 4 * (1015 - (c15 >> 1) - 16 * par + 4 * g - 32 * 31);
    const u32 gb[4] = {gb0, gb0 + 1024, gb0 + 2048, gb0 + 3072};
    u32x4 bq[32];
    u32x2 al[32], ah[32];
    f32x4 y[8];
    auto rdB = [&](auto M, auto T) { constexpr int q = 4 * T - M + 3; bq[q] = a_ds128<128 * q>(ub); };
    auto rdA = [&](auto M, auto H) {
      constexpr int k = (31 - M) >> 3, o = 32 * ((31 - M) & 7);
      if constexpr (H == 0) al[M] = a_ds2x32<o, o + 1>(gb[k]); else ah[M] = a_ds2x32<o + 2, o + 3>(gb[k]);
    };
    sfor<2>([&](auto M) { sfor<8>([&](auto T) { rdB(M, T); }); rdA(M, ic<0>{}); rdA(M, ic<1>{}); });
    sfor<32>([&](auto M) {
      constexpr int m = M, t0 = m >> 2, n = 8 - t0;
      wait_lgkm<(m <= 2 ? 10 : (m == 3 ? 4 : (m == 4 ? 6 : (2 * (31 - m) < 8 ? 2 * (31 - m) : 8))))>();
      sfor<n>([&](auto I) {
        constexpr int i = I, t = t0 + i;
        mfma16_h<m == 0>(y[t], cat4(al[m], ah[m]), bq[4 * t - m + 3]);
        if constexpr (m < 2) {
          rdB(ic<m + 2>{}, I);
          if constexpr (i >= 6) rdA(ic<m + 2>{}, ic<i - 6>{});
        } else if constexpr (m <= 4) {
          if constexpr (i < 4) rdA(ic<2 * m + (i >> 1)>{}, ic<i & 1>{});
        } else if constexpr (m + 5 < 32) {
          if constexpr (i < 2) rdA(ic<m + 5>{}, I);
        }
      });
    });
    asm volatile("s_nop 7\n\ts_nop 1" : "+v"(y[0]), "+v"(y[1]), "+v"(y[2]), "+v"(y[3]), "+v"(y[4]), "+v"(y[5]), "+v"(y[6]), "+v"(y[7]));
    STAMP(6);
    bar();
    auto send = [&](auto PAR) {
      FENCE();
#pragma unroll
      for (int i = 0; i < 4; ++i) lds_st<f32x4>(smem, S_EX + w * 4096 + i * 1024 + lane * 16, y[4 * (1 - PAR) + i]);
      FENCE();
    };
    if (par) send(ic<1>{}); else send(ic<0>{});
    bar();
    const float dsk = bf1(q_dsk);
    u32x2 ao[4];
    auto finish = [&](auto PAR) {
      FENCE();
#pragma unroll
      for (int i = 0; i < 4; ++i) {
        const f32x4 o = lds_ld<f32x4>(smem, S_EX + (w ^ 1) * 4096 + i * 1024 + lane * 16);
        const u32x2 uu = lds_ld<u32x2>(smem, S_U + kch * U_CH + (240 + 256 * (4 * PAR + i) + 16 * c15 + 4 * g) * 2);
        const f16x2 u01 = ash2(uu.x), u23 = ash2(uu.y);
        const float uf[4] = {(float)u01.x, (float)u01.y, (float)u23.x, (float)u23.y};
        float v[4];
#pragma unroll
        for (int e = 0; e < 4; ++e) v[e] = ffma1(uf[e], dsk, fmul1(fadd1(y[4 * PAR + i][e], o[e]), inv_sk));
        ao[i] = u32x2{cvt_bf2(v[0], v[1]), cvt_bf2(v[2], v[3])};
      }
      FENCE();
    };
    if (par) finish(ic<1>{}); else finish(ic<0>{});
#pragma unroll
    for (int i = 0; i < 4; ++i) {
      u32x2 o = ao[i];
      o.x = o.x == POISON ? 0xffc0ffc0u : o.x;
      o.y = o.y == POISON ? 0xffc0ffc0u : o.y;
      st64c(rWS, o, WS_A + ((4 * (4 * par + i) + (c15 >> 2)) * 512 + kd) * 128 + ((c15 & 3) * 16 + 4 * g) * 2);
    }
    STAMP(7);
    u32x4 xt0 = ld128(sX, xto, 0), xt1 = ld128(sX, xto + 32 * 1024, 0);
    u32 b2w = ld16(mk_srd(p.b2, DM * 2), t8 < 64 ? (64 * nt2 + t8) * 2 : OOB, 0);
    bar();
    sfor<16>([&](auto N) { st2_piece(ic<28 + N>{}); });
    const u32 ato = WS_A + (mt2 * 512 + nt2 * 64) * 128 + t8 * 16;
    u32x4 at0 = ld128c(sWS, ato, 0), at1 = ld128c(sWS, ato + 4096, 0);
    asm volatile("s_waitcnt vmcnt(10)" : "+v"(xt0), "+v"(xt1), "+v"(b2w)::"memory");
    bar();
    const int row0 = t8 >> 3, sl = t8 & 7;
    {
      const u32 o = S_XT + row0 * 128 + ((sl ^ (row0 & 7)) * 16);
      lds_st<u32x4>(smem, o, xt0);
      lds_st<u32x4>(smem, o + 32 * 128, xt1);
      lds_st<float>(smem, t8 < 64 ? S_B2 + t8 * 4 : S_MISC, bf1(b2w));
    }
    wait_vm<2>();
    bar();
    asm volatile("s_waitcnt vmcnt(0)" : "+v"(at0), "+v"(at1)::"memory");
#pragma unroll 1
    for (int it = 0; it < (1 << 16); ++it) {
      const u32 m0 = max(max(at0.x, at0.y), max(at0.z, at0.w)), m1 = max(max(at1.x, at1.y), max(at1.z, at1.w));
      if (!__any(max(m0, m1) == POISON)) break;
      at0 = ld128c(sWS, ato, 0); at1 = ld128c(sWS, ato + 4096, 0);
      asm volatile("s_waitcnt vmcnt(0)" : "+v"(at0), "+v"(at1)::"memory");
    }
    {
      const u32 o = S_AT + row0 * 128 + (((sl >> 1) ^ (row0 & 2)) * 32) + (sl & 1) * 16;
      lds_st<u32x4>(smem, o, at0);
      lds_st<u32x4>(smem, o + 32 * 128, at1);
    }
    bar();
  } else {
    u32x4 hf[22][2];
    const u32 hb = WS_H + ((mt2 * 2 + mh) * NUNIT + kh * 22) * 2048;
    const u32 hvo = lane * 16, hvz = kh ? OOB : hvo;
    auto ldh = [&](auto J, auto S) { hf[J][S] = ld128c(sWS, J == 21 ? hvz : hvo, hb + (J * 2 + S) * 1024); };
    sfor<12>([&](auto N) { st2_piece(ic<16 + N>{}); });
    {
      const u32 hfo = WS_HF + (((mt2 >> 1) * 4 + 2 * (mt2 & 1) + mh) * 32 + c31) * 4;
      u32 hfl = ld32c(sWS, hfo, 0);
      asm volatile("s_waitcnt vmcnt(0)" : "+v"(hfl)::"memory");
      spin_flag(sWS, hfo, epoch, hfl, kh ? c31 >= 16 : c31 <= 17);
    }
    sfor<4>([&](auto J) { ldh(J, ic<0>{}); ldh(J, ic<1>{}); });
    STAMP(6);
    bar();
    sfor<3>([&](auto J) { ldh(ic<4 + J>{}, ic<0>{}); ldh(ic<4 + J>{}, ic<1>{}); });
    bar();
    sfor<G2_LEAD - 7>([&](auto J) { ldh(ic<7 + J>{}, ic<0>{}); ldh(ic<7 + J>{}, ic<1>{}); });
    STAMP(7);
    u32 wa[2];
#pragma unroll
    for (int n = 0; n < 2; ++n) wa[n] = lb + kh * W2_HALF + (4 * hh + q4) * 128 + (((2 * n + b16) ^ (q4 & 2)) * 32) + 8 * p4;
    u32x2 wl[2][4], wh[2][4];
    auto rd = [&](auto J, auto I, auto H) {
      constexpr int j = J, i = I, off = (j % W2_NS) * 4096 + ((i >> 1) * 16 + H * 8) * 128;
      if constexpr (H == 0) wl[j & 1][i] = a_dstr<off>(wa[i & 1]); else wh[j & 1][i] = a_dstr<off>(wa[i & 1]);
    };
    sfor<4>([&](auto I) { rd(ic<0>{}, I, ic<0>{}); rd(ic<0>{}, I, ic<1>{}); });
    sfor<22>([&](auto J) {
      constexpr int j = J;
      constexpr bool last = j == 21, brk = j == 8 || j == 13 || j == 17;
      if constexpr (brk) bar();
      wait_vm<g2_wait_h(j)>();
      tie(hf[j][0]); tie(hf[j][1]);
      sfor<4>([&](auto I) {
        constexpr int i = I;
        if constexpr (!brk) wait_lgkm<(last ? 6 - 2 * i : 6)>();
        mfma32_vv<j == 0 && i < 2>(acc[i & 1], cat4(wl[j & 1][i], wh[j & 1][i]), hf[j][i >> 1]);
        if constexpr ((i & 1) == 0 && j + G2_LEAD <= 21) ldh(ic<j + G2_LEAD>{}, ic<(i >> 1)>{});
        if constexpr (!last) { rd(ic<j + 1>{}, I, ic<0>{}); rd(ic<j + 1>{}, I, ic<1>{}); }
      });
    });
    asm volatile("s_nop 7\n\ts_nop 3" : "+v"(acc[0]), "+v"(acc[1]));
    STAMP(8);
    auto send2 = [&](auto KH) {
      FENCE();
#pragma unroll
      for (int q = 0; q < 4; ++q) {
        const f32x16& as = acc[1 - KH];
        lds_st<f32x4>(smem, S_EX + w * 4096 + q * 1024 + lane * 16, f32x4{as[4 * q], as[4 * q + 1], as[4 * q + 2], as[4 * q + 3]});
      }
      FENCE();
    };
    if (kh) send2(ic<1>{}); else send2(ic<0>{});
    bar();
    const int tl = 32 * mh + c31;
    auto fin2 = [&](auto KH) {
      FENCE();
#pragma unroll
      for (int q = 0; q < 4; ++q) {
        const f32x16& ak = acc[KH];
        const f32x4 v = f32x4{ak[4 * q], ak[4 * q + 1], ak[4 * q + 2], ak[4 * q + 3]} + lds_ld<f32x4>(smem, S_EX + (w ^ 2) * 4096 + q * 1024 + lane * 16);
        const f32x4 bb = lds_ld<f32x4>(smem, S_B2 + (32 * KH + 8 * q + 4 * hh) * 4);
        const int row = 32 * KH + 8 * q + 4 * hh + q4;
        const u32x2 av = lds_tr(smem, S_AT + row * 128 + (((2 * mh + b16) ^ (row & 2)) * 32) + 8 * p4);
        const u32 po = tl * 128 + (((4 * KH + q) ^ (tl & 7)) * 16) + hh * 8;
        const u32x2 xv = lds_ld<u32x2>(smem, S_XT + po);
        const f32x2 o0 = (bf2(xv.x) + bf2(av.x)) + (f32x2{v[0], v[1]} + f32x2{bb.x, bb.y}), o1 = (bf2(xv.y) + bf2(av.y)) + (f32x2{v[2], v[3]} + f32x2{bb.z, bb.w});
        lds_st<u32x2>(smem, S_OT + po, u32x2{pk_bf16(o0), pk_bf16(o1)});
      }
      FENCE();
    };
    if (kh) fin2(ic<1>{}); else fin2(ic<0>{});
  }
  bar();
  {
    const int row = tid >> 3, sl = tid & 7;
    const u32x4 o = lds_ld<u32x4>(smem, S_OT + row * 128 + ((sl ^ (row & 7)) * 16));
    __builtin_amdgcn_raw_buffer_store_b128(o, rOUT, (64 * mt2 + row) * 1024 + nt2 * 128 + sl * 16, 0, 0);
  }
  STAMP(9);
}

extern "C" void kernel_launch(void* const* d_in, const int* in_sizes, int n_in, void* d_out, int out_size, void* d_ws,
                              size_t ws_size, hipStream_t stream) {
  Params p;
  p.x = (const u16*)d_in[0]; p.lar = (const u16*)d_in[1]; p.aim = (const u16*)d_in[2]; p.cre = (const u16*)d_in[3];
  p.cim = (const u16*)d_in[4]; p.ldt = (const u16*)d_in[5]; p.dsk = (const u16*)d_in[6];
  p.w1 = (const u16*)d_in[8]; p.b1 = (const u16*)d_in[9]; p.w2 = (const u16*)d_in[10]; p.b2 = (const u16*)d_in[11];
  p.out = (u16*)d_out; p.ws = (char*)d_ws;
  s4d_mixer_fused<<<256, 512, 0, stream>>>(p);
}
```

```cpp
#include <hip/hip_runtime.h>
#include <stdint.h>
#include <utility>

namespace {
typedef unsigned short u16;
typedef unsigned u32;
typedef unsigned long long u64;
typedef float f32x2 __attribute__((ext_vector_type(2)));
typedef float f32x4 __attribute__((ext_vector_type(4)));
typedef float f32x16 __attribute__((ext_vector_type(16)));
typedef u32 u32x2 __attribute__((ext_vector_type(2)));
typedef u32 u32x4 __attribute__((ext_vector_type(4)));
typedef short s16x4 __attribute__((ext_vector_type(4)));
typedef __bf16 bf16x2 __attribute__((ext_vector_type(2)));
typedef _Float16 f16x2 __attribute__((ext_vector_type(2)));
typedef _Float16 f16x8 __attribute__((ext_vector_type(8)));
#define LAS __attribute__((address_space(3)))
#define DEV __device__ __forceinline__
typedef __amdgpu_buffer_rsrc_t rsrc_t;

constexpr int SEQ = 2048, DM = 512, NMODE = 128, FF = 1376, NUNIT = 43;
constexpr u32 WS_E = 0, WS_LNF = 0x1000, WS_HF = 0x2000  , WS_STATS = 0x8000, WS_XN = 0x10000, WS_A = 0x210000, WS_H = 0x410000,
              WS_END = WS_H + 64 * NUNIT * 2048, WS_STAMP = 0xA00000;
constexpr u32 SLOT = 28672, SLOT_W = 16384;
constexpr u32 W2_NS = 14, W2_HALF = W2_NS * 4096;
constexpr u32 LDS_BYTES_ = 163840;
constexpr u32 S0 = 114688;
constexpr u32 S_DUMMY = LDS_BYTES_ - 256;
constexpr u32 S_LN = S0, LN_ROW = 1088;
constexpr u32 TAB_ROW = 544, TAB_CH = 57 * TAB_ROW;
constexpr u32 S_TAB = LDS_BYTES_ - 2 * TAB_CH - 1872;
constexpr u32 S_TB = 32768;
constexpr u32 S_KC = S_TAB - 2592, KC_CH = 1296;
constexpr u32 S_CSP = LDS_BYTES_ - 1872;
constexpr u32 S_B1 = S_CSP + 768;
constexpr u32 S_MISC = S_B1 + 384;
constexpr u32 S_CS = S_MISC + 64;
constexpr u32 S_G = S0, G_PAR = 4160, G_CH = 8320;
constexpr u32 S_U = S0 + 16640, U_CH = 4608;
constexpr u32 S_EX = S0;
constexpr u32 S_AT = S0 + 16384, S_XT = S0 + 24576, S_OT = S0 + 32768, S_B2 = S0 + 40960;
constexpr u32 LDS_BYTES = 163840;
constexpr u32 OOB = 0x80000000u;
constexpr u32 POISON = 0xffffffffu;

#define STAMP(k) do {} while (0)
#define CSTAMP(i) do {} while (0)
#define CDUMP(n, base) do {} while (0)

struct Params {
  const u16 *x, *lar, *aim, *cre, *cim, *ldt, *dsk, *w1, *b1, *w2, *b2;
  u16* out;
  char* ws;
};

DEV float asf(u32 v) { return __builtin_bit_cast(float, v); }
DEV u32 asu(float v) { return __builtin_bit_cast(u32, v); }
DEV f16x2 ash2(u32 v) { return __builtin_bit_cast(f16x2, v); }
DEV float bflo(u32 v) { return asf(v << 16); }
DEV float bfhi(u32 v) { return asf(v & 0xffff0000u); }
DEV f32x2 bf2(u32 v) { return f32x2{bflo(v), bfhi(v)}; }
DEV u32 pk_bf16(f32x2 v) { return __builtin_bit_cast(u32, __builtin_convertvector(v, bf16x2)); }
DEV u32 pk_f16(f32x2 v) { return __builtin_bit_cast(u32, __builtin_convertvector(v, f16x2)); }
DEV float rbf(float a) { return bflo(pk_bf16(f32x2{a, a})); }
DEV float bf1(u32 v) { return asf(v << 16); }
DEV f32x2 sp2(float a) { return f32x2{a, a}; }
DEV f32x2 fma2(f32x2 a, f32x2 b, f32x2 c) { return __builtin_elementwise_fma(a, b, c); }

template <int N> DEV void wait_vm() { asm volatile("s_waitcnt vmcnt(%0)" ::"n"(N) : "memory"); }
template <int N> DEV void wait_lgkm() { asm volatile("s_waitcnt lgkmcnt(%0)" ::"n"(N)); }
DEV void bar() { asm volatile("s_waitcnt lgkmcnt(0)\n\ts_barrier" ::: "memory"); }
DEV u32x4 mk_srd(const void* base, u32 bytes) {
  const u64 b = (u64)base;
  u32x4 s;
  s.x = __builtin_amdgcn_readfirstlane((u32)b);
  s.y = __builtin_amdgcn_readfirstlane((u32)(b >> 32)) & 0xffffu;
  s.z = bytes;
  s.w = 0x00020000u;
  return s;
}
DEV rsrc_t mk_rsrc(const void* base, u32 bytes) { return __builtin_amdgcn_make_buffer_rsrc((void*)base, 0, bytes, 0x00020000); }

#define BLOAD(INS, TY, NAME, FLAGS, CON)                                                                     \
  DEV TY NAME(u32x4 srd, u32 voff, u32 soff) {                                                               \
    TY r;                                                                                                    \
    asm volatile(INS " %0, %1, %2, %3 offen" FLAGS : CON(r) : "v"(voff), "s"(srd), "s"(soff) : "memory");     \
    return r;                                                                                                \
  }
BLOAD("buffer_load_dwordx4", u32x4, ld128, "", "=v")
BLOAD("buffer_load_dwordx4", u32x4, ld128c, " sc1", "=v")
BLOAD("buffer_load_dwordx4", u32x4, ld128c_a, " sc1", "=a")
BLOAD("buffer_load_dwordx2", u32x2, ld64, "", "=v")
BLOAD("buffer_load_dword", u32, ld32c, " sc1", "=v")
BLOAD("buffer_load_ushort", u32, ld16, "", "=v")
DEV void touch(const void* a, u32 lds_dummy) { asm volatile("s_mov_b32 m0, %1\n\ts_nop 0\n\tglobal_load_lds_dword %0, off" ::"v"(a), "s"(lds_dummy) : "memory"); }
template <class T> DEV void tie(T& v) { asm volatile("" : "+v"(v)); }

DEV void dma_m0(u32 lds_addr) { asm volatile("s_mov_b32 m0, %0" ::"s"(lds_addr) : "memory"); }
template <int IOFF> DEV void dma_go(u32x4 srd, u32 voff, u32 soff) {
  asm volatile("buffer_load_dwordx4 %0, %1, %2 offen offset:%3 lds" ::"v"(voff), "s"(srd), "s"(soff), "n"(IOFF) : "memory");
}
DEV void st128c(rsrc_t r, u32x4 v, u32 voff) { __builtin_amdgcn_raw_buffer_store_b128(v, r, voff, 0, 16); }
DEV void st64c(rsrc_t r, u32x2 v, u32 voff) { __builtin_amdgcn_raw_buffer_store_b64(v, r, voff, 0, 16); }
DEV void st32c(rsrc_t r, u32 v, u32 voff) { __builtin_amdgcn_raw_buffer_store_b32(v, r, voff, 0, 16); }
DEV void st64(rsrc_t r, u32x2 v, u32 voff) { __builtin_amdgcn_raw_buffer_store_b64(v, r, voff, 0, 0); }

template <class T> DEV T lds_ld(const char* smem, u32 off) { return *(const LAS T*)(smem + off); }
template <class T> DEV void lds_st(char* smem, u32 off, T v) { *(LAS T*)(smem + off) = v; }
DEV u32x2 lds_tr(const char* smem, u32 off) { return __builtin_bit_cast(u32x2, __builtin_amdgcn_ds_read_tr16_b64_v4i16((LAS s16x4*)(smem + off))); }

template <int I> using ic = std::integral_constant<int, I>;
template <class F, int... Is> DEV void sfor_impl(F&& f, std::integer_sequence<int, Is...>) { (f(ic<Is>{}), ...); }
template <int N, class F> DEV void sfor(F&& f) { sfor_impl(f, std::make_integer_sequence<int, N>{}); }
template <int OFF> DEV u32x4 a_ds128(u32 addr) { u32x4 r; asm volatile("ds_read_b128 %0, %1 offset:%2" : "=v"(r) : "v"(addr), "n"(OFF)); return r; }
template <int OFF> DEV u32x2 a_dstr(u32 addr) { u32x2 r; asm volatile("ds_read_b64_tr_b16 %0, %1 offset:%2" : "=v"(r) : "v"(addr), "n"(OFF)); return r; }
template <int O0, int O1> DEV u32x2 a_ds2x32(u32 addr) { u32x2 r; asm volatile("ds_read2_b32 %0, %1 offset0:%2 offset1:%3" : "=v"(r) : "v"(addr), "n"(O0), "n"(O1)); return r; }
DEV u32x4 cat4(u32x2 a, u32x2 b) { return __builtin_shufflevector(a, b, 0, 1, 2, 3); }
template <bool INIT> DEV void mfma32_vv(f32x16& c, u32x4 a, u32x4 b) {
  if constexpr (INIT) asm volatile("v_mfma_f32_32x32x16_bf16 %0, %1, %2, 0" : "=&v"(c) : "v"(a), "v"(b));
  else asm volatile("v_mfma_f32_32x32x16_bf16 %0, %1, %2, %0" : "+v"(c) : "v"(a), "v"(b));
}
template <bool INIT> DEV void mfma32_va(f32x16& c, u32x4 a, u32x4 b) {
  if constexpr (INIT) asm volatile("v_mfma_f32_32x32x16_bf16 %0, %1, %2, 0" : "=&v"(c) : "v"(a), "a"(b));
  else asm volatile("v_mfma_f32_32x32x16_bf16 %0, %1, %2, %0" : "+v"(c) : "v"(a), "a"(b));
}
template <bool INIT> DEV void mfma16_h(f32x4& c, u32x4 a, u32x4 b) {
  if constexpr (INIT) asm volatile("v_mfma_f32_16x16x32_f16 %0, %1, %2, 0" : "=&v"(c) : "v"(a), "v"(b));
  else asm volatile("v_mfma_f32_16x16x32_f16 %0, %1, %2, %0" : "+v"(c) : "v"(a), "v"(b));
}
DEV f32x4 mfma_h(u32x4 a, u32x4 b, f32x4 c) {
  return __builtin_amdgcn_mfma_f32_16x16x32_f16(__builtin_bit_cast(f16x8, a), __builtin_bit_cast(f16x8, b), c, 0, 0, 0);
}
template <bool INIT> DEV void mfma16_bf(f32x4& c, u32x4 a, u32x4 b) {
  if constexpr (INIT) asm volatile("v_mfma_f32_16x16x32_bf16 %0, %1, %2, 0" : "=&v"(c) : "v"(a), "v"(b));
  else asm volatile("v_mfma_f32_16x16x32_bf16 %0, %1, %2, %0" : "+v"(c) : "v"(a), "v"(b));
}
#define FENCE() __builtin_amdgcn_sched_barrier(0)

DEV void wave_sum4(float& a, float& b, float& c, float& d) {
#define S4(CTRL) "v_add_f32_dpp %0, %0, %0 " CTRL "\n\tv_add_f32_dpp %1, %1, %1 " CTRL "\n\tv_add_f32_dpp %2, %2, %2 " CTRL "\n\tv_add_f32_dpp %3, %3, %3 " CTRL "\n\t"
  asm volatile("s_nop 1\n\t" S4("row_shr:1 row_mask:0xf bank_mask:0xf bound_ctrl:1") S4("row_shr:2 row_mask:0xf bank_mask:0xf bound_ctrl:1")
               S4("row_shr:4 row_mask:0xf bank_mask:0xf bound_ctrl:1") S4("row_shr:8 row_mask:0xf bank_mask:0xf bound_ctrl:1")
               S4("row_bcast:15 row_mask:0xa bank_mask:0xf") S4("row_bcast:31 row_mask:0xc bank_mask:0xf") "s_nop 0"
               : "+v"(a), "+v"(b), "+v"(c), "+v"(d));
#undef S4
  a = asf(__builtin_amdgcn_readlane(asu(a), 63)); b = asf(__builtin_amdgcn_readlane(asu(b), 63));
  c = asf(__builtin_amdgcn_readlane(asu(c), 63)); d = asf(__builtin_amdgcn_readlane(asu(d), 63));
}
DEV void wave_sum2(float& a, float& b) {
#define S2(CTRL) "v_add_f32_dpp %0, %0, %0 " CTRL "\n\tv_add_f32_dpp %1, %1, %1 " CTRL "\n\ts_nop 0\n\t"
  asm volatile("s_nop 1\n\t" S2("row_shr:1 row_mask:0xf bank_mask:0xf bound_ctrl:1") S2("row_shr:2 row_mask:0xf bank_mask:0xf bound_ctrl:1")
               S2("row_shr:4 row_mask:0xf bank_mask:0xf bound_ctrl:1") S2("row_shr:8 row_mask:0xf bank_mask:0xf bound_ctrl:1")
               S2("row_bcast:15 row_mask:0xa bank_mask:0xf") S2("row_bcast:31 row_mask:0xc bank_mask:0xf")
               : "+v"(a), "+v"(b));
#undef S2
  a = asf(__builtin_amdgcn_readlane(asu(a), 63)); b = asf(__builtin_amdgcn_readlane(asu(b), 63));
}
template <int CTRL, int RM> DEV float dpp_add(float v) { return v + asf(__builtin_amdgcn_update_dpp(0, asu(v), CTRL, RM, 0xf, true)); }
DEV float wave_sum(float v) {
  v = dpp_add<0x111, 0xf>(v); v = dpp_add<0x112, 0xf>(v); v = dpp_add<0x114, 0xf>(v); v = dpp_add<0x118, 0xf>(v);
  v = dpp_add<0x142, 0xa>(v); v = dpp_add<0x143, 0xc>(v);
  return asf(__builtin_amdgcn_readlane(asu(v), 63));
}
DEV void cmul2(f32x2& r0, f32x2 a0, f32x2 b0, f32x2& r1, f32x2 a1, f32x2 b1) {
  f32x2 t0, t1;
  asm("v_pk_mul_f32 %0, %4, %5 op_sel_hi:[1,0]\n\tv_pk_mul_f32 %1, %6, %7 op_sel_hi:[1,0]\n\t"
      "v_pk_fma_f32 %2, %4, %5, %0 op_sel:[1,1,0] op_sel_hi:[0,1,1] neg_lo:[1,0,0]\n\t"
      "v_pk_fma_f32 %3, %6, %7, %1 op_sel:[1,1,0] op_sel_hi:[0,1,1] neg_lo:[1,0,0]"
      : "=&v"(t0), "=&v"(t1), "=&v"(r0), "=&v"(r1) : "v"(a0), "v"(b0), "v"(a1), "v"(b1));
}
DEV f32x2 cmul(f32x2 a, f32x2 b) {
  f32x2 t, r;
  asm("v_pk_mul_f32 %0, %2, %3 op_sel_hi:[1,0]\n\tv_pk_fma_f32 %1, %2, %3, %0 op_sel:[1,1,0] op_sel_hi:[0,1,1] neg_lo:[1,0,0]"
      : "=&v"(t), "=v"(r) : "v"(a), "v"(b));
  return r;
}
DEV u32 cvt_bf2(float a, float b) { u32 r; asm("v_cvt_pk_bf16_f32 %0, %1, %2" : "=v"(r) : "v"(a), "v"(b)); return r; }
DEV u32 cvt_h2(float a, float b) { u32 r; asm("v_cvt_pk_f16_f32 %0, %1, %2" : "=v"(r) : "v"(a), "v"(b)); return r; }
DEV float fadd1(float a, float b) { float r; asm("v_add_f32 %0, %1, %2" : "=v"(r) : "v"(a), "v"(b)); return r; }
DEV float fsub1(float a, float b) { float r; asm("v_sub_f32 %0, %1, %2" : "=v"(r) : "v"(a), "v"(b)); return r; }
DEV float fmul1(float a, float b) { float r; asm("v_mul_f32 %0, %1, %2" : "=v"(r) : "v"(a), "v"(b)); return r; }
DEV float ffma1(float a, float b, float c) { float r; asm("v_fma_f32 %0, %1, %2, %3" : "=v"(r) : "v"(a), "v"(b), "v"(c)); return r; }
DEV float rbf1(float a) { return bflo(cvt_bf2(a, a)); }
DEV u32 pk_f16_conj(f32x2 v) { u32 r; asm("v_cvt_pk_f16_f32 %0, %1, -%2" : "=v"(r) : "v"(v.x), "v"(v.y)); return r; }
DEV f32x2 gelu2(f32x2 v) {
  const f32x2 s = v * fma2(v * v, sp2(-0.10294323970f), sp2(-2.3022081986f));
  const f32x2 d = f32x2{__builtin_amdgcn_exp2f(s.x), __builtin_amdgcn_exp2f(s.y)} + sp2(1.0f);
  return v * f32x2{__builtin_amdgcn_rcpf(d.x), __builtin_amdgcn_rcpf(d.y)};
}
DEV void spin_flag(u32x4 srd, u32 voff, u32 epoch, u32 val, bool need) {
#pragma unroll 1
  for (int it = 0; it < (1 << 16); ++it) {
    if (__all(!need || val == epoch)) break;
    __builtin_amdgcn_s_sleep(1);
    val = ld32c(srd, voff, 0);
    asm volatile("s_waitcnt vmcnt(0)" : "+v"(val)::"memory");
  }
}

constexpr int G2_LEAD = 10;
constexpr int g2_wait_h(int j) { return 2 * ((j + G2_LEAD - 1 < 21 ? j + G2_LEAD - 1 : 21) - j); }

}

__global__ __launch_bounds__(512, 2) void s4d_mixer_fused(Params p) {
  __shared__ __attribute__((aligned(16))) char smem[LDS_BYTES];
  const int tid = threadIdx.x, lane = tid & 63, t8 = tid & 255;
  const int wid = __builtin_amdgcn_readfirstlane(tid >> 6);
  const int side = wid >> 2, w = wid & 3;
  const int b = blockIdx.x, g8 = b & 7, r = b >> 3;
  const int mt = 4 * (g8 >> 1) + (r >> 3), nt = 8 * (g8 & 1) + (r & 7);
  const int ustart = (nt * NUNIT) >> 4, unext = ((nt + 1) * NUNIT) >> 4;
  const int tg = 16 * mt + nt;
  const int cp = 32 * g8 + r;
  const int mt2 = 4 * g8 + (r >> 3), nt2 = r & 7;
  const int c15 = lane & 15, g = lane >> 4;
  const int c31 = lane & 31, hh = lane >> 5, b16 = (lane >> 4) & 1;
  const int q4 = (lane & 15) >> 2, p4 = lane & 3;
  const int kch = w >> 1, kd = 2 * cp + kch, kh = w >> 1, mh = w & 1;

  const u32x4 sX = mk_srd(p.x, SEQ * DM * 2), sWS = mk_srd(p.ws, WS_END);
  const rsrc_t rWS = mk_rsrc(p.ws, WS_END), rOUT = mk_rsrc(p.out, SEQ * DM * 2);
  const u32x4 sW1 = mk_srd(p.w1, DM * FF * 2), sW2 = mk_srd(p.w2, FF * DM * 2);
  const u32 lb = (u32)(uintptr_t)(LAS char*)smem;
  STAMP(0);

  u32x4 xr = ld128(sX, lane * 16, (8 * tg + 2 * w + side) * 1024);
  u32 vx[4], vw[3];
#pragma unroll
  for (int j = 0; j < 4; ++j) vx[j] = (8 * j + (lane >> 3)) * 1024 - j * 1024 + (((lane & 7) ^ ((4 * j + (lane >> 4)) & 7)) * 16);
#pragma unroll
  for (int j = 0; j < 3; ++j) {
    const int P = (3 * w + j) * 64 + lane, k = P / 12, sl = P - 12 * k;
    vw[j] = k * (FF * 2) + sl * 16 - j * 1024;
  }
  const u32 x_so = (mt * 128 + 32 * w) * 1024, x_lo = lb + 32 * w * 128, w_so = ustart * 64, w_lo = lb + SLOT_W + 3 * w * 1024;
  auto st1_m0 = [&](auto KC, auto J) {
    if constexpr (J == 0) dma_m0(x_lo + (KC & 3) * SLOT);
    if constexpr (J == 4) dma_m0(w_lo + (KC & 3) * SLOT);
  };
  auto st1_go = [&](auto KC, auto J) {
    if constexpr (J < 4) dma_go<J * 1024>(sX, vx[J], x_so + KC * 128); else dma_go<(J - 4) * 1024>(sW1, vw[J - 4], w_so + KC * 64 * (FF * 2));
  };
  auto stage = [&](auto KC) { sfor<7>([&](auto J) { if constexpr (J == 0 || J == 4) { st1_m0(KC, J); asm volatile("s_nop 0"); } st1_go(KC, J); }); };
  u32 eword = ld32c(sWS, WS_E, 0);
#define PRO 0
  auto stage_rt = [&](int kc) {
    dma_m0(x_lo + kc * SLOT); asm volatile("s_nop 0");
    sfor<4>([&](auto J) { dma_go<J * 1024>(sX, vx[J], x_so + kc * 128); });
    dma_m0(w_lo + kc * SLOT); asm volatile("s_nop 0");
    sfor<3>([&](auto J) { dma_go<J * 1024>(sW1, vw[J], w_so + kc * 64 * (FF * 2)); });
  };
  if constexpr (PRO == 0) { if (side == 0) { stage(ic<0>{}); stage(ic<1>{}); stage(ic<2>{}); stage(ic<3>{}); } }
  if constexpr (PRO == 1) stage_rt(side);
  if constexpr (PRO == 2) { if (side == 0) { stage(ic<0>{}); stage(ic<1>{}); } }
  {
    u64 pc;
    asm volatile("s_getpc_b64 %0" : "=s"(pc));
    const char* a;
    if (tid < 96) a = (const char*)p.x + (512 * (g8 >> 1) + 16 * r + tid / 6) * 1024 + (2 + tid % 6) * 128;
    else if (tid < 240) a = (const char*)p.w1 + (128 + 12 * r + (tid - 96) / 12) * (FF * 2) + (g8 & 1) * 1280 + ((tid - 96) % 12) * 120;
    else a = (const char*)pc + ((r * 64 + tid) % 208) * 128;
    touch(a, lb + S_DUMMY);
  }
  u32 ixp[5];
#pragma unroll
  for (int e = 0; e < 9; ++e) {
    const int l = 2047 - (8 * t8 + e);
    const u32 rb = pk_bf16(sp2((float)l)) & 0xffffu;
    const u32 ix = (l < 0 ? 641u : (l < 128 ? (u32)l : rb - 17024u)) * 2;
    if (e & 1) ixp[e >> 1] |= ix << 16; else ixp[e >> 1] = ix;
  }
#pragma unroll
  for (int e = 0; e < 5; ++e) tie(ixp[e]);
  if constexpr (PRO == 0) { if (side == 0) wait_vm<30>(); else wait_vm<2>(); }
  if constexpr (PRO == 1) wait_vm<9>();
  if constexpr (PRO == 2) { if (side == 0) wait_vm<16>(); else wait_vm<2>(); }
  tie(xr);
  STAMP(1);

  u32 epoch;
  {
    f32x2 x[4];
#pragma unroll
    for (int i = 0; i < 4; ++i) x[i] = bf2(xr[i]);
    const float x0 = asf(__builtin_amdgcn_readfirstlane(asu(x[0].x)));
    f32x2 s = {0.f, 0.f}, q = {0.f, 0.f};
#pragma unroll
    for (int i = 0; i < 4; ++i) { const f32x2 d = x[i] - sp2(x0); s += d; q = fma2(d, d, q); }
    float sm = s.x + s.y, sq = q.x + q.y;
    wave_sum2(sm, sq);
    const float md = sm * (1.0f / DM);
    const float var = fmaxf(__builtin_fmaf(-md, md, sq * (1.0f / DM)), 0.f);
    const float m1 = rbf(x0 + md);
    const float r1 = __builtin_amdgcn_rcpf(rbf(__builtin_amdgcn_sqrtf(rbf(rbf(var) + 1e-5f))));
    s = f32x2{0.f, 0.f}; q = f32x2{0.f, 0.f};
    u32x4 hv;
#pragma unroll
    for (int i = 0; i < 4; ++i) {
      const f32x2 n = bf2(pk_bf16(bf2(pk_bf16(x[i] - sp2(m1))) * sp2(r1)));
      s += n; q = fma2(n, n, q);
      hv[i] = pk_f16(n);
    }
    lds_st<u32x4>(smem, S_LN + (2 * w + side) * LN_ROW + lane * 16, hv);
    float s2 = s.x + s.y, q2 = q.x + q.y;
    wave_sum2(s2, q2);
    const float mean2 = s2 * (1.0f / DM);
    const float var2 = fmaxf(__builtin_fmaf(-mean2, mean2, q2 * (1.0f / DM)), 0.f);
    const float r2 = __builtin_amdgcn_rcpf(rbf(__builtin_amdgcn_sqrtf(rbf(rbf(var2) + 1e-5f))));
    if constexpr (PRO == 0) { if (side == 0) wait_vm<29>(); else wait_vm<1>(); }
    if constexpr (PRO == 1) wait_vm<8>();
    if constexpr (PRO == 2) { if (side == 0) wait_vm<15>(); else wait_vm<1>(); }
    tie(eword);
    epoch = __builtin_amdgcn_readfirstlane(eword) * 0x9E3779B1u + 0x7F4A7C15u;
    st128c(rWS, u32x4{asu(r1 * r2), epoch, asu(__builtin_fmaf(m1, r1, rbf(mean2)) * r2), epoch}, lane == 0 ? WS_STATS + (8 * tg + 2 * w + side) * 16 : OOB);
  }
  {
    const int ln = wid * 8 + (lane >> 3);
    st128c(rWS, u32x4{POISON, POISON, POISON, POISON}, WS_A + ((ln >> 1) * 512 + 2 * cp + (ln & 1)) * 128 + (lane & 7) * 16);
  }
  if constexpr (PRO == 1) stage_rt(2 + side);
  if constexpr (PRO == 2) { if (side == 0) { stage(ic<2>{}); stage(ic<3>{}); } }
  u32 q_lar, q_aim, q_cre, q_cim, q_cre2, q_cim2, q_ldt, q_dsk;
  u32x2 b1v;
  auto load_small = [&]() {
    const u32 vo = (64 * (w & 1) + lane) * 2, so = kd * NMODE * 2;
    const u32x4 sCr = mk_srd(p.cre, DM * NMODE * 2), sCi = mk_srd(p.cim, DM * NMODE * 2);
    q_lar = ld16(mk_srd(p.lar, DM * NMODE * 2), vo, so); q_aim = ld16(mk_srd(p.aim, DM * NMODE * 2), vo, so);
    q_cre = ld16(sCr, vo, so); q_cim = ld16(sCi, vo, so);
    q_cre2 = ld16(sCr, vo ^ 128, so); q_cim2 = ld16(sCi, vo ^ 128, so);
    q_ldt = ld16(mk_srd(p.ldt, DM * 2), 0, kd * 2); q_dsk = ld16(mk_srd(p.dsk, DM * 2), 0, kd * 2);
    b1v = ld64(mk_srd(p.b1, FF * 2), lane < 24 ? lane * 8 : OOB, ustart * 64);
  };
#define TIE_SMALL "+v"(q_lar), "+v"(q_aim), "+v"(q_cre), "+v"(q_cim), "+v"(q_cre2), "+v"(q_cim2), "+v"(q_ldt), "+v"(q_dsk), "+v"(b1v)
  f32x16 acc1[3];
  f32x4 csp[3];
  u32x4 stv;
  if (side == 0) {
    const u32 xa0 = lb + (32 * w + c31) * 128 + ((hh ^ ((c31 >> 1) & 7)) * 16);
    const u32 wa0 = lb + SLOT_W + (8 * hh + q4) * 192 + b16 * 32 + 8 * p4;
    u32 xa[2][4], wa[2];
#pragma unroll
    for (int s = 0; s < 4; ++s) { xa[0][s] = xa0 ^ (s * 32); xa[1][s] = xa[0][s] + 2 * SLOT; }
    wa[0] = wa0; wa[1] = wa0 + 2 * SLOT;
    u32x4 xf[2];
    u32x2 wl[2][3], wh[2][3];
    auto rd = [&](auto S, auto R) {
      constexpr int s = S, rr = R, buf = s & 1, kc = s >> 2, sl = kc & 3, so = (sl & 1) * SLOT, sp = s & 3;
      if constexpr (rr == 0) xf[buf] = a_ds128<so>(xa[sl >> 1][sp]);
      else {
        constexpr int u = (rr - 1) >> 1, hf = (rr - 1) & 1, off = so + u * 64 + (sp * 16 + hf * 4) * 192;
        if constexpr (hf == 0) wl[buf][u] = a_dstr<off>(wa[sl >> 1]); else wh[buf][u] = a_dstr<off>(wa[sl >> 1]);
      }
    };
    load_small();
    wait_vm<(PRO == 1 ? 19 : 33)>();
    bar();
    CSTAMP(0);
    sfor<7>([&](auto R) { rd(ic<0>{}, R); });
    sfor<32>([&](auto S) {
      constexpr int s = S, buf = s & 1, sp = s & 3, kc = s >> 2;
      constexpr bool last = s == 31, brk = sp == 2 && kc < 7;
      sfor<3>([&](auto U) {
        constexpr int u = U;
        constexpr int gi = sp == 2 ? (u == 2 ? 0 : -1) : (u == 0 ? -1 : (sp == 3 ? u : 2 * sp + 2 + u));
        constexpr int kcs = sp >= 2 ? kc + 3 : kc + 2;
        constexpr bool st = gi >= 0 && gi < 7 && kcs >= 4 && kcs < 8;
        if constexpr (u == 0) wait_lgkm<4>();
        else if constexpr (u == 1) {
          wait_lgkm<(last ? 0 : 3)>();
          if constexpr (brk) {
            constexpr int n = kc + 1;
            if constexpr (n == 1) { if constexpr (PRO != 1) wait_vm<26>(); }
            else if constexpr (n == 2) wait_vm<(PRO == 0 ? 19 : (PRO == 1 ? 9 : 16))>();
            else if constexpr (n == 3) { if constexpr (PRO != 1) wait_vm<(PRO == 0 ? 19 : 16)>(); }
            else if constexpr (n == 4) asm volatile("s_waitcnt vmcnt(7)" : TIE_SMALL :: "memory");
            else if constexpr (n > 4 && n < 7) wait_vm<7>();
            else if constexpr (n == 7) wait_vm<0>();
            CSTAMP(2 * n - 1);
            asm volatile("s_barrier" ::: "memory");
            CSTAMP(2 * n);
            if constexpr (n == 7) stv = ld128c(sWS, WS_STATS + (128 * mt + 32 * w + c31) * 16, 0);
          }
        }
        if constexpr (st && (gi == 0 || gi == 4)) st1_m0(ic<kcs>{}, ic<gi>{});
        mfma32_vv<s == 0>(acc1[u], cat4(wl[buf][u], wh[buf][u]), xf[buf]);
        if constexpr (st) st1_go(ic<kcs>{}, ic<gi>{});
        if constexpr (!last) {
          if constexpr (u == 0) sfor<3>([&](auto R) { rd(ic<s + 1>{}, R); });
          if constexpr (u == 1) sfor<2>([&](auto R) { rd(ic<s + 1>{}, ic<3 + R>{}); });
          if constexpr (u == 2) sfor<2>([&](auto R) { rd(ic<s + 1>{}, ic<5 + R>{}); });
        }
      });
    });
    asm volatile("s_nop 7\n\ts_nop 3" : "+v"(acc1[0]), "+v"(acc1[1]), "+v"(acc1[2]));
    CSTAMP(15); CDUMP(16, 0);
    STAMP(2);
    bar();
#pragma unroll
    for (int q = 0; q < 6; ++q) {
      const f32x16& a = acc1[q < 2 ? 1 : 2];
      const int o = q < 2 ? 8 + 4 * q : 4 * (q - 2);
      lds_st<f32x4>(smem, S_TB + w * 6144 + q * 1024 + lane * 16, f32x4{a[o], a[o + 1], a[o + 2], a[o + 3]});
    }
    asm volatile("s_waitcnt vmcnt(0)" : "+v"(stv)::"memory");
  } else {
    const u32 wc0 = lb + SLOT_W + (32 * (w >> 1) + 8 * g + q4) * 192 + 3 * (w & 1) * 32 + 8 * p4;
    u32x4 ones = {0x3f803f80u, 0x3f803f80u, 0x3f803f80u, 0x3f803f80u};
    tie(ones);
    u32x2 cl[3], ch[3];
    auto cs_rd = [&](auto KC) {
      const u32 a = wc0 + (KC & 3) * SLOT;
      sfor<3>([&](auto I) { cl[I] = a_dstr<I * 32>(a); ch[I] = a_dstr<I * 32 + 4 * 192>(a); });
      FENCE();
    };
    auto cs_sum = [&](auto KC) {
      FENCE();
      wait_lgkm<0>();
      sfor<3>([&](auto I) { mfma16_bf<KC == 0>(csp[I], cat4(cl[I], ch[I]), ones); });
    };
    load_small();
    bar();
    CSTAMP(0);
    cs_rd(ic<0>{});
#pragma unroll
    for (int i = 0; i < 2; ++i) {
      const u32 a = S_LN + q4 * LN_ROW + ((2 * w + i) * 64 + 16 * g + 4 * p4) * 2;
      st128c(rWS, cat4(lds_tr(smem, a), lds_tr(smem, a + 4 * LN_ROW)), WS_XN + tg * 8192 + ((2 * w + i) * 64 + lane) * 16);
    }
    cs_sum(ic<0>{});
    if constexpr (PRO == 1) wait_vm<21>();
    CSTAMP(1);
    bar();
    CSTAMP(2);
    cs_rd(ic<1>{}); cs_sum(ic<1>{});
    CSTAMP(3);
    bar();
    CSTAMP(4);
    cs_rd(ic<2>{}); cs_sum(ic<2>{});
    if constexpr (PRO == 1) wait_vm<11>();
    CSTAMP(5);
    bar();
    CSTAMP(6);
    cs_rd(ic<3>{}); cs_sum(ic<3>{});
    CSTAMP(7);
    bar();
    CSTAMP(8);
    cs_rd(ic<4>{}); cs_sum(ic<4>{});
    CSTAMP(9);
    asm volatile("s_waitcnt vmcnt(0)" : TIE_SMALL :: "memory");
    lds_st<f32x4>(smem, lane < 24 ? S_B1 + lane * 16 : S_MISC, f32x4{bflo(b1v.x), bfhi(b1v.x), bflo(b1v.y), bfhi(b1v.y)});
    bar();
    CSTAMP(10);
    st32c(rWS, epoch, t8 == 0 ? WS_LNF + tg * 4 : OOB);
    cs_rd(ic<5>{}); cs_sum(ic<5>{});
    CSTAMP(11);
    bar();
    CSTAMP(12);
    cs_rd(ic<6>{}); cs_sum(ic<6>{});
    CSTAMP(13);
    bar();
    CSTAMP(14);
    stv = ld128c(sWS, WS_STATS + (128 * mt + 32 * w + c31) * 16, 0);
    cs_rd(ic<7>{}); cs_sum(ic<7>{});
    asm volatile("s_nop 7\n\ts_nop 1" : "+v"(csp[0]), "+v"(csp[1]), "+v"(csp[2]));
#pragma unroll
    for (int i = 0; i < 3; ++i) lds_st<f32x4>(smem, c15 == 0 ? S_CSP + ((w >> 1) * 96 + (3 * (w & 1) + i) * 16 + 4 * g) * 4 : S_MISC, csp[i]);
    CSTAMP(15); CDUMP(16, 20);
    STAMP(2);
    bar();
    const int f = t8 < 96 ? t8 : 0;
    lds_st<float>(smem, t8 < 96 ? S_CS + t8 * 4 : S_MISC, lds_ld<float>(smem, S_CSP + f * 4) + lds_ld<float>(smem, S_CSP + 384 + f * 4));
    asm volatile("s_waitcnt vmcnt(0)" : "+v"(stv)::"memory");
  }
#pragma unroll 1
  for (int it = 0; it < (1 << 16); ++it) {
    if (__all(stv.y == epoch && stv.w == epoch)) break;
    __builtin_amdgcn_s_sleep(1);
    stv = ld128c(sWS, WS_STATS + (128 * mt + 32 * w + c31) * 16, 0);
    asm volatile("s_waitcnt vmcnt(0)" : "+v"(stv)::"memory");
  }
  bar();
  CSTAMP(0);
  STAMP(3);

  {
    f32x4 av[6];
    if (side) {
#pragma unroll
      for (int q = 0; q < 6; ++q) av[q] = lds_ld<f32x4>(smem, S_TB + w * 6144 + q * 1024 + lane * 16);
    } else {
#pragma unroll
      for (int q = 0; q < 6; ++q) {
        const f32x16& a = acc1[q >> 2];
        const int o = 4 * (q & 3);
        av[q] = f32x4{a[o], a[o + 1], a[o + 2], a[o + 3]};
      }
    }
    const f32x2 al = sp2(asf(stv.x)), nb = sp2(-asf(stv.z));
    const u32 hso = WS_H + ((mt * 4 + w) * NUNIT + ustart) * 2048 + 3 * side * 1024 + lane * 16;
    const u32 cb = hh * 16 + 3 * side * 64;
#pragma unroll
    for (int i = 0; i < 3; ++i) {
      u32x4 o;
#pragma unroll
      for (int q = 0; q < 2; ++q) {
        const f32x4 bi = lds_ld<f32x4>(smem, S_B1 + cb + (16 * i + 8 * q) * 4), cs = lds_ld<f32x4>(smem, S_CS + cb + (16 * i + 8 * q) * 4);
        const f32x4 a = av[2 * i + q];
        const f32x2 p0 = fma2(al, f32x2{a.x, a.y}, fma2(nb, f32x2{cs.x, cs.y}, f32x2{bi.x, bi.y}));
        const f32x2 p1 = fma2(al, f32x2{a.z, a.w}, fma2(nb, f32x2{cs.z, cs.w}, f32x2{bi.z, bi.w}));
        o[2 * q] = pk_bf16(gelu2(p0));
        o[2 * q + 1] = pk_bf16(gelu2(p1));
      }
      st128c(rWS, o, ustart + ((3 * side + i) >> 1) < unext ? hso + i * 1024 : OOB);
    }
  }
  CSTAMP(1);
  u32 lnf = ld32c(sWS, WS_LNF + t8 * 4, 0);
  {
    const int ix = tid < 480 ? tid : tid - 480, ri = 15 * r + (ix >> 5);
    touch((const char*)p.w2 + (ri < 256 ? 448 + ri : 896 + ri) * 1024 + (ix & 31) * 32, lb + S_DUMMY);
  }
  st32c(rWS, epoch, (b == 0 && tid == 0) ? WS_E : OOB);
  STAMP(4);
  const u32 vw2 = (lane >> 3) * 1024 + ((((lane & 7) >> 1) ^ ((lane >> 3) & 2)) * 32) + (lane & 1) * 16;
  const u32 vw2p[2] = {vw2, vw2 + 7168};
  const u32 vw2z[2] = {kh ? OOB : vw2p[0], kh ? OOB : vw2p[1]};
  const u32 w2_lo = lb + kh * W2_HALF + 2 * mh * 1024, w2_so = (kh * 22 * 32 + 16 * mh) * 1024 + nt2 * 128;
  auto st2_piece = [&](auto N) {
    constexpr int j = N >> 1, i = N & 1;
    FENCE();
    if constexpr (i == 0) { dma_m0(w2_lo + (j % W2_NS) * 4096); asm volatile("s_nop 0"); }
    dma_go<i * 1024>(sW2, j == 21 ? vw2z[i] : vw2p[i], w2_so + j * 32 * 1024);
    FENCE();
  };

  float inv_sk;
  {
    const int mh2 = w & 1;
    const float dt = rbf(expf(bf1(q_ldt)));
    const float zr = -rbf(expf(bf1(q_lar))) * dt, zi = bf1(q_aim) * dt;
    f32x2 C = f32x2{bf1(q_cre), bf1(q_cim)} * sp2(2.0f * dt);
    const float bd = wave_sum((__builtin_fabsf(C.x) + __builtin_fabsf(C.y)) + (__builtin_fabsf(bf1(q_cre2)) + __builtin_fabsf(bf1(q_cim2))) * (2.0f * dt));
    int se = 267 - (int)((asu(bd) >> 23) & 0xff);
    se = se < 1 ? 1 : (se > 253 ? 253 : se);
    C *= sp2(asf((u32)se << 23));
    inv_sk = asf((u32)(254 - se) << 23);
    const float e1 = zr * 1.44269504089f;
    const float ph = zi * 0.15915494309f;
    const float pl = __builtin_fmaf(zi, 0.15915494309f, -ph) + zi * 6.4206383e-9f;
    auto cexp = [&](float pw) {
      const float m = __builtin_amdgcn_exp2f(e1 * pw);
      const float a = __builtin_amdgcn_fractf(ph * pw) + pl * pw;
      return f32x2{__builtin_amdgcn_cosf(a), __builtin_amdgcn_sinf(a)} * sp2(m);
    };
    CSTAMP(2);
    const u32 tl = S_TAB + kch * TAB_CH + (64 * mh2 + lane) * 4;
    auto putX = [&](int row, f32x2 v) { lds_st<u32>(smem, tl + (16 + row) * TAB_ROW, pk_f16(v)); };
    if (side == 0) {
      f32x2 P[8], E[3], CE[8];
      P[1] = cexp(1.f); E[0] = cexp(128.f);
      CE[0] = C;
      cmul2(P[2], P[1], P[1], E[1], E[0], E[0]);
      cmul2(CE[1], C, E[0], P[3], P[2], P[1]);
      cmul2(P[4], P[2], P[2], E[2], E[1], E[1]);
      cmul2(CE[2], CE[0], E[1], CE[3], CE[1], E[1]);
      cmul2(P[5], P[4], P[1], P[6], P[4], P[2]);
      cmul2(CE[4], CE[0], E[2], CE[5], CE[1], E[2]);
      cmul2(CE[6], CE[2], E[2], CE[7], CE[3], E[2]);
      P[7] = cmul(P[4], P[3]);
      putX(0, CE[0]); putX(8, CE[1]);
#pragma unroll
      for (int i = 1; i < 8; ++i) {
        f32x2 a, c;
        cmul2(a, CE[0], P[i], c, CE[1], P[i]);
        putX(i, a); putX(8 + i, c);
      }
      putX(16, CE[2]); putX(20, CE[3]);
#pragma unroll
      for (int i = 1; i < 4; ++i) {
        f32x2 a, c;
        cmul2(a, CE[2], P[2 * i], c, CE[3], P[2 * i]);
        putX(16 + i, a); putX(20 + i, c);
      }
      putX(24, CE[4]); putX(26, CE[5]); putX(28, CE[6]); putX(30, CE[7]);
      f32x2 a, c;
      cmul2(a, CE[4], P[4], c, CE[5], P[4]); putX(25, a); putX(27, c);
      cmul2(a, CE[6], P[4], c, CE[7], P[4]); putX(29, a); putX(31, c);
    } else {
      f32x2 Y[16], E[5], CE[9];
      st2_piece(ic<0>{});
      Y[1] = cexp(8.f); E[0] = cexp(128.f);
      Y[0] = f32x2{1.f, 0.f};
      st2_piece(ic<1>{});
      auto putY = [&](int i) { lds_st<u32>(smem, tl + i * TAB_ROW, pk_f16_conj(Y[i])); };
      cmul2(Y[2], Y[1], Y[1], E[1], E[0], E[0]);
      st2_piece(ic<2>{});
      cmul2(Y[3], Y[2], Y[1], Y[4], Y[2], Y[2]);
      putY(0); putY(1); putY(2);
      st2_piece(ic<3>{});
      cmul2(E[2], E[1], E[1], Y[5], Y[4], Y[1]);
      cmul2(Y[6], Y[4], Y[2], Y[7], Y[4], Y[3]);
      putY(3); putY(4);
      st2_piece(ic<4>{});
      cmul2(Y[8], Y[4], Y[4], E[3], E[2], E[2]);
      putY(5); putY(6); putY(7);
      st2_piece(ic<5>{});
      cmul2(Y[9], Y[8], Y[1], Y[10], Y[8], Y[2]);
      cmul2(E[4], E[3], E[3], CE[0], C, E[3]);
      putY(8);
      st2_piece(ic<6>{});
      cmul2(Y[11], Y[8], Y[3], Y[12], Y[8], Y[4]);
      putY(9); putY(10);
      st2_piece(ic<7>{});
      cmul2(Y[13], Y[8], Y[5], Y[14], Y[8], Y[6]);
      putY(11); putY(12);
      st2_piece(ic<8>{});
      cmul2(Y[15], Y[8], Y[7], CE[1], CE[0], E[0]);
      putY(13); putY(14);
      st2_piece(ic<9>{});
      cmul2(CE[2], CE[0], E[1], CE[8], C, E[4]);
      putY(15); putX(32, CE[0]); putX(33, CE[1]);
      st2_piece(ic<10>{});
      cmul2(CE[3], CE[1], E[1], CE[4], CE[0], E[2]);
      putX(34, CE[2]); putX(40, CE[8]);
      st2_piece(ic<11>{});
      cmul2(CE[5], CE[1], E[2], CE[6], CE[2], E[2]);
      putX(35, CE[3]); putX(36, CE[4]);
      st2_piece(ic<12>{});
      CE[7] = cmul(CE[3], E[2]);
      putX(37, CE[5]); putX(38, CE[6]);
      st2_piece(ic<13>{});
      putX(39, CE[7]);
      st2_piece(ic<14>{});
    }
    CSTAMP(3);
    if (side) wait_vm<17>(); else wait_vm<2>();
    tie(lnf);
    st32c(rWS, epoch, lane == 0 ? WS_HF + (((mt * 4 + w) * 16 + nt) * 2 + side) * 4 : OOB);
    spin_flag(sWS, WS_LNF + t8 * 4, epoch, lnf, true);
    u32x4 ug = ld128c(sWS, WS_XN + t8 * 8192 + cp * 32 + side * 16, 0);
    if (side) st2_piece(ic<15>{});
    CSTAMP(4);
    bar();
    CSTAMP(5);
    const int tile = side ? 2 : mh2;
    const u32 kc = S_KC + kch * KC_CH;
    if (!(side && mh2)) {
      const u32 rd = S_TAB + kch * TAB_CH + c15 * TAB_ROW + g * 16;
      const u32 ra = S_TAB + kch * TAB_CH + (16 + min(16 * tile + c15, 40)) * TAB_ROW + g * 16;
      u32x4 yf[8], xf[8];
#pragma unroll
      for (int s = 0; s < 8; ++s) { yf[s] = lds_ld<u32x4>(smem, rd + s * 64); xf[s] = lds_ld<u32x4>(smem, ra + s * 64); }
      f32x4 a0 = {0, 0, 0, 0}, a1 = {0, 0, 0, 0};
#pragma unroll
      for (int s = 0; s < 4; ++s) { a0 = mfma_h(xf[2 * s], yf[2 * s], a0); a1 = mfma_h(xf[2 * s + 1], yf[2 * s + 1], a1); }
      const f32x4 v = a0 + a1;
      const u32 h01 = pk_f16(f32x2{v[0], v[1]}), h23 = pk_f16(f32x2{v[2], v[3]});
      if (tile == 0) {
        lds_st<u32x2>(smem, kc + (128 * (g >> 1) + 8 * c15 + 4 * (g & 1)) * 2, u32x2{h01, h23});
      } else if (tile == 1) {
        const u32 t1a = g < 2 ? 256 + 64 * g + 4 * c15 : 384 + 64 * (g - 2) + 2 * c15;
        lds_st<u32>(smem, kc + t1a * 2, h01);
        lds_st<u32>(smem, kc + (t1a + (g < 2 ? 2 : 32)) * 2, h23);
      } else {
        const u32 t2 = g < 2 ? 512 + 64 * g + c15 : 644, st2 = g < 2 ? 16 : 0;
        lds_st<u16>(smem, kc + ((g == 2 && c15 == 0) ? 640 : t2) * 2, (u16)h01);
        lds_st<u16>(smem, kc + (t2 + st2) * 2, (u16)(h01 >> 16));
        lds_st<u16>(smem, kc + (t2 + 2 * st2) * 2, (u16)h23);
        lds_st<u16>(smem, kc + (t2 + 3 * st2) * 2, (u16)(h23 >> 16));
      }
    } else if (lane == 0) lds_st<u16>(smem, kc + 641 * 2, (u16)0);
    CSTAMP(6);
    bar();
    CSTAMP(7);
    if (side) wait_vm<1>(); else wait_vm<0>();
    tie(ug);
    {
      u32 e[9];
#pragma unroll
      for (int i = 0; i < 9; ++i) e[i] = lds_ld<u16>(smem, S_KC + side * KC_CH + ((i & 1) ? ixp[i >> 1] >> 16 : ixp[i >> 1] & 0xffffu));
      const u32x4 a0 = {e[0] | (e[1] << 16), e[2] | (e[3] << 16), e[4] | (e[5] << 16), e[6] | (e[7] << 16)};
      const u32x4 a1 = {e[1] | (e[2] << 16), e[3] | (e[4] << 16), e[5] | (e[6] << 16), e[7] | (e[8] << 16)};
      lds_st<u32x4>(smem, S_G + side * G_CH + t8 * 16, a0);
      lds_st<u32x4>(smem, S_G + side * G_CH + G_PAR + t8 * 16, a1);
      const u32x4 z4 = {0, 0, 0, 0};
      if (t8 < 4) lds_st<u32x4>(smem, S_G + side * G_CH + (t8 >> 1) * G_PAR + 4096 + (t8 & 1) * 16, z4);
      if (t8 < 30) lds_st<u32x4>(smem, S_U + side * U_CH + t8 * 16, z4);
      lds_st<u32x4>(smem, S_U + side * U_CH + 480 + t8 * 16, ug);
    }
    CSTAMP(8);
    bar();
    CSTAMP(9);
    if (side) CDUMP(10, 48); else CDUMP(10, 38);
  }
  STAMP(5);

  const int ch0 = 64 * nt2 + 32 * kh + 4 * hh;
  const u32 xto = (64 * mt2 + (t8 >> 3)) * 1024 + nt2 * 128 + (t8 & 7) * 16;
  f32x16 acc[2];
  if (side == 0) {
    const int par = w & 1;
    const u32 ub = lb + S_U + kch * U_CH + 64 - 64 * par + 32 * c15 + 16 * g;
    const u32 gb0 = lb + S_G + kch * G_CH + ((c15 & 1) ? 0 : G_PAR) + 4 * (1015 - (c15 >> 1) - 16 * par + 4 * g - 32 * 31);
    const u32 gb[4] = {gb0, gb0 + 1024, gb0 + 2048, gb0 + 3072};
    u32x4 bq[32];
    u32x2 al[32], ah[32];
    f32x4 y[8];
    auto rdB = [&](auto M, auto T) { constexpr int q = 4 * T - M + 3; bq[q] = a_ds128<128 * q>(ub); };
    auto rdA = [&](auto M, auto H) {
      constexpr int k = (31 - M) >> 3, o = 32 * ((31 - M) & 7);
      if constexpr (H == 0) al[M] = a_ds2x32<o, o + 1>(gb[k]); else ah[M] = a_ds2x32<o + 2, o + 3>(gb[k]);
    };
    sfor<2>([&](auto M) { sfor<8>([&](auto T) { rdB(M, T); }); rdA(M, ic<0>{}); rdA(M, ic<1>{}); });
    sfor<32>([&](auto M) {
      constexpr int m = M, t0 = m >> 2, n = 8 - t0;
      wait_lgkm<(m <= 2 ? 10 : (m == 3 ? 4 : (m == 4 ? 6 : (2 * (31 - m) < 8 ? 2 * (31 - m) : 8))))>();
      sfor<n>([&](auto I) {
        constexpr int i = I, t = t0 + i;
        mfma16_h<m == 0>(y[t], cat4(al[m], ah[m]), bq[4 * t - m + 3]);
        if constexpr (m < 2) {
          rdB(ic<m + 2>{}, I);
          if constexpr (i >= 6) rdA(ic<m + 2>{}, ic<i - 6>{});
        } else if constexpr (m <= 4) {
          if constexpr (i < 4) rdA(ic<2 * m + (i >> 1)>{}, ic<i & 1>{});
        } else if constexpr (m + 5 < 32) {
          if constexpr (i < 2) rdA(ic<m + 5>{}, I);
        }
      });
    });
    asm volatile("s_nop 7\n\ts_nop 1" : "+v"(y[0]), "+v"(y[1]), "+v"(y[2]), "+v"(y[3]), "+v"(y[4]), "+v"(y[5]), "+v"(y[6]), "+v"(y[7]));
    STAMP(6);
    bar();
    auto send = [&](auto PAR) {
      FENCE();
#pragma unroll
      for (int i = 0; i < 4; ++i) lds_st<f32x4>(smem, S_EX + w * 4096 + i * 1024 + lane * 16, y[4 * (1 - PAR) + i]);
      FENCE();
    };
    if (par) send(ic<1>{}); else send(ic<0>{});
    bar();
    const float dsk = bf1(q_dsk);
    u32x2 ao[4];
    auto finish = [&](auto PAR) {
      FENCE();
#pragma unroll
      for (int i = 0; i < 4; ++i) {
        const f32x4 o = lds_ld<f32x4>(smem, S_EX + (w ^ 1) * 4096 + i * 1024 + lane * 16);
        const u32x2 uu = lds_ld<u32x2>(smem, S_U + kch * U_CH + (240 + 256 * (4 * PAR + i) + 16 * c15 + 4 * g) * 2);
        const f16x2 u01 = ash2(uu.x), u23 = ash2(uu.y);
        const float uf[4] = {(float)u01.x, (float)u01.y, (float)u23.x, (float)u23.y};
        float v[4];
#pragma unroll
        for (int e = 0; e < 4; ++e) v[e] = ffma1(uf[e], dsk, fmul1(fadd1(y[4 * PAR + i][e], o[e]), inv_sk));
        ao[i] = u32x2{cvt_bf2(v[0], v[1]), cvt_bf2(v[2], v[3])};
      }
      FENCE();
    };
    if (par) finish(ic<1>{}); else finish(ic<0>{});
#pragma unroll
    for (int i = 0; i < 4; ++i) {
      u32x2 o = ao[i];
      o.x = o.x == POISON ? 0xffc0ffc0u : o.x;
      o.y = o.y == POISON ? 0xffc0ffc0u : o.y;
      st64c(rWS, o, WS_A + ((4 * (4 * par + i) + (c15 >> 2)) * 512 + kd) * 128 + ((c15 & 3) * 16 + 4 * g) * 2);
    }
    STAMP(7);
    u32x4 xt0 = ld128(sX, xto, 0), xt1 = ld128(sX, xto + 32 * 1024, 0);
    u32 b2w = ld16(mk_srd(p.b2, DM * 2), t8 < 64 ? (64 * nt2 + t8) * 2 : OOB, 0);
    bar();
    sfor<16>([&](auto N) { st2_piece(ic<28 + N>{}); });
    const u32 ato = WS_A + (mt2 * 512 + nt2 * 64) * 128 + t8 * 16;
    u32x4 at0 = ld128c(sWS, ato, 0), at1 = ld128c(sWS, ato + 4096, 0);
    asm volatile("s_waitcnt vmcnt(10)" : "+v"(xt0), "+v"(xt1), "+v"(b2w)::"memory");
    bar();
    const int row0 = t8 >> 3, sl = t8 & 7;
    {
      const u32 o = S_XT + row0 * 128 + ((sl ^ (row0 & 7)) * 16);
      lds_st<u32x4>(smem, o, xt0);
      lds_st<u32x4>(smem, o + 32 * 128, xt1);
      lds_st<float>(smem, t8 < 64 ? S_B2 + t8 * 4 : S_MISC, bf1(b2w));
    }
    wait_vm<2>();
    bar();
    asm volatile("s_waitcnt vmcnt(0)" : "+v"(at0), "+v"(at1)::"memory");
#pragma unroll 1
    for (int it = 0; it < (1 << 16); ++it) {
      const u32 m0 = max(max(at0.x, at0.y), max(at0.z, at0.w)), m1 = max(max(at1.x, at1.y), max(at1.z, at1.w));
      if (!__any(max(m0, m1) == POISON)) break;
      at0 = ld128c(sWS, ato, 0); at1 = ld128c(sWS, ato + 4096, 0);
      asm volatile("s_waitcnt vmcnt(0)" : "+v"(at0), "+v"(at1)::"memory");
    }
    {
      const u32 o = S_AT + row0 * 128 + (((sl >> 1) ^ (row0 & 2)) * 32) + (sl & 1) * 16;
      lds_st<u32x4>(smem, o, at0);
      lds_st<u32x4>(smem, o + 32 * 128, at1);
    }
    bar();
  } else {
    u32x4 hf[22][2];
    const u32 hb = WS_H + ((mt2 * 2 + mh) * NUNIT + kh * 22) * 2048;
    const u32 hvo = lane * 16, hvz = kh ? OOB : hvo;
    auto ldh = [&](auto J, auto S) { hf[J][S] = ld128c(sWS, J == 21 ? hvz : hvo, hb + (J * 2 + S) * 1024); };
    sfor<12>([&](auto N) { st2_piece(ic<16 + N>{}); });
    {
      const u32 hfo = WS_HF + (((mt2 >> 1) * 4 + 2 * (mt2 & 1) + mh) * 32 + c31) * 4;
      u32 hfl = ld32c(sWS, hfo, 0);
      asm volatile("s_waitcnt vmcnt(0)" : "+v"(hfl)::"memory");
      spin_flag(sWS, hfo, epoch, hfl, kh ? c31 >= 16 : c31 <= 17);
    }
    sfor<4>([&](auto J) { ldh(J, ic<0>{}); ldh(J, ic<1>{}); });
    STAMP(6);
    bar();
    sfor<3>([&](auto J) { ldh(ic<4 + J>{}, ic<0>{}); ldh(ic<4 + J>{}, ic<1>{}); });
    bar();
    sfor<G2_LEAD - 7>([&](auto J) { ldh(ic<7 + J>{}, ic<0>{}); ldh(ic<7 + J>{}, ic<1>{}); });
    STAMP(7);
    u32 wa[2];
#pragma unroll
    for (int n = 0; n < 2; ++n) wa[n] = lb + kh * W2_HALF + (4 * hh + q4) * 128 + (((2 * n + b16) ^ (q4 & 2)) * 32) + 8 * p4;
    u32x2 wl[2][4], wh[2][4];
    auto rd = [&](auto J, auto I, auto H) {
      constexpr int j = J, i = I, off = (j % W2_NS) * 4096 + ((i >> 1) * 16 + H * 8) * 128;
      if constexpr (H == 0) wl[j & 1][i] = a_dstr<off>(wa[i & 1]); else wh[j & 1][i] = a_dstr<off>(wa[i & 1]);
    };
    sfor<4>([&](auto I) { rd(ic<0>{}, I, ic<0>{}); rd(ic<0>{}, I, ic<1>{}); });
    sfor<22>([&](auto J) {
      constexpr int j = J;
      constexpr bool last = j == 21, brk = j == 8 || j == 13 || j == 17;
      if constexpr (brk) bar();
      wait_vm<g2_wait_h(j)>();
      tie(hf[j][0]); tie(hf[j][1]);
      sfor<4>([&](auto I) {
        constexpr int i = I;
        if constexpr (!brk) wait_lgkm<(last ? 6 - 2 * i : 6)>();
        mfma32_vv<j == 0 && i < 2>(acc[i & 1], cat4(wl[j & 1][i], wh[j & 1][i]), hf[j][i >> 1]);
        if constexpr ((i & 1) == 0 && j + G2_LEAD <= 21) ldh(ic<j + G2_LEAD>{}, ic<(i >> 1)>{});
        if constexpr (!last) { rd(ic<j + 1>{}, I, ic<0>{}); rd(ic<j + 1>{}, I, ic<1>{}); }
      });
    });
    asm volatile("s_nop 7\n\ts_nop 3" : "+v"(acc[0]), "+v"(acc[1]));
    STAMP(8);
    auto send2 = [&](auto KH) {
      FENCE();
#pragma unroll
      for (int q = 0; q < 4; ++q) {
        const f32x16& as = acc[1 - KH];
        lds_st<f32x4>(smem, S_EX + w * 4096 + q * 1024 + lane * 16, f32x4{as[4 * q], as[4 * q + 1], as[4 * q + 2], as[4 * q + 3]});
      }
      FENCE();
    };
    if (kh) send2(ic<1>{}); else send2(ic<0>{});
    bar();
    const int tl = 32 * mh + c31;
    auto fin2 = [&](auto KH) {
      FENCE();
#pragma unroll
      for (int q = 0; q < 4; ++q) {
        const f32x16& ak = acc[KH];
        const f32x4 v = f32x4{ak[4 * q], ak[4 * q + 1], ak[4 * q + 2], ak[4 * q + 3]} + lds_ld<f32x4>(smem, S_EX + (w ^ 2) * 4096 + q * 1024 + lane * 16);
        const f32x4 bb = lds_ld<f32x4>(smem, S_B2 + (32 * KH + 8 * q + 4 * hh) * 4);
        const int row = 32 * KH + 8 * q + 4 * hh + q4;
        const u32x2 av = lds_tr(smem, S_AT + row * 128 + (((2 * mh + b16) ^ (row & 2)) * 32) + 8 * p4);
        const u32 po = tl * 128 + (((4 * KH + q) ^ (tl & 7)) * 16) + hh * 8;
        const u32x2 xv = lds_ld<u32x2>(smem, S_XT + po);
        const f32x2 o0 = (bf2(xv.x) + bf2(av.x)) + (f32x2{v[0], v[1]} + f32x2{bb.x, bb.y}), o1 = (bf2(xv.y) + bf2(av.y)) + (f32x2{v[2], v[3]} + f32x2{bb.z, bb.w});
        lds_st<u32x2>(smem, S_OT + po, u32x2{pk_bf16(o0), pk_bf16(o1)});
      }
      FENCE();
    };
    if (kh) fin2(ic<1>{}); else fin2(ic<0>{});
  }
  bar();
  {
    const int row = tid >> 3, sl = tid & 7;
    const u32x4 o = lds_ld<u32x4>(smem, S_OT + row * 128 + ((sl ^ (row & 7)) * 16));
    __builtin_amdgcn_raw_buffer_store_b128(o, rOUT, (64 * mt2 + row) * 1024 + nt2 * 128 + sl * 16, 0, 0);
  }
  STAMP(9);
}

extern "C" void kernel_launch(void* const* d_in, const int* in_sizes, int n_in, void* d_out, int out_size, void* d_ws,
                              size_t ws_size, hipStream_t stream) {
  Params p;
  p.x = (const u16*)d_in[0]; p.lar = (const u16*)d_in[1]; p.aim = (const u16*)d_in[2]; p.cre = (const u16*)d_in[3];
  p.cim = (const u16*)d_in[4]; p.ldt = (const u16*)d_in[5]; p.dsk = (const u16*)d_in[6];
  p.w1 = (const u16*)d_in[8]; p.b1 = (const u16*)d_in[9]; p.w2 = (const u16*)d_in[10]; p.b2 = (const u16*)d_in[11];
  p.out = (u16*)d_out; p.ws = (char*)d_ws;
  s4d_mixer_fused<<<256, 512, 0, stream>>>(p);
}
```

```cpp
#include <hip/hip_runtime.h>
#include <stdint.h>
#include <utility>

namespace {
typedef unsigned short u16;
typedef unsigned u32;
typedef unsigned long long u64;
typedef float f32x2 __attribute__((ext_vector_type(2)));
typedef float f32x4 __attribute__((ext_vector_type(4)));
typedef float f32x16 __attribute__((ext_vector_type(16)));
typedef u32 u32x2 __attribute__((ext_vector_type(2)));
typedef u32 u32x4 __attribute__((ext_vector_type(4)));
typedef short s16x4 __attribute__((ext_vector_type(4)));
typedef __bf16 bf16x2 __attribute__((ext_vector_type(2)));
typedef _Float16 f16x2 __attribute__((ext_vector_type(2)));
typedef _Float16 f16x8 __attribute__((ext_vector_type(8)));
#define LAS __attribute__((address_space(3)))
#define DEV __device__ __forceinline__
typedef __amdgpu_buffer_rsrc_t rsrc_t;

constexpr int SEQ = 2048, DM = 512, NMODE = 128, FF = 1376, NUNIT = 43;
constexpr u32 WS_E = 0, WS_LNF = 0x1000, WS_HF = 0x2000  , WS_STATS = 0x8000, WS_XN = 0x10000, WS_A = 0x210000, WS_H = 0x410000,
              WS_END = WS_H + 64 * NUNIT * 2048, WS_STAMP = 0xA00000;
constexpr u32 SLOT = 28672, SLOT_W = 16384;
constexpr u32 W2_NS = 14, W2_HALF = W2_NS * 4096;
constexpr u32 LDS_BYTES_ = 163840;
constexpr u32 S0 = 114688;
constexpr u32 S_DUMMY = LDS_BYTES_ - 256;
constexpr u32 S_LN = S0, LN_ROW = 1088;
constexpr u32 TAB_ROW = 544, TAB_CH = 57 * TAB_ROW;
constexpr u32 S_TAB = LDS_BYTES_ - 2 * TAB_CH - 1872;
constexpr u32 S_TB = 32768;
constexpr u32 S_KC = S_TAB - 2592, KC_CH = 1296;
constexpr u32 S_CSP = LDS_BYTES_ - 1872;
constexpr u32 S_B1 = S_CSP + 768;
constexpr u32 S_MISC = S_B1 + 384;
constexpr u32 S_CS = S_MISC + 64;
constexpr u32 S_G = S0, G_PAR = 4160, G_CH = 8320;
constexpr u32 S_U = S0 + 16640, U_CH = 4608;
constexpr u32 S_EX = S0;
constexpr u32 S_AT = S0 + 16384, S_XT = S0 + 24576, S_OT = S0 + 32768, S_B2 = S0 + 40960;
constexpr u32 LDS_BYTES = 163840;
constexpr u32 OOB = 0x80000000u;
constexpr u32 POISON = 0xffffffffu;

#define STAMP(k) do {} while (0)
#define CSTAMP(i) do {} while (0)
#define PSTAMP(i) do {} while (0)
#define PDUMP() do {} while (0)
#define CDUMP(n, base) do {} while (0)

struct Params {
  const u16 *x, *lar, *aim, *cre, *cim, *ldt, *dsk, *w1, *b1, *w2, *b2;
  u16* out;
  char* ws;
};

DEV float asf(u32 v) { return __builtin_bit_cast(float, v); }
DEV u32 asu(float v) { return __builtin_bit_cast(u32, v); }
DEV f16x2 ash2(u32 v) { return __builtin_bit_cast(f16x2, v); }
DEV float bflo(u32 v) { return asf(v << 16); }
DEV float bfhi(u32 v) { return asf(v & 0xffff0000u); }
DEV f32x2 bf2(u32 v) { return f32x2{bflo(v), bfhi(v)}; }
DEV u32 pk_bf16(f32x2 v) { return __builtin_bit_cast(u32, __builtin_convertvector(v, bf16x2)); }
DEV u32 pk_f16(f32x2 v) { return __builtin_bit_cast(u32, __builtin_convertvector(v, f16x2)); }
DEV float rbf(float a) { return bflo(pk_bf16(f32x2{a, a})); }
DEV float bf1(u32 v) { return asf(v << 16); }
DEV f32x2 sp2(float a) { return f32x2{a, a}; }
DEV f32x2 fma2(f32x2 a, f32x2 b, f32x2 c) { return __builtin_elementwise_fma(a, b, c); }

template <int N> DEV void wait_vm() { asm volatile("s_waitcnt vmcnt(%0)" ::"n"(N) : "memory"); }
template <int N> DEV void wait_lgkm() { asm volatile("s_waitcnt lgkmcnt(%0)" ::"n"(N)); }
DEV void bar() { asm volatile("s_waitcnt lgkmcnt(0)\n\ts_barrier" ::: "memory"); }
DEV u32x4 mk_srd(const void* base, u32 bytes) {
  const u64 b = (u64)base;
  u32x4 s;
  s.x = __builtin_amdgcn_readfirstlane((u32)b);
  s.y = __builtin_amdgcn_readfirstlane((u32)(b >> 32)) & 0xffffu;
  s.z = bytes;
  s.w = 0x00020000u;
  return s;
}
DEV rsrc_t mk_rsrc(const void* base, u32 bytes) { return __builtin_amdgcn_make_buffer_rsrc((void*)base, 0, bytes, 0x00020000); }

#define BLOAD(INS, TY, NAME, FLAGS, CON)                                                                     \
  DEV TY NAME(u32x4 srd, u32 voff, u32 soff) {                                                               \
    TY r;                                                                                                    \
    asm volatile(INS " %0, %1, %2, %3 offen" FLAGS : CON(r) : "v"(voff), "s"(srd), "s"(soff) : "memory");     \
    return r;                                                                                                \
  }
BLOAD("buffer_load_dwordx4", u32x4, ld128, "", "=v")
BLOAD("buffer_load_dwordx4", u32x4, ld128c, " sc1", "=v")
BLOAD("buffer_load_dwordx4", u32x4, ld128c_a, " sc1", "=a")
BLOAD("buffer_load_dwordx2", u32x2, ld64, "", "=v")
BLOAD("buffer_load_dword", u32, ld32c, " sc1", "=v")
BLOAD("buffer_load_ushort", u32, ld16, "", "=v")
DEV void touch(const void* a, u32 lds_dummy) { asm volatile("s_mov_b32 m0, %1\n\ts_nop 0\n\tglobal_load_lds_dword %0, off" ::"v"(a), "s"(lds_dummy) : "memory"); }
template <class T> DEV void tie(T& v) { asm volatile("" : "+v"(v)); }

DEV void dma_m0(u32 lds_addr) { asm volatile("s_mov_b32 m0, %0" ::"s"(lds_addr) : "memory"); }
template <int IOFF> DEV void dma_go(u32x4 srd, u32 voff, u32 soff) {
  asm volatile("buffer_load_dwordx4 %0, %1, %2 offen offset:%3 lds" ::"v"(voff), "s"(srd), "s"(soff), "n"(IOFF) : "memory");
}
DEV void st128c(rsrc_t r, u32x4 v, u32 voff) { __builtin_amdgcn_raw_buffer_store_b128(v, r, voff, 0, 16); }
DEV void st64c(rsrc_t r, u32x2 v, u32 voff) { __builtin_amdgcn_raw_buffer_store_b64(v, r, voff, 0, 16); }
DEV void st32c(rsrc_t r, u32 v, u32 voff) { __builtin_amdgcn_raw_buffer_store_b32(v, r, voff, 0, 16); }
DEV void st64(rsrc_t r, u32x2 v, u32 voff) { __builtin_amdgcn_raw_buffer_store_b64(v, r, voff, 0, 0); }

template <class T> DEV T lds_ld(const char* smem, u32 off) { return *(const LAS T*)(smem + off); }
template <class T> DEV void lds_st(char* smem, u32 off, T v) { *(LAS T*)(smem + off) = v; }
DEV u32x2 lds_tr(const char* smem, u32 off) { return __builtin_bit_cast(u32x2, __builtin_amdgcn_ds_read_tr16_b64_v4i16((LAS s16x4*)(smem + off))); }

template <int I> using ic = std::integral_constant<int, I>;
template <class F, int... Is> DEV void sfor_impl(F&& f, std::integer_sequence<int, Is...>) { (f(ic<Is>{}), ...); }
template <int N, class F> DEV void sfor(F&& f) { sfor_impl(f, std::make_integer_sequence<int, N>{}); }
template <int OFF> DEV u32x4 a_ds128(u32 addr) { u32x4 r; asm volatile("ds_read_b128 %0, %1 offset:%2" : "=v"(r) : "v"(addr), "n"(OFF)); return r; }
template <int OFF> DEV u32x2 a_dstr(u32 addr) { u32x2 r; asm volatile("ds_read_b64_tr_b16 %0, %1 offset:%2" : "=v"(r) : "v"(addr), "n"(OFF)); return r; }
template <int O0, int O1> DEV u32x2 a_ds2x32(u32 addr) { u32x2 r; asm volatile("ds_read2_b32 %0, %1 offset0:%2 offset1:%3" : "=v"(r) : "v"(addr), "n"(O0), "n"(O1)); return r; }
DEV u32x4 cat4(u32x2 a, u32x2 b) { return __builtin_shufflevector(a, b, 0, 1, 2, 3); }
template <bool INIT> DEV void mfma32_vv(f32x16& c, u32x4 a, u32x4 b) {
  if constexpr (INIT) asm volatile("v_mfma_f32_32x32x16_bf16 %0, %1, %2, 0" : "=&v"(c) : "v"(a), "v"(b));
  else asm volatile("v_mfma_f32_32x32x16_bf16 %0, %1, %2, %0" : "+v"(c) : "v"(a), "v"(b));
}
template <bool INIT> DEV void mfma32_va(f32x16& c, u32x4 a, u32x4 b) {
  if constexpr (INIT) asm volatile("v_mfma_f32_32x32x16_bf16 %0, %1, %2, 0" : "=&v"(c) : "v"(a), "a"(b));
  else asm volatile("v_mfma_f32_32x32x16_bf16 %0, %1, %2, %0" : "+v"(c) : "v"(a), "a"(b));
}
template <bool INIT> DEV void mfma16_h(f32x4& c, u32x4 a, u32x4 b) {
  if constexpr (INIT) asm volatile("v_mfma_f32_16x16x32_f16 %0, %1, %2, 0" : "=&v"(c) : "v"(a), "v"(b));
  else asm volatile("v_mfma_f32_16x16x32_f16 %0, %1, %2, %0" : "+v"(c) : "v"(a), "v"(b));
}
DEV f32x4 mfma_h(u32x4 a, u32x4 b, f32x4 c) {
  return __builtin_amdgcn_mfma_f32_16x16x32_f16(__builtin_bit_cast(f16x8, a), __builtin_bit_cast(f16x8, b), c, 0, 0, 0);
}
template <bool INIT> DEV void mfma16_bf(f32x4& c, u32x4 a, u32x4 b) {
  if constexpr (INIT) asm volatile("v_mfma_f32_16x16x32_bf16 %0, %1, %2, 0" : "=&v"(c) : "v"(a), "v"(b));
  else asm volatile("v_mfma_f32_16x16x32_bf16 %0, %1, %2, %0" : "+v"(c) : "v"(a), "v"(b));
}
#define FENCE() __builtin_amdgcn_sched_barrier(0)

DEV void wave_sum4(float& a, float& b, float& c, float& d) {
#define S4(CTRL) "v_add_f32_dpp %0, %0, %0 " CTRL "\n\tv_add_f32_dpp %1, %1, %1 " CTRL "\n\tv_add_f32_dpp %2, %2, %2 " CTRL "\n\tv_add_f32_dpp %3, %3, %3 " CTRL "\n\t"
  asm volatile("s_nop 1\n\t" S4("row_shr:1 row_mask:0xf bank_mask:0xf bound_ctrl:1") S4("row_shr:2 row_mask:0xf bank_mask:0xf bound_ctrl:1")
               S4("row_shr:4 row_mask:0xf bank_mask:0xf bound_ctrl:1") S4("row_shr:8 row_mask:0xf bank_mask:0xf bound_ctrl:1")
               S4("row_bcast:15 row_mask:0xa bank_mask:0xf") S4("row_bcast:31 row_mask:0xc bank_mask:0xf") "s_nop 0"
               : "+v"(a), "+v"(b), "+v"(c), "+v"(d));
#undef S4
  a = asf(__builtin_amdgcn_readlane(asu(a), 63)); b = asf(__builtin_amdgcn_readlane(asu(b), 63));
  c = asf(__builtin_amdgcn_readlane(asu(c), 63)); d = asf(__builtin_amdgcn_readlane(asu(d), 63));
}
DEV void wave_sum2(float& a, float& b) {
#define S2(CTRL) "v_add_f32_dpp %0, %0, %0 " CTRL "\n\tv_add_f32_dpp %1, %1, %1 " CTRL "\n\ts_nop 0\n\t"
  asm volatile("s_nop 1\n\t" S2("row_shr:1 row_mask:0xf bank_mask:0xf bound_ctrl:1") S2("row_shr:2 row_mask:0xf bank_mask:0xf bound_ctrl:1")
               S2("row_shr:4 row_mask:0xf bank_mask:0xf bound_ctrl:1") S2("row_shr:8 row_mask:0xf bank_mask:0xf bound_ctrl:1")
               S2("row_bcast:15 row_mask:0xa bank_mask:0xf") S2("row_bcast:31 row_mask:0xc bank_mask:0xf")
               : "+v"(a), "+v"(b));
#undef S2
  a = asf(__builtin_amdgcn_readlane(asu(a), 63)); b = asf(__builtin_amdgcn_readlane(asu(b), 63));
}
template <int CTRL, int RM> DEV float dpp_add(float v) { return v + asf(__builtin_amdgcn_update_dpp(0, asu(v), CTRL, RM, 0xf, true)); }
DEV float wave_sum(float v) {
  v = dpp_add<0x111, 0xf>(v); v = dpp_add<0x112, 0xf>(v); v = dpp_add<0x114, 0xf>(v); v = dpp_add<0x118, 0xf>(v);
  v = dpp_add<0x142, 0xa>(v); v = dpp_add<0x143, 0xc>(v);
  return asf(__builtin_amdgcn_readlane(asu(v), 63));
}
DEV void cmul2(f32x2& r0, f32x2 a0, f32x2 b0, f32x2& r1, f32x2 a1, f32x2 b1) {
  f32x2 t0, t1;
  asm("v_pk_mul_f32 %0, %4, %5 op_sel_hi:[1,0]\n\tv_pk_mul_f32 %1, %6, %7 op_sel_hi:[1,0]\n\t"
      "v_pk_fma_f32 %2, %4, %5, %0 op_sel:[1,1,0] op_sel_hi:[0,1,1] neg_lo:[1,0,0]\n\t"
      "v_pk_fma_f32 %3, %6, %7, %1 op_sel:[1,1,0] op_sel_hi:[0,1,1] neg_lo:[1,0,0]"
      : "=&v"(t0), "=&v"(t1), "=&v"(r0), "=&v"(r1) : "v"(a0), "v"(b0), "v"(a1), "v"(b1));
}
DEV f32x2 cmul(f32x2 a, f32x2 b) {
  f32x2 t, r;
  asm("v_pk_mul_f32 %0, %2, %3 op_sel_hi:[1,0]\n\tv_pk_fma_f32 %1, %2, %3, %0 op_sel:[1,1,0] op_sel_hi:[0,1,1] neg_lo:[1,0,0]"
      : "=&v"(t), "=v"(r) : "v"(a), "v"(b));
  return r;
}
DEV u32 cvt_bf2(float a, float b) { u32 r; asm("v_cvt_pk_bf16_f32 %0, %1, %2" : "=v"(r) : "v"(a), "v"(b)); return r; }
DEV u32 cvt_h2(float a, float b) { u32 r; asm("v_cvt_pk_f16_f32 %0, %1, %2" : "=v"(r) : "v"(a), "v"(b)); return r; }
DEV float fadd1(float a, float b) { float r; asm("v_add_f32 %0, %1, %2" : "=v"(r) : "v"(a), "v"(b)); return r; }
DEV float fsub1(float a, float b) { float r; asm("v_sub_f32 %0, %1, %2" : "=v"(r) : "v"(a), "v"(b)); return r; }
DEV float fmul1(float a, float b) { float r; asm("v_mul_f32 %0, %1, %2" : "=v"(r) : "v"(a), "v"(b)); return r; }
DEV float ffma1(float a, float b, float c) { float r; asm("v_fma_f32 %0, %1, %2, %3" : "=v"(r) : "v"(a), "v"(b), "v"(c)); return r; }
DEV float rbf1(float a) { return bflo(cvt_bf2(a, a)); }
DEV u32 pk_f16_conj(f32x2 v) { u32 r; asm("v_cvt_pk_f16_f32 %0, %1, -%2" : "=v"(r) : "v"(v.x), "v"(v.y)); return r; }
DEV f32x2 gelu2(f32x2 v) {
  const f32x2 s = v * fma2(v * v, sp2(-0.10294323970f), sp2(-2.3022081986f));
  const f32x2 d = f32x2{__builtin_amdgcn_exp2f(s.x), __builtin_amdgcn_exp2f(s.y)} + sp2(1.0f);
  return v * f32x2{__builtin_amdgcn_rcpf(d.x), __builtin_amdgcn_rcpf(d.y)};
}
DEV void spin_flag(u32x4 srd, u32 voff, u32 epoch, u32 val, bool need) {
#pragma unroll 1
  for (int it = 0; it < (1 << 16); ++it) {
    if (__all(!need || val == epoch)) break;
    __builtin_amdgcn_s_sleep(1);
    val = ld32c(srd, voff, 0);
    asm volatile("s_waitcnt vmcnt(0)" : "+v"(val)::"memory");
  }
}

constexpr int G2_LEAD = 10;
constexpr int g2_unit_ops(int j) { return (j + G2_LEAD <= 21 ? 2 : 0) + ((j >= 8 && j <= 11) ? 4 : 0); }
constexpr int g2_ops_before(int j) { int n = 2 * G2_LEAD; for (int i = 0; i < j; ++i) n += g2_unit_ops(i); return n; }
constexpr int g2_pos_h(int j) {
  if (j < G2_LEAD) return 2 * j + 2;
  const int i = j - G2_LEAD;
  return g2_ops_before(i) + 2 + ((i >= 8 && i <= 11) ? 2 : 0);
}
constexpr int g2_wait_h(int j) { return g2_ops_before(j) - g2_pos_h(j); }

}

__global__ __launch_bounds__(512, 2) void s4d_mixer_fused(Params p) {
  __shared__ __attribute__((aligned(16))) char smem[LDS_BYTES];
  const int tid = threadIdx.x, lane = tid & 63, t8 = tid & 255;
  const int wid = __builtin_amdgcn_readfirstlane(tid >> 6);
  const int side = wid >> 2, w = wid & 3;
  const int b = blockIdx.x, g8 = b & 7, r = b >> 3;
  const int mt = 4 * (g8 >> 1) + (r >> 3), nt = 8 * (g8 & 1) + (r & 7);
  const int ustart = (nt * NUNIT) >> 4, unext = ((nt + 1) * NUNIT) >> 4;
  const int tg = 16 * mt + nt;
  const int cp = 32 * g8 + r;
  const int mt2 = 4 * g8 + (r >> 3), nt2 = r & 7;
  const int c15 = lane & 15, g = lane >> 4;
  const int c31 = lane & 31, hh = lane >> 5, b16 = (lane >> 4) & 1;
  const int q4 = (lane & 15) >> 2, p4 = lane & 3;
  const int kch = w >> 1, kd = 2 * cp + kch, kh = w >> 1, mh = w & 1;

  const u32x4 sX = mk_srd(p.x, SEQ * DM * 2), sWS = mk_srd(p.ws, WS_END);
  const rsrc_t rWS = mk_rsrc(p.ws, WS_END), rOUT = mk_rsrc(p.out, SEQ * DM * 2);
  const u32x4 sW1 = mk_srd(p.w1, DM * FF * 2), sW2 = mk_srd(p.w2, FF * DM * 2);
  const u32 lb = (u32)(uintptr_t)(LAS char*)smem;
  STAMP(0);
  PSTAMP(0);

  auto load_eword = [&]() { u32 e; asm volatile("s_load_dword %0, %1, 0x0" : "=s"(e) : "s"(p.ws) : "memory"); return e; };
  u32 vx[4], vw[3];
#pragma unroll
  for (int j = 0; j < 4; ++j) vx[j] = (8 * j + (lane >> 3)) * 1024 - j * 1024 + (((lane & 7) ^ ((4 * j + (lane >> 4)) & 7)) * 16);
#pragma unroll
  for (int j = 0; j < 3; ++j) {
    const int P = (3 * w + j) * 64 + lane, k = P / 12, sl = P - 12 * k;
    vw[j] = k * (FF * 2) + sl * 16 - j * 1024;
  }
  const u32 x_so = (mt * 128 + 32 * w) * 1024, x_lo = lb + 32 * w * 128, w_so = ustart * 64, w_lo = lb + SLOT_W + 3 * w * 1024;
  auto st1_m0 = [&](auto KC, auto J) {
    if constexpr (J == 0) dma_m0(x_lo + (KC & 3) * SLOT);
    if constexpr (J == 4) dma_m0(w_lo + (KC & 3) * SLOT);
  };
  auto st1_go = [&](auto KC, auto J) {
    if constexpr (J < 4) dma_go<J * 1024>(sX, vx[J], x_so + KC * 128); else dma_go<(J - 4) * 1024>(sW1, vw[J - 4], w_so + KC * 64 * (FF * 2));
  };
  auto stage = [&](auto KC) { sfor<7>([&](auto J) { if constexpr (J == 0 || J == 4) { st1_m0(KC, J); asm volatile("s_nop 0"); } st1_go(KC, J); }); };
  u32 ixp[5];
  auto prep_ix = [&]() {
#pragma unroll
    for (int e = 0; e < 9; ++e) {
      const int l = 2047 - (8 * t8 + e);
      const u32 rb = pk_bf16(sp2((float)l)) & 0xffffu;
      const u32 ix = (l < 0 ? 641u : (l < 128 ? (u32)l : rb - 17024u)) * 2;
      if (e & 1) ixp[e >> 1] |= ix << 16; else ixp[e >> 1] = ix;
    }
#pragma unroll
    for (int e = 0; e < 5; ++e) tie(ixp[e]);
  };
  u32 epoch;
  u32 q_lar, q_aim, q_cre, q_cim, q_cre2, q_cim2, q_ldt, q_dsk;
  u32x2 b1v;
  auto load_small = [&]() {
    const u32 vo = (64 * (w & 1) + lane) * 2, so = kd * NMODE * 2;
    const u32x4 sCr = mk_srd(p.cre, DM * NMODE * 2), sCi = mk_srd(p.cim, DM * NMODE * 2);
    q_lar = ld16(mk_srd(p.lar, DM * NMODE * 2), vo, so); q_aim = ld16(mk_srd(p.aim, DM * NMODE * 2), vo, so);
    q_cre = ld16(sCr, vo, so); q_cim = ld16(sCi, vo, so);
    q_cre2 = ld16(sCr, vo ^ 128, so); q_cim2 = ld16(sCi, vo ^ 128, so);
    q_ldt = ld16(mk_srd(p.ldt, DM * 2), 0, kd * 2); q_dsk = ld16(mk_srd(p.dsk, DM * 2), 0, kd * 2);
    b1v = ld64(mk_srd(p.b1, FF * 2), lane < 24 ? lane * 8 : OOB, ustart * 64);
  };
#define TIE_SMALL "+v"(q_lar), "+v"(q_aim), "+v"(q_cre), "+v"(q_cim), "+v"(q_cre2), "+v"(q_cim2), "+v"(q_ldt), "+v"(q_dsk), "+v"(b1v)
  f32x16 acc1[3];
  f32x4 csp[3];
  u32x4 stv;
  if (side == 0) {
    u32x4 xr0 = ld128(sX, lane * 16, (8 * tg + 2 * w) * 1024), xr1 = ld128(sX, lane * 16, (8 * tg + 2 * w + 1) * 1024);
    {
      u64 pc;
      asm volatile("s_getpc_b64 %0" : "=s"(pc));
      const char* a;
      if (t8 < 96) a = (const char*)p.x + (512 * (g8 >> 1) + 16 * r + t8 / 6) * 1024 + (2 + t8 % 6) * 128;
      else if (t8 < 192) a = (const char*)p.w1 + (256 + 8 * r + (t8 - 96) / 12) * (FF * 2) + (g8 & 1) * 1280 + ((t8 - 96) % 12) * 120;
      else a = (const char*)pc + ((r * 64 + lane) % 208) * 128;
      touch(a, lb + S_DUMMY);
    }
    u32 eword = load_eword();
    prep_ix();
    asm volatile("s_waitcnt vmcnt(1)" : "+v"(xr0), "+v"(xr1)::"memory");
    PSTAMP(1);
    STAMP(1);
    {
      const u32x4 xr[2] = {xr0, xr1};
      f32x2 x[2][4];
      float x0[2], sm[2], sq[2];
#pragma unroll
      for (int t = 0; t < 2; ++t) {
#pragma unroll
        for (int i = 0; i < 4; ++i) x[t][i] = bf2(xr[t][i]);
        x0[t] = asf(__builtin_amdgcn_readfirstlane(asu(x[t][0].x)));
        f32x2 s = {0.f, 0.f}, q = {0.f, 0.f};
#pragma unroll
        for (int i = 0; i < 4; ++i) { const f32x2 d = x[t][i] - sp2(x0[t]); s += d; q = fma2(d, d, q); }
        sm[t] = s.x + s.y; sq[t] = q.x + q.y;
      }
      wave_sum4(sm[0], sq[0], sm[1], sq[1]);
      float m1[2], r1[2], s2[2], q2[2];
#pragma unroll
      for (int t = 0; t < 2; ++t) {
        const float md = sm[t] * (1.0f / DM);
        const float var = fmaxf(__builtin_fmaf(-md, md, sq[t] * (1.0f / DM)), 0.f);
        m1[t] = rbf(x0[t] + md);
        r1[t] = __builtin_amdgcn_rcpf(rbf(__builtin_amdgcn_sqrtf(rbf(rbf(var) + 1e-5f))));
      }
#pragma unroll
      for (int t = 0; t < 2; ++t) {
        f32x2 s = {0.f, 0.f}, q = {0.f, 0.f};
        u32x4 hv;
#pragma unroll
        for (int i = 0; i < 4; ++i) {
          const f32x2 n = bf2(pk_bf16(bf2(pk_bf16(x[t][i] - sp2(m1[t]))) * sp2(r1[t])));
          s += n; q = fma2(n, n, q);
          hv[i] = pk_f16(n);
        }
        lds_st<u32x4>(smem, S_LN + (2 * w + t) * LN_ROW + lane * 16, hv);
        s2[t] = s.x + s.y; q2[t] = q.x + q.y;
      }
      wave_sum4(s2[0], q2[0], s2[1], q2[1]);
      float al[2], be[2];
#pragma unroll
      for (int t = 0; t < 2; ++t) {
        const float mean2 = s2[t] * (1.0f / DM);
        const float var2 = fmaxf(__builtin_fmaf(-mean2, mean2, q2[t] * (1.0f / DM)), 0.f);
        const float r2 = __builtin_amdgcn_rcpf(rbf(__builtin_amdgcn_sqrtf(rbf(rbf(var2) + 1e-5f))));
        al[t] = r1[t] * r2;
        be[t] = __builtin_fmaf(m1[t], r1[t], rbf(mean2)) * r2;
      }
      PSTAMP(2);
      asm volatile("s_waitcnt lgkmcnt(0)" : "+s"(eword)::"memory");
      epoch = eword * 0x9E3779B1u + 0x7F4A7C15u;
      const u32x4 sv = {asu(lane ? al[1] : al[0]), epoch, asu(lane ? be[1] : be[0]), epoch};
      st128c(rWS, sv, lane < 2 ? WS_STATS + (8 * tg + 2 * w + lane) * 16 : OOB);
    }
    PSTAMP(3); PSTAMP(4);
    const u32 xa0 = lb + (32 * w + c31) * 128 + ((hh ^ ((c31 >> 1) & 7)) * 16);
    const u32 wa0 = lb + SLOT_W + (8 * hh + q4) * 192 + b16 * 32 + 8 * p4;
    u32 xa[2][4], wa[2];
#pragma unroll
    for (int s = 0; s < 4; ++s) { xa[0][s] = xa0 ^ (s * 32); xa[1][s] = xa[0][s] + 2 * SLOT; }
    wa[0] = wa0; wa[1] = wa0 + 2 * SLOT;
    u32x4 xf[2];
    u32x2 wl[2][3], wh[2][3];
    auto rd = [&](auto S, auto R) {
      constexpr int s = S, rr = R, buf = s & 1, kc = s >> 2, sl = kc & 3, so = (sl & 1) * SLOT, sp = s & 3;
      if constexpr (rr == 0) xf[buf] = a_ds128<so>(xa[sl >> 1][sp]);
      else {
        constexpr int u = (rr - 1) >> 1, hf = (rr - 1) & 1, off = so + u * 64 + (sp * 16 + hf * 4) * 192;
        if constexpr (hf == 0) wl[buf][u] = a_dstr<off>(wa[sl >> 1]); else wh[buf][u] = a_dstr<off>(wa[sl >> 1]);
      }
    };
    PSTAMP(5); PSTAMP(6);
    bar();
    PSTAMP(7); PDUMP();
    CSTAMP(0);
    sfor<7>([&](auto R) { rd(ic<0>{}, R); });
    sfor<32>([&](auto S) {
      constexpr int s = S, buf = s & 1, sp = s & 3, kc = s >> 2;
      constexpr bool last = s == 31, brk = sp == 2 && kc < 7;
      sfor<3>([&](auto U) {
        constexpr int u = U;
        if constexpr (u == 0) wait_lgkm<4>();
        else if constexpr (u == 1) {
          wait_lgkm<(last ? 0 : 3)>();
          if constexpr (brk) {
            constexpr int n = kc + 1;
            CSTAMP(2 * n - 1);
            asm volatile("s_barrier" ::: "memory");
            CSTAMP(2 * n);
            if constexpr (n == 4) load_small();
            if constexpr (n == 7) stv = ld128c(sWS, WS_STATS + (128 * mt + 32 * w + c31) * 16, 0);
          }
        }
        mfma32_vv<s == 0>(acc1[u], cat4(wl[buf][u], wh[buf][u]), xf[buf]);
        if constexpr (!last) {
          if constexpr (u == 0) sfor<3>([&](auto R) { rd(ic<s + 1>{}, R); });
          if constexpr (u == 1) sfor<2>([&](auto R) { rd(ic<s + 1>{}, ic<3 + R>{}); });
          if constexpr (u == 2) sfor<2>([&](auto R) { rd(ic<s + 1>{}, ic<5 + R>{}); });
        }
      });
    });
    asm volatile("s_nop 7\n\ts_nop 3" : "+v"(acc1[0]), "+v"(acc1[1]), "+v"(acc1[2]));
    CSTAMP(15); CDUMP(16, 0);
    STAMP(2);
    bar();
#pragma unroll
    for (int q = 0; q < 6; ++q) {
      const f32x16& a = acc1[q < 2 ? 1 : 2];
      const int o = q < 2 ? 8 + 4 * q : 4 * (q - 2);
      lds_st<f32x4>(smem, S_TB + w * 6144 + q * 1024 + lane * 16, f32x4{a[o], a[o + 1], a[o + 2], a[o + 3]});
    }
    asm volatile("s_waitcnt vmcnt(0)" : "+v"(stv), TIE_SMALL :: "memory");
  } else {
    const u32 wc0 = lb + SLOT_W + (32 * (w >> 1) + 8 * g + q4) * 192 + 3 * (w & 1) * 32 + 8 * p4;
    u32x4 ones = {0x3f803f80u, 0x3f803f80u, 0x3f803f80u, 0x3f803f80u};
    tie(ones);
    u32x2 cl[3], ch[3];
    auto cs_rd = [&](auto KC) {
      const u32 a = wc0 + (KC & 3) * SLOT;
      sfor<3>([&](auto I) { cl[I] = a_dstr<I * 32>(a); ch[I] = a_dstr<I * 32 + 4 * 192>(a); });
      FENCE();
    };
    auto cs_sum = [&](auto KC) {
      FENCE();
      wait_lgkm<0>();
      sfor<3>([&](auto I) { mfma16_bf<KC == 0>(csp[I], cat4(cl[I], ch[I]), ones); });
    };
    stage(ic<0>{}); stage(ic<1>{}); stage(ic<2>{});
    u32 eword = load_eword();
    prep_ix();
    asm volatile("s_waitcnt lgkmcnt(0)" : "+s"(eword)::"memory");
    epoch = eword * 0x9E3779B1u + 0x7F4A7C15u;
    PSTAMP(1); PSTAMP(2); PSTAMP(3); PSTAMP(4); PSTAMP(5);
    wait_vm<14>();
    PSTAMP(6);
    bar();
    PSTAMP(7); PDUMP();
    CSTAMP(0);
    STAMP(1);
    cs_rd(ic<0>{}); stage(ic<3>{}); cs_sum(ic<0>{});
    wait_vm<14>();
    CSTAMP(1);
    bar();
    CSTAMP(2);
    cs_rd(ic<1>{}); cs_sum(ic<1>{});
    wait_vm<7>();
    CSTAMP(3);
    bar();
    CSTAMP(4);
    cs_rd(ic<2>{}); stage(ic<4>{}); cs_sum(ic<2>{});
    wait_vm<7>();
    CSTAMP(5);
    bar();
    CSTAMP(6);
    cs_rd(ic<3>{}); stage(ic<5>{}); cs_sum(ic<3>{});
    wait_vm<7>();
    CSTAMP(7);
    bar();
    CSTAMP(8);
    cs_rd(ic<4>{}); stage(ic<6>{}); cs_sum(ic<4>{});
    wait_vm<7>();
    CSTAMP(9);
    bar();
    CSTAMP(10);
    cs_rd(ic<5>{}); stage(ic<7>{}); cs_sum(ic<5>{});
    wait_vm<7>();
    CSTAMP(11);
    bar();
    CSTAMP(12);
    cs_rd(ic<6>{});
#pragma unroll
    for (int i = 0; i < 2; ++i) {
      const u32 a = S_LN + q4 * LN_ROW + ((2 * w + i) * 64 + 16 * g + 4 * p4) * 2;
      st128c(rWS, cat4(lds_tr(smem, a), lds_tr(smem, a + 4 * LN_ROW)), WS_XN + tg * 8192 + ((2 * w + i) * 64 + lane) * 16);
    }
    {
#pragma unroll
      for (int i = 0; i < 2; ++i) {
        const int ln = (2 * w + i) * 8 + (lane >> 3);
        st128c(rWS, u32x4{POISON, POISON, POISON, POISON}, WS_A + ((ln >> 1) * 512 + 2 * cp + (ln & 1)) * 128 + (lane & 7) * 16);
      }
    }
    load_small();
    cs_sum(ic<6>{});
    wait_vm<13>();
    CSTAMP(13);
    bar();
    CSTAMP(14);
    stv = ld128c(sWS, WS_STATS + (128 * mt + 32 * w + c31) * 16, 0);
    cs_rd(ic<7>{}); cs_sum(ic<7>{});
    asm volatile("s_nop 7\n\ts_nop 1" : "+v"(csp[0]), "+v"(csp[1]), "+v"(csp[2]));
#pragma unroll
    for (int i = 0; i < 3; ++i) lds_st<f32x4>(smem, c15 == 0 ? S_CSP + ((w >> 1) * 96 + (3 * (w & 1) + i) * 16 + 4 * g) * 4 : S_MISC, csp[i]);
    CSTAMP(15); CDUMP(16, 20);
    STAMP(2);
    asm volatile("s_waitcnt vmcnt(1)" : TIE_SMALL :: "memory");
    lds_st<f32x4>(smem, lane < 24 ? S_B1 + lane * 16 : S_MISC, f32x4{bflo(b1v.x), bfhi(b1v.x), bflo(b1v.y), bfhi(b1v.y)});
    bar();
    st32c(rWS, epoch, t8 == 0 ? WS_LNF + tg * 4 : OOB);
    const int f = t8 < 96 ? t8 : 0;
    lds_st<float>(smem, t8 < 96 ? S_CS + t8 * 4 : S_MISC, lds_ld<float>(smem, S_CSP + f * 4) + lds_ld<float>(smem, S_CSP + 384 + f * 4));
    asm volatile("s_waitcnt vmcnt(1)" : "+v"(stv)::"memory");
  }
#pragma unroll 1
  for (int it = 0; it < (1 << 16); ++it) {
    if (__all(stv.y == epoch && stv.w == epoch)) break;
    __builtin_amdgcn_s_sleep(1);
    stv = ld128c(sWS, WS_STATS + (128 * mt + 32 * w + c31) * 16, 0);
    asm volatile("s_waitcnt vmcnt(0)" : "+v"(stv)::"memory");
  }
  bar();
  CSTAMP(0);
  STAMP(3);

  {
    f32x4 av[6];
    if (side) {
#pragma unroll
      for (int q = 0; q < 6; ++q) av[q] = lds_ld<f32x4>(smem, S_TB + w * 6144 + q * 1024 + lane * 16);
    } else {
#pragma unroll
      for (int q = 0; q < 6; ++q) {
        const f32x16& a = acc1[q >> 2];
        const int o = 4 * (q & 3);
        av[q] = f32x4{a[o], a[o + 1], a[o + 2], a[o + 3]};
      }
    }
    const f32x2 al = sp2(asf(stv.x)), nb = sp2(-asf(stv.z));
    const u32 hso = WS_H + ((mt * 4 + w) * NUNIT + ustart) * 2048 + 3 * side * 1024 + lane * 16;
    const u32 cb = S_B1 + hh * 16 + 3 * side * 64;
    f32x4 bi[6], cs[6];
#pragma unroll
    for (int q = 0; q < 6; ++q) { bi[q] = lds_ld<f32x4>(smem, cb + q * 32); cs[q] = lds_ld<f32x4>(smem, cb + (S_CS - S_B1) + q * 32); }
    f32x2 v[12], t[12];
#pragma unroll
    for (int q = 0; q < 6; ++q) { t[2 * q] = fma2(nb, f32x2{cs[q].x, cs[q].y}, f32x2{bi[q].x, bi[q].y}); t[2 * q + 1] = fma2(nb, f32x2{cs[q].z, cs[q].w}, f32x2{bi[q].z, bi[q].w}); }
    FENCE();
#pragma unroll
    for (int q = 0; q < 6; ++q) { v[2 * q] = fma2(al, f32x2{av[q].x, av[q].y}, t[2 * q]); v[2 * q + 1] = fma2(al, f32x2{av[q].z, av[q].w}, t[2 * q + 1]); }
    FENCE();
#pragma unroll
    for (int k = 0; k < 12; ++k) t[k] = v[k] * v[k];
    FENCE();
#pragma unroll
    for (int k = 0; k < 12; ++k) t[k] = fma2(t[k], sp2(-0.10294323970f), sp2(-2.3022081986f));
    FENCE();
#pragma unroll
    for (int k = 0; k < 12; ++k) t[k] = v[k] * t[k];
    FENCE();
#pragma unroll
    for (int k = 0; k < 12; ++k) t[k] = f32x2{__builtin_amdgcn_exp2f(t[k].x), __builtin_amdgcn_exp2f(t[k].y)};
    FENCE();
#pragma unroll
    for (int k = 0; k < 12; ++k) t[k] += sp2(1.0f);
    FENCE();
#pragma unroll
    for (int k = 0; k < 12; ++k) t[k] = f32x2{__builtin_amdgcn_rcpf(t[k].x), __builtin_amdgcn_rcpf(t[k].y)};
    FENCE();
#pragma unroll
    for (int k = 0; k < 12; ++k) v[k] *= t[k];
    FENCE();
#pragma unroll
    for (int i = 0; i < 3; ++i)
      st128c(rWS, u32x4{pk_bf16(v[4 * i]), pk_bf16(v[4 * i + 1]), pk_bf16(v[4 * i + 2]), pk_bf16(v[4 * i + 3])},
             ustart + ((3 * side + i) >> 1) < unext ? hso + i * 1024 : OOB);
  }
  CSTAMP(1);
  u32 lnf = ld32c(sWS, WS_LNF + t8 * 4, 0);
  {
    const int ix = tid < 480 ? tid : tid - 480, ri = 15 * r + (ix >> 5);
    touch((const char*)p.w2 + (ri < 256 ? 448 + ri : 896 + ri) * 1024 + (ix & 31) * 32, lb + S_DUMMY);
  }
  st32c(rWS, epoch, (b == 0 && tid == 0) ? WS_E : OOB);
  STAMP(4);
  const u32 vw2 = (lane >> 3) * 1024 + ((((lane & 7) >> 1) ^ ((lane >> 3) & 2)) * 32) + (lane & 1) * 16;
  const u32 vw2p[2] = {vw2, vw2 + 7168};
  const u32 vw2z[2] = {kh ? OOB : vw2p[0], kh ? OOB : vw2p[1]};
  const u32 w2_lo = lb + kh * W2_HALF + 2 * mh * 1024, w2_so = (kh * 22 * 32 + 16 * mh) * 1024 + nt2 * 128;
  auto st2_piece = [&](auto N) {
    constexpr int j = N >> 1, i = N & 1;
    FENCE();
    if constexpr (i == 0) { dma_m0(w2_lo + (j % W2_NS) * 4096); asm volatile("s_nop 0"); }
    dma_go<i * 1024>(sW2, j == 21 ? vw2z[i] : vw2p[i], w2_so + j * 32 * 1024);
    FENCE();
  };

  float inv_sk;
  {
    const int mh2 = w & 1;
    const float dt = rbf(expf(bf1(q_ldt)));
    const float zr = -rbf(expf(bf1(q_lar))) * dt, zi = bf1(q_aim) * dt;
    f32x2 C = f32x2{bf1(q_cre), bf1(q_cim)} * sp2(2.0f * dt);
    const float bd = wave_sum((__builtin_fabsf(C.x) + __builtin_fabsf(C.y)) + (__builtin_fabsf(bf1(q_cre2)) + __builtin_fabsf(bf1(q_cim2))) * (2.0f * dt));
    int se = 267 - (int)((asu(bd) >> 23) & 0xff);
    se = se < 1 ? 1 : (se > 253 ? 253 : se);
    C *= sp2(asf((u32)se << 23));
    inv_sk = asf((u32)(254 - se) << 23);
    const float e1 = zr * 1.44269504089f;
    const float ph = zi * 0.15915494309f;
    const float pl = __builtin_fmaf(zi, 0.15915494309f, -ph) + zi * 6.4206383e-9f;
    auto cexp = [&](float pw) {
      const float m = __builtin_amdgcn_exp2f(e1 * pw);
      const float a = __builtin_amdgcn_fractf(ph * pw) + pl * pw;
      return f32x2{__builtin_amdgcn_cosf(a), __builtin_amdgcn_sinf(a)} * sp2(m);
    };
    CSTAMP(2);
    const u32 tl = S_TAB + kch * TAB_CH + (64 * mh2 + lane) * 4;
    auto putX = [&](int row, f32x2 v) { lds_st<u32>(smem, tl + (16 + row) * TAB_ROW, pk_f16(v)); };
    if (side == 0) {
      f32x2 P[8], E[3], CE[8];
      P[1] = cexp(1.f); E[0] = cexp(128.f);
      CE[0] = C;
      cmul2(P[2], P[1], P[1], E[1], E[0], E[0]);
      cmul2(CE[1], C, E[0], P[3], P[2], P[1]);
      cmul2(P[4], P[2], P[2], E[2], E[1], E[1]);
      cmul2(CE[2], CE[0], E[1], CE[3], CE[1], E[1]);
      cmul2(P[5], P[4], P[1], P[6], P[4], P[2]);
      cmul2(CE[4], CE[0], E[2], CE[5], CE[1], E[2]);
      cmul2(CE[6], CE[2], E[2], CE[7], CE[3], E[2]);
      P[7] = cmul(P[4], P[3]);
      putX(0, CE[0]); putX(8, CE[1]);
#pragma unroll
      for (int i = 1; i < 8; ++i) {
        f32x2 a, c;
        cmul2(a, CE[0], P[i], c, CE[1], P[i]);
        putX(i, a); putX(8 + i, c);
      }
      putX(16, CE[2]); putX(20, CE[3]);
#pragma unroll
      for (int i = 1; i < 4; ++i) {
        f32x2 a, c;
        cmul2(a, CE[2], P[2 * i], c, CE[3], P[2 * i]);
        putX(16 + i, a); putX(20 + i, c);
      }
      putX(24, CE[4]); putX(26, CE[5]); putX(28, CE[6]); putX(30, CE[7]);
      f32x2 a, c;
      cmul2(a, CE[4], P[4], c, CE[5], P[4]); putX(25, a); putX(27, c);
      cmul2(a, CE[6], P[4], c, CE[7], P[4]); putX(29, a); putX(31, c);
    } else {
      f32x2 Y[16], E[5], CE[9];
      st2_piece(ic<0>{});
      Y[1] = cexp(8.f); E[0] = cexp(128.f);
      Y[0] = f32x2{1.f, 0.f};
      st2_piece(ic<1>{});
      auto putY = [&](int i) { lds_st<u32>(smem, tl + i * TAB_ROW, pk_f16_conj(Y[i])); };
      cmul2(Y[2], Y[1], Y[1], E[1], E[0], E[0]);
      st2_piece(ic<2>{});
      cmul2(Y[3], Y[2], Y[1], Y[4], Y[2], Y[2]);
      putY(0); putY(1); putY(2);
      st2_piece(ic<3>{});
      cmul2(E[2], E[1], E[1], Y[5], Y[4], Y[1]);
      cmul2(Y[6], Y[4], Y[2], Y[7], Y[4], Y[3]);
      putY(3); putY(4);
      st2_piece(ic<4>{});
      cmul2(Y[8], Y[4], Y[4], E[3], E[2], E[2]);
      putY(5); putY(6); putY(7);
      st2_piece(ic<5>{});
      cmul2(Y[9], Y[8], Y[1], Y[10], Y[8], Y[2]);
      cmul2(E[4], E[3], E[3], CE[0], C, E[3]);
      putY(8);
      st2_piece(ic<6>{});
      cmul2(Y[11], Y[8], Y[3], Y[12], Y[8], Y[4]);
      putY(9); putY(10);
      st2_piece(ic<7>{});
      cmul2(Y[13], Y[8], Y[5], Y[14], Y[8], Y[6]);
      putY(11); putY(12);
      st2_piece(ic<8>{});
      cmul2(Y[15], Y[8], Y[7], CE[1], CE[0], E[0]);
      putY(13); putY(14);
      st2_piece(ic<9>{});
      cmul2(CE[2], CE[0], E[1], CE[8], C, E[4]);
      putY(15); putX(32, CE[0]); putX(33, CE[1]);
      st2_piece(ic<10>{});
      cmul2(CE[3], CE[1], E[1], CE[4], CE[0], E[2]);
      putX(34, CE[2]); putX(40, CE[8]);
      st2_piece(ic<11>{});
      cmul2(CE[5], CE[1], E[2], CE[6], CE[2], E[2]);
      putX(35, CE[3]); putX(36, CE[4]);
      st2_piece(ic<12>{});
      CE[7] = cmul(CE[3], E[2]);
      putX(37, CE[5]); putX(38, CE[6]);
      st2_piece(ic<13>{});
      putX(39, CE[7]);
      st2_piece(ic<14>{});
    }
    CSTAMP(3);
    if (side) wait_vm<17>(); else wait_vm<2>();
    tie(lnf);
    st32c(rWS, epoch, lane == 0 ? WS_HF + (((mt * 4 + w) * 16 + nt) * 2 + side) * 4 : OOB);
    spin_flag(sWS, WS_LNF + t8 * 4, epoch, lnf, true);
    u32x4 ug = ld128c(sWS, WS_XN + t8 * 8192 + cp * 32 + side * 16, 0);
    if (side) st2_piece(ic<15>{});
    CSTAMP(4);
    bar();
    CSTAMP(5);
    const int tile = side ? 2 : mh2;
    const u32 kc = S_KC + kch * KC_CH;
    if (!(side && mh2)) {
      const u32 rd = S_TAB + kch * TAB_CH + c15 * TAB_ROW + g * 16;
      const u32 ra = S_TAB + kch * TAB_CH + (16 + min(16 * tile + c15, 40)) * TAB_ROW + g * 16;
      u32x4 yf[8], xf[8];
#pragma unroll
      for (int s = 0; s < 8; ++s) { yf[s] = lds_ld<u32x4>(smem, rd + s * 64); xf[s] = lds_ld<u32x4>(smem, ra + s * 64); }
      f32x4 a0 = {0, 0, 0, 0}, a1 = {0, 0, 0, 0};
#pragma unroll
      for (int s = 0; s < 4; ++s) { a0 = mfma_h(xf[2 * s], yf[2 * s], a0); a1 = mfma_h(xf[2 * s + 1], yf[2 * s + 1], a1); }
      const f32x4 v = a0 + a1;
      const u32 h01 = pk_f16(f32x2{v[0], v[1]}), h23 = pk_f16(f32x2{v[2], v[3]});
      if (tile == 0) {
        lds_st<u32x2>(smem, kc + (128 * (g >> 1) + 8 * c15 + 4 * (g & 1)) * 2, u32x2{h01, h23});
      } else if (tile == 1) {
        const u32 t1a = g < 2 ? 256 + 64 * g + 4 * c15 : 384 + 64 * (g - 2) + 2 * c15;
        lds_st<u32>(smem, kc + t1a * 2, h01);
        lds_st<u32>(smem, kc + (t1a + (g < 2 ? 2 : 32)) * 2, h23);
      } else {
        const u32 t2 = g < 2 ? 512 + 64 * g + c15 : 644, st2 = g < 2 ? 16 : 0;
        lds_st<u16>(smem, kc + ((g == 2 && c15 == 0) ? 640 : t2) * 2, (u16)h01);
        lds_st<u16>(smem, kc + (t2 + st2) * 2, (u16)(h01 >> 16));
        lds_st<u16>(smem, kc + (t2 + 2 * st2) * 2, (u16)h23);
        lds_st<u16>(smem, kc + (t2 + 3 * st2) * 2, (u16)(h23 >> 16));
      }
    } else if (lane == 0) lds_st<u16>(smem, kc + 641 * 2, (u16)0);
    CSTAMP(6);
    bar();
    CSTAMP(7);
    if (side) wait_vm<1>(); else wait_vm<0>();
    tie(ug);
    {
      u32 e[9];
#pragma unroll
      for (int i = 0; i < 9; ++i) e[i] = lds_ld<u16>(smem, S_KC + side * KC_CH + ((i & 1) ? ixp[i >> 1] >> 16 : ixp[i >> 1] & 0xffffu));
      const u32x4 a0 = {e[0] | (e[1] << 16), e[2] | (e[3] << 16), e[4] | (e[5] << 16), e[6] | (e[7] << 16)};
      const u32x4 a1 = {e[1] | (e[2] << 16), e[3] | (e[4] << 16), e[5] | (e[6] << 16), e[7] | (e[8] << 16)};
      lds_st<u32x4>(smem, S_G + side * G_CH + t8 * 16, a0);
      lds_st<u32x4>(smem, S_G + side * G_CH + G_PAR + t8 * 16, a1);
      const u32x4 z4 = {0, 0, 0, 0};
      if (t8 < 4) lds_st<u32x4>(smem, S_G + side * G_CH + (t8 >> 1) * G_PAR + 4096 + (t8 & 1) * 16, z4);
      if (t8 < 30) lds_st<u32x4>(smem, S_U + side * U_CH + t8 * 16, z4);
      lds_st<u32x4>(smem, S_U + side * U_CH + 480 + t8 * 16, ug);
    }
    CSTAMP(8);
    bar();
    CSTAMP(9);
    if (side) CDUMP(10, 48); else CDUMP(10, 38);
  }
  STAMP(5);

  const int ch0 = 64 * nt2 + 32 * kh + 4 * hh;
  const u32 xto = (64 * mt2 + (t8 >> 3)) * 1024 + nt2 * 128 + (t8 & 7) * 16;
  f32x16 acc[2];
  if (side == 0) {
    const int par = w & 1;
    const u32 ub = lb + S_U + kch * U_CH + 64 - 64 * par + 32 * c15 + 16 * g;
    const u32 gb0 = lb + S_G + kch * G_CH + ((c15 & 1) ? 0 : G_PAR) + 4 * (1015 - (c15 >> 1) - 16 * par + 4 * g - 32 * 31);
    const u32 gb[4] = {gb0, gb0 + 1024, gb0 + 2048, gb0 + 3072};
    u32x4 bq[32];
    u32x2 al[32], ah[32];
    f32x4 y[8];
    auto rdB = [&](auto M, auto T) { constexpr int q = 4 * T - M + 3; bq[q] = a_ds128<128 * q>(ub); };
    auto rdA = [&](auto M, auto H) {
      constexpr int k = (31 - M) >> 3, o = 32 * ((31 - M) & 7);
      if constexpr (H == 0) al[M] = a_ds2x32<o, o + 1>(gb[k]); else ah[M] = a_ds2x32<o + 2, o + 3>(gb[k]);
    };
    sfor<2>([&](auto M) { sfor<8>([&](auto T) { rdB(M, T); }); rdA(M, ic<0>{}); rdA(M, ic<1>{}); });
    sfor<32>([&](auto M) {
      constexpr int m = M, t0 = m >> 2, n = 8 - t0;
      wait_lgkm<(m <= 2 ? 10 : (m == 3 ? 4 : (m == 4 ? 6 : (2 * (31 - m) < 8 ? 2 * (31 - m) : 8))))>();
      sfor<n>([&](auto I) {
        constexpr int i = I, t = t0 + i;
        mfma16_h<m == 0>(y[t], cat4(al[m], ah[m]), bq[4 * t - m + 3]);
        if constexpr (m < 2) {
          rdB(ic<m + 2>{}, I);
          if constexpr (i >= 6) rdA(ic<m + 2>{}, ic<i - 6>{});
        } else if constexpr (m <= 4) {
          if constexpr (i < 4) rdA(ic<2 * m + (i >> 1)>{}, ic<i & 1>{});
        } else if constexpr (m + 5 < 32) {
          if constexpr (i < 2) rdA(ic<m + 5>{}, I);
        }
      });
    });
    asm volatile("s_nop 7\n\ts_nop 1" : "+v"(y[0]), "+v"(y[1]), "+v"(y[2]), "+v"(y[3]), "+v"(y[4]), "+v"(y[5]), "+v"(y[6]), "+v"(y[7]));
    STAMP(6);
    bar();
    auto send = [&](auto PAR) {
      FENCE();
#pragma unroll
      for (int i = 0; i < 4; ++i) lds_st<f32x4>(smem, S_EX + w * 4096 + i * 1024 + lane * 16, y[4 * (1 - PAR) + i]);
      FENCE();
    };
    if (par) send(ic<1>{}); else send(ic<0>{});
    bar();
    const float dsk = bf1(q_dsk);
    u32x2 ao[4];
    auto finish = [&](auto PAR) {
      FENCE();
#pragma unroll
      for (int i = 0; i < 4; ++i) {
        const f32x4 o = lds_ld<f32x4>(smem, S_EX + (w ^ 1) * 4096 + i * 1024 + lane * 16);
        const u32x2 uu = lds_ld<u32x2>(smem, S_U + kch * U_CH + (240 + 256 * (4 * PAR + i) + 16 * c15 + 4 * g) * 2);
        const f16x2 u01 = ash2(uu.x), u23 = ash2(uu.y);
        const float uf[4] = {(float)u01.x, (float)u01.y, (float)u23.x, (float)u23.y};
        float v[4];
#pragma unroll
        for (int e = 0; e < 4; ++e) v[e] = ffma1(uf[e], dsk, fmul1(fadd1(y[4 * PAR + i][e], o[e]), inv_sk));
        ao[i] = u32x2{cvt_bf2(v[0], v[1]), cvt_bf2(v[2], v[3])};
      }
      FENCE();
    };
    if (par) finish(ic<1>{}); else finish(ic<0>{});
#pragma unroll
    for (int i = 0; i < 4; ++i) {
      u32x2 o = ao[i];
      o.x = o.x == POISON ? 0xffc0ffc0u : o.x;
      o.y = o.y == POISON ? 0xffc0ffc0u : o.y;
      st64c(rWS, o, WS_A + ((4 * (4 * par + i) + (c15 >> 2)) * 512 + kd) * 128 + ((c15 & 3) * 16 + 4 * g) * 2);
    }
    STAMP(7);
    u32x4 xt0 = ld128(sX, xto, 0), xt1 = ld128(sX, xto + 32 * 1024, 0);
    u32 b2w = ld16(mk_srd(p.b2, DM * 2), t8 < 64 ? (64 * nt2 + t8) * 2 : OOB, 0);
    bar();
    const u32 ato = WS_A + (mt2 * 512 + nt2 * 64) * 128 + t8 * 16;
    asm volatile("s_waitcnt vmcnt(0)" : "+v"(xt0), "+v"(xt1), "+v"(b2w)::"memory");
    const int row0 = t8 >> 3, sl = t8 & 7;
    {
      const u32 o = S_XT + row0 * 128 + ((sl ^ (row0 & 7)) * 16);
      lds_st<u32x4>(smem, o, xt0);
      lds_st<u32x4>(smem, o + 32 * 128, xt1);
      lds_st<float>(smem, t8 < 64 ? S_B2 + t8 * 4 : S_MISC, bf1(b2w));
    }
    bar();
    u32x4 at0 = ld128c(sWS, ato, 0), at1 = ld128c(sWS, ato + 4096, 0);
    bar();
    asm volatile("s_waitcnt vmcnt(0)" : "+v"(at0), "+v"(at1)::"memory");
#pragma unroll 1
    for (int it = 0; it < (1 << 16); ++it) {
      const u32 m0 = max(max(at0.x, at0.y), max(at0.z, at0.w)), m1 = max(max(at1.x, at1.y), max(at1.z, at1.w));
      if (!__any(max(m0, m1) == POISON)) break;
      at0 = ld128c(sWS, ato, 0); at1 = ld128c(sWS, ato + 4096, 0);
      asm volatile("s_waitcnt vmcnt(0)" : "+v"(at0), "+v"(at1)::"memory");
    }
    {
      const u32 o = S_AT + row0 * 128 + (((sl >> 1) ^ (row0 & 2)) * 32) + (sl & 1) * 16;
      lds_st<u32x4>(smem, o, at0);
      lds_st<u32x4>(smem, o + 32 * 128, at1);
    }
    bar();
  } else {
    u32x4 hf[22][2];
    const u32 hb = WS_H + ((mt2 * 2 + mh) * NUNIT + kh * 22) * 2048;
    const u32 hvo = lane * 16, hvz = kh ? OOB : hvo;
    auto ldh = [&](auto J, auto S) { hf[J][S] = ld128c(sWS, J == 21 ? hvz : hvo, hb + (J * 2 + S) * 1024); };
    sfor<12>([&](auto N) { st2_piece(ic<16 + N>{}); });
    {
      const u32 hfo = WS_HF + (((mt2 >> 1) * 4 + 2 * (mt2 & 1) + mh) * 32 + c31) * 4;
      u32 hfl = ld32c(sWS, hfo, 0);
      asm volatile("s_waitcnt vmcnt(0)" : "+v"(hfl)::"memory");
      spin_flag(sWS, hfo, epoch, hfl, kh ? c31 >= 16 : c31 <= 17);
    }
    sfor<4>([&](auto J) { ldh(J, ic<0>{}); ldh(J, ic<1>{}); });
    STAMP(6);
    bar();
    sfor<3>([&](auto J) { ldh(ic<4 + J>{}, ic<0>{}); ldh(ic<4 + J>{}, ic<1>{}); });
    bar();
    sfor<G2_LEAD - 7>([&](auto J) { ldh(ic<7 + J>{}, ic<0>{}); ldh(ic<7 + J>{}, ic<1>{}); });
    STAMP(7);
    u32 wa[2];
#pragma unroll
    for (int n = 0; n < 2; ++n) wa[n] = lb + kh * W2_HALF + (4 * hh + q4) * 128 + (((2 * n + b16) ^ (q4 & 2)) * 32) + 8 * p4;
    u32x2 wl[2][4], wh[2][4];
    auto rd = [&](auto J, auto I, auto H) {
      constexpr int j = J, i = I, off = (j % W2_NS) * 4096 + ((i >> 1) * 16 + H * 8) * 128;
      if constexpr (H == 0) wl[j & 1][i] = a_dstr<off>(wa[i & 1]); else wh[j & 1][i] = a_dstr<off>(wa[i & 1]);
    };
    sfor<4>([&](auto I) { rd(ic<0>{}, I, ic<0>{}); rd(ic<0>{}, I, ic<1>{}); });
    sfor<22>([&](auto J) {
      constexpr int j = J;
      constexpr bool last = j == 21, brk = j == 8 || j == 13 || j == 17;
      if constexpr (j == 13) wait_vm<g2_ops_before(13) - g2_ops_before(10)>();
      if constexpr (j == 17) wait_vm<g2_ops_before(17) - g2_ops_before(12)>();
      if constexpr (brk) bar();
      wait_vm<g2_wait_h(j)>();
      tie(hf[j][0]); tie(hf[j][1]);
      sfor<4>([&](auto I) {
        constexpr int i = I;
        constexpr bool stg = j >= 8 && j <= 11;
        constexpr int ju = stg ? 14 + 2 * (j - 8) + (i >> 1) : 14, pi = i & 1;
        if constexpr (!brk) wait_lgkm<(last ? 6 - 2 * i : 6)>();
        if constexpr (stg && pi == 0) dma_m0(w2_lo + (ju % W2_NS) * 4096);
        mfma32_vv<j == 0 && i < 2>(acc[i & 1], cat4(wl[j & 1][i], wh[j & 1][i]), hf[j][i >> 1]);
        if constexpr ((i & 1) == 0 && j + G2_LEAD <= 21) ldh(ic<j + G2_LEAD>{}, ic<(i >> 1)>{});
        if constexpr (stg) dma_go<pi * 1024>(sW2, ju == 21 ? vw2z[pi] : vw2p[pi], w2_so + ju * 32 * 1024);
        if constexpr (!last) { rd(ic<j + 1>{}, I, ic<0>{}); rd(ic<j + 1>{}, I, ic<1>{}); }
      });
    });
    asm volatile("s_nop 7\n\ts_nop 3" : "+v"(acc[0]), "+v"(acc[1]));
    STAMP(8);
    auto send2 = [&](auto KH) {
      FENCE();
#pragma unroll
      for (int q = 0; q < 4; ++q) {
        const f32x16& as = acc[1 - KH];
        lds_st<f32x4>(smem, S_EX + w * 4096 + q * 1024 + lane * 16, f32x4{as[4 * q], as[4 * q + 1], as[4 * q + 2], as[4 * q + 3]});
      }
      FENCE();
    };
    if (kh) send2(ic<1>{}); else send2(ic<0>{});
    bar();
    const int tl = 32 * mh + c31;
    auto fin2 = [&](auto KH) {
      FENCE();
      const f32x16& ak = acc[KH];
      f32x4 pv[4], bb[4];
      u32x2 av[4], xv[4];
      u32 po[4];
#pragma unroll
      for (int q = 0; q < 4; ++q) {
        const int row = 32 * KH + 8 * q + 4 * hh + q4;
        po[q] = tl * 128 + (((4 * KH + q) ^ (tl & 7)) * 16) + hh * 8;
        pv[q] = lds_ld<f32x4>(smem, S_EX + (w ^ 2) * 4096 + q * 1024 + lane * 16);
        bb[q] = lds_ld<f32x4>(smem, S_B2 + (32 * KH + 8 * q + 4 * hh) * 4);
        av[q] = lds_tr(smem, S_AT + row * 128 + (((2 * mh + b16) ^ (row & 2)) * 32) + 8 * p4);
        xv[q] = lds_ld<u32x2>(smem, S_XT + po[q]);
      }
      FENCE();
      f32x2 o[8];
#pragma unroll
      for (int q = 0; q < 4; ++q) {
        o[2 * q] = f32x2{ak[4 * q], ak[4 * q + 1]} + f32x2{bb[q].x, bb[q].y};
        o[2 * q + 1] = f32x2{ak[4 * q + 2], ak[4 * q + 3]} + f32x2{bb[q].z, bb[q].w};
      }
      FENCE();
#pragma unroll
      for (int q = 0; q < 4; ++q) { o[2 * q] += f32x2{pv[q].x, pv[q].y}; o[2 * q + 1] += f32x2{pv[q].z, pv[q].w}; }
      FENCE();
#pragma unroll
      for (int q = 0; q < 4; ++q) { o[2 * q] += bf2(xv[q].x) + bf2(av[q].x); o[2 * q + 1] += bf2(xv[q].y) + bf2(av[q].y); }
      FENCE();
#pragma unroll
      for (int q = 0; q < 4; ++q) lds_st<u32x2>(smem, S_OT + po[q], u32x2{pk_bf16(o[2 * q]), pk_bf16(o[2 * q + 1])});
      FENCE();
    };
    if (kh) fin2(ic<1>{}); else fin2(ic<0>{});
  }
  bar();
  {
    const int row = tid >> 3, sl = tid & 7;
    const u32x4 o = lds_ld<u32x4>(smem, S_OT + row * 128 + ((sl ^ (row & 7)) * 16));
    __builtin_amdgcn_raw_buffer_store_b128(o, rOUT, (64 * mt2 + row) * 1024 + nt2 * 128 + sl * 16, 0, 0);
  }
  STAMP(9);
}

extern "C" void kernel_launch(void* const* d_in, const int* in_sizes, int n_in, void* d_out, int out_size, void* d_ws,
                              size_t ws_size, hipStream_t stream) {
  Params p;
  p.x = (const u16*)d_in[0]; p.lar = (const u16*)d_in[1]; p.aim = (const u16*)d_in[2]; p.cre = (const u16*)d_in[3];
  p.cim = (const u16*)d_in[4]; p.ldt = (const u16*)d_in[5]; p.dsk = (const u16*)d_in[6];
  p.w1 = (const u16*)d_in[8]; p.b1 = (const u16*)d_in[9]; p.w2 = (const u16*)d_in[10]; p.b2 = (const u16*)d_in[11];
  p.out = (u16*)d_out; p.ws = (char*)d_ws;
  s4d_mixer_fused<<<256, 512, 0, stream>>>(p);
}
```

```cpp
#include <hip/hip_runtime.h>
#include <stdint.h>
#include <utility>

namespace {
typedef unsigned short u16;
typedef unsigned u32;
typedef unsigned long long u64;
typedef float f32x2 __attribute__((ext_vector_type(2)));
typedef float f32x4 __attribute__((ext_vector_type(4)));
typedef float f32x16 __attribute__((ext_vector_type(16)));
typedef u32 u32x2 __attribute__((ext_vector_type(2)));
typedef u32 u32x4 __attribute__((ext_vector_type(4)));
typedef short s16x4 __attribute__((ext_vector_type(4)));
typedef __bf16 bf16x2 __attribute__((ext_vector_type(2)));
typedef _Float16 f16x2 __attribute__((ext_vector_type(2)));
typedef _Float16 f16x8 __attribute__((ext_vector_type(8)));
#define LAS __attribute__((address_space(3)))
#define DEV __device__ __forceinline__
typedef __amdgpu_buffer_rsrc_t rsrc_t;

constexpr int SEQ = 2048, DM = 512, NMODE = 128, FF = 1376, NUNIT = 43;
constexpr u32 WS_E = 0, WS_LNF = 0x1000, WS_HF = 0x2000  , WS_STATS = 0x8000, WS_XN = 0x10000, WS_A = 0x210000, WS_H = 0x410000,
              WS_END = WS_H + 64 * NUNIT * 2048, WS_STAMP = 0xA00000;
constexpr u32 SLOT = 28672, SLOT_W = 16384;
constexpr u32 W2_NS = 14, W2_HALF = W2_NS * 4096;
constexpr u32 LDS_BYTES_ = 163840;
constexpr u32 S0 = 114688;
constexpr u32 S_DUMMY = LDS_BYTES_ - 256;
constexpr u32 S_LN = S0, LN_ROW = 1088;
constexpr u32 TAB_ROW = 544, TAB_CH = 57 * TAB_ROW;
constexpr u32 S_TAB = LDS_BYTES_ - 2 * TAB_CH - 1872;
constexpr u32 S_TB = 32768;
constexpr u32 S_KC = S_TAB - 2592, KC_CH = 1296;
constexpr u32 S_CSP = LDS_BYTES_ - 1872;
constexpr u32 S_B1 = S_CSP + 768;
constexpr u32 S_MISC = S_B1 + 384;
constexpr u32 S_CS = S_MISC + 64;
constexpr u32 S_G = S0, G_PAR = 4160, G_CH = 8320;
constexpr u32 S_U = S0 + 16640, U_CH = 4608;
constexpr u32 S_EX = S0, S_EXC = S0 + 25856;
constexpr u32 S_AT = S0 + 16384, S_XT = S0 + 24576, S_OT = S0 + 32768, S_B2 = S0 + 40960;
constexpr u32 LDS_BYTES = 163840;
constexpr u32 OOB = 0x80000000u;
constexpr u32 POISON = 0xffffffffu;

#define STAMP(k) do {} while (0)
#define CSTAMP(i) do {} while (0)
#define PSTAMP(i) do {} while (0)
#define PDUMP() do {} while (0)
#define CDUMP(n, base) do {} while (0)

struct Params {
  const u16 *x, *lar, *aim, *cre, *cim, *ldt, *dsk, *w1, *b1, *w2, *b2;
  u16* out;
  char* ws;
};

DEV float asf(u32 v) { return __builtin_bit_cast(float, v); }
DEV u32 asu(float v) { return __builtin_bit_cast(u32, v); }
DEV f16x2 ash2(u32 v) { return __builtin_bit_cast(f16x2, v); }
DEV float bflo(u32 v) { return asf(v << 16); }
DEV float bfhi(u32 v) { return asf(v & 0xffff0000u); }
DEV f32x2 bf2(u32 v) { return f32x2{bflo(v), bfhi(v)}; }
DEV u32 pk_bf16(f32x2 v) { return __builtin_bit_cast(u32, __builtin_convertvector(v, bf16x2)); }
DEV u32 pk_f16(f32x2 v) { return __builtin_bit_cast(u32, __builtin_convertvector(v, f16x2)); }
DEV float rbf(float a) { return bflo(pk_bf16(f32x2{a, a})); }
DEV float bf1(u32 v) { return asf(v << 16); }
DEV f32x2 sp2(float a) { return f32x2{a, a}; }
DEV f32x2 fma2(f32x2 a, f32x2 b, f32x2 c) { return __builtin_elementwise_fma(a, b, c); }

template <int N> DEV void wait_vm() { asm volatile("s_waitcnt vmcnt(%0)" ::"n"(N) : "memory"); }
template <int N> DEV void wait_lgkm() { asm volatile("s_waitcnt lgkmcnt(%0)" ::"n"(N)); }
DEV void bar() { asm volatile("s_waitcnt lgkmcnt(0)\n\ts_barrier" ::: "memory"); }
DEV u32x4 mk_srd(const void* base, u32 bytes) {
  const u64 b = (u64)base;
  u32x4 s;
  s.x = __builtin_amdgcn_readfirstlane((u32)b);
  s.y = __builtin_amdgcn_readfirstlane((u32)(b >> 32)) & 0xffffu;
  s.z = bytes;
  s.w = 0x00020000u;
  return s;
}
DEV rsrc_t mk_rsrc(const void* base, u32 bytes) { return __builtin_amdgcn_make_buffer_rsrc((void*)base, 0, bytes, 0x00020000); }

#define BLOAD(INS, TY, NAME, FLAGS, CON)                                                                     \
  DEV TY NAME(u32x4 srd, u32 voff, u32 soff) {                                                               \
    TY r;                                                                                                    \
    asm volatile(INS " %0, %1, %2, %3 offen" FLAGS : CON(r) : "v"(voff), "s"(srd), "s"(soff) : "memory");     \
    return r;                                                                                                \
  }
BLOAD("buffer_load_dwordx4", u32x4, ld128, "", "=v")
BLOAD("buffer_load_dwordx4", u32x4, ld128c, " sc1", "=v")
BLOAD("buffer_load_dwordx4", u32x4, ld128c_a, " sc1", "=a")
BLOAD("buffer_load_dwordx2", u32x2, ld64, "", "=v")
BLOAD("buffer_load_dword", u32, ld32c, " sc1", "=v")
BLOAD("buffer_load_ushort", u32, ld16, "", "=v")
DEV void touch(const void* a, u32 lds_dummy) { asm volatile("s_mov_b32 m0, %1\n\ts_nop 0\n\tglobal_load_lds_dword %0, off" ::"v"(a), "s"(lds_dummy) : "memory"); }
template <class T> DEV void tie(T& v) { asm volatile("" : "+v"(v)); }

DEV void dma_m0(u32 lds_addr) { asm volatile("s_mov_b32 m0, %0" ::"s"(lds_addr) : "memory"); }
template <int IOFF> DEV void dma_go(u32x4 srd, u32 voff, u32 soff) {
  asm volatile("buffer_load_dwordx4 %0, %1, %2 offen offset:%3 lds" ::"v"(voff), "s"(srd), "s"(soff), "n"(IOFF) : "memory");
}
DEV void st128c(rsrc_t r, u32x4 v, u32 voff) { __builtin_amdgcn_raw_buffer_store_b128(v, r, voff, 0, 16); }
DEV void st64c(rsrc_t r, u32x2 v, u32 voff) { __builtin_amdgcn_raw_buffer_store_b64(v, r, voff, 0, 16); }
DEV void st32c(rsrc_t r, u32 v, u32 voff) { __builtin_amdgcn_raw_buffer_store_b32(v, r, voff, 0, 16); }
DEV void st64(rsrc_t r, u32x2 v, u32 voff) { __builtin_amdgcn_raw_buffer_store_b64(v, r, voff, 0, 0); }

template <class T> DEV T lds_ld(const char* smem, u32 off) { return *(const LAS T*)(smem + off); }
template <class T> DEV void lds_st(char* smem, u32 off, T v) { *(LAS T*)(smem + off) = v; }
DEV u32x2 lds_tr(const char* smem, u32 off) { return __builtin_bit_cast(u32x2, __builtin_amdgcn_ds_read_tr16_b64_v4i16((LAS s16x4*)(smem + off))); }

template <int I> using ic = std::integral_constant<int, I>;
template <class F, int... Is> DEV void sfor_impl(F&& f, std::integer_sequence<int, Is...>) { (f(ic<Is>{}), ...); }
template <int N, class F> DEV void sfor(F&& f) { sfor_impl(f, std::make_integer_sequence<int, N>{}); }
template <int OFF> DEV u32x4 a_ds128(u32 addr) { u32x4 r; asm volatile("ds_read_b128 %0, %1 offset:%2" : "=v"(r) : "v"(addr), "n"(OFF)); return r; }
template <int OFF> DEV u32x2 a_dstr(u32 addr) { u32x2 r; asm volatile("ds_read_b64_tr_b16 %0, %1 offset:%2" : "=v"(r) : "v"(addr), "n"(OFF)); return r; }
template <int O0, int O1> DEV u32x2 a_ds2x32(u32 addr) { u32x2 r; asm volatile("ds_read2_b32 %0, %1 offset0:%2 offset1:%3" : "=v"(r) : "v"(addr), "n"(O0), "n"(O1)); return r; }
DEV u32x4 cat4(u32x2 a, u32x2 b) { return __builtin_shufflevector(a, b, 0, 1, 2, 3); }
template <bool INIT> DEV void mfma32_vv(f32x16& c, u32x4 a, u32x4 b) {
  if constexpr (INIT) asm volatile("v_mfma_f32_32x32x16_bf16 %0, %1, %2, 0" : "=&v"(c) : "v"(a), "v"(b));
  else asm volatile("v_mfma_f32_32x32x16_bf16 %0, %1, %2, %0" : "+v"(c) : "v"(a), "v"(b));
}
template <bool INIT> DEV void mfma32_va(f32x16& c, u32x4 a, u32x4 b) {
  if constexpr (INIT) asm volatile("v_mfma_f32_32x32x16_bf16 %0, %1, %2, 0" : "=&v"(c) : "v"(a), "a"(b));
  else asm volatile("v_mfma_f32_32x32x16_bf16 %0, %1, %2, %0" : "+v"(c) : "v"(a), "a"(b));
}
template <bool INIT> DEV void mfma16_h(f32x4& c, u32x4 a, u32x4 b) {
  if constexpr (INIT) asm volatile("v_mfma_f32_16x16x32_f16 %0, %1, %2, 0" : "=&v"(c) : "v"(a), "v"(b));
  else asm volatile("v_mfma_f32_16x16x32_f16 %0, %1, %2, %0" : "+v"(c) : "v"(a), "v"(b));
}
DEV f32x4 mfma_h(u32x4 a, u32x4 b, f32x4 c) {
  return __builtin_amdgcn_mfma_f32_16x16x32_f16(__builtin_bit_cast(f16x8, a), __builtin_bit_cast(f16x8, b), c, 0, 0, 0);
}
template <bool INIT> DEV void mfma16_bf(f32x4& c, u32x4 a, u32x4 b) {
  if constexpr (INIT) asm volatile("v_mfma_f32_16x16x32_bf16 %0, %1, %2, 0" : "=&v"(c) : "v"(a), "v"(b));
  else asm volatile("v_mfma_f32_16x16x32_bf16 %0, %1, %2, %0" : "+v"(c) : "v"(a), "v"(b));
}
#define FENCE() __builtin_amdgcn_sched_barrier(0)

DEV void wave_sum4(float& a, float& b, float& c, float& d) {
#define S4(CTRL) "v_add_f32_dpp %0, %0, %0 " CTRL "\n\tv_add_f32_dpp %1, %1, %1 " CTRL "\n\tv_add_f32_dpp %2, %2, %2 " CTRL "\n\tv_add_f32_dpp %3, %3, %3 " CTRL "\n\t"
  asm volatile("s_nop 1\n\t" S4("row_shr:1 row_mask:0xf bank_mask:0xf bound_ctrl:1") S4("row_shr:2 row_mask:0xf bank_mask:0xf bound_ctrl:1")
               S4("row_shr:4 row_mask:0xf bank_mask:0xf bound_ctrl:1") S4("row_shr:8 row_mask:0xf bank_mask:0xf bound_ctrl:1")
               S4("row_bcast:15 row_mask:0xa bank_mask:0xf") S4("row_bcast:31 row_mask:0xc bank_mask:0xf") "s_nop 0"
               : "+v"(a), "+v"(b), "+v"(c), "+v"(d));
#undef S4
  a = asf(__builtin_amdgcn_readlane(asu(a), 63)); b = asf(__builtin_amdgcn_readlane(asu(b), 63));
  c = asf(__builtin_amdgcn_readlane(asu(c), 63)); d = asf(__builtin_amdgcn_readlane(asu(d), 63));
}
DEV void wave_sum2(float& a, float& b) {
#define S2(CTRL) "v_add_f32_dpp %0, %0, %0 " CTRL "\n\tv_add_f32_dpp %1, %1, %1 " CTRL "\n\ts_nop 0\n\t"
  asm volatile("s_nop 1\n\t" S2("row_shr:1 row_mask:0xf bank_mask:0xf bound_ctrl:1") S2("row_shr:2 row_mask:0xf bank_mask:0xf bound_ctrl:1")
               S2("row_shr:4 row_mask:0xf bank_mask:0xf bound_ctrl:1") S2("row_shr:8 row_mask:0xf bank_mask:0xf bound_ctrl:1")
               S2("row_bcast:15 row_mask:0xa bank_mask:0xf") S2("row_bcast:31 row_mask:0xc bank_mask:0xf")
               : "+v"(a), "+v"(b));
#undef S2
  a = asf(__builtin_amdgcn_readlane(asu(a), 63)); b = asf(__builtin_amdgcn_readlane(asu(b), 63));
}
template <int CTRL, int RM> DEV float dpp_add(float v) { return v + asf(__builtin_amdgcn_update_dpp(0, asu(v), CTRL, RM, 0xf, true)); }
DEV float wave_sum(float v) {
  v = dpp_add<0x111, 0xf>(v); v = dpp_add<0x112, 0xf>(v); v = dpp_add<0x114, 0xf>(v); v = dpp_add<0x118, 0xf>(v);
  v = dpp_add<0x142, 0xa>(v); v = dpp_add<0x143, 0xc>(v);
  return asf(__builtin_amdgcn_readlane(asu(v), 63));
}
DEV void cmul2(f32x2& r0, f32x2 a0, f32x2 b0, f32x2& r1, f32x2 a1, f32x2 b1) {
  f32x2 t0, t1;
  asm("v_pk_mul_f32 %0, %4, %5 op_sel_hi:[1,0]\n\tv_pk_mul_f32 %1, %6, %7 op_sel_hi:[1,0]\n\t"
      "v_pk_fma_f32 %2, %4, %5, %0 op_sel:[1,1,0] op_sel_hi:[0,1,1] neg_lo:[1,0,0]\n\t"
      "v_pk_fma_f32 %3, %6, %7, %1 op_sel:[1,1,0] op_sel_hi:[0,1,1] neg_lo:[1,0,0]"
      : "=&v"(t0), "=&v"(t1), "=&v"(r0), "=&v"(r1) : "v"(a0), "v"(b0), "v"(a1), "v"(b1));
}
DEV f32x2 cmul(f32x2 a, f32x2 b) {
  f32x2 t, r;
  asm("v_pk_mul_f32 %0, %2, %3 op_sel_hi:[1,0]\n\tv_pk_fma_f32 %1, %2, %3, %0 op_sel:[1,1,0] op_sel_hi:[0,1,1] neg_lo:[1,0,0]"
      : "=&v"(t), "=v"(r) : "v"(a), "v"(b));
  return r;
}
DEV u32 cvt_bf2(float a, float b) { u32 r; asm("v_cvt_pk_bf16_f32 %0, %1, %2" : "=v"(r) : "v"(a), "v"(b)); return r; }
DEV u32 cvt_h2(float a, float b) { u32 r; asm("v_cvt_pk_f16_f32 %0, %1, %2" : "=v"(r) : "v"(a), "v"(b)); return r; }
DEV float fadd1(float a, float b) { float r; asm("v_add_f32 %0, %1, %2" : "=v"(r) : "v"(a), "v"(b)); return r; }
DEV float fsub1(float a, float b) { float r; asm("v_sub_f32 %0, %1, %2" : "=v"(r) : "v"(a), "v"(b)); return r; }
DEV float fmul1(float a, float b) { float r; asm("v_mul_f32 %0, %1, %2" : "=v"(r) : "v"(a), "v"(b)); return r; }
DEV float ffma1(float a, float b, float c) { float r; asm("v_fma_f32 %0, %1, %2, %3" : "=v"(r) : "v"(a), "v"(b), "v"(c)); return r; }
DEV float rbf1(float a) { return bflo(cvt_bf2(a, a)); }
DEV u32 pk_f16_conj(f32x2 v) { u32 r; asm("v_cvt_pk_f16_f32 %0, %1, -%2" : "=v"(r) : "v"(v.x), "v"(v.y)); return r; }
DEV f32x2 gelu2(f32x2 v) {
  const f32x2 s = v * fma2(v * v, sp2(-0.10294323970f), sp2(-2.3022081986f));
  const f32x2 d = f32x2{__builtin_amdgcn_exp2f(s.x), __builtin_amdgcn_exp2f(s.y)} + sp2(1.0f);
  return v * f32x2{__builtin_amdgcn_rcpf(d.x), __builtin_amdgcn_rcpf(d.y)};
}
DEV void spin_flag(u32x4 srd, u32 voff, u32 epoch, u32 val, bool need) {
#pragma unroll 1
  for (int it = 0; it < (1 << 16); ++it) {
    if (__all(!need || val == epoch)) break;
    __builtin_amdgcn_s_sleep(1);
    val = ld32c(srd, voff, 0);
    asm volatile("s_waitcnt vmcnt(0)" : "+v"(val)::"memory");
  }
}

constexpr int G2_LEAD = 6;
constexpr int g2_unit_ops(int j) { return (j + G2_LEAD <= 21 ? 2 : 0) + (j <= 5 ? 2 : 0) + ((j >= 7 && j <= 10) ? 4 : 0); }
constexpr int g2_ops_before(int j) { int n = 2 * G2_LEAD; for (int i = 0; i < j; ++i) n += g2_unit_ops(i); return n; }
constexpr int g2_pos_h(int j) {
  if (j < G2_LEAD) return 2 * j + 2;
  const int i = j - G2_LEAD;
  return g2_ops_before(i) + 2 + (i <= 5 ? 1 : ((i >= 7 && i <= 10) ? 2 : 0));
}
constexpr int g2_wait_h(int j) { return g2_ops_before(j) - g2_pos_h(j); }

}

__global__ __launch_bounds__(512, 2) void s4d_mixer_fused(Params p) {
  __shared__ __attribute__((aligned(16))) char smem[LDS_BYTES];
  const int tid = threadIdx.x, lane = tid & 63, t8 = tid & 255;
  const int wid = __builtin_amdgcn_readfirstlane(tid >> 6);
  const int side = wid >> 2, w = wid & 3;
  const int b = blockIdx.x, g8 = b & 7, r = b >> 3;
  const int mt = 4 * (g8 >> 1) + (r >> 3), nt = 8 * (g8 & 1) + (r & 7);
  const int ustart = (nt * NUNIT) >> 4, unext = ((nt + 1) * NUNIT) >> 4;
  const int tg = 16 * mt + nt;
  const int cp = 32 * g8 + r;
  const int mt2 = 4 * g8 + (r >> 3), nt2 = r & 7;
  const int c15 = lane & 15, g = lane >> 4;
  const int c31 = lane & 31, hh = lane >> 5, b16 = (lane >> 4) & 1;
  const int q4 = (lane & 15) >> 2, p4 = lane & 3;
  const int kch = w >> 1, kd = 2 * cp + kch, kh = w >> 1, mh = w & 1;

  const u32x4 sX = mk_srd(p.x, SEQ * DM * 2), sWS = mk_srd(p.ws, WS_END);
  const rsrc_t rWS = mk_rsrc(p.ws, WS_END), rOUT = mk_rsrc(p.out, SEQ * DM * 2);
  const u32x4 sW1 = mk_srd(p.w1, DM * FF * 2), sW2 = mk_srd(p.w2, FF * DM * 2);
  const u32 lb = (u32)(uintptr_t)(LAS char*)smem;
  STAMP(0);
  PSTAMP(0);

  auto load_eword = [&]() { u32 e; asm volatile("s_load_dword %0, %1, 0x0" : "=s"(e) : "s"(p.ws) : "memory"); return e; };
  u32 vx[4], vw[3];
#pragma unroll
  for (int j = 0; j < 4; ++j) vx[j] = (8 * j + (lane >> 3)) * 1024 - j * 1024 + (((lane & 7) ^ ((4 * j + (lane >> 4)) & 7)) * 16);
#pragma unroll
  for (int j = 0; j < 3; ++j) {
    const int P = (3 * w + j) * 64 + lane, k = P / 12, sl = P - 12 * k;
    vw[j] = k * (FF * 2) + sl * 16 - j * 1024;
  }
  const u32 x_so = (mt * 128 + 32 * w) * 1024, x_lo = lb + 32 * w * 128, w_so = ustart * 64, w_lo = lb + SLOT_W + 3 * w * 1024;
  auto st1_m0 = [&](auto KC, auto J) {
    if constexpr (J == 0) dma_m0(x_lo + (KC & 3) * SLOT);
    if constexpr (J == 4) dma_m0(w_lo + (KC & 3) * SLOT);
  };
  auto st1_go = [&](auto KC, auto J) {
    if constexpr (J < 4) dma_go<J * 1024>(sX, vx[J], x_so + KC * 128); else dma_go<(J - 4) * 1024>(sW1, vw[J - 4], w_so + KC * 64 * (FF * 2));
  };
  auto stage = [&](auto KC) { sfor<7>([&](auto J) { if constexpr (J == 0 || J == 4) { st1_m0(KC, J); asm volatile("s_nop 0"); } st1_go(KC, J); }); };
  u32 ixp[5];
  auto prep_ix = [&]() {
#pragma unroll
    for (int e = 0; e < 9; ++e) {
      const int l = 2047 - (8 * t8 + e);
      const u32 rb = pk_bf16(sp2((float)l)) & 0xffffu;
      const u32 ix = (l < 0 ? 641u : (l < 128 ? (u32)l : rb - 17024u)) * 2;
      if (e & 1) ixp[e >> 1] |= ix << 16; else ixp[e >> 1] = ix;
    }
#pragma unroll
    for (int e = 0; e < 5; ++e) tie(ixp[e]);
  };
  u32 epoch;
  u32 q_lar, q_aim, q_cre, q_cim, q_cre2, q_cim2, q_ldt, q_dsk;
  u32x2 b1v;
  auto load_small = [&]() {
    const u32 vo = (64 * (w & 1) + lane) * 2, so = kd * NMODE * 2;
    const u32x4 sCr = mk_srd(p.cre, DM * NMODE * 2), sCi = mk_srd(p.cim, DM * NMODE * 2);
    q_lar = ld16(mk_srd(p.lar, DM * NMODE * 2), vo, so); q_aim = ld16(mk_srd(p.aim, DM * NMODE * 2), vo, so);
    q_cre = ld16(sCr, vo, so); q_cim = ld16(sCi, vo, so);
    q_cre2 = ld16(sCr, vo ^ 128, so); q_cim2 = ld16(sCi, vo ^ 128, so);
    q_ldt = ld16(mk_srd(p.ldt, DM * 2), 0, kd * 2); q_dsk = ld16(mk_srd(p.dsk, DM * 2), 0, kd * 2);
    b1v = ld64(mk_srd(p.b1, FF * 2), lane < 24 ? lane * 8 : OOB, ustart * 64);
  };
#define TIE_SMALL "+v"(q_lar), "+v"(q_aim), "+v"(q_cre), "+v"(q_cim), "+v"(q_cre2), "+v"(q_cim2), "+v"(q_ldt), "+v"(q_dsk), "+v"(b1v)
  f32x16 acc1[3];
  f32x4 csp[3];
  u32x4 stv;
  if (side == 0) {
    u32x4 xr0 = ld128(sX, lane * 16, (8 * tg + 2 * w) * 1024), xr1 = ld128(sX, lane * 16, (8 * tg + 2 * w + 1) * 1024);
    {
      u64 pc;
      asm volatile("s_getpc_b64 %0" : "=s"(pc));
      const char* a;
      if (t8 < 96) a = (const char*)p.x + (512 * (g8 >> 1) + 16 * r + t8 / 6) * 1024 + (2 + t8 % 6) * 128;
      else if (t8 < 192) a = (const char*)p.w1 + (256 + 8 * r + (t8 - 96) / 12) * (FF * 2) + (g8 & 1) * 1280 + ((t8 - 96) % 12) * 120;
      else a = (const char*)pc + ((r * 64 + lane) % 208) * 128;
      touch(a, lb + S_DUMMY);
    }
    u32 eword = load_eword();
    prep_ix();
    asm volatile("s_waitcnt vmcnt(1)" : "+v"(xr0), "+v"(xr1)::"memory");
    PSTAMP(1);
    STAMP(1);
    {
      const u32x4 xr[2] = {xr0, xr1};
      f32x2 x[2][4];
      float x0[2], sm[2], sq[2];
#pragma unroll
      for (int t = 0; t < 2; ++t) {
#pragma unroll
        for (int i = 0; i < 4; ++i) x[t][i] = bf2(xr[t][i]);
        x0[t] = asf(__builtin_amdgcn_readfirstlane(asu(x[t][0].x)));
        f32x2 s = {0.f, 0.f}, q = {0.f, 0.f};
#pragma unroll
        for (int i = 0; i < 4; ++i) { const f32x2 d = x[t][i] - sp2(x0[t]); s += d; q = fma2(d, d, q); }
        sm[t] = s.x + s.y; sq[t] = q.x + q.y;
      }
      wave_sum4(sm[0], sq[0], sm[1], sq[1]);
      float m1[2], r1[2], s2[2], q2[2];
#pragma unroll
      for (int t = 0; t < 2; ++t) {
        const float md = sm[t] * (1.0f / DM);
        const float var = fmaxf(__builtin_fmaf(-md, md, sq[t] * (1.0f / DM)), 0.f);
        m1[t] = rbf(x0[t] + md);
        r1[t] = __builtin_amdgcn_rcpf(rbf(__builtin_amdgcn_sqrtf(rbf(rbf(var) + 1e-5f))));
      }
#pragma unroll
      for (int t = 0; t < 2; ++t) {
        f32x2 s = {0.f, 0.f}, q = {0.f, 0.f};
        u32x4 hv;
#pragma unroll
        for (int i = 0; i < 4; ++i) {
          const f32x2 n = bf2(pk_bf16(bf2(pk_bf16(x[t][i] - sp2(m1[t]))) * sp2(r1[t])));
          s += n; q = fma2(n, n, q);
          hv[i] = pk_f16(n);
        }
        lds_st<u32x4>(smem, S_LN + (2 * w + t) * LN_ROW + lane * 16, hv);
        s2[t] = s.x + s.y; q2[t] = q.x + q.y;
      }
      wave_sum4(s2[0], q2[0], s2[1], q2[1]);
      float al[2], be[2];
#pragma unroll
      for (int t = 0; t < 2; ++t) {
        const float mean2 = s2[t] * (1.0f / DM);
        const float var2 = fmaxf(__builtin_fmaf(-mean2, mean2, q2[t] * (1.0f / DM)), 0.f);
        const float r2 = __builtin_amdgcn_rcpf(rbf(__builtin_amdgcn_sqrtf(rbf(rbf(var2) + 1e-5f))));
        al[t] = r1[t] * r2;
        be[t] = __builtin_fmaf(m1[t], r1[t], rbf(mean2)) * r2;
      }
      PSTAMP(2);
      asm volatile("s_waitcnt lgkmcnt(0)" : "+s"(eword)::"memory");
      epoch = eword * 0x9E3779B1u + 0x7F4A7C15u;
      const u32x4 sv = {asu(lane ? al[1] : al[0]), epoch, asu(lane ? be[1] : be[0]), epoch};
      st128c(rWS, sv, lane < 2 ? WS_STATS + (8 * tg + 2 * w + lane) * 16 : OOB);
    }
    PSTAMP(3); PSTAMP(4);
    const u32 xa0 = lb + (32 * w + c31) * 128 + ((hh ^ ((c31 >> 1) & 7)) * 16);
    const u32 wa0 = lb + SLOT_W + (8 * hh + q4) * 192 + b16 * 32 + 8 * p4;
    u32 xa[2][4], wa[2];
#pragma unroll
    for (int s = 0; s < 4; ++s) { xa[0][s] = xa0 ^ (s * 32); xa[1][s] = xa[0][s] + 2 * SLOT; }
    wa[0] = wa0; wa[1] = wa0 + 2 * SLOT;
    u32x4 xf[2];
    u32x2 wl[2][3], wh[2][3];
    auto rd = [&](auto S, auto R) {
      constexpr int s = S, rr = R, buf = s & 1, kc = s >> 2, sl = kc & 3, so = (sl & 1) * SLOT, sp = s & 3;
      if constexpr (rr == 0) xf[buf] = a_ds128<so>(xa[sl >> 1][sp]);
      else {
        constexpr int u = (rr - 1) >> 1, hf = (rr - 1) & 1, off = so + u * 64 + (sp * 16 + hf * 4) * 192;
        if constexpr (hf == 0) wl[buf][u] = a_dstr<off>(wa[sl >> 1]); else wh[buf][u] = a_dstr<off>(wa[sl >> 1]);
      }
    };
    PSTAMP(5); PSTAMP(6);
    bar();
    PSTAMP(7); PDUMP();
    CSTAMP(0);
    sfor<7>([&](auto R) { rd(ic<0>{}, R); });
    sfor<32>([&](auto S) {
      constexpr int s = S, buf = s & 1, sp = s & 3, kc = s >> 2;
      constexpr bool last = s == 31, brk = sp == 2 && kc < 7;
      sfor<3>([&](auto U) {
        constexpr int u = U;
        if constexpr (u == 0) wait_lgkm<4>();
        else if constexpr (u == 1) {
          wait_lgkm<(last ? 0 : 3)>();
          if constexpr (brk) {
            constexpr int n = kc + 1;
            CSTAMP(2 * n - 1);
            asm volatile("s_barrier" ::: "memory");
            CSTAMP(2 * n);
            if constexpr (n == 4) load_small();
            if constexpr (n == 7) stv = ld128c(sWS, WS_STATS + (128 * mt + 32 * w + c31) * 16, 0);
          }
        }
        mfma32_vv<s == 0>(acc1[u], cat4(wl[buf][u], wh[buf][u]), xf[buf]);
        if constexpr (!last) {
          if constexpr (u == 0) sfor<3>([&](auto R) { rd(ic<s + 1>{}, R); });
          if constexpr (u == 1) sfor<2>([&](auto R) { rd(ic<s + 1>{}, ic<3 + R>{}); });
          if constexpr (u == 2) sfor<2>([&](auto R) { rd(ic<s + 1>{}, ic<5 + R>{}); });
        }
      });
    });
    asm volatile("s_nop 7\n\ts_nop 3" : "+v"(acc1[0]), "+v"(acc1[1]), "+v"(acc1[2]));
    CSTAMP(15); CDUMP(16, 0);
    STAMP(2);
    bar();
#pragma unroll
    for (int q = 0; q < 6; ++q) {
      const f32x16& a = acc1[q < 2 ? 1 : 2];
      const int o = q < 2 ? 8 + 4 * q : 4 * (q - 2);
      lds_st<f32x4>(smem, S_TB + w * 6144 + q * 1024 + lane * 16, f32x4{a[o], a[o + 1], a[o + 2], a[o + 3]});
    }
    asm volatile("s_waitcnt vmcnt(0)" : "+v"(stv), TIE_SMALL :: "memory");
  } else {
    const u32 wc0 = lb + SLOT_W + (32 * (w >> 1) + 8 * g + q4) * 192 + 3 * (w & 1) * 32 + 8 * p4;
    u32x4 ones = {0x3f803f80u, 0x3f803f80u, 0x3f803f80u, 0x3f803f80u};
    tie(ones);
    u32x2 cl[3], ch[3];
    auto cs_rd = [&](auto KC) {
      const u32 a = wc0 + (KC & 3) * SLOT;
      sfor<3>([&](auto I) { cl[I] = a_dstr<I * 32>(a); ch[I] = a_dstr<I * 32 + 4 * 192>(a); });
      FENCE();
    };
    auto cs_sum = [&](auto KC) {
      FENCE();
      wait_lgkm<0>();
      sfor<3>([&](auto I) { mfma16_bf<KC == 0>(csp[I], cat4(cl[I], ch[I]), ones); });
    };
    stage(ic<0>{}); stage(ic<1>{}); stage(ic<2>{});
    u32 eword = load_eword();
    prep_ix();
    asm volatile("s_waitcnt lgkmcnt(0)" : "+s"(eword)::"memory");
    epoch = eword * 0x9E3779B1u + 0x7F4A7C15u;
    PSTAMP(1); PSTAMP(2); PSTAMP(3); PSTAMP(4); PSTAMP(5);
    wait_vm<14>();
    PSTAMP(6);
    bar();
    PSTAMP(7); PDUMP();
    CSTAMP(0);
    STAMP(1);
    cs_rd(ic<0>{}); stage(ic<3>{}); cs_sum(ic<0>{});
    wait_vm<14>();
    CSTAMP(1);
    bar();
    CSTAMP(2);
    cs_rd(ic<1>{});
#pragma unroll
    for (int i = 0; i < 2; ++i) {
      const u32 a = S_LN + q4 * LN_ROW + ((2 * w + i) * 64 + 16 * g + 4 * p4) * 2;
      st128c(rWS, cat4(lds_tr(smem, a), lds_tr(smem, a + 4 * LN_ROW)), WS_XN + tg * 8192 + ((2 * w + i) * 64 + lane) * 16);
    }
    {
#pragma unroll
      for (int i = 0; i < 2; ++i) {
        const int ln = (2 * w + i) * 8 + (lane >> 3);
        st128c(rWS, u32x4{POISON, POISON, POISON, POISON}, WS_A + ((ln >> 1) * 512 + 2 * cp + (ln & 1)) * 128 + (lane & 7) * 16);
      }
    }
    load_small();
    cs_sum(ic<1>{});
    wait_vm<20>();
    CSTAMP(3);
    bar();
    CSTAMP(4);
    cs_rd(ic<2>{}); stage(ic<4>{}); cs_sum(ic<2>{});
    wait_vm<20>();
    CSTAMP(5);
    bar();
    CSTAMP(6);
    cs_rd(ic<3>{}); stage(ic<5>{}); cs_sum(ic<3>{});
    asm volatile("s_waitcnt vmcnt(7)" : TIE_SMALL :: "memory");
    CSTAMP(7);
    bar();
    CSTAMP(8);
    st32c(rWS, epoch, t8 == 0 ? WS_LNF + tg * 4 : OOB);
    lds_st<f32x4>(smem, lane < 24 ? S_B1 + lane * 16 : S_MISC, f32x4{bflo(b1v.x), bfhi(b1v.x), bflo(b1v.y), bfhi(b1v.y)});
    cs_rd(ic<4>{}); stage(ic<6>{}); cs_sum(ic<4>{});
    wait_vm<8>();
    CSTAMP(9);
    bar();
    CSTAMP(10);
    cs_rd(ic<5>{}); stage(ic<7>{}); cs_sum(ic<5>{});
    wait_vm<7>();
    CSTAMP(11);
    bar();
    CSTAMP(12);
    cs_rd(ic<6>{});
#pragma unroll
    for (int i = 0; i < 2; ++i) {
      const int ix = min(i * 256 + t8, 343);
      touch((const char*)p.w2 + (43 * r + (ix >> 3)) * 1024 + (ix & 7) * 128, lb + S_DUMMY);
    }
    cs_sum(ic<6>{});
    wait_vm<2>();
    CSTAMP(13);
    bar();
    CSTAMP(14);
    stv = ld128c(sWS, WS_STATS + (128 * mt + 32 * w + c31) * 16, 0);
    cs_rd(ic<7>{}); cs_sum(ic<7>{});
    asm volatile("s_nop 7\n\ts_nop 1" : "+v"(csp[0]), "+v"(csp[1]), "+v"(csp[2]));
#pragma unroll
    for (int i = 0; i < 3; ++i) lds_st<f32x4>(smem, c15 == 0 ? S_CSP + ((w >> 1) * 96 + (3 * (w & 1) + i) * 16 + 4 * g) * 4 : S_MISC, csp[i]);
    CSTAMP(15); CDUMP(16, 20);
    STAMP(2);
    bar();
    const int f = t8 < 96 ? t8 : 0;
    lds_st<float>(smem, t8 < 96 ? S_CS + t8 * 4 : S_MISC, lds_ld<float>(smem, S_CSP + f * 4) + lds_ld<float>(smem, S_CSP + 384 + f * 4));
    asm volatile("s_waitcnt vmcnt(0)" : "+v"(stv)::"memory");
  }
#pragma unroll 1
  for (int it = 0; it < (1 << 16); ++it) {
    if (__all(stv.y == epoch && stv.w == epoch)) break;
    __builtin_amdgcn_s_sleep(1);
    stv = ld128c(sWS, WS_STATS + (128 * mt + 32 * w + c31) * 16, 0);
    asm volatile("s_waitcnt vmcnt(0)" : "+v"(stv)::"memory");
  }
  bar();
  CSTAMP(0);
  STAMP(3);

  const u32 vw2 = (lane >> 3) * 1024 + ((((lane & 7) >> 1) ^ ((lane >> 3) & 2)) * 32) + (lane & 1) * 16;
  const u32 vw2p[2] = {vw2, vw2 + 7168};
  const u32 vw2z[2] = {kh ? OOB : vw2p[0], kh ? OOB : vw2p[1]};
  const u32 w2_lo = lb + kh * W2_HALF + 2 * mh * 1024, w2_so = (kh * 22 * 32 + 16 * mh) * 1024 + nt2 * 128;
  auto st2_piece = [&](auto N) {
    constexpr int j = N >> 1, i = N & 1;
    FENCE();
    if constexpr (i == 0) { dma_m0(w2_lo + (j % W2_NS) * 4096); asm volatile("s_nop 0"); }
    dma_go<i * 1024>(sW2, j == 21 ? vw2z[i] : vw2p[i], w2_so + j * 32 * 1024);
    FENCE();
  };

  {
    f32x4 av[6];
    if (side) {
#pragma unroll
      for (int q = 0; q < 6; ++q) av[q] = lds_ld<f32x4>(smem, S_TB + w * 6144 + q * 1024 + lane * 16);
    } else {
#pragma unroll
      for (int q = 0; q < 6; ++q) {
        const f32x16& a = acc1[q >> 2];
        const int o = 4 * (q & 3);
        av[q] = f32x4{a[o], a[o + 1], a[o + 2], a[o + 3]};
      }
    }
    const f32x2 al = sp2(asf(stv.x)), nb = sp2(-asf(stv.z));
    const u32 hso = WS_H + ((mt * 4 + w) * NUNIT + ustart) * 2048 + 3 * side * 1024 + lane * 16;
    const u32 cb = S_B1 + hh * 16 + 3 * side * 64;
    f32x4 bi[6], cs[6];
#pragma unroll
    for (int q = 0; q < 6; ++q) { bi[q] = lds_ld<f32x4>(smem, cb + q * 32); cs[q] = lds_ld<f32x4>(smem, cb + (S_CS - S_B1) + q * 32); }
    f32x2 v[12], t[12];
#pragma unroll
    for (int q = 0; q < 6; ++q) { t[2 * q] = fma2(nb, f32x2{cs[q].x, cs[q].y}, f32x2{bi[q].x, bi[q].y}); t[2 * q + 1] = fma2(nb, f32x2{cs[q].z, cs[q].w}, f32x2{bi[q].z, bi[q].w}); }
    if (side == 0) { st2_piece(ic<0>{}); st2_piece(ic<1>{}); }
    FENCE();
#pragma unroll
    for (int q = 0; q < 6; ++q) { v[2 * q] = fma2(al, f32x2{av[q].x, av[q].y}, t[2 * q]); v[2 * q + 1] = fma2(al, f32x2{av[q].z, av[q].w}, t[2 * q + 1]); }
    if (side == 0) { st2_piece(ic<2>{}); st2_piece(ic<3>{}); }
    FENCE();
#pragma unroll
    for (int k = 0; k < 12; ++k) t[k] = v[k] * v[k];
    if (side == 0) { st2_piece(ic<4>{}); st2_piece(ic<5>{}); }
    FENCE();
#pragma unroll
    for (int k = 0; k < 12; ++k) t[k] = fma2(t[k], sp2(-0.10294323970f), sp2(-2.3022081986f));
    if (side == 0) { st2_piece(ic<6>{}); st2_piece(ic<7>{}); }
    FENCE();
#pragma unroll
    for (int k = 0; k < 12; ++k) t[k] = v[k] * t[k];
    if (side == 0) { st2_piece(ic<8>{}); st2_piece(ic<9>{}); }
    FENCE();
#pragma unroll
    for (int k = 0; k < 12; ++k) t[k] = f32x2{__builtin_amdgcn_exp2f(t[k].x), __builtin_amdgcn_exp2f(t[k].y)};
    if (side == 0) { st2_piece(ic<10>{}); st2_piece(ic<11>{}); }
    FENCE();
#pragma unroll
    for (int k = 0; k < 12; ++k) t[k] += sp2(1.0f);
    if (side == 0) { st2_piece(ic<12>{}); st2_piece(ic<13>{}); }
    FENCE();
#pragma unroll
    for (int k = 0; k < 12; ++k) t[k] = f32x2{__builtin_amdgcn_rcpf(t[k].x), __builtin_amdgcn_rcpf(t[k].y)};
    if (side == 0) { st2_piece(ic<14>{}); st2_piece(ic<15>{}); }
    FENCE();
#pragma unroll
    for (int k = 0; k < 12; ++k) v[k] *= t[k];
    FENCE();
#pragma unroll
    for (int i = 0; i < 3; ++i)
      st128c(rWS, u32x4{pk_bf16(v[4 * i]), pk_bf16(v[4 * i + 1]), pk_bf16(v[4 * i + 2]), pk_bf16(v[4 * i + 3])},
             ustart + ((3 * side + i) >> 1) < unext ? hso + i * 1024 : OOB);
  }
  CSTAMP(1);
  u32 lnf = ld32c(sWS, WS_LNF + t8 * 4, 0);
  st32c(rWS, epoch, (b == 0 && tid == 0) ? WS_E : OOB);
  STAMP(4);
  float inv_sk;
  {
    const int mh2 = w & 1;
    const float dt = rbf(expf(bf1(q_ldt)));
    const float zr = -rbf(expf(bf1(q_lar))) * dt, zi = bf1(q_aim) * dt;
    f32x2 C = f32x2{bf1(q_cre), bf1(q_cim)} * sp2(2.0f * dt);
    const float bd = wave_sum((__builtin_fabsf(C.x) + __builtin_fabsf(C.y)) + (__builtin_fabsf(bf1(q_cre2)) + __builtin_fabsf(bf1(q_cim2))) * (2.0f * dt));
    int se = 267 - (int)((asu(bd) >> 23) & 0xff);
    se = se < 1 ? 1 : (se > 253 ? 253 : se);
    C *= sp2(asf((u32)se << 23));
    inv_sk = asf((u32)(254 - se) << 23);
    const float e1 = zr * 1.44269504089f;
    const float ph = zi * 0.15915494309f;
    const float pl = __builtin_fmaf(zi, 0.15915494309f, -ph) + zi * 6.4206383e-9f;
    auto cexp = [&](float pw) {
      const float m = __builtin_amdgcn_exp2f(e1 * pw);
      const float a = __builtin_amdgcn_fractf(ph * pw) + pl * pw;
      return f32x2{__builtin_amdgcn_cosf(a), __builtin_amdgcn_sinf(a)} * sp2(m);
    };
    CSTAMP(2);
    const u32 tl = S_TAB + kch * TAB_CH + (64 * mh2 + lane) * 4;
    auto putX = [&](int row, f32x2 v) { lds_st<u32>(smem, tl + (16 + row) * TAB_ROW, pk_f16(v)); };
    if (side == 0) {
      f32x2 P[8], E[3], CE[8];
      P[1] = cexp(1.f); E[0] = cexp(128.f);
      CE[0] = C;
      cmul2(P[2], P[1], P[1], E[1], E[0], E[0]);
      cmul2(CE[1], C, E[0], P[3], P[2], P[1]);
      cmul2(P[4], P[2], P[2], E[2], E[1], E[1]);
      cmul2(CE[2], CE[0], E[1], CE[3], CE[1], E[1]);
      cmul2(P[5], P[4], P[1], P[6], P[4], P[2]);
      cmul2(CE[4], CE[0], E[2], CE[5], CE[1], E[2]);
      cmul2(CE[6], CE[2], E[2], CE[7], CE[3], E[2]);
      P[7] = cmul(P[4], P[3]);
      putX(0, CE[0]); putX(8, CE[1]);
#pragma unroll
      for (int i = 1; i < 8; ++i) {
        f32x2 a, c;
        cmul2(a, CE[0], P[i], c, CE[1], P[i]);
        putX(i, a); putX(8 + i, c);
      }
      putX(16, CE[2]); putX(20, CE[3]);
#pragma unroll
      for (int i = 1; i < 4; ++i) {
        f32x2 a, c;
        cmul2(a, CE[2], P[2 * i], c, CE[3], P[2 * i]);
        putX(16 + i, a); putX(20 + i, c);
      }
      putX(24, CE[4]); putX(26, CE[5]); putX(28, CE[6]); putX(30, CE[7]);
      f32x2 a, c;
      cmul2(a, CE[4], P[4], c, CE[5], P[4]); putX(25, a); putX(27, c);
      cmul2(a, CE[6], P[4], c, CE[7], P[4]); putX(29, a); putX(31, c);
    } else {
      f32x2 Y[16], E[5], CE[9];
      Y[1] = cexp(8.f); E[0] = cexp(128.f);
      Y[0] = f32x2{1.f, 0.f};
      auto putY = [&](int i) { lds_st<u32>(smem, tl + i * TAB_ROW, pk_f16_conj(Y[i])); };
      cmul2(Y[2], Y[1], Y[1], E[1], E[0], E[0]);
      cmul2(Y[3], Y[2], Y[1], Y[4], Y[2], Y[2]);
      putY(0); putY(1); putY(2);
      cmul2(E[2], E[1], E[1], Y[5], Y[4], Y[1]);
      cmul2(Y[6], Y[4], Y[2], Y[7], Y[4], Y[3]);
      putY(3); putY(4);
      cmul2(Y[8], Y[4], Y[4], E[3], E[2], E[2]);
      putY(5); putY(6); putY(7);
      cmul2(Y[9], Y[8], Y[1], Y[10], Y[8], Y[2]);
      cmul2(E[4], E[3], E[3], CE[0], C, E[3]);
      putY(8);
      cmul2(Y[11], Y[8], Y[3], Y[12], Y[8], Y[4]);
      putY(9); putY(10);
      cmul2(Y[13], Y[8], Y[5], Y[14], Y[8], Y[6]);
      putY(11); putY(12);
      cmul2(Y[15], Y[8], Y[7], CE[1], CE[0], E[0]);
      putY(13); putY(14);
      cmul2(CE[2], CE[0], E[1], CE[8], C, E[4]);
      putY(15); putX(32, CE[0]); putX(33, CE[1]);
      cmul2(CE[3], CE[1], E[1], CE[4], CE[0], E[2]);
      putX(34, CE[2]); putX(40, CE[8]);
      cmul2(CE[5], CE[1], E[2], CE[6], CE[2], E[2]);
      putX(35, CE[3]); putX(36, CE[4]);
      CE[7] = cmul(CE[3], E[2]);
      putX(37, CE[5]); putX(38, CE[6]);
      putX(39, CE[7]);
    }
    CSTAMP(3);
    asm volatile("s_waitcnt vmcnt(1)" : "+v"(lnf)::"memory");
    st32c(rWS, epoch, lane == 0 ? WS_HF + (((mt * 4 + w) * 16 + nt) * 2 + side) * 4 : OOB);
    spin_flag(sWS, WS_LNF + t8 * 4, epoch, lnf, true);
    u32x4 ug = ld128c(sWS, WS_XN + t8 * 8192 + cp * 32 + side * 16, 0);
    CSTAMP(4);
    bar();
    CSTAMP(5);
    const int tile = side ? 2 : mh2;
    const u32 kc = S_KC + kch * KC_CH;
    if (!(side && mh2)) {
      const u32 rd = S_TAB + kch * TAB_CH + c15 * TAB_ROW + g * 16;
      const u32 ra = S_TAB + kch * TAB_CH + (16 + min(16 * tile + c15, 40)) * TAB_ROW + g * 16;
      u32x4 yf[8], xf[8];
#pragma unroll
      for (int s = 0; s < 8; ++s) { yf[s] = lds_ld<u32x4>(smem, rd + s * 64); xf[s] = lds_ld<u32x4>(smem, ra + s * 64); }
      f32x4 a0 = {0, 0, 0, 0}, a1 = {0, 0, 0, 0};
#pragma unroll
      for (int s = 0; s < 4; ++s) { a0 = mfma_h(xf[2 * s], yf[2 * s], a0); a1 = mfma_h(xf[2 * s + 1], yf[2 * s + 1], a1); }
      const f32x4 v = a0 + a1;
      const u32 h01 = pk_f16(f32x2{v[0], v[1]}), h23 = pk_f16(f32x2{v[2], v[3]});
      if (tile == 0) {
        lds_st<u32x2>(smem, kc + (128 * (g >> 1) + 8 * c15 + 4 * (g & 1)) * 2, u32x2{h01, h23});
      } else if (tile == 1) {
        const u32 t1a = g < 2 ? 256 + 64 * g + 4 * c15 : 384 + 64 * (g - 2) + 2 * c15;
        lds_st<u32>(smem, kc + t1a * 2, h01);
        lds_st<u32>(smem, kc + (t1a + (g < 2 ? 2 : 32)) * 2, h23);
      } else {
        const u32 t2 = g < 2 ? 512 + 64 * g + c15 : 644, st2 = g < 2 ? 16 : 0;
        lds_st<u16>(smem, kc + ((g == 2 && c15 == 0) ? 640 : t2) * 2, (u16)h01);
        lds_st<u16>(smem, kc + (t2 + st2) * 2, (u16)(h01 >> 16));
        lds_st<u16>(smem, kc + (t2 + 2 * st2) * 2, (u16)h23);
        lds_st<u16>(smem, kc + (t2 + 3 * st2) * 2, (u16)(h23 >> 16));
      }
    } else if (lane == 0) lds_st<u16>(smem, kc + 641 * 2, (u16)0);
    CSTAMP(6);
    bar();
    CSTAMP(7);
    asm volatile("s_waitcnt vmcnt(0)" : "+v"(ug)::"memory");
    {
      u32 e[9];
#pragma unroll
      for (int i = 0; i < 9; ++i) e[i] = lds_ld<u16>(smem, S_KC + side * KC_CH + ((i & 1) ? ixp[i >> 1] >> 16 : ixp[i >> 1] & 0xffffu));
      const u32x4 a0 = {e[0] | (e[1] << 16), e[2] | (e[3] << 16), e[4] | (e[5] << 16), e[6] | (e[7] << 16)};
      const u32x4 a1 = {e[1] | (e[2] << 16), e[3] | (e[4] << 16), e[5] | (e[6] << 16), e[7] | (e[8] << 16)};
      lds_st<u32x4>(smem, S_G + side * G_CH + t8 * 16, a0);
      lds_st<u32x4>(smem, S_G + side * G_CH + G_PAR + t8 * 16, a1);
      const u32x4 z4 = {0, 0, 0, 0};
      if (t8 < 4) lds_st<u32x4>(smem, S_G + side * G_CH + (t8 >> 1) * G_PAR + 4096 + (t8 & 1) * 16, z4);
      if (t8 < 30) lds_st<u32x4>(smem, S_U + side * U_CH + t8 * 16, z4);
      lds_st<u32x4>(smem, S_U + side * U_CH + 480 + t8 * 16, ug);
    }
    CSTAMP(8);
    bar();
    CSTAMP(9);
    if (side) CDUMP(10, 48); else CDUMP(10, 38);
  }
  STAMP(5);

  const int ch0 = 64 * nt2 + 32 * kh + 4 * hh;
  const u32 xto = (64 * mt2 + (t8 >> 3)) * 1024 + nt2 * 128 + (t8 & 7) * 16;
  f32x16 acc[2];
  if (side == 0) {
    const int par = w & 1;
    const u32 ub = lb + S_U + kch * U_CH + 64 - 64 * par + 32 * c15 + 16 * g;
    const u32 gb0 = lb + S_G + kch * G_CH + ((c15 & 1) ? 0 : G_PAR) + 4 * (1015 - (c15 >> 1) - 16 * par + 4 * g - 32 * 31);
    const u32 gb[4] = {gb0, gb0 + 1024, gb0 + 2048, gb0 + 3072};
    u32x4 bq[32];
    u32x2 al[32], ah[32];
    f32x4 y[8];
    auto rdB = [&](auto M, auto T) { constexpr int q = 4 * T - M + 3; bq[q] = a_ds128<128 * q>(ub); };
    auto rdA = [&](auto M, auto H) {
      constexpr int k = (31 - M) >> 3, o = 32 * ((31 - M) & 7);
      if constexpr (H == 0) al[M] = a_ds2x32<o, o + 1>(gb[k]); else ah[M] = a_ds2x32<o + 2, o + 3>(gb[k]);
    };
    sfor<2>([&](auto M) { sfor<8>([&](auto T) { rdB(M, T); }); rdA(M, ic<0>{}); rdA(M, ic<1>{}); });
    sfor<32>([&](auto M) {
      constexpr int m = M, t0 = m >> 2, n = 8 - t0;
      wait_lgkm<(m <= 2 ? 10 : (m == 3 ? 4 : (m == 4 ? 6 : (2 * (31 - m) < 8 ? 2 * (31 - m) : 8))))>();
      sfor<n>([&](auto I) {
        constexpr int i = I, t = t0 + i;
        mfma16_h<m == 0>(y[t], cat4(al[m], ah[m]), bq[4 * t - m + 3]);
        if constexpr (m < 2) {
          rdB(ic<m + 2>{}, I);
          if constexpr (i >= 6) rdA(ic<m + 2>{}, ic<i - 6>{});
        } else if constexpr (m <= 4) {
          if constexpr (i < 4) rdA(ic<2 * m + (i >> 1)>{}, ic<i & 1>{});
        } else if constexpr (m + 5 < 32) {
          if constexpr (i < 2) rdA(ic<m + 5>{}, I);
        }
      });
    });
    asm volatile("s_nop 7\n\ts_nop 1" : "+v"(y[0]), "+v"(y[1]), "+v"(y[2]), "+v"(y[3]), "+v"(y[4]), "+v"(y[5]), "+v"(y[6]), "+v"(y[7]));
    STAMP(6);
    auto send = [&](auto PAR) {
      FENCE();
#pragma unroll
      for (int i = 0; i < 4; ++i) lds_st<f32x4>(smem, S_EXC + w * 4096 + i * 1024 + lane * 16, y[4 * (1 - PAR) + i]);
      FENCE();
    };
    if (par) send(ic<1>{}); else send(ic<0>{});
    bar();
    const float dsk = bf1(q_dsk);
    u32x2 ao[4];
    auto finish = [&](auto PAR) {
      FENCE();
#pragma unroll
      for (int i = 0; i < 4; ++i) {
        const f32x4 o = lds_ld<f32x4>(smem, S_EXC + (w ^ 1) * 4096 + i * 1024 + lane * 16);
        const u32x2 uu = lds_ld<u32x2>(smem, S_U + kch * U_CH + (240 + 256 * (4 * PAR + i) + 16 * c15 + 4 * g) * 2);
        const f16x2 u01 = ash2(uu.x), u23 = ash2(uu.y);
        const float uf[4] = {(float)u01.x, (float)u01.y, (float)u23.x, (float)u23.y};
        float v[4];
#pragma unroll
        for (int e = 0; e < 4; ++e) v[e] = ffma1(uf[e], dsk, fmul1(fadd1(y[4 * PAR + i][e], o[e]), inv_sk));
        ao[i] = u32x2{cvt_bf2(v[0], v[1]), cvt_bf2(v[2], v[3])};
      }
      FENCE();
    };
    if (par) finish(ic<1>{}); else finish(ic<0>{});
#pragma unroll
    for (int i = 0; i < 4; ++i) {
      u32x2 o = ao[i];
      o.x = o.x == POISON ? 0xffc0ffc0u : o.x;
      o.y = o.y == POISON ? 0xffc0ffc0u : o.y;
      st64c(rWS, o, WS_A + ((4 * (4 * par + i) + (c15 >> 2)) * 512 + kd) * 128 + ((c15 & 3) * 16 + 4 * g) * 2);
    }
    STAMP(7);
    u32x4 xt0 = ld128(sX, xto, 0), xt1 = ld128(sX, xto + 32 * 1024, 0);
    u32 b2w = ld16(mk_srd(p.b2, DM * 2), t8 < 64 ? (64 * nt2 + t8) * 2 : OOB, 0);
    bar();
    const u32 ato = WS_A + (mt2 * 512 + nt2 * 64) * 128 + t8 * 16;
    asm volatile("s_waitcnt vmcnt(0)" : "+v"(xt0), "+v"(xt1), "+v"(b2w)::"memory");
    const int row0 = t8 >> 3, sl = t8 & 7;
    {
      const u32 o = S_XT + row0 * 128 + ((sl ^ (row0 & 7)) * 16);
      lds_st<u32x4>(smem, o, xt0);
      lds_st<u32x4>(smem, o + 32 * 128, xt1);
      lds_st<float>(smem, t8 < 64 ? S_B2 + t8 * 4 : S_MISC, bf1(b2w));
    }
    bar();
    u32x4 at0 = ld128c(sWS, ato, 0), at1 = ld128c(sWS, ato + 4096, 0);
    asm volatile("s_waitcnt vmcnt(0)" : "+v"(at0), "+v"(at1)::"memory");
#pragma unroll 1
    for (int it = 0; it < (1 << 16); ++it) {
      const u32 m0 = max(max(at0.x, at0.y), max(at0.z, at0.w)), m1 = max(max(at1.x, at1.y), max(at1.z, at1.w));
      if (!__any(max(m0, m1) == POISON)) break;
      at0 = ld128c(sWS, ato, 0); at1 = ld128c(sWS, ato + 4096, 0);
      asm volatile("s_waitcnt vmcnt(0)" : "+v"(at0), "+v"(at1)::"memory");
    }
    {
      const u32 o = S_AT + row0 * 128 + (((sl >> 1) ^ (row0 & 2)) * 32) + (sl & 1) * 16;
      lds_st<u32x4>(smem, o, at0);
      lds_st<u32x4>(smem, o + 32 * 128, at1);
    }
    bar();
  } else {
    u32x4 hf[22][2];
    const u32 hb = WS_H + ((mt2 * 2 + mh) * NUNIT + kh * 22) * 2048;
    const u32 hvo = lane * 16, hvz = kh ? OOB : hvo;
    auto ldh = [&](auto J, auto S) { hf[J][S] = ld128c(sWS, J == 21 ? hvz : hvo, hb + (J * 2 + S) * 1024); };
    {
      const u32 hfo = WS_HF + (((mt2 >> 1) * 4 + 2 * (mt2 & 1) + mh) * 32 + c31) * 4;
      u32 hfl = ld32c(sWS, hfo, 0);
      asm volatile("s_waitcnt vmcnt(0)" : "+v"(hfl)::"memory");
      spin_flag(sWS, hfo, epoch, hfl, kh ? c31 >= 16 : c31 <= 17);
    }
    STAMP(6);
    sfor<G2_LEAD>([&](auto J) { ldh(J, ic<0>{}); ldh(J, ic<1>{}); });
    STAMP(7);
    u32 wa[2];
#pragma unroll
    for (int n = 0; n < 2; ++n) wa[n] = lb + kh * W2_HALF + (4 * hh + q4) * 128 + (((2 * n + b16) ^ (q4 & 2)) * 32) + 8 * p4;
    u32x2 wl[2][4], wh[2][4];
    auto rd = [&](auto J, auto I, auto H) {
      constexpr int j = J, i = I, off = (j % W2_NS) * 4096 + ((i >> 1) * 16 + H * 8) * 128;
      if constexpr (H == 0) wl[j & 1][i] = a_dstr<off>(wa[i & 1]); else wh[j & 1][i] = a_dstr<off>(wa[i & 1]);
    };
    sfor<4>([&](auto I) { rd(ic<0>{}, I, ic<0>{}); rd(ic<0>{}, I, ic<1>{}); });
    sfor<22>([&](auto J) {
      constexpr int j = J;
      constexpr bool last = j == 21, brk = j == 7 || j == 13 || j == 17;
      if constexpr (j == 7) wait_vm<g2_ops_before(7) - g2_ops_before(6)>();
      if constexpr (j == 13) wait_vm<g2_ops_before(13) - g2_ops_before(9)>();
      if constexpr (j == 17) wait_vm<g2_ops_before(17) - g2_ops_before(11)>();
      if constexpr (brk) bar();
      wait_vm<g2_wait_h(j)>();
      tie(hf[j][0]); tie(hf[j][1]);
      sfor<4>([&](auto I) {
        constexpr int i = I;
        constexpr bool late = j <= 5 && (i & 1), refill = j >= 7 && j <= 10, stg = late || refill;
        constexpr int ju = late ? 8 + j : (refill ? 14 + 2 * (j - 7) + (i >> 1) : 14), pi = late ? (i >> 1) : (i & 1);
        if constexpr (!brk) wait_lgkm<(last ? 6 - 2 * i : 6)>();
        if constexpr (stg && pi == 0) dma_m0(w2_lo + (ju % W2_NS) * 4096);
        mfma32_vv<j == 0 && i < 2>(acc[i & 1], cat4(wl[j & 1][i], wh[j & 1][i]), hf[j][i >> 1]);
        if constexpr ((i & 1) == 0 && j + G2_LEAD <= 21) ldh(ic<j + G2_LEAD>{}, ic<(i >> 1)>{});
        if constexpr (stg) dma_go<pi * 1024>(sW2, ju == 21 ? vw2z[pi] : vw2p[pi], w2_so + ju * 32 * 1024);
        if constexpr (!last) { rd(ic<j + 1>{}, I, ic<0>{}); rd(ic<j + 1>{}, I, ic<1>{}); }
      });
    });
    asm volatile("s_nop 7\n\ts_nop 3" : "+v"(acc[0]), "+v"(acc[1]));
    STAMP(8);
    auto send2 = [&](auto KH) {
      FENCE();
#pragma unroll
      for (int q = 0; q < 4; ++q) {
        const f32x16& as = acc[1 - KH];
        lds_st<f32x4>(smem, S_EX + w * 4096 + q * 1024 + lane * 16, f32x4{as[4 * q], as[4 * q + 1], as[4 * q + 2], as[4 * q + 3]});
      }
      FENCE();
    };
    if (kh) send2(ic<1>{}); else send2(ic<0>{});
    bar();
    const int tl = 32 * mh + c31;
    auto fin2 = [&](auto KH) {
      FENCE();
      const f32x16& ak = acc[KH];
      f32x4 pv[4], bb[4];
      u32x2 av[4], xv[4];
      u32 po[4];
#pragma unroll
      for (int q = 0; q < 4; ++q) {
        const int row = 32 * KH + 8 * q + 4 * hh + q4;
        po[q] = tl * 128 + (((4 * KH + q) ^ (tl & 7)) * 16) + hh * 8;
        pv[q] = lds_ld<f32x4>(smem, S_EX + (w ^ 2) * 4096 + q * 1024 + lane * 16);
        bb[q] = lds_ld<f32x4>(smem, S_B2 + (32 * KH + 8 * q + 4 * hh) * 4);
        av[q] = lds_tr(smem, S_AT + row * 128 + (((2 * mh + b16) ^ (row & 2)) * 32) + 8 * p4);
        xv[q] = lds_ld<u32x2>(smem, S_XT + po[q]);
      }
      FENCE();
      f32x2 o[8];
#pragma unroll
      for (int q = 0; q < 4; ++q) {
        o[2 * q] = f32x2{ak[4 * q], ak[4 * q + 1]} + f32x2{bb[q].x, bb[q].y};
        o[2 * q + 1] = f32x2{ak[4 * q + 2], ak[4 * q + 3]} + f32x2{bb[q].z, bb[q].w};
      }
      FENCE();
#pragma unroll
      for (int q = 0; q < 4; ++q) { o[2 * q] += f32x2{pv[q].x, pv[q].y}; o[2 * q + 1] += f32x2{pv[q].z, pv[q].w}; }
      FENCE();
#pragma unroll
      for (int q = 0; q < 4; ++q) { o[2 * q] += bf2(xv[q].x) + bf2(av[q].x); o[2 * q + 1] += bf2(xv[q].y) + bf2(av[q].y); }
      FENCE();
#pragma unroll
      for (int q = 0; q < 4; ++q) lds_st<u32x2>(smem, S_OT + po[q], u32x2{pk_bf16(o[2 * q]), pk_bf16(o[2 * q + 1])});
      FENCE();
    };
    if (kh) fin2(ic<1>{}); else fin2(ic<0>{});
  }
  bar();
  {
    const int row = tid >> 3, sl = tid & 7;
    const u32x4 o = lds_ld<u32x4>(smem, S_OT + row * 128 + ((sl ^ (row & 7)) * 16));
    __builtin_amdgcn_raw_buffer_store_b128(o, rOUT, (64 * mt2 + row) * 1024 + nt2 * 128 + sl * 16, 0, 0);
  }
  STAMP(9);
}

extern "C" void kernel_launch(void* const* d_in, const int* in_sizes, int n_in, void* d_out, int out_size, void* d_ws,
                              size_t ws_size, hipStream_t stream) {
  Params p;
  p.x = (const u16*)d_in[0]; p.lar = (const u16*)d_in[1]; p.aim = (const u16*)d_in[2]; p.cre = (const u16*)d_in[3];
  p.cim = (const u16*)d_in[4]; p.ldt = (const u16*)d_in[5]; p.dsk = (const u16*)d_in[6];
  p.w1 = (const u16*)d_in[8]; p.b1 = (const u16*)d_in[9]; p.w2 = (const u16*)d_in[10]; p.b2 = (const u16*)d_in[11];
  p.out = (u16*)d_out; p.ws = (char*)d_ws;
  s4d_mixer_fused<<<256, 512, 0, stream>>>(p);
}
```

```cpp
#include <hip/hip_runtime.h>
#include <stdint.h>
#include <utility>

namespace {
typedef unsigned short u16;
typedef unsigned u32;
typedef unsigned long long u64;
typedef float f32x2 __attribute__((ext_vector_type(2)));
typedef float f32x4 __attribute__((ext_vector_type(4)));
typedef float f32x16 __attribute__((ext_vector_type(16)));
typedef u32 u32x2 __attribute__((ext_vector_type(2)));
typedef u32 u32x4 __attribute__((ext_vector_type(4)));
typedef short s16x4 __attribute__((ext_vector_type(4)));
typedef __bf16 bf16x2 __attribute__((ext_vector_type(2)));
typedef _Float16 f16x2 __attribute__((ext_vector_type(2)));
typedef _Float16 f16x8 __attribute__((ext_vector_type(8)));
#define LAS __attribute__((address_space(3)))
#define DEV __device__ __forceinline__
typedef __amdgpu_buffer_rsrc_t rsrc_t;

constexpr int SEQ = 2048, DM = 512, NMODE = 128, FF = 1376, NUNIT = 43;
constexpr u32 WS_E = 0, WS_LNF = 0x1000, WS_HF = 0x2000  , WS_STATS = 0x8000, WS_XN = 0x10000, WS_A = 0x210000, WS_H = 0x410000,
              WS_END = WS_H + 64 * NUNIT * 2048, WS_STAMP = 0xA00000;
constexpr u32 SLOT = 28672, SLOT_W = 16384;
constexpr u32 W2_NS = 14, W2_HALF = W2_NS * 4096;
constexpr u32 LDS_BYTES_ = 163840;
constexpr u32 S0 = 114688;
constexpr u32 S_DUMMY = LDS_BYTES_ - 256;
constexpr u32 S_LN = S0, LN_ROW = 1088;
constexpr u32 TAB_ROW = 544, TAB_CH = 57 * TAB_ROW;
constexpr u32 S_KC = LDS_BYTES_ - 256 - 2592, KC_CH = 1296;
constexpr u32 S_TAB = S_KC - 2 * TAB_CH;
constexpr u32 S_TB = 32768;
constexpr u32 S_CSP = S_KC;
constexpr u32 S_B1 = S_CSP + 768;
constexpr u32 S_MISC = S_B1 + 384;
constexpr u32 S_CS = S_MISC + 64;
constexpr u32 S_G = S0, G_PAR = 4160, G_CH = 8320;
constexpr u32 S_U = S0 + 16640, U_CH = 4608;
constexpr u32 S_EX = S0, S_EXC = S0 + 25856;
constexpr u32 S_AT = S0 + 16384, S_XT = S0 + 24576, S_OT = S0 + 32768, S_B2 = S0 + 40960;
constexpr u32 LDS_BYTES = 163840;
constexpr u32 OOB = 0x80000000u;
constexpr u32 POISON = 0xffffffffu;

#define STAMP(k) do {} while (0)
#define CSTAMP(i) do {} while (0)
#define PSTAMP(i) do {} while (0)
#define PDUMP() do {} while (0)
#define CDUMP(n, base) do {} while (0)

struct Params {
  const u16 *x, *lar, *aim, *cre, *cim, *ldt, *dsk, *w1, *b1, *w2, *b2;
  u16* out;
  char* ws;
};

DEV float asf(u32 v) { return __builtin_bit_cast(float, v); }
DEV u32 asu(float v) { return __builtin_bit_cast(u32, v); }
DEV f16x2 ash2(u32 v) { return __builtin_bit_cast(f16x2, v); }
DEV float bflo(u32 v) { return asf(v << 16); }
DEV float bfhi(u32 v) { return asf(v & 0xffff0000u); }
DEV f32x2 bf2(u32 v) { return f32x2{bflo(v), bfhi(v)}; }
DEV u32 pk_bf16(f32x2 v) { return __builtin_bit_cast(u32, __builtin_convertvector(v, bf16x2)); }
DEV u32 pk_f16(f32x2 v) { return __builtin_bit_cast(u32, __builtin_convertvector(v, f16x2)); }
DEV float rbf(float a) { return bflo(pk_bf16(f32x2{a, a})); }
DEV float bf1(u32 v) { return asf(v << 16); }
DEV f32x2 sp2(float a) { return f32x2{a, a}; }
DEV f32x2 fma2(f32x2 a, f32x2 b, f32x2 c) { return __builtin_elementwise_fma(a, b, c); }

template <int N> DEV void wait_vm() { asm volatile("s_waitcnt vmcnt(%0)" ::"n"(N) : "memory"); }
template <int N> DEV void wait_lgkm() { asm volatile("s_waitcnt lgkmcnt(%0)" ::"n"(N)); }
DEV void bar() { asm volatile("s_waitcnt lgkmcnt(0)\n\ts_barrier" ::: "memory"); }
DEV u32x4 mk_srd(const void* base, u32 bytes) {
  const u64 b = (u64)base;
  u32x4 s;
  s.x = __builtin_amdgcn_readfirstlane((u32)b);
  s.y = __builtin_amdgcn_readfirstlane((u32)(b >> 32)) & 0xffffu;
  s.z = bytes;
  s.w = 0x00020000u;
  return s;
}
DEV rsrc_t mk_rsrc(const void* base, u32 bytes) { return __builtin_amdgcn_make_buffer_rsrc((void*)base, 0, bytes, 0x00020000); }

#define BLOAD(INS, TY, NAME, FLAGS, CON)                                                                     \
  DEV TY NAME(u32x4 srd, u32 voff, u32 soff) {                                                               \
    TY r;                                                                                                    \
    asm volatile(INS " %0, %1, %2, %3 offen" FLAGS : CON(r) : "v"(voff), "s"(srd), "s"(soff) : "memory");     \
    return r;                                                                                                \
  }
BLOAD("buffer_load_dwordx4", u32x4, ld128, "", "=v")
BLOAD("buffer_load_dwordx4", u32x4, ld128c, " sc1", "=v")
BLOAD("buffer_load_dwordx4", u32x4, ld128c_a, " sc1", "=a")
BLOAD("buffer_load_dwordx2", u32x2, ld64, "", "=v")
BLOAD("buffer_load_dword", u32, ld32c, " sc1", "=v")
BLOAD("buffer_load_ushort", u32, ld16, "", "=v")
DEV void touch(const void* a, u32 lds_dummy) { asm volatile("s_mov_b32 m0, %1\n\ts_nop 0\n\tglobal_load_lds_dword %0, off" ::"v"(a), "s"(lds_dummy) : "memory"); }
template <class T> DEV void tie(T& v) { asm volatile("" : "+v"(v)); }

DEV void dma_m0(u32 lds_addr) { asm volatile("s_mov_b32 m0, %0" ::"s"(lds_addr) : "memory"); }
template <int IOFF> DEV void dma_go(u32x4 srd, u32 voff, u32 soff) {
  asm volatile("buffer_load_dwordx4 %0, %1, %2 offen offset:%3 lds" ::"v"(voff), "s"(srd), "s"(soff), "n"(IOFF) : "memory");
}
DEV void st128c(rsrc_t r, u32x4 v, u32 voff) { __builtin_amdgcn_raw_buffer_store_b128(v, r, voff, 0, 16); }
DEV void st64c(rsrc_t r, u32x2 v, u32 voff) { __builtin_amdgcn_raw_buffer_store_b64(v, r, voff, 0, 16); }
DEV void st32c(rsrc_t r, u32 v, u32 voff) { __builtin_amdgcn_raw_buffer_store_b32(v, r, voff, 0, 16); }
DEV void st64(rsrc_t r, u32x2 v, u32 voff) { __builtin_amdgcn_raw_buffer_store_b64(v, r, voff, 0, 0); }

template <class T> DEV T lds_ld(const char* smem, u32 off) { return *(const LAS T*)(smem + off); }
template <class T> DEV void lds_st(char* smem, u32 off, T v) { *(LAS T*)(smem + off) = v; }
DEV u32x2 lds_tr(const char* smem, u32 off) { return __builtin_bit_cast(u32x2, __builtin_amdgcn_ds_read_tr16_b64_v4i16((LAS s16x4*)(smem + off))); }

template <int I> using ic = std::integral_constant<int, I>;
template <class F, int... Is> DEV void sfor_impl(F&& f, std::integer_sequence<int, Is...>) { (f(ic<Is>{}), ...); }
template <int N, class F> DEV void sfor(F&& f) { sfor_impl(f, std::make_integer_sequence<int, N>{}); }
template <int OFF> DEV u32x4 a_ds128(u32 addr) { u32x4 r; asm volatile("ds_read_b128 %0, %1 offset:%2" : "=v"(r) : "v"(addr), "n"(OFF)); return r; }
template <int OFF> DEV u32x2 a_dstr(u32 addr) { u32x2 r; asm volatile("ds_read_b64_tr_b16 %0, %1 offset:%2" : "=v"(r) : "v"(addr), "n"(OFF)); return r; }
template <int O0, int O1> DEV u32x2 a_ds2x32(u32 addr) { u32x2 r; asm volatile("ds_read2_b32 %0, %1 offset0:%2 offset1:%3" : "=v"(r) : "v"(addr), "n"(O0), "n"(O1)); return r; }
DEV u32x4 cat4(u32x2 a, u32x2 b) { return __builtin_shufflevector(a, b, 0, 1, 2, 3); }
template <bool INIT> DEV void mfma32_vv(f32x16& c, u32x4 a, u32x4 b) {
  if constexpr (INIT) asm volatile("v_mfma_f32_32x32x16_bf16 %0, %1, %2, 0" : "=&v"(c) : "v"(a), "v"(b));
  else asm volatile("v_mfma_f32_32x32x16_bf16 %0, %1, %2, %0" : "+v"(c) : "v"(a), "v"(b));
}
template <bool INIT> DEV void mfma32_va(f32x16& c, u32x4 a, u32x4 b) {
  if constexpr (INIT) asm volatile("v_mfma_f32_32x32x16_bf16 %0, %1, %2, 0" : "=&v"(c) : "v"(a), "a"(b));
  else asm volatile("v_mfma_f32_32x32x16_bf16 %0, %1, %2, %0" : "+v"(c) : "v"(a), "a"(b));
}
template <bool INIT> DEV void mfma16_h(f32x4& c, u32x4 a, u32x4 b) {
  if constexpr (INIT) asm volatile("v_mfma_f32_16x16x32_f16 %0, %1, %2, 0" : "=&v"(c) : "v"(a), "v"(b));
  else asm volatile("v_mfma_f32_16x16x32_f16 %0, %1, %2, %0" : "+v"(c) : "v"(a), "v"(b));
}
DEV f32x4 mfma_h(u32x4 a, u32x4 b, f32x4 c) {
  return __builtin_amdgcn_mfma_f32_16x16x32_f16(__builtin_bit_cast(f16x8, a), __builtin_bit_cast(f16x8, b), c, 0, 0, 0);
}
template <bool INIT> DEV void mfma16_bf(f32x4& c, u32x4 a, u32x4 b) {
  if constexpr (INIT) asm volatile("v_mfma_f32_16x16x32_bf16 %0, %1, %2, 0" : "=&v"(c) : "v"(a), "v"(b));
  else asm volatile("v_mfma_f32_16x16x32_bf16 %0, %1, %2, %0" : "+v"(c) : "v"(a), "v"(b));
}
#define FENCE() __builtin_amdgcn_sched_barrier(0)

DEV void wave_sum4(float& a, float& b, float& c, float& d) {
#define S4(CTRL) "v_add_f32_dpp %0, %0, %0 " CTRL "\n\tv_add_f32_dpp %1, %1, %1 " CTRL "\n\tv_add_f32_dpp %2, %2, %2 " CTRL "\n\tv_add_f32_dpp %3, %3, %3 " CTRL "\n\t"
  asm volatile("s_nop 1\n\t" S4("row_shr:1 row_mask:0xf bank_mask:0xf bound_ctrl:1") S4("row_shr:2 row_mask:0xf bank_mask:0xf bound_ctrl:1")
               S4("row_shr:4 row_mask:0xf bank_mask:0xf bound_ctrl:1") S4("row_shr:8 row_mask:0xf bank_mask:0xf bound_ctrl:1")
               S4("row_bcast:15 row_mask:0xa bank_mask:0xf") S4("row_bcast:31 row_mask:0xc bank_mask:0xf") "s_nop 0"
               : "+v"(a), "+v"(b), "+v"(c), "+v"(d));
#undef S4
  a = asf(__builtin_amdgcn_readlane(asu(a), 63)); b = asf(__builtin_amdgcn_readlane(asu(b), 63));
  c = asf(__builtin_amdgcn_readlane(asu(c), 63)); d = asf(__builtin_amdgcn_readlane(asu(d), 63));
}
DEV void wave_sum2(float& a, float& b) {
#define S2(CTRL) "v_add_f32_dpp %0, %0, %0 " CTRL "\n\tv_add_f32_dpp %1, %1, %1 " CTRL "\n\ts_nop 0\n\t"
  asm volatile("s_nop 1\n\t" S2("row_shr:1 row_mask:0xf bank_mask:0xf bound_ctrl:1") S2("row_shr:2 row_mask:0xf bank_mask:0xf bound_ctrl:1")
               S2("row_shr:4 row_mask:0xf bank_mask:0xf bound_ctrl:1") S2("row_shr:8 row_mask:0xf bank_mask:0xf bound_ctrl:1")
               S2("row_bcast:15 row_mask:0xa bank_mask:0xf") S2("row_bcast:31 row_mask:0xc bank_mask:0xf")
               : "+v"(a), "+v"(b));
#undef S2
  a = asf(__builtin_amdgcn_readlane(asu(a), 63)); b = asf(__builtin_amdgcn_readlane(asu(b), 63));
}
template <int CTRL, int RM> DEV float dpp_add(float v) { return v + asf(__builtin_amdgcn_update_dpp(0, asu(v), CTRL, RM, 0xf, true)); }
DEV float wave_sum(float v) {
  v = dpp_add<0x111, 0xf>(v); v = dpp_add<0x112, 0xf>(v); v = dpp_add<0x114, 0xf>(v); v = dpp_add<0x118, 0xf>(v);
  v = dpp_add<0x142, 0xa>(v); v = dpp_add<0x143, 0xc>(v);
  return asf(__builtin_amdgcn_readlane(asu(v), 63));
}
DEV void cmul2(f32x2& r0, f32x2 a0, f32x2 b0, f32x2& r1, f32x2 a1, f32x2 b1) {
  f32x2 t0, t1;
  asm("v_pk_mul_f32 %0, %4, %5 op_sel_hi:[1,0]\n\tv_pk_mul_f32 %1, %6, %7 op_sel_hi:[1,0]\n\t"
      "v_pk_fma_f32 %2, %4, %5, %0 op_sel:[1,1,0] op_sel_hi:[0,1,1] neg_lo:[1,0,0]\n\t"
      "v_pk_fma_f32 %3, %6, %7, %1 op_sel:[1,1,0] op_sel_hi:[0,1,1] neg_lo:[1,0,0]"
      : "=&v"(t0), "=&v"(t1), "=&v"(r0), "=&v"(r1) : "v"(a0), "v"(b0), "v"(a1), "v"(b1));
}
DEV f32x2 cmul(f32x2 a, f32x2 b) {
  f32x2 t, r;
  asm("v_pk_mul_f32 %0, %2, %3 op_sel_hi:[1,0]\n\tv_pk_fma_f32 %1, %2, %3, %0 op_sel:[1,1,0] op_sel_hi:[0,1,1] neg_lo:[1,0,0]"
      : "=&v"(t), "=v"(r) : "v"(a), "v"(b));
  return r;
}
DEV u32 cvt_bf2(float a, float b) { u32 r; asm("v_cvt_pk_bf16_f32 %0, %1, %2" : "=v"(r) : "v"(a), "v"(b)); return r; }
DEV u32 cvt_h2(float a, float b) { u32 r; asm("v_cvt_pk_f16_f32 %0, %1, %2" : "=v"(r) : "v"(a), "v"(b)); return r; }
DEV float fadd1(float a, float b) { float r; asm("v_add_f32 %0, %1, %2" : "=v"(r) : "v"(a), "v"(b)); return r; }
DEV float fsub1(float a, float b) { float r; asm("v_sub_f32 %0, %1, %2" : "=v"(r) : "v"(a), "v"(b)); return r; }
DEV float fmul1(float a, float b) { float r; asm("v_mul_f32 %0, %1, %2" : "=v"(r) : "v"(a), "v"(b)); return r; }
DEV float ffma1(float a, float b, float c) { float r; asm("v_fma_f32 %0, %1, %2, %3" : "=v"(r) : "v"(a), "v"(b), "v"(c)); return r; }
DEV float rbf1(float a) { return bflo(cvt_bf2(a, a)); }
DEV u32 pk_f16_conj(f32x2 v) { u32 r; asm("v_cvt_pk_f16_f32 %0, %1, -%2" : "=v"(r) : "v"(v.x), "v"(v.y)); return r; }
DEV f32x2 gelu2(f32x2 v) {
  const f32x2 s = v * fma2(v * v, sp2(-0.10294323970f), sp2(-2.3022081986f));
  const f32x2 d = f32x2{__builtin_amdgcn_exp2f(s.x), __builtin_amdgcn_exp2f(s.y)} + sp2(1.0f);
  return v * f32x2{__builtin_amdgcn_rcpf(d.x), __builtin_amdgcn_rcpf(d.y)};
}
DEV void spin_flag(u32x4 srd, u32 voff, u32 epoch, u32 val, bool need) {
#pragma unroll 1
  for (int it = 0; it < (1 << 16); ++it) {
    if (__all(!need || val == epoch)) break;
    __builtin_amdgcn_s_sleep(1);
    val = ld32c(srd, voff, 0);
    asm volatile("s_waitcnt vmcnt(0)" : "+v"(val)::"memory");
  }
}

constexpr int G2_LEAD = 8;
constexpr int g2_unit_ops(int j) { return (j + G2_LEAD <= 21 ? 2 : 0) + ((j >= 7 && j <= 10) ? 4 : 0); }
constexpr int g2_ops_before(int j) { int n = 2 * G2_LEAD; for (int i = 0; i < j; ++i) n += g2_unit_ops(i); return n; }
constexpr int g2_pos_h(int j) {
  if (j < G2_LEAD) return 2 * j + 2;
  const int i = j - G2_LEAD;
  return g2_ops_before(i) + 2 + ((i >= 7 && i <= 10) ? 2 : 0);
}
constexpr int g2_wait_h(int j) { return g2_ops_before(j) - g2_pos_h(j); }

}

__global__ __launch_bounds__(512, 2) void s4d_mixer_fused(Params p) {
  __shared__ __attribute__((aligned(16))) char smem[LDS_BYTES];
  const int tid = threadIdx.x, lane = tid & 63, t8 = tid & 255;
  const int wid = __builtin_amdgcn_readfirstlane(tid >> 6);
  const int side = wid >> 2, w = wid & 3;
  const int b = blockIdx.x, g8 = b & 7, r = b >> 3;
  const int mt = 4 * (g8 >> 1) + (r >> 3), nt = 8 * (g8 & 1) + (r & 7);
  const int ustart = (nt * NUNIT) >> 4, unext = ((nt + 1) * NUNIT) >> 4;
  const int tg = 16 * mt + nt;
  const int cp = 32 * g8 + r;
  const int mt2 = 4 * g8 + (r >> 3), nt2 = r & 7;
  const int c15 = lane & 15, g = lane >> 4;
  const int c31 = lane & 31, hh = lane >> 5, b16 = (lane >> 4) & 1;
  const int q4 = (lane & 15) >> 2, p4 = lane & 3;
  const int kch = w >> 1, kd = 2 * cp + kch, kh = w >> 1, mh = w & 1;

  const u32x4 sX = mk_srd(p.x, SEQ * DM * 2), sWS = mk_srd(p.ws, WS_END);
  const rsrc_t rWS = mk_rsrc(p.ws, WS_END), rOUT = mk_rsrc(p.out, SEQ * DM * 2);
  const u32x4 sW1 = mk_srd(p.w1, DM * FF * 2), sW2 = mk_srd(p.w2, FF * DM * 2);
  const u32 lb = (u32)(uintptr_t)(LAS char*)smem;
  STAMP(0);
  PSTAMP(0);

  auto load_eword = [&]() { u32 e; asm volatile("s_load_dword %0, %1, 0x0" : "=s"(e) : "s"(p.ws) : "memory"); return e; };
  u32 vx[4], vw[3];
#pragma unroll
  for (int j = 0; j < 4; ++j) vx[j] = (8 * j + (lane >> 3)) * 1024 - j * 1024 + (((lane & 7) ^ ((4 * j + (lane >> 4)) & 7)) * 16);
#pragma unroll
  for (int j = 0; j < 3; ++j) {
    const int P = (3 * w + j) * 64 + lane, k = P / 12, sl = P - 12 * k;
    vw[j] = k * (FF * 2) + sl * 16 - j * 1024;
  }
  const u32 x_so = (mt * 128 + 32 * w) * 1024, x_lo = lb + 32 * w * 128, w_so = ustart * 64, w_lo = lb + SLOT_W + 3 * w * 1024;
  auto st1_m0 = [&](auto KC, auto J) {
    if constexpr (J == 0) dma_m0(x_lo + (KC & 3) * SLOT);
    if constexpr (J == 4) dma_m0(w_lo + (KC & 3) * SLOT);
  };
  auto st1_go = [&](auto KC, auto J) {
    if constexpr (J < 4) dma_go<J * 1024>(sX, vx[J], x_so + KC * 128); else dma_go<(J - 4) * 1024>(sW1, vw[J - 4], w_so + KC * 64 * (FF * 2));
  };
  auto stage = [&](auto KC) { sfor<7>([&](auto J) { if constexpr (J == 0 || J == 4) { st1_m0(KC, J); asm volatile("s_nop 0"); } st1_go(KC, J); }); };
  u32 ixp[5];
  auto prep_ix = [&]() {
#pragma unroll
    for (int e = 0; e < 9; ++e) {
      const int l = 2047 - (8 * t8 + e);
      const u32 rb = pk_bf16(sp2((float)l)) & 0xffffu;
      const u32 ix = (l < 0 ? 641u : (l < 128 ? (u32)l : rb - 17024u)) * 2;
      if (e & 1) ixp[e >> 1] |= ix << 16; else ixp[e >> 1] = ix;
    }
#pragma unroll
    for (int e = 0; e < 5; ++e) tie(ixp[e]);
  };
  u32 epoch;
  u32 q_lar, q_aim, q_cre, q_cim, q_cre2, q_cim2, q_ldt, q_dsk;
  u32x2 b1v;
  auto load_small = [&]() {
    const u32 vo = (64 * (w & 1) + lane) * 2, so = kd * NMODE * 2;
    const u32x4 sCr = mk_srd(p.cre, DM * NMODE * 2), sCi = mk_srd(p.cim, DM * NMODE * 2);
    q_lar = ld16(mk_srd(p.lar, DM * NMODE * 2), vo, so); q_aim = ld16(mk_srd(p.aim, DM * NMODE * 2), vo, so);
    q_cre = ld16(sCr, vo, so); q_cim = ld16(sCi, vo, so);
    q_cre2 = ld16(sCr, vo ^ 128, so); q_cim2 = ld16(sCi, vo ^ 128, so);
    q_ldt = ld16(mk_srd(p.ldt, DM * 2), 0, kd * 2); q_dsk = ld16(mk_srd(p.dsk, DM * 2), 0, kd * 2);
    b1v = ld64(mk_srd(p.b1, FF * 2), lane < 24 ? lane * 8 : OOB, ustart * 64);
  };
#define TIE_SMALL "+v"(q_lar), "+v"(q_aim), "+v"(q_cre), "+v"(q_cim), "+v"(q_cre2), "+v"(q_cim2), "+v"(q_ldt), "+v"(q_dsk), "+v"(b1v)
  f32x16 acc1[3];
  f32x4 csp[3];
  u32x4 stv;
  if (side == 0) {
    u32x4 xr0 = ld128(sX, lane * 16, (8 * tg + 2 * w) * 1024), xr1 = ld128(sX, lane * 16, (8 * tg + 2 * w + 1) * 1024);
    {
      u64 pc;
      asm volatile("s_getpc_b64 %0" : "=s"(pc));
      const char* a;
      if (t8 < 96) a = (const char*)p.x + (512 * (g8 >> 1) + 16 * r + t8 / 6) * 1024 + (2 + t8 % 6) * 128;
      else if (t8 < 192) a = (const char*)p.w1 + (256 + 8 * r + (t8 - 96) / 12) * (FF * 2) + (g8 & 1) * 1280 + ((t8 - 96) % 12) * 120;
      else a = (const char*)pc + ((r * 64 + lane) % 208) * 128;
      touch(a, lb + S_DUMMY);
    }
    u32 eword = load_eword();
    prep_ix();
    asm volatile("s_waitcnt vmcnt(1)" : "+v"(xr0), "+v"(xr1)::"memory");
    PSTAMP(1);
    STAMP(1);
    {
      const u32x4 xr[2] = {xr0, xr1};
      f32x2 x[2][4];
      float x0[2], sm[2], sq[2];
#pragma unroll
      for (int t = 0; t < 2; ++t) {
#pragma unroll
        for (int i = 0; i < 4; ++i) x[t][i] = bf2(xr[t][i]);
        x0[t] = asf(__builtin_amdgcn_readfirstlane(asu(x[t][0].x)));
        f32x2 s = {0.f, 0.f}, q = {0.f, 0.f};
#pragma unroll
        for (int i = 0; i < 4; ++i) { const f32x2 d = x[t][i] - sp2(x0[t]); s += d; q = fma2(d, d, q); }
        sm[t] = s.x + s.y; sq[t] = q.x + q.y;
      }
      wave_sum4(sm[0], sq[0], sm[1], sq[1]);
      float m1[2], r1[2], s2[2], q2[2];
#pragma unroll
      for (int t = 0; t < 2; ++t) {
        const float md = sm[t] * (1.0f / DM);
        const float var = fmaxf(__builtin_fmaf(-md, md, sq[t] * (1.0f / DM)), 0.f);
        m1[t] = rbf(x0[t] + md);
        r1[t] = __builtin_amdgcn_rcpf(rbf(__builtin_amdgcn_sqrtf(rbf(rbf(var) + 1e-5f))));
      }
#pragma unroll
      for (int t = 0; t < 2; ++t) {
        f32x2 s = {0.f, 0.f}, q = {0.f, 0.f};
        u32x4 hv;
#pragma unroll
        for (int i = 0; i < 4; ++i) {
          const f32x2 n = bf2(pk_bf16(bf2(pk_bf16(x[t][i] - sp2(m1[t]))) * sp2(r1[t])));
          s += n; q = fma2(n, n, q);
          hv[i] = pk_f16(n);
        }
        lds_st<u32x4>(smem, S_LN + (2 * w + t) * LN_ROW + lane * 16, hv);
        s2[t] = s.x + s.y; q2[t] = q.x + q.y;
      }
      wave_sum4(s2[0], q2[0], s2[1], q2[1]);
      float al[2], be[2];
#pragma unroll
      for (int t = 0; t < 2; ++t) {
        const float mean2 = s2[t] * (1.0f / DM);
        const float var2 = fmaxf(__builtin_fmaf(-mean2, mean2, q2[t] * (1.0f / DM)), 0.f);
        const float r2 = __builtin_amdgcn_rcpf(rbf(__builtin_amdgcn_sqrtf(rbf(rbf(var2) + 1e-5f))));
        al[t] = r1[t] * r2;
        be[t] = __builtin_fmaf(m1[t], r1[t], rbf(mean2)) * r2;
      }
      PSTAMP(2);
      asm volatile("s_waitcnt lgkmcnt(0)" : "+s"(eword)::"memory");
      epoch = eword * 0x9E3779B1u + 0x7F4A7C15u;
      const u32x4 sv = {asu(lane ? al[1] : al[0]), epoch, asu(lane ? be[1] : be[0]), epoch};
      st128c(rWS, sv, lane < 2 ? WS_STATS + (8 * tg + 2 * w + lane) * 16 : OOB);
    }
    PSTAMP(3); PSTAMP(4);
    const u32 xa0 = lb + (32 * w + c31) * 128 + ((hh ^ ((c31 >> 1) & 7)) * 16);
    const u32 wa0 = lb + SLOT_W + (8 * hh + q4) * 192 + b16 * 32 + 8 * p4;
    u32 xa[2][4], wa[2];
#pragma unroll
    for (int s = 0; s < 4; ++s) { xa[0][s] = xa0 ^ (s * 32); xa[1][s] = xa[0][s] + 2 * SLOT; }
    wa[0] = wa0; wa[1] = wa0 + 2 * SLOT;
    u32x4 xf[2];
    u32x2 wl[2][3], wh[2][3];
    auto rd = [&](auto S, auto R) {
      constexpr int s = S, rr = R, buf = s & 1, kc = s >> 2, sl = kc & 3, so = (sl & 1) * SLOT, sp = s & 3;
      if constexpr (rr == 0) xf[buf] = a_ds128<so>(xa[sl >> 1][sp]);
      else {
        constexpr int u = (rr - 1) >> 1, hf = (rr - 1) & 1, off = so + u * 64 + (sp * 16 + hf * 4) * 192;
        if constexpr (hf == 0) wl[buf][u] = a_dstr<off>(wa[sl >> 1]); else wh[buf][u] = a_dstr<off>(wa[sl >> 1]);
      }
    };
    PSTAMP(5); PSTAMP(6);
    bar();
    PSTAMP(7); PDUMP();
    CSTAMP(0);
    sfor<7>([&](auto R) { rd(ic<0>{}, R); });
    sfor<32>([&](auto S) {
      constexpr int s = S, buf = s & 1, sp = s & 3, kc = s >> 2;
      constexpr bool last = s == 31, brk = sp == 2 && kc < 7;
      sfor<3>([&](auto U) {
        constexpr int u = U;
        if constexpr (u == 0) wait_lgkm<4>();
        else if constexpr (u == 1) {
          wait_lgkm<(last ? 0 : 3)>();
          if constexpr (brk) {
            constexpr int n = kc + 1;
            CSTAMP(2 * n - 1);
            asm volatile("s_barrier" ::: "memory");
            CSTAMP(2 * n);
            if constexpr (n == 4) load_small();
            if constexpr (n == 7) stv = ld128c(sWS, WS_STATS + (128 * mt + 32 * w + c31) * 16, 0);
          }
        }
        mfma32_vv<s == 0>(acc1[u], cat4(wl[buf][u], wh[buf][u]), xf[buf]);
        if constexpr (!last) {
          if constexpr (u == 0) sfor<3>([&](auto R) { rd(ic<s + 1>{}, R); });
          if constexpr (u == 1) sfor<2>([&](auto R) { rd(ic<s + 1>{}, ic<3 + R>{}); });
          if constexpr (u == 2) sfor<2>([&](auto R) { rd(ic<s + 1>{}, ic<5 + R>{}); });
        }
      });
    });
    asm volatile("s_nop 7\n\ts_nop 3" : "+v"(acc1[0]), "+v"(acc1[1]), "+v"(acc1[2]));
    CSTAMP(15); CDUMP(16, 0);
    STAMP(2);
    bar();
#pragma unroll
    for (int q = 0; q < 6; ++q) {
      const f32x16& a = acc1[q < 2 ? 1 : 2];
      const int o = q < 2 ? 8 + 4 * q : 4 * (q - 2);
      lds_st<f32x4>(smem, S_TB + w * 6144 + q * 1024 + lane * 16, f32x4{a[o], a[o + 1], a[o + 2], a[o + 3]});
    }
    asm volatile("s_waitcnt vmcnt(0)" : "+v"(stv), TIE_SMALL :: "memory");
  } else {
    const u32 wc0 = lb + SLOT_W + (32 * (w >> 1) + 8 * g + q4) * 192 + 3 * (w & 1) * 32 + 8 * p4;
    u32x4 ones = {0x3f803f80u, 0x3f803f80u, 0x3f803f80u, 0x3f803f80u};
    tie(ones);
    u32x2 cl[3], ch[3];
    auto cs_rd = [&](auto KC) {
      const u32 a = wc0 + (KC & 3) * SLOT;
      sfor<3>([&](auto I) { cl[I] = a_dstr<I * 32>(a); ch[I] = a_dstr<I * 32 + 4 * 192>(a); });
      FENCE();
    };
    auto cs_sum = [&](auto KC) {
      FENCE();
      wait_lgkm<0>();
      sfor<3>([&](auto I) { mfma16_bf<KC == 0>(csp[I], cat4(cl[I], ch[I]), ones); });
    };
    stage(ic<0>{}); stage(ic<1>{}); stage(ic<2>{});
    u32 eword = load_eword();
    prep_ix();
    asm volatile("s_waitcnt lgkmcnt(0)" : "+s"(eword)::"memory");
    epoch = eword * 0x9E3779B1u + 0x7F4A7C15u;
    PSTAMP(1); PSTAMP(2); PSTAMP(3); PSTAMP(4); PSTAMP(5);
    wait_vm<14>();
    PSTAMP(6);
    bar();
    PSTAMP(7); PDUMP();
    CSTAMP(0);
    STAMP(1);
    cs_rd(ic<0>{});
#pragma unroll
    for (int i = 0; i < 2; ++i) {
      const u32 a = S_LN + q4 * LN_ROW + ((2 * w + i) * 64 + 16 * g + 4 * p4) * 2;
      st128c(rWS, cat4(lds_tr(smem, a), lds_tr(smem, a + 4 * LN_ROW)), WS_XN + tg * 8192 + ((2 * w + i) * 64 + lane) * 16);
    }
    {
#pragma unroll
      for (int i = 0; i < 2; ++i) {
        const int ln = (2 * w + i) * 8 + (lane >> 3);
        st128c(rWS, u32x4{POISON, POISON, POISON, POISON}, WS_A + ((ln >> 1) * 512 + 2 * cp + (ln & 1)) * 128 + (lane & 7) * 16);
      }
    }
    stage(ic<3>{}); cs_sum(ic<0>{});
    wait_vm<18>();
    CSTAMP(1);
    bar();
    CSTAMP(2);
    cs_rd(ic<1>{});
    load_small();
    cs_sum(ic<1>{});
    wait_vm<20>();
    CSTAMP(3);
    bar();
    CSTAMP(4);
    cs_rd(ic<2>{}); stage(ic<4>{}); cs_sum(ic<2>{});
    wait_vm<16>();
    CSTAMP(5);
    bar();
    CSTAMP(6);
    st32c(rWS, epoch, t8 == 0 ? WS_LNF + tg * 4 : OOB);
    cs_rd(ic<3>{}); stage(ic<5>{}); cs_sum(ic<3>{});
    asm volatile("s_waitcnt vmcnt(8)" : TIE_SMALL :: "memory");
    CSTAMP(7);
    bar();
    CSTAMP(8);
    lds_st<f32x4>(smem, lane < 24 ? S_B1 + lane * 16 : S_MISC, f32x4{bflo(b1v.x), bfhi(b1v.x), bflo(b1v.y), bfhi(b1v.y)});
    cs_rd(ic<4>{}); stage(ic<6>{}); cs_sum(ic<4>{});
    wait_vm<7>();
    CSTAMP(9);
    bar();
    CSTAMP(10);
    cs_rd(ic<5>{}); stage(ic<7>{}); cs_sum(ic<5>{});
    wait_vm<7>();
    CSTAMP(11);
    bar();
    CSTAMP(12);
    cs_rd(ic<6>{});
#pragma unroll
    for (int i = 0; i < 2; ++i) {
      const int ix = min(i * 256 + t8, 343);
      touch((const char*)p.w2 + (43 * r + (ix >> 3)) * 1024 + (ix & 7) * 128, lb + S_DUMMY);
    }
    cs_sum(ic<6>{});
    wait_vm<2>();
    CSTAMP(13);
    bar();
    CSTAMP(14);
    stv = ld128c(sWS, WS_STATS + (128 * mt + 32 * w + c31) * 16, 0);
    cs_rd(ic<7>{}); cs_sum(ic<7>{});
    asm volatile("s_nop 7\n\ts_nop 1" : "+v"(csp[0]), "+v"(csp[1]), "+v"(csp[2]));
#pragma unroll
    for (int i = 0; i < 3; ++i) lds_st<f32x4>(smem, c15 == 0 ? S_CSP + ((w >> 1) * 96 + (3 * (w & 1) + i) * 16 + 4 * g) * 4 : S_MISC, csp[i]);
    CSTAMP(15); CDUMP(16, 20);
    STAMP(2);
    bar();
    const int f = t8 < 96 ? t8 : 0;
    lds_st<float>(smem, t8 < 96 ? S_CS + t8 * 4 : S_MISC, lds_ld<float>(smem, S_CSP + f * 4) + lds_ld<float>(smem, S_CSP + 384 + f * 4));
    asm volatile("s_waitcnt vmcnt(0)" : "+v"(stv)::"memory");
  }
#pragma unroll 1
  for (int it = 0; it < (1 << 16); ++it) {
    if (__all(stv.y == epoch && stv.w == epoch)) break;
    __builtin_amdgcn_s_sleep(1);
    stv = ld128c(sWS, WS_STATS + (128 * mt + 32 * w + c31) * 16, 0);
    asm volatile("s_waitcnt vmcnt(0)" : "+v"(stv)::"memory");
  }
  bar();
  CSTAMP(0);
  STAMP(3);

  const u32 vw2 = (lane >> 3) * 1024 + ((((lane & 7) >> 1) ^ ((lane >> 3) & 2)) * 32) + (lane & 1) * 16;
  const u32 vw2p[2] = {vw2, vw2 + 7168};
  const u32 vw2z[2] = {kh ? OOB : vw2p[0], kh ? OOB : vw2p[1]};
  const u32 w2_lo = lb + kh * W2_HALF + 2 * mh * 1024, w2_so = (kh * 22 * 32 + 16 * mh) * 1024 + nt2 * 128;
  auto st2_piece = [&](auto N) {
    constexpr int j = N >> 1, i = N & 1;
    FENCE();
    if constexpr (i == 0) { dma_m0(w2_lo + (j % W2_NS) * 4096); asm volatile("s_nop 0"); }
    dma_go<i * 1024>(sW2, j == 21 ? vw2z[i] : vw2p[i], w2_so + j * 32 * 1024);
    FENCE();
  };

  {
    f32x4 av[6];
    if (side) {
#pragma unroll
      for (int q = 0; q < 6; ++q) av[q] = lds_ld<f32x4>(smem, S_TB + w * 6144 + q * 1024 + lane * 16);
    } else {
#pragma unroll
      for (int q = 0; q < 6; ++q) {
        const f32x16& a = acc1[q >> 2];
        const int o = 4 * (q & 3);
        av[q] = f32x4{a[o], a[o + 1], a[o + 2], a[o + 3]};
      }
    }
    const f32x2 al = sp2(asf(stv.x)), nb = sp2(-asf(stv.z));
    const u32 hso = WS_H + ((mt * 4 + w) * NUNIT + ustart) * 2048 + 3 * side * 1024 + lane * 16;
    const u32 cb = S_B1 + hh * 16 + 3 * side * 64;
    f32x4 bi[6], cs[6];
#pragma unroll
    for (int q = 0; q < 6; ++q) { bi[q] = lds_ld<f32x4>(smem, cb + q * 32); cs[q] = lds_ld<f32x4>(smem, cb + (S_CS - S_B1) + q * 32); }
    f32x2 v[12], t[12];
#pragma unroll
    for (int q = 0; q < 6; ++q) { t[2 * q] = fma2(nb, f32x2{cs[q].x, cs[q].y}, f32x2{bi[q].x, bi[q].y}); t[2 * q + 1] = fma2(nb, f32x2{cs[q].z, cs[q].w}, f32x2{bi[q].z, bi[q].w}); }
    if (side == 0) { st2_piece(ic<0>{}); st2_piece(ic<1>{}); }
    FENCE();
#pragma unroll
    for (int q = 0; q < 6; ++q) { v[2 * q] = fma2(al, f32x2{av[q].x, av[q].y}, t[2 * q]); v[2 * q + 1] = fma2(al, f32x2{av[q].z, av[q].w}, t[2 * q + 1]); }
    if (side == 0) { st2_piece(ic<2>{}); st2_piece(ic<3>{}); }
    FENCE();
#pragma unroll
    for (int k = 0; k < 12; ++k) t[k] = v[k] * v[k];
    if (side == 0) { st2_piece(ic<4>{}); st2_piece(ic<5>{}); }
    FENCE();
#pragma unroll
    for (int k = 0; k < 12; ++k) t[k] = fma2(t[k], sp2(-0.10294323970f), sp2(-2.3022081986f));
    if (side == 0) { st2_piece(ic<6>{}); st2_piece(ic<7>{}); }
    FENCE();
#pragma unroll
    for (int k = 0; k < 12; ++k) t[k] = v[k] * t[k];
    if (side == 0) { st2_piece(ic<8>{}); st2_piece(ic<9>{}); }
    FENCE();
#pragma unroll
    for (int k = 0; k < 12; ++k) t[k] = f32x2{__builtin_amdgcn_exp2f(t[k].x), __builtin_amdgcn_exp2f(t[k].y)};
    if (side == 0) { st2_piece(ic<10>{}); st2_piece(ic<11>{}); }
    FENCE();
#pragma unroll
    for (int k = 0; k < 12; ++k) t[k] += sp2(1.0f);
    if (side == 0) { st2_piece(ic<12>{}); st2_piece(ic<13>{}); }
    FENCE();
#pragma unroll
    for (int k = 0; k < 12; ++k) t[k] = f32x2{__builtin_amdgcn_rcpf(t[k].x), __builtin_amdgcn_rcpf(t[k].y)};
    if (side == 0) { st2_piece(ic<14>{}); st2_piece(ic<15>{}); }
    FENCE();
#pragma unroll
    for (int k = 0; k < 12; ++k) v[k] *= t[k];
    FENCE();
#pragma unroll
    for (int i = 0; i < 3; ++i)
      st128c(rWS, u32x4{pk_bf16(v[4 * i]), pk_bf16(v[4 * i + 1]), pk_bf16(v[4 * i + 2]), pk_bf16(v[4 * i + 3])},
             ustart + ((3 * side + i) >> 1) < unext ? hso + i * 1024 : OOB);
  }
  CSTAMP(1);
  u32 lnf = ld32c(sWS, WS_LNF + t8 * 4, 0);
  st32c(rWS, epoch, (b == 0 && tid == 0) ? WS_E : OOB);
  STAMP(4);
  float inv_sk;
  {
    const int mh2 = w & 1;
    const float dt = rbf(expf(bf1(q_ldt)));
    const float zr = -rbf(expf(bf1(q_lar))) * dt, zi = bf1(q_aim) * dt;
    f32x2 C = f32x2{bf1(q_cre), bf1(q_cim)} * sp2(2.0f * dt);
    const float bd = wave_sum((__builtin_fabsf(C.x) + __builtin_fabsf(C.y)) + (__builtin_fabsf(bf1(q_cre2)) + __builtin_fabsf(bf1(q_cim2))) * (2.0f * dt));
    int se = 267 - (int)((asu(bd) >> 23) & 0xff);
    se = se < 1 ? 1 : (se > 253 ? 253 : se);
    C *= sp2(asf((u32)se << 23));
    inv_sk = asf((u32)(254 - se) << 23);
    const float e1 = zr * 1.44269504089f;
    const float ph = zi * 0.15915494309f;
    const float pl = __builtin_fmaf(zi, 0.15915494309f, -ph) + zi * 6.4206383e-9f;
    auto cexp = [&](float pw) {
      const float m = __builtin_amdgcn_exp2f(e1 * pw);
      const float a = __builtin_amdgcn_fractf(ph * pw) + pl * pw;
      return f32x2{__builtin_amdgcn_cosf(a), __builtin_amdgcn_sinf(a)} * sp2(m);
    };
    CSTAMP(2);
    const u32 tl = S_TAB + kch * TAB_CH + (64 * mh2 + lane) * 4;
    auto putX = [&](int row, f32x2 v) { lds_st<u32>(smem, tl + (16 + row) * TAB_ROW, pk_f16(v)); };
    if (side == 0) {
      f32x2 P[8], E[3], CE[8];
      P[1] = cexp(1.f); E[0] = cexp(128.f);
      CE[0] = C;
      cmul2(P[2], P[1], P[1], E[1], E[0], E[0]);
      cmul2(CE[1], C, E[0], P[3], P[2], P[1]);
      cmul2(P[4], P[2], P[2], E[2], E[1], E[1]);
      cmul2(CE[2], CE[0], E[1], CE[3], CE[1], E[1]);
      cmul2(P[5], P[4], P[1], P[6], P[4], P[2]);
      cmul2(CE[4], CE[0], E[2], CE[5], CE[1], E[2]);
      cmul2(CE[6], CE[2], E[2], CE[7], CE[3], E[2]);
      P[7] = cmul(P[4], P[3]);
      putX(0, CE[0]); putX(8, CE[1]);
#pragma unroll
      for (int i = 1; i < 8; ++i) {
        f32x2 a, c;
        cmul2(a, CE[0], P[i], c, CE[1], P[i]);
        putX(i, a); putX(8 + i, c);
      }
      putX(16, CE[2]); putX(20, CE[3]);
#pragma unroll
      for (int i = 1; i < 4; ++i) {
        f32x2 a, c;
        cmul2(a, CE[2], P[2 * i], c, CE[3], P[2 * i]);
        putX(16 + i, a); putX(20 + i, c);
      }
      putX(24, CE[4]); putX(26, CE[5]); putX(28, CE[6]); putX(30, CE[7]);
      f32x2 a, c;
      cmul2(a, CE[4], P[4], c, CE[5], P[4]); putX(25, a); putX(27, c);
      cmul2(a, CE[6], P[4], c, CE[7], P[4]); putX(29, a); putX(31, c);
    } else {
      f32x2 Y[16], E[5], CE[9];
      Y[1] = cexp(8.f); E[0] = cexp(128.f);
      Y[0] = f32x2{1.f, 0.f};
      auto putY = [&](int i) { lds_st<u32>(smem, tl + i * TAB_ROW, pk_f16_conj(Y[i])); };
      cmul2(Y[2], Y[1], Y[1], E[1], E[0], E[0]);
      cmul2(Y[3], Y[2], Y[1], Y[4], Y[2], Y[2]);
      putY(0); putY(1); putY(2);
      cmul2(E[2], E[1], E[1], Y[5], Y[4], Y[1]);
      cmul2(Y[6], Y[4], Y[2], Y[7], Y[4], Y[3]);
      putY(3); putY(4);
      cmul2(Y[8], Y[4], Y[4], E[3], E[2], E[2]);
      putY(5); putY(6); putY(7);
      cmul2(Y[9], Y[8], Y[1], Y[10], Y[8], Y[2]);
      cmul2(E[4], E[3], E[3], CE[0], C, E[3]);
      putY(8);
      cmul2(Y[11], Y[8], Y[3], Y[12], Y[8], Y[4]);
      putY(9); putY(10);
      cmul2(Y[13], Y[8], Y[5], Y[14], Y[8], Y[6]);
      putY(11); putY(12);
      cmul2(Y[15], Y[8], Y[7], CE[1], CE[0], E[0]);
      putY(13); putY(14);
      cmul2(CE[2], CE[0], E[1], CE[8], C, E[4]);
      putY(15); putX(32, CE[0]); putX(33, CE[1]);
      cmul2(CE[3], CE[1], E[1], CE[4], CE[0], E[2]);
      putX(34, CE[2]); putX(40, CE[8]);
      cmul2(CE[5], CE[1], E[2], CE[6], CE[2], E[2]);
      putX(35, CE[3]); putX(36, CE[4]);
      CE[7] = cmul(CE[3], E[2]);
      putX(37, CE[5]); putX(38, CE[6]);
      putX(39, CE[7]);
    }
    CSTAMP(3);
    asm volatile("s_waitcnt vmcnt(1)" : "+v"(lnf)::"memory");
    st32c(rWS, epoch, lane == 0 ? WS_HF + (((mt * 4 + w) * 16 + nt) * 2 + side) * 4 : OOB);
    spin_flag(sWS, WS_LNF + t8 * 4, epoch, lnf, true);
    u32x4 ug = ld128c(sWS, WS_XN + t8 * 8192 + cp * 32 + side * 16, 0);
    CSTAMP(4);
    bar();
    CSTAMP(5);
    const int tile = side ? 2 : mh2;
    const u32 kc = S_KC + kch * KC_CH;
    if (!(side && mh2)) {
      const u32 rd = S_TAB + kch * TAB_CH + c15 * TAB_ROW + g * 16;
      const u32 ra = S_TAB + kch * TAB_CH + (16 + min(16 * tile + c15, 40)) * TAB_ROW + g * 16;
      u32x4 yf[8], xf[8];
#pragma unroll
      for (int s = 0; s < 8; ++s) { yf[s] = lds_ld<u32x4>(smem, rd + s * 64); xf[s] = lds_ld<u32x4>(smem, ra + s * 64); }
      if (side == 0) {
        sfor<4>([&](auto N) { st2_piece(ic<16 + N>{}); });
        if (kh == 0) sfor<8>([&](auto N) { st2_piece(ic<20 + N>{}); });
      }
      f32x4 a0 = {0, 0, 0, 0}, a1 = {0, 0, 0, 0};
#pragma unroll
      for (int s = 0; s < 4; ++s) { a0 = mfma_h(xf[2 * s], yf[2 * s], a0); a1 = mfma_h(xf[2 * s + 1], yf[2 * s + 1], a1); }
      const f32x4 v = a0 + a1;
      const u32 h01 = pk_f16(f32x2{v[0], v[1]}), h23 = pk_f16(f32x2{v[2], v[3]});
      if (tile == 0) {
        lds_st<u32x2>(smem, kc + (128 * (g >> 1) + 8 * c15 + 4 * (g & 1)) * 2, u32x2{h01, h23});
      } else if (tile == 1) {
        const u32 t1a = g < 2 ? 256 + 64 * g + 4 * c15 : 384 + 64 * (g - 2) + 2 * c15;
        lds_st<u32>(smem, kc + t1a * 2, h01);
        lds_st<u32>(smem, kc + (t1a + (g < 2 ? 2 : 32)) * 2, h23);
      } else {
        const u32 t2 = g < 2 ? 512 + 64 * g + c15 : 644, st2 = g < 2 ? 16 : 0;
        lds_st<u16>(smem, kc + ((g == 2 && c15 == 0) ? 640 : t2) * 2, (u16)h01);
        lds_st<u16>(smem, kc + (t2 + st2) * 2, (u16)(h01 >> 16));
        lds_st<u16>(smem, kc + (t2 + 2 * st2) * 2, (u16)h23);
        lds_st<u16>(smem, kc + (t2 + 3 * st2) * 2, (u16)(h23 >> 16));
      }
    } else if (lane == 0) lds_st<u16>(smem, kc + 641 * 2, (u16)0);
    CSTAMP(6);
    bar();
    CSTAMP(7);
    if (side) wait_vm<0>(); else if (kh) wait_vm<4>(); else wait_vm<12>();
    tie(ug);
    if (side == 0 && kh) sfor<8>([&](auto N) { st2_piece(ic<20 + N>{}); });
    {
      u32 e[9];
#pragma unroll
      for (int i = 0; i < 9; ++i) e[i] = lds_ld<u16>(smem, S_KC + side * KC_CH + ((i & 1) ? ixp[i >> 1] >> 16 : ixp[i >> 1] & 0xffffu));
      const u32x4 a0 = {e[0] | (e[1] << 16), e[2] | (e[3] << 16), e[4] | (e[5] << 16), e[6] | (e[7] << 16)};
      const u32x4 a1 = {e[1] | (e[2] << 16), e[3] | (e[4] << 16), e[5] | (e[6] << 16), e[7] | (e[8] << 16)};
      lds_st<u32x4>(smem, S_G + side * G_CH + t8 * 16, a0);
      lds_st<u32x4>(smem, S_G + side * G_CH + G_PAR + t8 * 16, a1);
      const u32x4 z4 = {0, 0, 0, 0};
      if (t8 < 4) lds_st<u32x4>(smem, S_G + side * G_CH + (t8 >> 1) * G_PAR + 4096 + (t8 & 1) * 16, z4);
      if (t8 < 30) lds_st<u32x4>(smem, S_U + side * U_CH + t8 * 16, z4);
      lds_st<u32x4>(smem, S_U + side * U_CH + 480 + t8 * 16, ug);
    }
    CSTAMP(8);
  }

  const int ch0 = 64 * nt2 + 32 * kh + 4 * hh;
  const u32 xto = (64 * mt2 + (t8 >> 3)) * 1024 + nt2 * 128 + (t8 & 7) * 16;
  f32x16 acc[2];
  if (side == 0) {
    bar();
    CSTAMP(9); CDUMP(10, 38);
    STAMP(5);
    const int par = w & 1;
    const u32 ub = lb + S_U + kch * U_CH + 64 - 64 * par + 32 * c15 + 16 * g;
    const u32 gb0 = lb + S_G + kch * G_CH + ((c15 & 1) ? 0 : G_PAR) + 4 * (1015 - (c15 >> 1) - 16 * par + 4 * g - 32 * 31);
    const u32 gb[4] = {gb0, gb0 + 1024, gb0 + 2048, gb0 + 3072};
    u32x4 bq[32];
    u32x2 al[32], ah[32];
    f32x4 y[8];
    auto rdB = [&](auto M, auto T) { constexpr int q = 4 * T - M + 3; bq[q] = a_ds128<128 * q>(ub); };
    auto rdA = [&](auto M, auto H) {
      constexpr int k = (31 - M) >> 3, o = 32 * ((31 - M) & 7);
      if constexpr (H == 0) al[M] = a_ds2x32<o, o + 1>(gb[k]); else ah[M] = a_ds2x32<o + 2, o + 3>(gb[k]);
    };
    sfor<2>([&](auto M) { sfor<8>([&](auto T) { rdB(M, T); }); rdA(M, ic<0>{}); rdA(M, ic<1>{}); });
    sfor<32>([&](auto M) {
      constexpr int m = M, t0 = m >> 2, n = 8 - t0;
      wait_lgkm<(m <= 2 ? 10 : (m == 3 ? 4 : (m == 4 ? 6 : (2 * (31 - m) < 8 ? 2 * (31 - m) : 8))))>();
      sfor<n>([&](auto I) {
        constexpr int i = I, t = t0 + i;
        mfma16_h<m == 0>(y[t], cat4(al[m], ah[m]), bq[4 * t - m + 3]);
        if constexpr (m < 2) {
          rdB(ic<m + 2>{}, I);
          if constexpr (i >= 6) rdA(ic<m + 2>{}, ic<i - 6>{});
        } else if constexpr (m <= 4) {
          if constexpr (i < 4) rdA(ic<2 * m + (i >> 1)>{}, ic<i & 1>{});
        } else if constexpr (m + 5 < 32) {
          if constexpr (i < 2) rdA(ic<m + 5>{}, I);
        }
      });
    });
    asm volatile("s_nop 7\n\ts_nop 1" : "+v"(y[0]), "+v"(y[1]), "+v"(y[2]), "+v"(y[3]), "+v"(y[4]), "+v"(y[5]), "+v"(y[6]), "+v"(y[7]));
    STAMP(6);
    auto send = [&](auto PAR) {
      FENCE();
#pragma unroll
      for (int i = 0; i < 4; ++i) lds_st<f32x4>(smem, S_EXC + w * 4096 + i * 1024 + lane * 16, y[4 * (1 - PAR) + i]);
      FENCE();
    };
    if (par) send(ic<1>{}); else send(ic<0>{});
    wait_vm<0>();
    bar();
    const float dsk = bf1(q_dsk);
    u32x2 ao[4];
    auto finish = [&](auto PAR) {
      FENCE();
#pragma unroll
      for (int i = 0; i < 4; ++i) {
        const f32x4 o = lds_ld<f32x4>(smem, S_EXC + (w ^ 1) * 4096 + i * 1024 + lane * 16);
        const u32x2 uu = lds_ld<u32x2>(smem, S_U + kch * U_CH + (240 + 256 * (4 * PAR + i) + 16 * c15 + 4 * g) * 2);
        const f16x2 u01 = ash2(uu.x), u23 = ash2(uu.y);
        const float uf[4] = {(float)u01.x, (float)u01.y, (float)u23.x, (float)u23.y};
        float v[4];
#pragma unroll
        for (int e = 0; e < 4; ++e) v[e] = ffma1(uf[e], dsk, fmul1(fadd1(y[4 * PAR + i][e], o[e]), inv_sk));
        ao[i] = u32x2{cvt_bf2(v[0], v[1]), cvt_bf2(v[2], v[3])};
      }
      FENCE();
    };
    if (par) finish(ic<1>{}); else finish(ic<0>{});
#pragma unroll
    for (int i = 0; i < 4; ++i) {
      u32x2 o = ao[i];
      o.x = o.x == POISON ? 0xffc0ffc0u : o.x;
      o.y = o.y == POISON ? 0xffc0ffc0u : o.y;
      st64c(rWS, o, WS_A + ((4 * (4 * par + i) + (c15 >> 2)) * 512 + kd) * 128 + ((c15 & 3) * 16 + 4 * g) * 2);
    }
    STAMP(7);
    u32x4 xt0 = ld128(sX, xto, 0), xt1 = ld128(sX, xto + 32 * 1024, 0);
    u32 b2w = ld16(mk_srd(p.b2, DM * 2), t8 < 64 ? (64 * nt2 + t8) * 2 : OOB, 0);
    bar();
    const u32 ato = WS_A + (mt2 * 512 + nt2 * 64) * 128 + t8 * 16;
    asm volatile("s_waitcnt vmcnt(0)" : "+v"(xt0), "+v"(xt1), "+v"(b2w)::"memory");
    const int row0 = t8 >> 3, sl = t8 & 7;
    {
      const u32 o = S_XT + row0 * 128 + ((sl ^ (row0 & 7)) * 16);
      lds_st<u32x4>(smem, o, xt0);
      lds_st<u32x4>(smem, o + 32 * 128, xt1);
      lds_st<float>(smem, t8 < 64 ? S_B2 + t8 * 4 : S_MISC, bf1(b2w));
    }
    bar();
    u32x4 at0 = ld128c(sWS, ato, 0), at1 = ld128c(sWS, ato + 4096, 0);
    asm volatile("s_waitcnt vmcnt(0)" : "+v"(at0), "+v"(at1)::"memory");
#pragma unroll 1
    for (int it = 0; it < (1 << 16); ++it) {
      const u32 m0 = max(max(at0.x, at0.y), max(at0.z, at0.w)), m1 = max(max(at1.x, at1.y), max(at1.z, at1.w));
      if (!__any(max(m0, m1) == POISON)) break;
      at0 = ld128c(sWS, ato, 0); at1 = ld128c(sWS, ato + 4096, 0);
      asm volatile("s_waitcnt vmcnt(0)" : "+v"(at0), "+v"(at1)::"memory");
    }
    {
      const u32 o = S_AT + row0 * 128 + (((sl >> 1) ^ (row0 & 2)) * 32) + (sl & 1) * 16;
      lds_st<u32x4>(smem, o, at0);
      lds_st<u32x4>(smem, o + 32 * 128, at1);
    }
    bar();
  } else {
    u32x4 hf[22][2];
    const u32 hb = WS_H + ((mt2 * 2 + mh) * NUNIT + kh * 22) * 2048;
    const u32 hvo = lane * 16, hvz = kh ? OOB : hvo;
    auto ldh = [&](auto J, auto S) { hf[J][S] = ld128c(sWS, J == 21 ? hvz : hvo, hb + (J * 2 + S) * 1024); };
    {
      const u32 hfo = WS_HF + (((mt2 >> 1) * 4 + 2 * (mt2 & 1) + mh) * 32 + c31) * 4;
      u32 hfl = ld32c(sWS, hfo, 0);
      bar();
      CSTAMP(9); CDUMP(10, 48);
      STAMP(5);
      asm volatile("s_waitcnt vmcnt(0)" : "+v"(hfl)::"memory");
      spin_flag(sWS, hfo, epoch, hfl, kh ? c31 >= 16 : c31 <= 17);
    }
    STAMP(6);
    sfor<G2_LEAD>([&](auto J) { ldh(J, ic<0>{}); ldh(J, ic<1>{}); });
    STAMP(7);
    u32 wa[2];
#pragma unroll
    for (int n = 0; n < 2; ++n) wa[n] = lb + kh * W2_HALF + (4 * hh + q4) * 128 + (((2 * n + b16) ^ (q4 & 2)) * 32) + 8 * p4;
    u32x2 wl[2][4], wh[2][4];
    auto rd = [&](auto J, auto I, auto H) {
      constexpr int j = J, i = I, off = (j % W2_NS) * 4096 + ((i >> 1) * 16 + H * 8) * 128;
      if constexpr (H == 0) wl[j & 1][i] = a_dstr<off>(wa[i & 1]); else wh[j & 1][i] = a_dstr<off>(wa[i & 1]);
    };
    sfor<4>([&](auto I) { rd(ic<0>{}, I, ic<0>{}); rd(ic<0>{}, I, ic<1>{}); });
    sfor<22>([&](auto J) {
      constexpr int j = J;
      constexpr bool last = j == 21, brk = j == 7 || j == 13 || j == 17;
      if constexpr (j == 13) wait_vm<g2_ops_before(13) - g2_ops_before(9)>();
      if constexpr (j == 17) wait_vm<g2_ops_before(17) - g2_ops_before(11)>();
      if constexpr (brk) bar();
      wait_vm<g2_wait_h(j)>();
      tie(hf[j][0]); tie(hf[j][1]);
      sfor<4>([&](auto I) {
        constexpr int i = I;
        constexpr bool stg = j >= 7 && j <= 10;
        constexpr int ju = stg ? 14 + 2 * (j - 7) + (i >> 1) : 14, pi = i & 1;
        if constexpr (!brk) wait_lgkm<(last ? 6 - 2 * i : 6)>();
        if constexpr (stg && pi == 0) dma_m0(w2_lo + (ju % W2_NS) * 4096);
        mfma32_vv<j == 0 && i < 2>(acc[i & 1], cat4(wl[j & 1][i], wh[j & 1][i]), hf[j][i >> 1]);
        if constexpr ((i & 1) == 0 && j + G2_LEAD <= 21) ldh(ic<j + G2_LEAD>{}, ic<(i >> 1)>{});
        if constexpr (stg) dma_go<pi * 1024>(sW2, ju == 21 ? vw2z[pi] : vw2p[pi], w2_so + ju * 32 * 1024);
        if constexpr (!last) { rd(ic<j + 1>{}, I, ic<0>{}); rd(ic<j + 1>{}, I, ic<1>{}); }
      });
    });
    asm volatile("s_nop 7\n\ts_nop 3" : "+v"(acc[0]), "+v"(acc[1]));
    STAMP(8);
    auto send2 = [&](auto KH) {
      FENCE();
#pragma unroll
      for (int q = 0; q < 4; ++q) {
        const f32x16& as = acc[1 - KH];
        lds_st<f32x4>(smem, S_EX + w * 4096 + q * 1024 + lane * 16, f32x4{as[4 * q], as[4 * q + 1], as[4 * q + 2], as[4 * q + 3]});
      }
      FENCE();
    };
    if (kh) send2(ic<1>{}); else send2(ic<0>{});
    bar();
    const int tl = 32 * mh + c31;
    auto fin2 = [&](auto KH) {
      FENCE();
      const f32x16& ak = acc[KH];
      f32x4 pv[4], bb[4];
      u32x2 av[4], xv[4];
      u32 po[4];
#pragma unroll
      for (int q = 0; q < 4; ++q) {
        const int row = 32 * KH + 8 * q + 4 * hh + q4;
        po[q] = tl * 128 + (((4 * KH + q) ^ (tl & 7)) * 16) + hh * 8;
        pv[q] = lds_ld<f32x4>(smem, S_EX + (w ^ 2) * 4096 + q * 1024 + lane * 16);
        bb[q] = lds_ld<f32x4>(smem, S_B2 + (32 * KH + 8 * q + 4 * hh) * 4);
        av[q] = lds_tr(smem, S_AT + row * 128 + (((2 * mh + b16) ^ (row & 2)) * 32) + 8 * p4);
        xv[q] = lds_ld<u32x2>(smem, S_XT + po[q]);
      }
      FENCE();
      f32x2 o[8];
#pragma unroll
      for (int q = 0; q < 4; ++q) {
        o[2 * q] = f32x2{ak[4 * q], ak[4 * q + 1]} + f32x2{bb[q].x, bb[q].y};
        o[2 * q + 1] = f32x2{ak[4 * q + 2], ak[4 * q + 3]} + f32x2{bb[q].z, bb[q].w};
      }
      FENCE();
#pragma unroll
      for (int q = 0; q < 4; ++q) { o[2 * q] += f32x2{pv[q].x, pv[q].y}; o[2 * q + 1] += f32x2{pv[q].z, pv[q].w}; }
      FENCE();
#pragma unroll
      for (int q = 0; q < 4; ++q) { o[2 * q] += bf2(xv[q].x) + bf2(av[q].x); o[2 * q + 1] += bf2(xv[q].y) + bf2(av[q].y); }
      FENCE();
#pragma unroll
      for (int q = 0; q < 4; ++q) lds_st<u32x2>(smem, S_OT + po[q], u32x2{pk_bf16(o[2 * q]), pk_bf16(o[2 * q + 1])});
      FENCE();
    };
    if (kh) fin2(ic<1>{}); else fin2(ic<0>{});
  }
  bar();
  {
    const int row = tid >> 3, sl = tid & 7;
    const u32x4 o = lds_ld<u32x4>(smem, S_OT + row * 128 + ((sl ^ (row & 7)) * 16));
    __builtin_amdgcn_raw_buffer_store_b128(o, rOUT, (64 * mt2 + row) * 1024 + nt2 * 128 + sl * 16, 0, 0);
  }
  STAMP(9);
}

extern "C" void kernel_launch(void* const* d_in, const int* in_sizes, int n_in, void* d_out, int out_size, void* d_ws,
                              size_t ws_size, hipStream_t stream) {
  Params p;
  p.x = (const u16*)d_in[0]; p.lar = (const u16*)d_in[1]; p.aim = (const u16*)d_in[2]; p.cre = (const u16*)d_in[3];
  p.cim = (const u16*)d_in[4]; p.ldt = (const u16*)d_in[5]; p.dsk = (const u16*)d_in[6];
  p.w1 = (const u16*)d_in[8]; p.b1 = (const u16*)d_in[9]; p.w2 = (const u16*)d_in[10]; p.b2 = (const u16*)d_in[11];
  p.out = (u16*)d_out; p.ws = (char*)d_ws;
  s4d_mixer_fused<<<256, 512, 0, stream>>>(p);
}
```

```cpp
#include <hip/hip_runtime.h>
#include <stdint.h>
#include <utility>

namespace {
typedef unsigned short u16;
typedef unsigned u32;
typedef unsigned long long u64;
typedef float f32x2 __attribute__((ext_vector_type(2)));
typedef float f32x4 __attribute__((ext_vector_type(4)));
typedef float f32x16 __attribute__((ext_vector_type(16)));
typedef u32 u32x2 __attribute__((ext_vector_type(2)));
typedef u32 u32x4 __attribute__((ext_vector_type(4)));
typedef short s16x4 __attribute__((ext_vector_type(4)));
typedef __bf16 bf16x2 __attribute__((ext_vector_type(2)));
typedef _Float16 f16x2 __attribute__((ext_vector_type(2)));
typedef _Float16 f16x8 __attribute__((ext_vector_type(8)));
#define LAS __attribute__((address_space(3)))
#define DEV __device__ __forceinline__
typedef __amdgpu_buffer_rsrc_t rsrc_t;

constexpr int SEQ = 2048, DM = 512, NMODE = 128, FF = 1376, NUNIT = 43;
constexpr u32 WS_E = 0, WS_LNF = 0x1000  , WS_HF = 0x2000  , WS_STATS = 0x8000, WS_XN = 0x10000, WS_A = 0x210000, WS_H = 0x410000,
              WS_END = WS_H + 64 * NUNIT * 2048, WS_STAMP = 0xA00000;
constexpr u32 SLOT = 28672, SLOT_W = 16384;
constexpr u32 W2_NS = 14, W2_HALF = W2_NS * 4096;
constexpr u32 LDS_BYTES_ = 163840;
constexpr u32 S0 = 114688;
constexpr u32 S_DUMMY = LDS_BYTES_ - 256;
constexpr u32 S_LN = S0, LN_ROW = 1088;
constexpr u32 TAB_ROW = 544, TAB_CH = 57 * TAB_ROW;
constexpr u32 S_KC = LDS_BYTES_ - 256 - 2592, KC_CH = 1296;
constexpr u32 S_TAB = S_KC - 2 * TAB_CH;
constexpr u32 S_KS = S0 + 8704;
constexpr u32 S_CSP = S_KC;
constexpr u32 S_B1 = S_CSP + 768;
constexpr u32 S_MISC = S_B1 + 384;
constexpr u32 S_CS = S_MISC + 64;
constexpr u32 S_G = S0, G_PAR = 4160, G_CH = 8320;
constexpr u32 S_U = S0 + 16640, U_CH = 4608;
constexpr u32 S_EX = S0, S_EXC = S0 + 25856;
constexpr u32 S_AT = S0 + 16384, S_XT = S0 + 24576, S_OT = S0 + 32768, S_B2 = S0 + 40960;
constexpr u32 LDS_BYTES = 163840;
constexpr u32 OOB = 0x80000000u;
constexpr u32 POISON = 0xffffffffu;

#define STAMP(k) do {} while (0)
#define CSTAMP(i) do {} while (0)
#define PSTAMP(i) do {} while (0)
#define PDUMP() do {} while (0)
#define CDUMP(n, base) do {} while (0)
#define CDUMP2(n, base) do {} while (0)

struct Params {
  const u16 *x, *lar, *aim, *cre, *cim, *ldt, *dsk, *w1, *b1, *w2, *b2;
  u16* out;
  char* ws;
};

DEV float asf(u32 v) { return __builtin_bit_cast(float, v); }
DEV u32 asu(float v) { return __builtin_bit_cast(u32, v); }
DEV f16x2 ash2(u32 v) { return __builtin_bit_cast(f16x2, v); }
DEV float bflo(u32 v) { return asf(v << 16); }
DEV float bfhi(u32 v) { return asf(v & 0xffff0000u); }
DEV f32x2 bf2(u32 v) { return f32x2{bflo(v), bfhi(v)}; }
DEV u32 pk_bf16(f32x2 v) { return __builtin_bit_cast(u32, __builtin_convertvector(v, bf16x2)); }
DEV u32 pk_f16(f32x2 v) { return __builtin_bit_cast(u32, __builtin_convertvector(v, f16x2)); }
DEV float rbf(float a) { return bflo(pk_bf16(f32x2{a, a})); }
DEV float bf1(u32 v) { return asf(v << 16); }
DEV f32x2 sp2(float a) { return f32x2{a, a}; }
DEV f32x2 fma2(f32x2 a, f32x2 b, f32x2 c) { return __builtin_elementwise_fma(a, b, c); }

template <int N> DEV void wait_vm() { asm volatile("s_waitcnt vmcnt(%0)" ::"n"(N) : "memory"); }
template <int N> DEV void wait_lgkm() { asm volatile("s_waitcnt lgkmcnt(%0)" ::"n"(N)); }
DEV void bar() { asm volatile("s_waitcnt lgkmcnt(0)\n\ts_barrier" ::: "memory"); }
DEV u32x4 mk_srd(const void* base, u32 bytes) {
  const u64 b = (u64)base;
  u32x4 s;
  s.x = __builtin_amdgcn_readfirstlane((u32)b);
  s.y = __builtin_amdgcn_readfirstlane((u32)(b >> 32)) & 0xffffu;
  s.z = bytes;
  s.w = 0x00020000u;
  return s;
}
DEV rsrc_t mk_rsrc(const void* base, u32 bytes) { return __builtin_amdgcn_make_buffer_rsrc((void*)base, 0, bytes, 0x00020000); }

#define BLOAD(INS, TY, NAME, FLAGS, CON)                                                                     \
  DEV TY NAME(u32x4 srd, u32 voff, u32 soff) {                                                               \
    TY r;                                                                                                    \
    asm volatile(INS " %0, %1, %2, %3 offen" FLAGS : CON(r) : "v"(voff), "s"(srd), "s"(soff) : "memory");     \
    return r;                                                                                                \
  }
BLOAD("buffer_load_dwordx4", u32x4, ld128, "", "=v")
BLOAD("buffer_load_dwordx4", u32x4, ld128c, " sc1", "=v")
BLOAD("buffer_load_dwordx4", u32x4, ld128c_a, " sc1", "=a")
BLOAD("buffer_load_dwordx2", u32x2, ld64, "", "=v")
BLOAD("buffer_load_dword", u32, ld32c, " sc1", "=v")
BLOAD("buffer_load_ushort", u32, ld16, "", "=v")
DEV void touch(const void* a, u32 lds_dummy) { asm volatile("s_mov_b32 m0, %1\n\ts_nop 0\n\tglobal_load_lds_dword %0, off" ::"v"(a), "s"(lds_dummy) : "memory"); }
template <class T> DEV void tie(T& v) { asm volatile("" : "+v"(v)); }

DEV void dma_m0(u32 lds_addr) { asm volatile("s_mov_b32 m0, %0" ::"s"(lds_addr) : "memory"); }
template <int IOFF> DEV void dma_go(u32x4 srd, u32 voff, u32 soff) {
  asm volatile("buffer_load_dwordx4 %0, %1, %2 offen offset:%3 lds" ::"v"(voff), "s"(srd), "s"(soff), "n"(IOFF) : "memory");
}
DEV void st128c(rsrc_t r, u32x4 v, u32 voff) { __builtin_amdgcn_raw_buffer_store_b128(v, r, voff, 0, 16); }
DEV void st64c(rsrc_t r, u32x2 v, u32 voff) { __builtin_amdgcn_raw_buffer_store_b64(v, r, voff, 0, 16); }
DEV void st32c(rsrc_t r, u32 v, u32 voff) { __builtin_amdgcn_raw_buffer_store_b32(v, r, voff, 0, 16); }
DEV void st64(rsrc_t r, u32x2 v, u32 voff) { __builtin_amdgcn_raw_buffer_store_b64(v, r, voff, 0, 0); }

template <class T> DEV T lds_ld(const char* smem, u32 off) { return *(const LAS T*)(smem + off); }
template <class T> DEV void lds_st(char* smem, u32 off, T v) { *(LAS T*)(smem + off) = v; }
DEV u32x2 lds_tr(const char* smem, u32 off) { return __builtin_bit_cast(u32x2, __builtin_amdgcn_ds_read_tr16_b64_v4i16((LAS s16x4*)(smem + off))); }

template <int I> using ic = std::integral_constant<int, I>;
template <class F, int... Is> DEV void sfor_impl(F&& f, std::integer_sequence<int, Is...>) { (f(ic<Is>{}), ...); }
template <int N, class F> DEV void sfor(F&& f) { sfor_impl(f, std::make_integer_sequence<int, N>{}); }
template <int OFF> DEV u32x4 a_ds128(u32 addr) { u32x4 r; asm volatile("ds_read_b128 %0, %1 offset:%2" : "=v"(r) : "v"(addr), "n"(OFF)); return r; }
template <int OFF> DEV u32x2 a_dstr(u32 addr) { u32x2 r; asm volatile("ds_read_b64_tr_b16 %0, %1 offset:%2" : "=v"(r) : "v"(addr), "n"(OFF)); return r; }
template <int O0, int O1> DEV u32x2 a_ds2x32(u32 addr) { u32x2 r; asm volatile("ds_read2_b32 %0, %1 offset0:%2 offset1:%3" : "=v"(r) : "v"(addr), "n"(O0), "n"(O1)); return r; }
DEV u32x4 cat4(u32x2 a, u32x2 b) { return __builtin_shufflevector(a, b, 0, 1, 2, 3); }
template <bool INIT> DEV void mfma32_vv(f32x16& c, u32x4 a, u32x4 b) {
  if constexpr (INIT) asm volatile("v_mfma_f32_32x32x16_bf16 %0, %1, %2, 0" : "=&v"(c) : "v"(a), "v"(b));
  else asm volatile("v_mfma_f32_32x32x16_bf16 %0, %1, %2, %0" : "+v"(c) : "v"(a), "v"(b));
}
template <bool INIT> DEV void mfma32_va(f32x16& c, u32x4 a, u32x4 b) {
  if constexpr (INIT) asm volatile("v_mfma_f32_32x32x16_bf16 %0, %1, %2, 0" : "=&v"(c) : "v"(a), "a"(b));
  else asm volatile("v_mfma_f32_32x32x16_bf16 %0, %1, %2, %0" : "+v"(c) : "v"(a), "a"(b));
}
template <bool INIT> DEV void mfma16_h(f32x4& c, u32x4 a, u32x4 b) {
  if constexpr (INIT) asm volatile("v_mfma_f32_16x16x32_f16 %0, %1, %2, 0" : "=&v"(c) : "v"(a), "v"(b));
  else asm volatile("v_mfma_f32_16x16x32_f16 %0, %1, %2, %0" : "+v"(c) : "v"(a), "v"(b));
}
DEV f32x4 mfma_h(u32x4 a, u32x4 b, f32x4 c) {
  return __builtin_amdgcn_mfma_f32_16x16x32_f16(__builtin_bit_cast(f16x8, a), __builtin_bit_cast(f16x8, b), c, 0, 0, 0);
}
template <bool INIT> DEV void mfma16_bf(f32x4& c, u32x4 a, u32x4 b) {
  if constexpr (INIT) asm volatile("v_mfma_f32_16x16x32_bf16 %0, %1, %2, 0" : "=&v"(c) : "v"(a), "v"(b));
  else asm volatile("v_mfma_f32_16x16x32_bf16 %0, %1, %2, %0" : "+v"(c) : "v"(a), "v"(b));
}
#define FENCE() __builtin_amdgcn_sched_barrier(0)

DEV void wave_sum4(float& a, float& b, float& c, float& d) {
#define S4(CTRL) "v_add_f32_dpp %0, %0, %0 " CTRL "\n\tv_add_f32_dpp %1, %1, %1 " CTRL "\n\tv_add_f32_dpp %2, %2, %2 " CTRL "\n\tv_add_f32_dpp %3, %3, %3 " CTRL "\n\t"
  asm volatile("s_nop 1\n\t" S4("row_shr:1 row_mask:0xf bank_mask:0xf bound_ctrl:1") S4("row_shr:2 row_mask:0xf bank_mask:0xf bound_ctrl:1")
               S4("row_shr:4 row_mask:0xf bank_mask:0xf bound_ctrl:1") S4("row_shr:8 row_mask:0xf bank_mask:0xf bound_ctrl:1")
               S4("row_bcast:15 row_mask:0xa bank_mask:0xf") S4("row_bcast:31 row_mask:0xc bank_mask:0xf") "s_nop 0"
               : "+v"(a), "+v"(b), "+v"(c), "+v"(d));
#undef S4
  a = asf(__builtin_amdgcn_readlane(asu(a), 63)); b = asf(__builtin_amdgcn_readlane(asu(b), 63));
  c = asf(__builtin_amdgcn_readlane(asu(c), 63)); d = asf(__builtin_amdgcn_readlane(asu(d), 63));
}
DEV void wave_sum2(float& a, float& b) {
#define S2(CTRL) "v_add_f32_dpp %0, %0, %0 " CTRL "\n\tv_add_f32_dpp %1, %1, %1 " CTRL "\n\ts_nop 0\n\t"
  asm volatile("s_nop 1\n\t" S2("row_shr:1 row_mask:0xf bank_mask:0xf bound_ctrl:1") S2("row_shr:2 row_mask:0xf bank_mask:0xf bound_ctrl:1")
               S2("row_shr:4 row_mask:0xf bank_mask:0xf bound_ctrl:1") S2("row_shr:8 row_mask:0xf bank_mask:0xf bound_ctrl:1")
               S2("row_bcast:15 row_mask:0xa bank_mask:0xf") S2("row_bcast:31 row_mask:0xc bank_mask:0xf")
               : "+v"(a), "+v"(b));
#undef S2
  a = asf(__builtin_amdgcn_readlane(asu(a), 63)); b = asf(__builtin_amdgcn_readlane(asu(b), 63));
}
template <int CTRL, int RM> DEV float dpp_add(float v) { return v + asf(__builtin_amdgcn_update_dpp(0, asu(v), CTRL, RM, 0xf, true)); }
DEV float wave_sum(float v) {
  v = dpp_add<0x111, 0xf>(v); v = dpp_add<0x112, 0xf>(v); v = dpp_add<0x114, 0xf>(v); v = dpp_add<0x118, 0xf>(v);
  v = dpp_add<0x142, 0xa>(v); v = dpp_add<0x143, 0xc>(v);
  return asf(__builtin_amdgcn_readlane(asu(v), 63));
}
DEV void cmul2(f32x2& r0, f32x2 a0, f32x2 b0, f32x2& r1, f32x2 a1, f32x2 b1) {
  f32x2 t0, t1;
  asm("v_pk_mul_f32 %0, %4, %5 op_sel_hi:[1,0]\n\tv_pk_mul_f32 %1, %6, %7 op_sel_hi:[1,0]\n\t"
      "v_pk_fma_f32 %2, %4, %5, %0 op_sel:[1,1,0] op_sel_hi:[0,1,1] neg_lo:[1,0,0]\n\t"
      "v_pk_fma_f32 %3, %6, %7, %1 op_sel:[1,1,0] op_sel_hi:[0,1,1] neg_lo:[1,0,0]"
      : "=&v"(t0), "=&v"(t1), "=&v"(r0), "=&v"(r1) : "v"(a0), "v"(b0), "v"(a1), "v"(b1));
}
DEV f32x2 cmul(f32x2 a, f32x2 b) {
  f32x2 t, r;
  asm("v_pk_mul_f32 %0, %2, %3 op_sel_hi:[1,0]\n\tv_pk_fma_f32 %1, %2, %3, %0 op_sel:[1,1,0] op_sel_hi:[0,1,1] neg_lo:[1,0,0]"
      : "=&v"(t), "=v"(r) : "v"(a), "v"(b));
  return r;
}
DEV u32 cvt_bf2(float a, float b) { u32 r; asm("v_cvt_pk_bf16_f32 %0, %1, %2" : "=v"(r) : "v"(a), "v"(b)); return r; }
DEV u32 cvt_h2(float a, float b) { u32 r; asm("v_cvt_pk_f16_f32 %0, %1, %2" : "=v"(r) : "v"(a), "v"(b)); return r; }
DEV float fadd1(float a, float b) { float r; asm("v_add_f32 %0, %1, %2" : "=v"(r) : "v"(a), "v"(b)); return r; }
DEV float fsub1(float a, float b) { float r; asm("v_sub_f32 %0, %1, %2" : "=v"(r) : "v"(a), "v"(b)); return r; }
DEV float fmul1(float a, float b) { float r; asm("v_mul_f32 %0, %1, %2" : "=v"(r) : "v"(a), "v"(b)); return r; }
DEV float ffma1(float a, float b, float c) { float r; asm("v_fma_f32 %0, %1, %2, %3" : "=v"(r) : "v"(a), "v"(b), "v"(c)); return r; }
DEV float rbf1(float a) { return bflo(cvt_bf2(a, a)); }
DEV u32 pk_f16_conj(f32x2 v) { u32 r; asm("v_cvt_pk_f16_f32 %0, %1, -%2" : "=v"(r) : "v"(v.x), "v"(v.y)); return r; }
DEV f32x2 gelu2(f32x2 v) {
  const f32x2 s = v * fma2(v * v, sp2(-0.10294323970f), sp2(-2.3022081986f));
  const f32x2 d = f32x2{__builtin_amdgcn_exp2f(s.x), __builtin_amdgcn_exp2f(s.y)} + sp2(1.0f);
  return v * f32x2{__builtin_amdgcn_rcpf(d.x), __builtin_amdgcn_rcpf(d.y)};
}
DEV void spin_flag(u32x4 srd, u32 voff, u32 epoch, u32 val, bool need) {
#pragma unroll 1
  for (int it = 0; it < (1 << 16); ++it) {
    if (__all(!need || val == epoch)) break;
    __builtin_amdgcn_s_sleep(1);
    val = ld32c(srd, voff, 0);
    asm volatile("s_waitcnt vmcnt(0)" : "+v"(val)::"memory");
  }
}

constexpr int G2_LEAD = 8;
constexpr int g2_unit_ops(int j) { return (j + G2_LEAD <= 21 ? 2 : 0) + ((j >= 7 && j <= 10) ? 4 : 0); }
constexpr int g2_ops_before(int j) { int n = 2 * G2_LEAD; for (int i = 0; i < j; ++i) n += g2_unit_ops(i); return n; }
constexpr int g2_pos_h(int j) {
  if (j < G2_LEAD) return 2 * j + 2;
  const int i = j - G2_LEAD;
  return g2_ops_before(i) + 2 + ((i >= 7 && i <= 10) ? 2 : 0);
}
constexpr int g2_wait_h(int j) { return g2_ops_before(j) - g2_pos_h(j); }

}

__global__ __launch_bounds__(512, 2) void s4d_mixer_fused(Params p) {
  __shared__ __attribute__((aligned(16))) char smem[LDS_BYTES];
  const int tid = threadIdx.x, lane = tid & 63, t8 = tid & 255;
  const int wid = __builtin_amdgcn_readfirstlane(tid >> 6);
  const int side = wid >> 2, w = wid & 3;
  const int b = blockIdx.x, g8 = b & 7, r = b >> 3;
  const int mt = 4 * (g8 >> 1) + (r >> 3), nt = 8 * (g8 & 1) + (r & 7);
  const int ustart = (nt * NUNIT) >> 4, unext = ((nt + 1) * NUNIT) >> 4;
  const int tg = 16 * mt + nt;
  const int cp = 32 * g8 + r;
  const int mt2 = 4 * g8 + (r >> 3), nt2 = r & 7;
  const int c15 = lane & 15, g = lane >> 4;
  const int c31 = lane & 31, hh = lane >> 5, b16 = (lane >> 4) & 1;
  const int q4 = (lane & 15) >> 2, p4 = lane & 3;
  const int kch = w >> 1, kd = 2 * cp + kch, kh = w >> 1, mh = w & 1;

  const u32x4 sX = mk_srd(p.x, SEQ * DM * 2), sWS = mk_srd(p.ws, WS_END);
  const rsrc_t rWS = mk_rsrc(p.ws, WS_END), rOUT = mk_rsrc(p.out, SEQ * DM * 2);
  const u32x4 sW1 = mk_srd(p.w1, DM * FF * 2), sW2 = mk_srd(p.w2, FF * DM * 2);
  const u32 lb = (u32)(uintptr_t)(LAS char*)smem;
  STAMP(0);
  PSTAMP(0);

  auto load_eword = [&]() { u32 e; asm volatile("s_load_dword %0, %1, 0x0" : "=s"(e) : "s"(p.ws) : "memory"); return e; };
  u32 vx[4], vw[3];
#pragma unroll
  for (int j = 0; j < 4; ++j) vx[j] = (8 * j + (lane >> 3)) * 1024 - j * 1024 + (((lane & 7) ^ ((4 * j + (lane >> 4)) & 7)) * 16);
#pragma unroll
  for (int j = 0; j < 3; ++j) {
    const int P = (3 * w + j) * 64 + lane, k = P / 12, sl = P - 12 * k;
    vw[j] = k * (FF * 2) + sl * 16 - j * 1024;
  }
  const u32 x_so = (mt * 128 + 32 * w) * 1024, x_lo = lb + 32 * w * 128, w_so = ustart * 64, w_lo = lb + SLOT_W + 3 * w * 1024;
  auto st1_m0 = [&](auto KC, auto J) {
    if constexpr (J == 0) dma_m0(x_lo + (KC & 3) * SLOT);
    if constexpr (J == 4) dma_m0(w_lo + (KC & 3) * SLOT);
  };
  auto st1_go = [&](auto KC, auto J) {
    if constexpr (J < 4) dma_go<J * 1024>(sX, vx[J], x_so + KC * 128); else dma_go<(J - 4) * 1024>(sW1, vw[J - 4], w_so + KC * 64 * (FF * 2));
  };
  auto stage = [&](auto KC) { sfor<7>([&](auto J) { if constexpr (J == 0 || J == 4) { st1_m0(KC, J); asm volatile("s_nop 0"); } st1_go(KC, J); }); };
  u32 ixp[5];
  auto prep_ix = [&]() {
#pragma unroll
    for (int e = 0; e < 9; ++e) {
      const int l = 2047 - (8 * t8 + e);
      const u32 rb = pk_bf16(sp2((float)l)) & 0xffffu;
      const u32 ix = (l < 0 ? 641u : (l < 128 ? (u32)l : rb - 17024u)) * 2;
      if (e & 1) ixp[e >> 1] |= ix << 16; else ixp[e >> 1] = ix;
    }
#pragma unroll
    for (int e = 0; e < 5; ++e) tie(ixp[e]);
  };
  u32 epoch;
  u32 q_lar, q_aim, q_cre, q_cim, q_cre2, q_cim2, q_ldt, q_dsk;
  u32x2 b1v;
  auto load_small = [&]() {
    const u32 vo = (64 * (w & 1) + lane) * 2, so = kd * NMODE * 2;
    const u32x4 sCr = mk_srd(p.cre, DM * NMODE * 2), sCi = mk_srd(p.cim, DM * NMODE * 2);
    q_lar = ld16(mk_srd(p.lar, DM * NMODE * 2), vo, so); q_aim = ld16(mk_srd(p.aim, DM * NMODE * 2), vo, so);
    q_cre = ld16(sCr, vo, so); q_cim = ld16(sCi, vo, so);
    q_cre2 = ld16(sCr, vo ^ 128, so); q_cim2 = ld16(sCi, vo ^ 128, so);
    q_ldt = ld16(mk_srd(p.ldt, DM * 2), 0, kd * 2);
    b1v = ld64(mk_srd(p.b1, FF * 2), lane < 24 ? lane * 8 : OOB, ustart * 64);
  };
#define TIE_SMALL "+v"(q_lar), "+v"(q_aim), "+v"(q_cre), "+v"(q_cim), "+v"(q_cre2), "+v"(q_cim2), "+v"(q_ldt), "+v"(b1v)
  f32x16 acc1[3];
  f32x4 csp[3];
  u32x4 stv;
  f32x4 ks0;
  f32x2 ks1;
  if (side == 0) {
    u32x4 xr0 = ld128(sX, lane * 16, (8 * tg + 2 * w) * 1024), xr1 = ld128(sX, lane * 16, (8 * tg + 2 * w + 1) * 1024);
    {
      u64 pc;
      asm volatile("s_getpc_b64 %0" : "=s"(pc));
      const char* a;
      if (t8 < 96) a = (const char*)p.x + (512 * (g8 >> 1) + 16 * r + t8 / 6) * 1024 + (2 + t8 % 6) * 128;
      else if (t8 < 192) a = (const char*)p.w1 + (256 + 8 * r + (t8 - 96) / 12) * (FF * 2) + (g8 & 1) * 1280 + ((t8 - 96) % 12) * 120;
      else a = (const char*)pc + ((r * 64 + lane) % 208) * 128;
      touch(a, lb + S_DUMMY);
    }
    u32 eword = load_eword();
    prep_ix();
    asm volatile("s_waitcnt vmcnt(1)" : "+v"(xr0), "+v"(xr1)::"memory");
    PSTAMP(1);
    STAMP(1);
    {
      const u32x4 xr[2] = {xr0, xr1};
      f32x2 x[2][4];
      float x0[2], sm[2], sq[2];
#pragma unroll
      for (int t = 0; t < 2; ++t) {
#pragma unroll
        for (int i = 0; i < 4; ++i) x[t][i] = bf2(xr[t][i]);
        x0[t] = asf(__builtin_amdgcn_readfirstlane(asu(x[t][0].x)));
        f32x2 s = {0.f, 0.f}, q = {0.f, 0.f};
#pragma unroll
        for (int i = 0; i < 4; ++i) { const f32x2 d = x[t][i] - sp2(x0[t]); s += d; q = fma2(d, d, q); }
        sm[t] = s.x + s.y; sq[t] = q.x + q.y;
      }
      wave_sum4(sm[0], sq[0], sm[1], sq[1]);
      float m1[2], r1[2], s2[2], q2[2];
#pragma unroll
      for (int t = 0; t < 2; ++t) {
        const float md = sm[t] * (1.0f / DM);
        const float var = fmaxf(__builtin_fmaf(-md, md, sq[t] * (1.0f / DM)), 0.f);
        m1[t] = rbf(x0[t] + md);
        r1[t] = __builtin_amdgcn_rcpf(rbf(__builtin_amdgcn_sqrtf(rbf(rbf(var) + 1e-5f))));
      }
#pragma unroll
      for (int t = 0; t < 2; ++t) {
        f32x2 s = {0.f, 0.f}, q = {0.f, 0.f};
        u32x4 hv;
#pragma unroll
        for (int i = 0; i < 4; ++i) {
          const f32x2 n = bf2(pk_bf16(bf2(pk_bf16(x[t][i] - sp2(m1[t]))) * sp2(r1[t])));
          s += n; q = fma2(n, n, q);
          hv[i] = pk_f16(n);
        }
        lds_st<u32x4>(smem, S_LN + (2 * w + t) * LN_ROW + lane * 16, hv);
        s2[t] = s.x + s.y; q2[t] = q.x + q.y;
      }
      wave_sum4(s2[0], q2[0], s2[1], q2[1]);
      float al[2], be[2];
#pragma unroll
      for (int t = 0; t < 2; ++t) {
        const float mean2 = s2[t] * (1.0f / DM);
        const float var2 = fmaxf(__builtin_fmaf(-mean2, mean2, q2[t] * (1.0f / DM)), 0.f);
        const float r2 = __builtin_amdgcn_rcpf(rbf(__builtin_amdgcn_sqrtf(rbf(rbf(var2) + 1e-5f))));
        al[t] = r1[t] * r2;
        be[t] = __builtin_fmaf(m1[t], r1[t], rbf(mean2)) * r2;
      }
      PSTAMP(2);
      asm volatile("s_waitcnt lgkmcnt(0)" : "+s"(eword)::"memory");
      epoch = eword * 0x9E3779B1u + 0x7F4A7C15u;
      const u32x4 sv = {asu(lane ? al[1] : al[0]), epoch, asu(lane ? be[1] : be[0]), epoch};
      st128c(rWS, sv, lane < 2 ? WS_STATS + (8 * tg + 2 * w + lane) * 16 : OOB);
    }
    PSTAMP(3); PSTAMP(4);
    const u32 xa0 = lb + (32 * w + c31) * 128 + ((hh ^ ((c31 >> 1) & 7)) * 16);
    const u32 wa0 = lb + SLOT_W + (8 * hh + q4) * 192 + b16 * 32 + 8 * p4;
    u32 xa[2][4], wa[2];
#pragma unroll
    for (int s = 0; s < 4; ++s) { xa[0][s] = xa0 ^ (s * 32); xa[1][s] = xa[0][s] + 2 * SLOT; }
    wa[0] = wa0; wa[1] = wa0 + 2 * SLOT;
    u32x4 xf[2];
    u32x2 wl[2][3], wh[2][3];
    auto rd = [&](auto S, auto R) {
      constexpr int s = S, rr = R, buf = s & 1, kc = s >> 2, sl = kc & 3, so = (sl & 1) * SLOT, sp = s & 3;
      if constexpr (rr == 0) xf[buf] = a_ds128<so>(xa[sl >> 1][sp]);
      else {
        constexpr int u = (rr - 1) >> 1, hf = (rr - 1) & 1, off = so + u * 64 + (sp * 16 + hf * 4) * 192;
        if constexpr (hf == 0) wl[buf][u] = a_dstr<off>(wa[sl >> 1]); else wh[buf][u] = a_dstr<off>(wa[sl >> 1]);
      }
    };
    PSTAMP(5); PSTAMP(6);
    bar();
    PSTAMP(7); PDUMP();
    CSTAMP(0);
    sfor<7>([&](auto R) { rd(ic<0>{}, R); });
    sfor<32>([&](auto S) {
      constexpr int s = S, buf = s & 1, sp = s & 3, kc = s >> 2;
      constexpr bool last = s == 31, brk = sp == 2 && kc < 7;
      sfor<3>([&](auto U) {
        constexpr int u = U;
        if constexpr (u == 0) wait_lgkm<4>();
        else if constexpr (u == 1) {
          wait_lgkm<(last ? 0 : 3)>();
          if constexpr (brk) {
            constexpr int n = kc + 1;
            CSTAMP(2 * n - 1);
            asm volatile("s_barrier" ::: "memory");
            CSTAMP(2 * n);
            if constexpr (n == 4) q_dsk = ld16(mk_srd(p.dsk, DM * 2), 0, kd * 2);
            if constexpr (n == 6) stv = ld128c(sWS, WS_STATS + (128 * mt + 32 * w + c31) * 16, 0);
          }
        }
        mfma32_vv<s == 0>(acc1[u], cat4(wl[buf][u], wh[buf][u]), xf[buf]);
        if constexpr (!last) {
          if constexpr (u == 0) sfor<3>([&](auto R) { rd(ic<s + 1>{}, R); });
          if constexpr (u == 1) sfor<2>([&](auto R) { rd(ic<s + 1>{}, ic<3 + R>{}); });
          if constexpr (u == 2) sfor<2>([&](auto R) { rd(ic<s + 1>{}, ic<5 + R>{}); });
        }
      });
    });
    asm volatile("s_nop 7\n\ts_nop 3" : "+v"(acc1[0]), "+v"(acc1[1]), "+v"(acc1[2]));
    CSTAMP(15); CDUMP(16, 0);
    STAMP(2);
    bar();
    ks0 = lds_ld<f32x4>(smem, S_KS + t8 * 32); ks1 = lds_ld<f32x2>(smem, S_KS + t8 * 32 + 16);
    asm volatile("s_waitcnt vmcnt(0)" : "+v"(stv), "+v"(q_dsk) :: "memory");
  } else {
    const u32 wc0 = lb + SLOT_W + (32 * (w >> 1) + 8 * g + q4) * 192 + 3 * (w & 1) * 32 + 8 * p4;
    u32x4 ones = {0x3f803f80u, 0x3f803f80u, 0x3f803f80u, 0x3f803f80u};
    tie(ones);
    u32x2 cl[3], ch[3];
    auto cs_rd = [&](auto KC) {
      const u32 a = wc0 + (KC & 3) * SLOT;
      sfor<3>([&](auto I) { cl[I] = a_dstr<I * 32>(a); ch[I] = a_dstr<I * 32 + 4 * 192>(a); });
      FENCE();
    };
    auto cs_sum = [&](auto KC) {
      FENCE();
      wait_lgkm<0>();
      sfor<3>([&](auto I) { mfma16_bf<KC == 0>(csp[I], cat4(cl[I], ch[I]), ones); });
    };
    stage(ic<0>{}); stage(ic<1>{}); stage(ic<2>{});
    __builtin_amdgcn_s_setprio(1);
    u32 eword = load_eword();
    prep_ix();
    asm volatile("s_waitcnt lgkmcnt(0)" : "+s"(eword)::"memory");
    epoch = eword * 0x9E3779B1u + 0x7F4A7C15u;
    PSTAMP(1); PSTAMP(2); PSTAMP(3); PSTAMP(4); PSTAMP(5);
    wait_vm<14>();
    PSTAMP(6);
    bar();
    PSTAMP(7); PDUMP();
    CSTAMP(0);
    STAMP(1);
    cs_rd(ic<0>{}); stage(ic<3>{}); cs_sum(ic<0>{});
    wait_vm<14>();
    CSTAMP(1);
    bar();
    CSTAMP(2);
    cs_rd(ic<1>{});
    load_small();
    cs_sum(ic<1>{});
    wait_vm<15>();
    CSTAMP(3);
    bar();
    CSTAMP(4);
    cs_rd(ic<2>{}); stage(ic<4>{}); cs_sum(ic<2>{});
    wait_vm<15>();
    CSTAMP(5);
    bar();
    CSTAMP(6);
    cs_rd(ic<3>{}); stage(ic<5>{}); cs_sum(ic<3>{});
    asm volatile("s_waitcnt vmcnt(7)" : TIE_SMALL :: "memory");
    CSTAMP(7);
    bar();
    CSTAMP(8);
    lds_st<f32x4>(smem, lane < 24 ? S_B1 + lane * 16 : S_MISC, f32x4{bflo(b1v.x), bfhi(b1v.x), bflo(b1v.y), bfhi(b1v.y)});
    cs_rd(ic<4>{}); stage(ic<6>{}); cs_sum(ic<4>{});
    wait_vm<7>();
    CSTAMP(9);
    bar();
    CSTAMP(10);
    cs_rd(ic<5>{}); stage(ic<7>{}); cs_sum(ic<5>{});
    wait_vm<7>();
    CSTAMP(11);
    bar();
    CSTAMP(12);
    cs_rd(ic<6>{});
#pragma unroll
    for (int i = 0; i < 2; ++i) {
      const u32 a = S_LN + q4 * LN_ROW + ((2 * w + i) * 64 + 16 * g + 4 * p4) * 2;
      st128c(rWS, cat4(lds_tr(smem, a), lds_tr(smem, a + 4 * LN_ROW)), WS_XN + tg * 8192 + ((2 * w + i) * 64 + lane) * 16);
    }
    {
#pragma unroll
      for (int i = 0; i < 2; ++i) {
        const int ln = (2 * w + i) * 8 + (lane >> 3);
        st128c(rWS, u32x4{POISON, POISON, POISON, POISON}, WS_A + ((ln >> 1) * 512 + 2 * cp + (ln & 1)) * 128 + (lane & 7) * 16);
      }
    }
#pragma unroll
    for (int i = 0; i < 2; ++i) {
      const int ix = min(i * 256 + t8, 343);
      touch((const char*)p.w2 + (43 * r + (ix >> 3)) * 1024 + (ix & 7) * 128, lb + S_DUMMY);
    }
    const float dt = rbf1(expf(bf1(q_ldt)));
    const float ar = rbf1(expf(bf1(q_lar)));
    const float dt2 = fadd1(dt, dt);
    const float e1 = fmul1(fmul1(ar, dt), -1.44269504089f), zi = fmul1(bf1(q_aim), dt);
    const float ph = fmul1(zi, 0.15915494309f);
    const float pl = ffma1(zi, 6.4206383e-9f, ffma1(zi, 0.15915494309f, -ph));
    cs_sum(ic<6>{});
    wait_vm<6>();
    CSTAMP(13);
    bar();
    CSTAMP(14);
    cs_rd(ic<7>{});
    {
      const float cr = fmul1(bf1(q_cre), dt2), ci = fmul1(bf1(q_cim), dt2);
      const float own = fadd1(__builtin_fabsf(cr), __builtin_fabsf(ci)), oth = fadd1(__builtin_fabsf(bf1(q_cre2)), __builtin_fabsf(bf1(q_cim2)));
      const float bd = wave_sum(ffma1(oth, dt2, own));
      int se = 267 - (int)((asu(bd) >> 23) & 0xff);
      se = se < 1 ? 1 : (se > 253 ? 253 : se);
      const float sc = asf((u32)se << 23);
      ks0 = f32x4{e1, ph, pl, fmul1(cr, sc)};
      ks1 = f32x2{fmul1(ci, sc), asf((u32)(254 - se) << 23)};
      lds_st<f32x4>(smem, S_KS + t8 * 32, ks0); lds_st<f32x2>(smem, S_KS + t8 * 32 + 16, ks1);
    }
    cs_sum(ic<7>{});
    asm volatile("s_nop 7\n\ts_nop 1" : "+v"(csp[0]), "+v"(csp[1]), "+v"(csp[2]));
#pragma unroll
    for (int i = 0; i < 3; ++i) lds_st<f32x4>(smem, c15 == 0 ? S_CSP + ((w >> 1) * 96 + (3 * (w & 1) + i) * 16 + 4 * g) * 4 : S_MISC, csp[i]);
    __builtin_amdgcn_s_setprio(0);
    CSTAMP(15); CDUMP(16, 20);
    STAMP(2);
    bar();
    const int f = t8 < 96 ? t8 : 0;
    lds_st<float>(smem, t8 < 96 ? S_CS + t8 * 4 : S_MISC, lds_ld<float>(smem, S_CSP + f * 4) + lds_ld<float>(smem, S_CSP + 384 + f * 4));
  }
  if (side == 0) {
#pragma unroll 1
    for (int it = 0; it < (1 << 16); ++it) {
      if (__all(stv.y == epoch && stv.w == epoch)) break;
      __builtin_amdgcn_s_sleep(1);
      stv = ld128c(sWS, WS_STATS + (128 * mt + 32 * w + c31) * 16, 0);
      asm volatile("s_waitcnt vmcnt(0)" : "+v"(stv)::"memory");
    }
  }
  bar();
  CSTAMP(0);
  STAMP(3);

  const u32 vw2 = (lane >> 3) * 1024 + ((((lane & 7) >> 1) ^ ((lane >> 3) & 2)) * 32) + (lane & 1) * 16;
  const u32 vw2p[2] = {vw2, vw2 + 7168};
  const u32 vw2z[2] = {kh ? OOB : vw2p[0], kh ? OOB : vw2p[1]};
  const u32 w2_lo = lb + kh * W2_HALF + 2 * mh * 1024, w2_so = (kh * 22 * 32 + 16 * mh) * 1024 + nt2 * 128;
  auto st2_piece = [&](auto N) {
    constexpr int j = N >> 1, i = N & 1;
    FENCE();
    if constexpr (i == 0) { dma_m0(w2_lo + (j % W2_NS) * 4096); asm volatile("s_nop 0"); }
    dma_go<i * 1024>(sW2, j == 21 ? vw2z[i] : vw2p[i], w2_so + j * 32 * 1024);
    FENCE();
  };

  if (side == 0) {
    __builtin_amdgcn_s_setprio(2);
    const f32x2 al = sp2(asf(stv.x)), nb = sp2(-asf(stv.z));
    const u32 hso = WS_H + ((mt * 4 + w) * NUNIT + ustart) * 2048 + lane * 16;
    f32x2 v[24], t[24];
    CSTAMP(10);
    sfor<12>([&](auto Q) {
      const f32x4 bi = lds_ld<f32x4>(smem, S_B1 + hh * 16 + Q * 32), cs = lds_ld<f32x4>(smem, S_CS + hh * 16 + Q * 32);
      t[2 * Q] = fma2(nb, f32x2{cs.x, cs.y}, f32x2{bi.x, bi.y}); t[2 * Q + 1] = fma2(nb, f32x2{cs.z, cs.w}, f32x2{bi.z, bi.w});
    });
    FENCE();
    CSTAMP(11);
#pragma unroll
    for (int q = 0; q < 12; ++q) {
      const f32x16& a = acc1[q >> 2];
      const int o = 4 * (q & 3);
      v[2 * q] = fma2(al, f32x2{a[o], a[o + 1]}, t[2 * q]); v[2 * q + 1] = fma2(al, f32x2{a[o + 2], a[o + 3]}, t[2 * q + 1]);
    }
    FENCE();
#pragma unroll
    for (int k = 0; k < 24; ++k) t[k] = v[k] * v[k];
    FENCE();
#pragma unroll
    for (int k = 0; k < 24; ++k) t[k] = fma2(t[k], sp2(-0.10294323970f), sp2(-2.3022081986f));
    FENCE();
#pragma unroll
    for (int k = 0; k < 24; ++k) t[k] = v[k] * t[k];
    FENCE();
#pragma unroll
    for (int k = 0; k < 24; ++k) t[k] = f32x2{__builtin_amdgcn_exp2f(t[k].x), __builtin_amdgcn_exp2f(t[k].y)};
    FENCE();
#pragma unroll
    for (int k = 0; k < 24; ++k) t[k] += sp2(1.0f);
    FENCE();
#pragma unroll
    for (int k = 0; k < 24; ++k) t[k] = f32x2{__builtin_amdgcn_rcpf(t[k].x), __builtin_amdgcn_rcpf(t[k].y)};
    FENCE();
#pragma unroll
    for (int k = 0; k < 24; ++k) v[k] *= t[k];
    FENCE();
    CSTAMP(12);
#pragma unroll
    for (int i = 0; i < 6; ++i)
      st128c(rWS, u32x4{pk_bf16(v[4 * i]), pk_bf16(v[4 * i + 1]), pk_bf16(v[4 * i + 2]), pk_bf16(v[4 * i + 3])},
             ustart + (i >> 1) < unext ? hso + i * 1024 : OOB);
    __builtin_amdgcn_s_setprio(0);
    CSTAMP(13);
  } else {
    wait_vm<2>();
    st32c(rWS, epoch, lane == 0 ? WS_LNF + (tg * 4 + w) * 4 : OOB);
    sfor<20>([&](auto N) { st2_piece(N); });
  }
  CSTAMP(1);
  const u32 lnfo = WS_LNF + (t8 * 4 + (g8 >> 1)) * 4;
  u32 lnf = ld32c(sWS, lnfo, 0);
  STAMP(4);
  float inv_sk;
  {
    const int mh2 = w & 1;
    const float e1 = ks0.x, ph = ks0.y, pl = ks0.z;
    const f32x2 C = {ks0.w, ks1.x};
    inv_sk = ks1.y;
    auto cexp = [&](float pw) {
      const float m = __builtin_amdgcn_exp2f(e1 * pw);
      const float a = __builtin_amdgcn_fractf(ph * pw) + pl * pw;
      return f32x2{__builtin_amdgcn_cosf(a), __builtin_amdgcn_sinf(a)} * sp2(m);
    };
    CSTAMP(2);
    const u32 tl = S_TAB + kch * TAB_CH + (64 * mh2 + lane) * 4;
    auto putX = [&](int row, f32x2 v) { lds_st<u32>(smem, tl + (16 + row) * TAB_ROW, pk_f16(v)); };
    if (side == 0) {
      f32x2 P[8], E[3], CE[8];
      P[1] = cexp(1.f); E[0] = cexp(128.f);
      CE[0] = C;
      cmul2(P[2], P[1], P[1], E[1], E[0], E[0]);
      cmul2(CE[1], C, E[0], P[3], P[2], P[1]);
      cmul2(P[4], P[2], P[2], E[2], E[1], E[1]);
      cmul2(CE[2], CE[0], E[1], CE[3], CE[1], E[1]);
      cmul2(P[5], P[4], P[1], P[6], P[4], P[2]);
      cmul2(CE[4], CE[0], E[2], CE[5], CE[1], E[2]);
      cmul2(CE[6], CE[2], E[2], CE[7], CE[3], E[2]);
      P[7] = cmul(P[4], P[3]);
      putX(0, CE[0]); putX(8, CE[1]);
#pragma unroll
      for (int i = 1; i < 8; ++i) {
        f32x2 a, c;
        cmul2(a, CE[0], P[i], c, CE[1], P[i]);
        putX(i, a); putX(8 + i, c);
      }
      putX(16, CE[2]); putX(20, CE[3]);
#pragma unroll
      for (int i = 1; i < 4; ++i) {
        f32x2 a, c;
        cmul2(a, CE[2], P[2 * i], c, CE[3], P[2 * i]);
        putX(16 + i, a); putX(20 + i, c);
      }
      putX(24, CE[4]); putX(26, CE[5]); putX(28, CE[6]); putX(30, CE[7]);
      f32x2 a, c;
      cmul2(a, CE[4], P[4], c, CE[5], P[4]); putX(25, a); putX(27, c);
      cmul2(a, CE[6], P[4], c, CE[7], P[4]); putX(29, a); putX(31, c);
    } else {
      f32x2 Y[16], E[5], CE[9];
      Y[1] = cexp(8.f); E[0] = cexp(128.f);
      Y[0] = f32x2{1.f, 0.f};
      auto putY = [&](int i) { lds_st<u32>(smem, tl + i * TAB_ROW, pk_f16_conj(Y[i])); };
      cmul2(Y[2], Y[1], Y[1], E[1], E[0], E[0]);
      cmul2(Y[3], Y[2], Y[1], Y[4], Y[2], Y[2]);
      putY(0); putY(1); putY(2);
      cmul2(E[2], E[1], E[1], Y[5], Y[4], Y[1]);
      cmul2(Y[6], Y[4], Y[2], Y[7], Y[4], Y[3]);
      putY(3); putY(4);
      cmul2(Y[8], Y[4], Y[4], E[3], E[2], E[2]);
      putY(5); putY(6); putY(7);
      cmul2(Y[9], Y[8], Y[1], Y[10], Y[8], Y[2]);
      cmul2(E[4], E[3], E[3], CE[0], C, E[3]);
      putY(8);
      cmul2(Y[11], Y[8], Y[3], Y[12], Y[8], Y[4]);
      putY(9); putY(10);
      cmul2(Y[13], Y[8], Y[5], Y[14], Y[8], Y[6]);
      putY(11); putY(12);
      cmul2(Y[15], Y[8], Y[7], CE[1], CE[0], E[0]);
      putY(13); putY(14);
      cmul2(CE[2], CE[0], E[1], CE[8], C, E[4]);
      putY(15); putX(32, CE[0]); putX(33, CE[1]);
      cmul2(CE[3], CE[1], E[1], CE[4], CE[0], E[2]);
      putX(34, CE[2]); putX(40, CE[8]);
      cmul2(CE[5], CE[1], E[2], CE[6], CE[2], E[2]);
      putX(35, CE[3]); putX(36, CE[4]);
      CE[7] = cmul(CE[3], E[2]);
      putX(37, CE[5]); putX(38, CE[6]);
      putX(39, CE[7]);
    }
    CSTAMP(3);
    asm volatile("s_waitcnt vmcnt(0)" : "+v"(lnf)::"memory");
    st32c(rWS, epoch, (side == 0 && lane == 0) ? WS_HF + ((mt * 4 + w) * 16 + nt) * 4 : OOB);
    spin_flag(sWS, lnfo, epoch, lnf, true);
    u32x4 ug = ld128c(sWS, WS_XN + t8 * 8192 + cp * 32 + side * 16, 0);
    CSTAMP(4);
    bar();
    st32c(rWS, epoch, (b == 0 && tid == 0) ? WS_E : OOB);
    CSTAMP(5);
    if (side && kh == 0) sfor<8>([&](auto N) { st2_piece(ic<20 + N>{}); });
    const int tile = side ? 2 : mh2;
    const u32 kc = S_KC + kch * KC_CH;
    if (!(side && mh2)) {
      const u32 rd = S_TAB + kch * TAB_CH + c15 * TAB_ROW + g * 16;
      const u32 ra = S_TAB + kch * TAB_CH + (16 + min(16 * tile + c15, 40)) * TAB_ROW + g * 16;
      u32x4 yf[8], xf[8];
#pragma unroll
      for (int s = 0; s < 8; ++s) { yf[s] = lds_ld<u32x4>(smem, rd + s * 64); xf[s] = lds_ld<u32x4>(smem, ra + s * 64); }
      f32x4 a0 = {0, 0, 0, 0}, a1 = {0, 0, 0, 0};
#pragma unroll
      for (int s = 0; s < 4; ++s) { a0 = mfma_h(xf[2 * s], yf[2 * s], a0); a1 = mfma_h(xf[2 * s + 1], yf[2 * s + 1], a1); }
      const f32x4 v = a0 + a1;
      const u32 h01 = pk_f16(f32x2{v[0], v[1]}), h23 = pk_f16(f32x2{v[2], v[3]});
      if (tile == 0) {
        lds_st<u32x2>(smem, kc + (128 * (g >> 1) + 8 * c15 + 4 * (g & 1)) * 2, u32x2{h01, h23});
      } else if (tile == 1) {
        const u32 t1a = g < 2 ? 256 + 64 * g + 4 * c15 : 384 + 64 * (g - 2) + 2 * c15;
        lds_st<u32>(smem, kc + t1a * 2, h01);
        lds_st<u32>(smem, kc + (t1a + (g < 2 ? 2 : 32)) * 2, h23);
      } else {
        const u32 t2 = g < 2 ? 512 + 64 * g + c15 : 644, st2 = g < 2 ? 16 : 0;
        lds_st<u16>(smem, kc + ((g == 2 && c15 == 0) ? 640 : t2) * 2, (u16)h01);
        lds_st<u16>(smem, kc + (t2 + st2) * 2, (u16)(h01 >> 16));
        lds_st<u16>(smem, kc + (t2 + 2 * st2) * 2, (u16)h23);
        lds_st<u16>(smem, kc + (t2 + 3 * st2) * 2, (u16)(h23 >> 16));
      }
    } else if (lane == 0) lds_st<u16>(smem, kc + 641 * 2, (u16)0);
    CSTAMP(6);
    bar();
    CSTAMP(7);
    if (side && kh == 0) wait_vm<8>(); else wait_vm<0>();
    tie(ug);
    if (side && kh) sfor<8>([&](auto N) { st2_piece(ic<20 + N>{}); });
    {
      u32 e[9];
#pragma unroll
      for (int i = 0; i < 9; ++i) e[i] = lds_ld<u16>(smem, S_KC + side * KC_CH + ((i & 1) ? ixp[i >> 1] >> 16 : ixp[i >> 1] & 0xffffu));
      const u32x4 a0 = {e[0] | (e[1] << 16), e[2] | (e[3] << 16), e[4] | (e[5] << 16), e[6] | (e[7] << 16)};
      const u32x4 a1 = {e[1] | (e[2] << 16), e[3] | (e[4] << 16), e[5] | (e[6] << 16), e[7] | (e[8] << 16)};
      lds_st<u32x4>(smem, S_G + side * G_CH + t8 * 16, a0);
      lds_st<u32x4>(smem, S_G + side * G_CH + G_PAR + t8 * 16, a1);
      const u32x4 z4 = {0, 0, 0, 0};
      if (t8 < 4) lds_st<u32x4>(smem, S_G + side * G_CH + (t8 >> 1) * G_PAR + 4096 + (t8 & 1) * 16, z4);
      if (t8 < 30) lds_st<u32x4>(smem, S_U + side * U_CH + t8 * 16, z4);
      lds_st<u32x4>(smem, S_U + side * U_CH + 480 + t8 * 16, ug);
    }
    CSTAMP(8);
  }

  const int ch0 = 64 * nt2 + 32 * kh + 4 * hh;
  const u32 xto = (64 * mt2 + (t8 >> 3)) * 1024 + nt2 * 128 + (t8 & 7) * 16;
  f32x16 acc[2];
  if (side == 0) {
    bar();
    CSTAMP(9); CDUMP(14, 34);
    STAMP(5);
    const int par = w & 1;
    const u32 ub = lb + S_U + kch * U_CH + 64 - 64 * par + 32 * c15 + 16 * g;
    const u32 gb0 = lb + S_G + kch * G_CH + ((c15 & 1) ? 0 : G_PAR) + 4 * (1015 - (c15 >> 1) - 16 * par + 4 * g - 32 * 31);
    const u32 gb[4] = {gb0, gb0 + 1024, gb0 + 2048, gb0 + 3072};
    u32x4 bq[32];
    u32x2 al[32], ah[32];
    f32x4 y[8];
    auto rdB = [&](auto M, auto T) { constexpr int q = 4 * T - M + 3; bq[q] = a_ds128<128 * q>(ub); };
    auto rdA = [&](auto M, auto H) {
      constexpr int k = (31 - M) >> 3, o = 32 * ((31 - M) & 7);
      if constexpr (H == 0) al[M] = a_ds2x32<o, o + 1>(gb[k]); else ah[M] = a_ds2x32<o + 2, o + 3>(gb[k]);
    };
    sfor<2>([&](auto M) { sfor<8>([&](auto T) { rdB(M, T); }); rdA(M, ic<0>{}); rdA(M, ic<1>{}); });
    sfor<32>([&](auto M) {
      constexpr int m = M, t0 = m >> 2, n = 8 - t0;
      wait_lgkm<(m <= 2 ? 10 : (m == 3 ? 4 : (m == 4 ? 6 : (2 * (31 - m) < 8 ? 2 * (31 - m) : 8))))>();
      sfor<n>([&](auto I) {
        constexpr int i = I, t = t0 + i;
        mfma16_h<m == 0>(y[t], cat4(al[m], ah[m]), bq[4 * t - m + 3]);
        if constexpr (m < 2) {
          rdB(ic<m + 2>{}, I);
          if constexpr (i >= 6) rdA(ic<m + 2>{}, ic<i - 6>{});
        } else if constexpr (m <= 4) {
          if constexpr (i < 4) rdA(ic<2 * m + (i >> 1)>{}, ic<i & 1>{});
        } else if constexpr (m + 5 < 32) {
          if constexpr (i < 2) rdA(ic<m + 5>{}, I);
        }
      });
    });
    asm volatile("s_nop 7\n\ts_nop 1" : "+v"(y[0]), "+v"(y[1]), "+v"(y[2]), "+v"(y[3]), "+v"(y[4]), "+v"(y[5]), "+v"(y[6]), "+v"(y[7]));
    STAMP(6);
    CSTAMP(0);
    auto send = [&](auto PAR) {
      FENCE();
#pragma unroll
      for (int i = 0; i < 4; ++i) lds_st<f32x4>(smem, S_EXC + w * 4096 + i * 1024 + lane * 16, y[4 * (1 - PAR) + i]);
      FENCE();
    };
    if (par) send(ic<1>{}); else send(ic<0>{});
    CSTAMP(1);
    bar();
    CSTAMP(2);
    const float dsk = bf1(q_dsk);
    u32x2 ao[4];
    auto finish = [&](auto PAR) {
      FENCE();
#pragma unroll
      for (int i = 0; i < 4; ++i) {
        const f32x4 o = lds_ld<f32x4>(smem, S_EXC + (w ^ 1) * 4096 + i * 1024 + lane * 16);
        const u32x2 uu = lds_ld<u32x2>(smem, S_U + kch * U_CH + (240 + 256 * (4 * PAR + i) + 16 * c15 + 4 * g) * 2);
        const f16x2 u01 = ash2(uu.x), u23 = ash2(uu.y);
        const float uf[4] = {(float)u01.x, (float)u01.y, (float)u23.x, (float)u23.y};
        float v[4];
#pragma unroll
        for (int e = 0; e < 4; ++e) v[e] = ffma1(uf[e], dsk, fmul1(fadd1(y[4 * PAR + i][e], o[e]), inv_sk));
        ao[i] = u32x2{cvt_bf2(v[0], v[1]), cvt_bf2(v[2], v[3])};
      }
      FENCE();
    };
    if (par) finish(ic<1>{}); else finish(ic<0>{});
    CSTAMP(3);
#pragma unroll
    for (int i = 0; i < 4; ++i) {
      u32x2 o = ao[i];
      o.x = o.x == POISON ? 0xffc0ffc0u : o.x;
      o.y = o.y == POISON ? 0xffc0ffc0u : o.y;
      st64c(rWS, o, WS_A + ((4 * (4 * par + i) + (c15 >> 2)) * 512 + kd) * 128 + ((c15 & 3) * 16 + 4 * g) * 2);
    }
    STAMP(7);
    u32x4 xt0 = ld128(sX, xto, 0), xt1 = ld128(sX, xto + 32 * 1024, 0);
    u32 b2w = ld16(mk_srd(p.b2, DM * 2), t8 < 64 ? (64 * nt2 + t8) * 2 : OOB, 0);
    CSTAMP(4);
    bar();
    CSTAMP(5);
    const u32 ato = WS_A + (mt2 * 512 + nt2 * 64) * 128 + t8 * 16;
    asm volatile("s_waitcnt vmcnt(0)" : "+v"(xt0), "+v"(xt1), "+v"(b2w)::"memory");
    const int row0 = t8 >> 3, sl = t8 & 7;
    {
      const u32 o = S_XT + row0 * 128 + ((sl ^ (row0 & 7)) * 16);
      lds_st<u32x4>(smem, o, xt0);
      lds_st<u32x4>(smem, o + 32 * 128, xt1);
      lds_st<float>(smem, t8 < 64 ? S_B2 + t8 * 4 : S_MISC, bf1(b2w));
    }
    CSTAMP(6);
    bar();
    CSTAMP(7);
    u32x4 at0 = ld128c(sWS, ato, 0), at1 = ld128c(sWS, ato + 4096, 0);
    asm volatile("s_waitcnt vmcnt(0)" : "+v"(at0), "+v"(at1)::"memory");
#pragma unroll 1
    for (int it = 0; it < (1 << 16); ++it) {
      const u32 m0 = max(max(at0.x, at0.y), max(at0.z, at0.w)), m1 = max(max(at1.x, at1.y), max(at1.z, at1.w));
      if (!__any(max(m0, m1) == POISON)) break;
      at0 = ld128c(sWS, ato, 0); at1 = ld128c(sWS, ato + 4096, 0);
      asm volatile("s_waitcnt vmcnt(0)" : "+v"(at0), "+v"(at1)::"memory");
    }
    CSTAMP(8);
    {
      const u32 o = S_AT + row0 * 128 + (((sl >> 1) ^ (row0 & 2)) * 32) + (sl & 1) * 16;
      lds_st<u32x4>(smem, o, at0);
      lds_st<u32x4>(smem, o + 32 * 128, at1);
    }
    CSTAMP(9);
    bar();
    CSTAMP(10); CDUMP2(11, 0);
  } else {
    u32x4 hf[22][2];
    const u32 hb = WS_H + ((mt2 * 2 + mh) * NUNIT + kh * 22) * 2048;
    const u32 hvo = lane * 16, hvz = kh ? OOB : hvo;
    auto ldh = [&](auto J, auto S) { hf[J][S] = ld128c(sWS, J == 21 ? hvz : hvo, hb + (J * 2 + S) * 1024); };
    {
      const u32 hfo = WS_HF + (((mt2 >> 1) * 4 + 2 * (mt2 & 1) + mh) * 16 + c15) * 4;
      u32 hfl = ld32c(sWS, hfo, 0);
      bar();
      CSTAMP(9); CDUMP(10, 48);
      STAMP(5);
      asm volatile("s_waitcnt vmcnt(0)" : "+v"(hfl)::"memory");
      spin_flag(sWS, hfo, epoch, hfl, kh ? c15 >= 8 : c15 <= 8);
    }
    STAMP(6);
    sfor<G2_LEAD>([&](auto J) { ldh(J, ic<0>{}); ldh(J, ic<1>{}); });
    STAMP(7);
    u32 wa[2];
#pragma unroll
    for (int n = 0; n < 2; ++n) wa[n] = lb + kh * W2_HALF + (4 * hh + q4) * 128 + (((2 * n + b16) ^ (q4 & 2)) * 32) + 8 * p4;
    u32x2 wl[2][4], wh[2][4];
    auto rd = [&](auto J, auto I, auto H) {
      constexpr int j = J, i = I, off = (j % W2_NS) * 4096 + ((i >> 1) * 16 + H * 8) * 128;
      if constexpr (H == 0) wl[j & 1][i] = a_dstr<off>(wa[i & 1]); else wh[j & 1][i] = a_dstr<off>(wa[i & 1]);
    };
    CSTAMP(0);
    sfor<4>([&](auto I) { rd(ic<0>{}, I, ic<0>{}); rd(ic<0>{}, I, ic<1>{}); });
    sfor<22>([&](auto J) {
      constexpr int j = J;
      constexpr bool last = j == 21, brk = j == 7 || j == 13 || j == 17;
      if constexpr (j == 13) wait_vm<g2_ops_before(13) - g2_ops_before(9)>();
      if constexpr (j == 17) wait_vm<g2_ops_before(17) - g2_ops_before(11)>();
      if constexpr (brk) CSTAMP(j == 7 ? 1 : (j == 13 ? 3 : 5));
      if constexpr (brk) bar();
      if constexpr (brk) CSTAMP(j == 7 ? 2 : (j == 13 ? 4 : 6));
      wait_vm<g2_wait_h(j)>();
      tie(hf[j][0]); tie(hf[j][1]);
      sfor<4>([&](auto I) {
        constexpr int i = I;
        constexpr bool stg = j >= 7 && j <= 10;
        constexpr int ju = stg ? 14 + 2 * (j - 7) + (i >> 1) : 14, pi = i & 1;
        if constexpr (!brk) wait_lgkm<(last ? 6 - 2 * i : 6)>();
        if constexpr (stg && pi == 0) dma_m0(w2_lo + (ju % W2_NS) * 4096);
        mfma32_vv<j == 0 && i < 2>(acc[i & 1], cat4(wl[j & 1][i], wh[j & 1][i]), hf[j][i >> 1]);
        if constexpr ((i & 1) == 0 && j + G2_LEAD <= 21) ldh(ic<j + G2_LEAD>{}, ic<(i >> 1)>{});
        if constexpr (stg) dma_go<pi * 1024>(sW2, ju == 21 ? vw2z[pi] : vw2p[pi], w2_so + ju * 32 * 1024);
        if constexpr (!last) { rd(ic<j + 1>{}, I, ic<0>{}); rd(ic<j + 1>{}, I, ic<1>{}); }
      });
    });
    asm volatile("s_nop 7\n\ts_nop 3" : "+v"(acc[0]), "+v"(acc[1]));
    CSTAMP(7);
    STAMP(8);
    auto send2 = [&](auto KH) {
      FENCE();
#pragma unroll
      for (int q = 0; q < 4; ++q) {
        const f32x16& as = acc[1 - KH];
        lds_st<f32x4>(smem, S_EX + w * 4096 + q * 1024 + lane * 16, f32x4{as[4 * q], as[4 * q + 1], as[4 * q + 2], as[4 * q + 3]});
      }
      FENCE();
    };
    if (kh) send2(ic<1>{}); else send2(ic<0>{});
    CSTAMP(8);
    bar();
    CSTAMP(9);
    const int tl = 32 * mh + c31;
    auto fin2 = [&](auto KH) {
      FENCE();
      const f32x16& ak = acc[KH];
      f32x4 pv[4], bb[4];
      u32x2 av[4], xv[4];
      u32 po[4];
#pragma unroll
      for (int q = 0; q < 4; ++q) {
        const int row = 32 * KH + 8 * q + 4 * hh + q4;
        po[q] = tl * 128 + (((4 * KH + q) ^ (tl & 7)) * 16) + hh * 8;
        pv[q] = lds_ld<f32x4>(smem, S_EX + (w ^ 2) * 4096 + q * 1024 + lane * 16);
        bb[q] = lds_ld<f32x4>(smem, S_B2 + (32 * KH + 8 * q + 4 * hh) * 4);
        av[q] = lds_tr(smem, S_AT + row * 128 + (((2 * mh + b16) ^ (row & 2)) * 32) + 8 * p4);
        xv[q] = lds_ld<u32x2>(smem, S_XT + po[q]);
      }
      FENCE();
      f32x2 o[8];
#pragma unroll
      for (int q = 0; q < 4; ++q) {
        o[2 * q] = f32x2{ak[4 * q], ak[4 * q + 1]} + f32x2{bb[q].x, bb[q].y};
        o[2 * q + 1] = f32x2{ak[4 * q + 2], ak[4 * q + 3]} + f32x2{bb[q].z, bb[q].w};
      }
      FENCE();
#pragma unroll
      for (int q = 0; q < 4; ++q) { o[2 * q] += f32x2{pv[q].x, pv[q].y}; o[2 * q + 1] += f32x2{pv[q].z, pv[q].w}; }
      FENCE();
#pragma unroll
      for (int q = 0; q < 4; ++q) { o[2 * q] += bf2(xv[q].x) + bf2(av[q].x); o[2 * q + 1] += bf2(xv[q].y) + bf2(av[q].y); }
      FENCE();
#pragma unroll
      for (int q = 0; q < 4; ++q) lds_st<u32x2>(smem, S_OT + po[q], u32x2{pk_bf16(o[2 * q]), pk_bf16(o[2 * q + 1])});
      FENCE();
    };
    if (kh) fin2(ic<1>{}); else fin2(ic<0>{});
    CSTAMP(10); CDUMP2(11, 16);
  }
  bar();
  {
    const int row = tid >> 3, sl = tid & 7;
    const u32x4 o = lds_ld<u32x4>(smem, S_OT + row * 128 + ((sl ^ (row & 7)) * 16));
    __builtin_amdgcn_raw_buffer_store_b128(o, rOUT, (64 * mt2 + row) * 1024 + nt2 * 128 + sl * 16, 0, 0);
  }
  STAMP(9);
}

extern "C" void kernel_launch(void* const* d_in, const int* in_sizes, int n_in, void* d_out, int out_size, void* d_ws,
                              size_t ws_size, hipStream_t stream) {
  Params p;
  p.x = (const u16*)d_in[0]; p.lar = (const u16*)d_in[1]; p.aim = (const u16*)d_in[2]; p.cre = (const u16*)d_in[3];
  p.cim = (const u16*)d_in[4]; p.ldt = (const u16*)d_in[5]; p.dsk = (const u16*)d_in[6];
  p.w1 = (const u16*)d_in[8]; p.b1 = (const u16*)d_in[9]; p.w2 = (const u16*)d_in[10]; p.b2 = (const u16*)d_in[11];
  p.out = (u16*)d_out; p.ws = (char*)d_ws;
  s4d_mixer_fused<<<256, 512, 0, stream>>>(p);
}
```

```cpp
#include <hip/hip_runtime.h>
#include <stdint.h>
#include <utility>

namespace {
typedef unsigned short u16;
typedef unsigned u32;
typedef unsigned long long u64;
typedef float f32x2 __attribute__((ext_vector_type(2)));
typedef float f32x4 __attribute__((ext_vector_type(4)));
typedef float f32x16 __attribute__((ext_vector_type(16)));
typedef u32 u32x2 __attribute__((ext_vector_type(2)));
typedef u32 u32x4 __attribute__((ext_vector_type(4)));
typedef short s16x4 __attribute__((ext_vector_type(4)));
typedef __bf16 bf16x2 __attribute__((ext_vector_type(2)));
typedef _Float16 f16x2 __attribute__((ext_vector_type(2)));
typedef _Float16 f16x8 __attribute__((ext_vector_type(8)));
#define LAS __attribute__((address_space(3)))
#define DEV __device__ __forceinline__
typedef __amdgpu_buffer_rsrc_t rsrc_t;

constexpr int SEQ = 2048, DM = 512, NMODE = 128, FF = 1376, NUNIT = 43;
constexpr u32 WS_E = 0, WS_LNF = 0x1000  , WS_HF = 0x2000  , WS_PF = 0x5000  , WS_STATS = 0x8000, WS_XN = 0x10000, WS_A = 0x210000, WS_H = 0x410000,
              WS_END = WS_H + 64 * NUNIT * 2048, WS_STAMP = 0xA00000;
constexpr u32 SLOT = 28672, SLOT_W = 16384;
constexpr u32 W2_NS = 14, W2_HALF = W2_NS * 4096;
constexpr u32 LDS_BYTES_ = 163840;
constexpr u32 S0 = 114688;
constexpr u32 S_DUMMY = LDS_BYTES_ - 256;
constexpr u32 S_SYNC = LDS_BYTES_ - 272;
constexpr u32 S_LN = S0, LN_ROW = 1088;
constexpr u32 TAB_ROW = 544, TAB_CH = 57 * TAB_ROW;
constexpr u32 S_KC = LDS_BYTES_ - 272 - 2592, KC_CH = 1296;
constexpr u32 S_TAB = S_KC - 2 * TAB_CH;
constexpr u32 S_CSP = S_KC;
constexpr u32 S_B1 = S_CSP + 768;
constexpr u32 S_MISC = S_B1 + 384;
constexpr u32 S_CS = S_MISC + 64;
constexpr u32 S_G = S0, G_PAR = 4160, G_CH = 8320;
constexpr u32 S_U = S0 + 16640, U_CH = 4608;
constexpr u32 S_EX = S0, S_EXC = S0 + 25856;
constexpr u32 S_AT = S0 + 16384, S_XT = S0 + 24576, S_OT = S0 + 32768, S_B2 = S0 + 40960;
constexpr u32 LDS_BYTES = 163840;
constexpr u32 OOB = 0x80000000u;
constexpr u32 POISON = 0xffffffffu;
constexpr int CODE_LINES = 200;
constexpr int SPIN_MAX = 1 << 13;

#define STAMP(k) do {} while (0)
#define CSTAMP(i) do {} while (0)
#define PSTAMP(i) do {} while (0)
#define PDUMP() do {} while (0)
#define CDUMP(n, base) do {} while (0)
#define CDUMP2(n, base) do {} while (0)

struct Params {
  const u16 *x, *lar, *aim, *cre, *cim, *ldt, *dsk, *w1, *b1, *w2, *b2;
  u16* out;
  char* ws;
};

DEV float asf(u32 v) { return __builtin_bit_cast(float, v); }
DEV u32 asu(float v) { return __builtin_bit_cast(u32, v); }
DEV f16x2 ash2(u32 v) { return __builtin_bit_cast(f16x2, v); }
DEV float bflo(u32 v) { return asf(v << 16); }
DEV float bfhi(u32 v) { return asf(v & 0xffff0000u); }
DEV f32x2 bf2(u32 v) { return f32x2{bflo(v), bfhi(v)}; }
DEV u32 pk_bf16(f32x2 v) { return __builtin_bit_cast(u32, __builtin_convertvector(v, bf16x2)); }
DEV u32 pk_f16(f32x2 v) { return __builtin_bit_cast(u32, __builtin_convertvector(v, f16x2)); }
DEV float rbf(float a) { return bflo(pk_bf16(f32x2{a, a})); }
DEV float bf1(u32 v) { return asf(v << 16); }
DEV f32x2 sp2(float a) { return f32x2{a, a}; }
DEV f32x2 fma2(f32x2 a, f32x2 b, f32x2 c) { return __builtin_elementwise_fma(a, b, c); }

template <int N> DEV void wait_vm() { asm volatile("s_waitcnt vmcnt(%0)" ::"n"(N) : "memory"); }
template <int N> DEV void wait_lgkm() { asm volatile("s_waitcnt lgkmcnt(%0)" ::"n"(N)); }
DEV void bar() { asm volatile("s_waitcnt lgkmcnt(0)\n\ts_barrier" ::: "memory"); }
DEV u32x4 mk_srd(const void* base, u32 bytes) {
  const u64 b = (u64)base;
  u32x4 s;
  s.x = __builtin_amdgcn_readfirstlane((u32)b);
  s.y = __builtin_amdgcn_readfirstlane((u32)(b >> 32)) & 0xffffu;
  s.z = bytes;
  s.w = 0x00020000u;
  return s;
}
DEV rsrc_t mk_rsrc(const void* base, u32 bytes) { return __builtin_amdgcn_make_buffer_rsrc((void*)base, 0, bytes, 0x00020000); }

#define BLOAD(INS, TY, NAME, FLAGS, CON)                                                                     \
  DEV TY NAME(u32x4 srd, u32 voff, u32 soff) {                                                               \
    TY r;                                                                                                    \
    asm volatile(INS " %0, %1, %2, %3 offen" FLAGS : CON(r) : "v"(voff), "s"(srd), "s"(soff) : "memory");     \
    return r;                                                                                                \
  }
BLOAD("buffer_load_dwordx4", u32x4, ld128, "", "=v")
BLOAD("buffer_load_dwordx4", u32x4, ld128c, " sc1", "=v")
BLOAD("buffer_load_dwordx4", u32x4, ld128c_a, " sc1", "=a")
BLOAD("buffer_load_dwordx2", u32x2, ld64, "", "=v")
BLOAD("buffer_load_dword", u32, ld32c, " sc1", "=v")
BLOAD("buffer_load_ushort", u32, ld16, "", "=v")
DEV void touch(const void* a, u32 lds_dummy) { asm volatile("s_mov_b32 m0, %1\n\ts_nop 0\n\tglobal_load_lds_dword %0, off" ::"v"(a), "s"(lds_dummy) : "memory"); }
template <class T> DEV void tie(T& v) { asm volatile("" : "+v"(v)); }

DEV void dma_m0(u32 lds_addr) { asm volatile("s_mov_b32 m0, %0" ::"s"(lds_addr) : "memory"); }
template <int IOFF> DEV void dma_go(u32x4 srd, u32 voff, u32 soff) {
  asm volatile("buffer_load_dwordx4 %0, %1, %2 offen offset:%3 lds" ::"v"(voff), "s"(srd), "s"(soff), "n"(IOFF) : "memory");
}
DEV void st128c(rsrc_t r, u32x4 v, u32 voff) { __builtin_amdgcn_raw_buffer_store_b128(v, r, voff, 0, 16); }
DEV void st64c(rsrc_t r, u32x2 v, u32 voff) { __builtin_amdgcn_raw_buffer_store_b64(v, r, voff, 0, 16); }
DEV void st32c(rsrc_t r, u32 v, u32 voff) { __builtin_amdgcn_raw_buffer_store_b32(v, r, voff, 0, 16); }
DEV void st64(rsrc_t r, u32x2 v, u32 voff) { __builtin_amdgcn_raw_buffer_store_b64(v, r, voff, 0, 0); }

template <class T> DEV T lds_ld(const char* smem, u32 off) { return *(const LAS T*)(smem + off); }
template <class T> DEV void lds_st(char* smem, u32 off, T v) { *(LAS T*)(smem + off) = v; }
DEV u32x2 lds_tr(const char* smem, u32 off) { return __builtin_bit_cast(u32x2, __builtin_amdgcn_ds_read_tr16_b64_v4i16((LAS s16x4*)(smem + off))); }

template <int I> using ic = std::integral_constant<int, I>;
template <class F, int... Is> DEV void sfor_impl(F&& f, std::integer_sequence<int, Is...>) { (f(ic<Is>{}), ...); }
template <int N, class F> DEV void sfor(F&& f) { sfor_impl(f, std::make_integer_sequence<int, N>{}); }
template <int OFF> DEV u32x4 a_ds128(u32 addr) { u32x4 r; asm volatile("ds_read_b128 %0, %1 offset:%2" : "=v"(r) : "v"(addr), "n"(OFF)); return r; }
template <int OFF> DEV u32x2 a_dstr(u32 addr) { u32x2 r; asm volatile("ds_read_b64_tr_b16 %0, %1 offset:%2" : "=v"(r) : "v"(addr), "n"(OFF)); return r; }
template <int O0, int O1> DEV u32x2 a_ds2x32(u32 addr) { u32x2 r; asm volatile("ds_read2_b32 %0, %1 offset0:%2 offset1:%3" : "=v"(r) : "v"(addr), "n"(O0), "n"(O1)); return r; }
DEV u32x4 cat4(u32x2 a, u32x2 b) { return __builtin_shufflevector(a, b, 0, 1, 2, 3); }
template <bool INIT> DEV void mfma32_vv(f32x16& c, u32x4 a, u32x4 b) {
  if constexpr (INIT) asm volatile("v_mfma_f32_32x32x16_bf16 %0, %1, %2, 0" : "=&v"(c) : "v"(a), "v"(b));
  else asm volatile("v_mfma_f32_32x32x16_bf16 %0, %1, %2, %0" : "+v"(c) : "v"(a), "v"(b));
}
template <bool INIT> DEV void mfma32_va(f32x16& c, u32x4 a, u32x4 b) {
  if constexpr (INIT) asm volatile("v_mfma_f32_32x32x16_bf16 %0, %1, %2, 0" : "=&v"(c) : "v"(a), "a"(b));
  else asm volatile("v_mfma_f32_32x32x16_bf16 %0, %1, %2, %0" : "+v"(c) : "v"(a), "a"(b));
}
template <bool INIT> DEV void mfma16_h(f32x4& c, u32x4 a, u32x4 b) {
  if constexpr (INIT) asm volatile("v_mfma_f32_16x16x32_f16 %0, %1, %2, 0" : "=&v"(c) : "v"(a), "v"(b));
  else asm volatile("v_mfma_f32_16x16x32_f16 %0, %1, %2, %0" : "+v"(c) : "v"(a), "v"(b));
}
DEV f32x4 mfma_h(u32x4 a, u32x4 b, f32x4 c) {
  return __builtin_amdgcn_mfma_f32_16x16x32_f16(__builtin_bit_cast(f16x8, a), __builtin_bit_cast(f16x8, b), c, 0, 0, 0);
}
template <bool INIT> DEV void mfma16_bf(f32x4& c, u32x4 a, u32x4 b) {
  if constexpr (INIT) asm volatile("v_mfma_f32_16x16x32_bf16 %0, %1, %2, 0" : "=&v"(c) : "v"(a), "v"(b));
  else asm volatile("v_mfma_f32_16x16x32_bf16 %0, %1, %2, %0" : "+v"(c) : "v"(a), "v"(b));
}
#define FENCE() __builtin_amdgcn_sched_barrier(0)

DEV void wave_sum4(float& a, float& b, float& c, float& d) {
#define S4(CTRL) "v_add_f32_dpp %0, %0, %0 " CTRL "\n\tv_add_f32_dpp %1, %1, %1 " CTRL "\n\tv_add_f32_dpp %2, %2, %2 " CTRL "\n\tv_add_f32_dpp %3, %3, %3 " CTRL "\n\t"
  asm volatile("s_nop 1\n\t" S4("row_shr:1 row_mask:0xf bank_mask:0xf bound_ctrl:1") S4("row_shr:2 row_mask:0xf bank_mask:0xf bound_ctrl:1")
               S4("row_shr:4 row_mask:0xf bank_mask:0xf bound_ctrl:1") S4("row_shr:8 row_mask:0xf bank_mask:0xf bound_ctrl:1")
               S4("row_bcast:15 row_mask:0xa bank_mask:0xf") S4("row_bcast:31 row_mask:0xc bank_mask:0xf") "s_nop 0"
               : "+v"(a), "+v"(b), "+v"(c), "+v"(d));
#undef S4
  a = asf(__builtin_amdgcn_readlane(asu(a), 63)); b = asf(__builtin_amdgcn_readlane(asu(b), 63));
  c = asf(__builtin_amdgcn_readlane(asu(c), 63)); d = asf(__builtin_amdgcn_readlane(asu(d), 63));
}
DEV void wave_sum2(float& a, float& b) {
#define S2(CTRL) "v_add_f32_dpp %0, %0, %0 " CTRL "\n\tv_add_f32_dpp %1, %1, %1 " CTRL "\n\ts_nop 0\n\t"
  asm volatile("s_nop 1\n\t" S2("row_shr:1 row_mask:0xf bank_mask:0xf bound_ctrl:1") S2("row_shr:2 row_mask:0xf bank_mask:0xf bound_ctrl:1")
               S2("row_shr:4 row_mask:0xf bank_mask:0xf bound_ctrl:1") S2("row_shr:8 row_mask:0xf bank_mask:0xf bound_ctrl:1")
               S2("row_bcast:15 row_mask:0xa bank_mask:0xf") S2("row_bcast:31 row_mask:0xc bank_mask:0xf")
               : "+v"(a), "+v"(b));
#undef S2
  a = asf(__builtin_amdgcn_readlane(asu(a), 63)); b = asf(__builtin_amdgcn_readlane(asu(b), 63));
}
template <int CTRL, int RM> DEV float dpp_add(float v) { return v + asf(__builtin_amdgcn_update_dpp(0, asu(v), CTRL, RM, 0xf, true)); }
DEV float wave_sum(float v) {
  v = dpp_add<0x111, 0xf>(v); v = dpp_add<0x112, 0xf>(v); v = dpp_add<0x114, 0xf>(v); v = dpp_add<0x118, 0xf>(v);
  v = dpp_add<0x142, 0xa>(v); v = dpp_add<0x143, 0xc>(v);
  return asf(__builtin_amdgcn_readlane(asu(v), 63));
}
DEV void cmul2(f32x2& r0, f32x2 a0, f32x2 b0, f32x2& r1, f32x2 a1, f32x2 b1) {
  f32x2 t0, t1;
  asm("v_pk_mul_f32 %0, %4, %5 op_sel_hi:[1,0]\n\tv_pk_mul_f32 %1, %6, %7 op_sel_hi:[1,0]\n\t"
      "v_pk_fma_f32 %2, %4, %5, %0 op_sel:[1,1,0] op_sel_hi:[0,1,1] neg_lo:[1,0,0]\n\t"
      "v_pk_fma_f32 %3, %6, %7, %1 op_sel:[1,1,0] op_sel_hi:[0,1,1] neg_lo:[1,0,0]"
      : "=&v"(t0), "=&v"(t1), "=&v"(r0), "=&v"(r1) : "v"(a0), "v"(b0), "v"(a1), "v"(b1));
}
DEV f32x2 cmul(f32x2 a, f32x2 b) {
  f32x2 t, r;
  asm("v_pk_mul_f32 %0, %2, %3 op_sel_hi:[1,0]\n\tv_pk_fma_f32 %1, %2, %3, %0 op_sel:[1,1,0] op_sel_hi:[0,1,1] neg_lo:[1,0,0]"
      : "=&v"(t), "=v"(r) : "v"(a), "v"(b));
  return r;
}
DEV u32 cvt_bf2(float a, float b) { u32 r; asm("v_cvt_pk_bf16_f32 %0, %1, %2" : "=v"(r) : "v"(a), "v"(b)); return r; }
DEV u32 cvt_h2(float a, float b) { u32 r; asm("v_cvt_pk_f16_f32 %0, %1, %2" : "=v"(r) : "v"(a), "v"(b)); return r; }
DEV float fadd1(float a, float b) { float r; asm("v_add_f32 %0, %1, %2" : "=v"(r) : "v"(a), "v"(b)); return r; }
DEV float fsub1(float a, float b) { float r; asm("v_sub_f32 %0, %1, %2" : "=v"(r) : "v"(a), "v"(b)); return r; }
DEV float fmul1(float a, float b) { float r; asm("v_mul_f32 %0, %1, %2" : "=v"(r) : "v"(a), "v"(b)); return r; }
DEV float ffma1(float a, float b, float c) { float r; asm("v_fma_f32 %0, %1, %2, %3" : "=v"(r) : "v"(a), "v"(b), "v"(c)); return r; }
DEV float rbf1(float a) { return bflo(cvt_bf2(a, a)); }
DEV u32 pk_f16_conj(f32x2 v) { u32 r; asm("v_cvt_pk_f16_f32 %0, %1, -%2" : "=v"(r) : "v"(v.x), "v"(v.y)); return r; }
DEV f32x2 gelu2(f32x2 v) {
  const f32x2 s = v * fma2(v * v, sp2(-0.10294323970f), sp2(-2.3022081986f));
  const f32x2 d = f32x2{__builtin_amdgcn_exp2f(s.x), __builtin_amdgcn_exp2f(s.y)} + sp2(1.0f);
  return v * f32x2{__builtin_amdgcn_rcpf(d.x), __builtin_amdgcn_rcpf(d.y)};
}
DEV void side_sync4(char* smem, int w, u32 tag) {
  asm volatile("s_waitcnt lgkmcnt(0)" ::: "memory");
  lds_st<u32>(smem, S_SYNC + w * 4, tag);
#pragma unroll 1
  for (int it = 0; it < (1 << 16); ++it) {
    asm volatile("" ::: "memory");
    const u32x4 v = lds_ld<u32x4>(smem, S_SYNC);
    if (__builtin_amdgcn_readfirstlane((v.x == tag && v.y == tag && v.z == tag && v.w == tag) ? 1u : 0u)) break;
  }
  asm volatile("" ::: "memory");
}
DEV void spin_flag(u32x4 srd, u32 voff, u32 epoch, u32 val, bool need) {
#pragma unroll 1
  for (int it = 0; it < SPIN_MAX; ++it) {
    if (__all(!need || val == epoch)) break;
    __builtin_amdgcn_s_sleep(1);
    val = ld32c(srd, voff, 0);
    asm volatile("s_waitcnt vmcnt(0)" : "+v"(val)::"memory");
  }
}

constexpr int G2_LEAD = 8;
constexpr int g2_unit_ops(int j) { return (j + G2_LEAD <= 21 ? 2 : 0) + ((j >= 7 && j <= 10) ? 4 : 0); }
constexpr int g2_ops_before(int j) { int n = 2 * G2_LEAD; for (int i = 0; i < j; ++i) n += g2_unit_ops(i); return n; }
constexpr int g2_pos_h(int j) {
  if (j < G2_LEAD) return 2 * j + 2;
  const int i = j - G2_LEAD;
  return g2_ops_before(i) + 2 + ((i >= 7 && i <= 10) ? 2 : 0);
}
constexpr int g2_wait_h(int j) { return g2_ops_before(j) - g2_pos_h(j); }

}

__global__ __launch_bounds__(512, 2) void s4d_mixer_fused(Params p) {
  __shared__ __attribute__((aligned(16))) char smem[LDS_BYTES];
  u64 pc0;
  asm volatile("s_getpc_b64 %0" : "=s"(pc0));
  const int tid = threadIdx.x, lane = tid & 63, t8 = tid & 255;
  const int wid = __builtin_amdgcn_readfirstlane(tid >> 6);
  const int side = wid >> 2, w = wid & 3;
  const int b = blockIdx.x, g8 = b & 7, r = b >> 3;
  const int mt = 4 * (g8 >> 1) + (r >> 3), nt = 8 * (g8 & 1) + (r & 7);
  const int ustart = (nt * NUNIT) >> 4, unext = ((nt + 1) * NUNIT) >> 4;
  const int tg = 16 * mt + nt;
  const int cp = 32 * g8 + r;
  const int mt2 = 4 * g8 + (r >> 3), nt2 = r & 7;
  const int c15 = lane & 15, g = lane >> 4;
  const int c31 = lane & 31, hh = lane >> 5, b16 = (lane >> 4) & 1;
  const int q4 = (lane & 15) >> 2, p4 = lane & 3;
  const int kch = w >> 1, kd = 2 * cp + kch, kh = w >> 1, mh = w & 1;

  const u32x4 sX = mk_srd(p.x, SEQ * DM * 2), sWS = mk_srd(p.ws, WS_END);
  const rsrc_t rWS = mk_rsrc(p.ws, WS_END), rOUT = mk_rsrc(p.out, SEQ * DM * 2);
  const u32x4 sW1 = mk_srd(p.w1, DM * FF * 2), sW2 = mk_srd(p.w2, FF * DM * 2);
  const u32 lb = (u32)(uintptr_t)(LAS char*)smem;
  const u64 amp0 = __builtin_amdgcn_s_memrealtime();
  if (threadIdx.x == 0) __hip_atomic_store((u64*)(p.ws + 0xA80000) + blockIdx.x, amp0, __ATOMIC_RELAXED, __HIP_MEMORY_SCOPE_AGENT);
  u64 ampk = amp0;
  PSTAMP(0);

  auto load_eword = [&]() { u32 e; asm volatile("s_load_dword %0, %1, 0x0" : "=s"(e) : "s"(p.ws) : "memory"); return e; };
  u32 vx[4], vw[3];
#pragma unroll
  for (int j = 0; j < 4; ++j) vx[j] = (8 * j + (lane >> 3)) * 1024 - j * 1024 + (((lane & 7) ^ ((4 * j + (lane >> 4)) & 7)) * 16);
#pragma unroll
  for (int j = 0; j < 3; ++j) {
    const int P = (3 * w + j) * 64 + lane, k = P / 12, sl = P - 12 * k;
    vw[j] = k * (FF * 2) + sl * 16 - j * 1024;
  }
  const u32 x_so = (mt * 128 + 32 * w) * 1024, x_lo = lb + 32 * w * 128, w_so = ustart * 64, w_lo = lb + SLOT_W + 3 * w * 1024;
  auto st1_m0 = [&](auto KC, auto J) {
    if constexpr (J == 0) dma_m0(x_lo + (KC & 3) * SLOT);
    if constexpr (J == 4) dma_m0(w_lo + (KC & 3) * SLOT);
  };
  auto st1_go = [&](auto KC, auto J) {
    if constexpr (J < 4) dma_go<J * 1024>(sX, vx[J], x_so + KC * 128); else dma_go<(J - 4) * 1024>(sW1, vw[J - 4], w_so + KC * 64 * (FF * 2));
  };
  auto stage = [&](auto KC) { sfor<7>([&](auto J) { if constexpr (J == 0 || J == 4) { st1_m0(KC, J); asm volatile("s_nop 0"); } st1_go(KC, J); }); };
  u32 ixp[5];
  auto prep_ix = [&]() {
#pragma unroll
    for (int e = 0; e < 9; ++e) {
      const int l = 2047 - (8 * t8 + e);
      const u32 rb = pk_bf16(sp2((float)l)) & 0xffffu;
      const u32 ix = (l < 0 ? 641u : (l < 128 ? (u32)l : rb - 17024u)) * 2;
      if (e & 1) ixp[e >> 1] |= ix << 16; else ixp[e >> 1] = ix;
    }
#pragma unroll
    for (int e = 0; e < 5; ++e) tie(ixp[e]);
  };
  u32 epoch;
  u32 q_dsk;
  u32x2 b1v;
  f32x16 acc1[3];
  f32x4 csp[3];
  u32x4 stv;
  float inv_sk;
  if (side == 0) {
    u32x4 xr0 = ld128(sX, lane * 16, (8 * tg + 2 * w) * 1024), xr1 = ld128(sX, lane * 16, (8 * tg + 2 * w + 1) * 1024);
    u32 q_lar, q_aim, q_cre, q_cim, q_cre2, q_cim2, q_ldt;
    {
      const u32 vo = (64 * (w & 1) + lane) * 2, so = kd * NMODE * 2;
      const u32x4 sCr = mk_srd(p.cre, DM * NMODE * 2), sCi = mk_srd(p.cim, DM * NMODE * 2);
      q_lar = ld16(mk_srd(p.lar, DM * NMODE * 2), vo, so); q_aim = ld16(mk_srd(p.aim, DM * NMODE * 2), vo, so);
      q_cre = ld16(sCr, vo, so); q_cim = ld16(sCi, vo, so);
      q_cre2 = ld16(sCr, vo ^ 128, so); q_cim2 = ld16(sCi, vo ^ 128, so);
      q_ldt = ld16(mk_srd(p.ldt, DM * 2), 0, kd * 2); q_dsk = ld16(mk_srd(p.dsk, DM * 2), 0, kd * 2);
    }
    {
      const char* a;
      if (t8 < 96) a = (const char*)p.x + (512 * (g8 >> 1) + 16 * r + t8 / 6) * 1024 + (2 + t8 % 6) * 128;
      else if (t8 < 192) a = (const char*)p.w1 + (256 + 8 * r + (t8 - 96) / 12) * (FF * 2) + (g8 & 1) * 1280 + ((t8 - 96) % 12) * 120;
      else a = (const char*)pc0 + ((r * 64 + lane) % CODE_LINES) * 128;
      touch(a, lb + S_DUMMY);
    }
    u32 eword = load_eword();
    asm volatile("s_waitcnt vmcnt(9)" : "+v"(xr0), "+v"(xr1)::"memory");
    PSTAMP(1);
    ampk = __builtin_amdgcn_s_memrealtime();
    {
      const u32x4 xr[2] = {xr0, xr1};
      f32x2 x[2][4];
      float x0[2], sm[2], sq[2];
#pragma unroll
      for (int t = 0; t < 2; ++t) {
#pragma unroll
        for (int i = 0; i < 4; ++i) x[t][i] = bf2(xr[t][i]);
        x0[t] = asf(__builtin_amdgcn_readfirstlane(asu(x[t][0].x)));
        f32x2 s = {0.f, 0.f}, q = {0.f, 0.f};
#pragma unroll
        for (int i = 0; i < 4; ++i) { const f32x2 d = x[t][i] - sp2(x0[t]); s += d; q = fma2(d, d, q); }
        sm[t] = s.x + s.y; sq[t] = q.x + q.y;
      }
      wave_sum4(sm[0], sq[0], sm[1], sq[1]);
      float m1[2], r1[2], s2[2], q2[2];
#pragma unroll
      for (int t = 0; t < 2; ++t) {
        const float md = sm[t] * (1.0f / DM);
        const float var = fmaxf(__builtin_fmaf(-md, md, sq[t] * (1.0f / DM)), 0.f);
        m1[t] = rbf(x0[t] + md);
        r1[t] = __builtin_amdgcn_rcpf(rbf(__builtin_amdgcn_sqrtf(rbf(rbf(var) + 1e-5f))));
      }
#pragma unroll
      for (int t = 0; t < 2; ++t) {
        f32x2 s = {0.f, 0.f}, q = {0.f, 0.f};
        u32x4 hv;
#pragma unroll
        for (int i = 0; i < 4; ++i) {
          const f32x2 n = bf2(pk_bf16(bf2(pk_bf16(x[t][i] - sp2(m1[t]))) * sp2(r1[t])));
          s += n; q = fma2(n, n, q);
          hv[i] = pk_f16(n);
        }
        lds_st<u32x4>(smem, S_LN + (2 * w + t) * LN_ROW + lane * 16, hv);
        s2[t] = s.x + s.y; q2[t] = q.x + q.y;
      }
      wave_sum4(s2[0], q2[0], s2[1], q2[1]);
      float al[2], be[2];
#pragma unroll
      for (int t = 0; t < 2; ++t) {
        const float mean2 = s2[t] * (1.0f / DM);
        const float var2 = fmaxf(__builtin_fmaf(-mean2, mean2, q2[t] * (1.0f / DM)), 0.f);
        const float r2 = __builtin_amdgcn_rcpf(rbf(__builtin_amdgcn_sqrtf(rbf(rbf(var2) + 1e-5f))));
        al[t] = r1[t] * r2;
        be[t] = __builtin_fmaf(m1[t], r1[t], rbf(mean2)) * r2;
      }
      PSTAMP(2);
      asm volatile("s_waitcnt lgkmcnt(0)" : "+s"(eword)::"memory");
      epoch = eword * 0x9E3779B1u + 0x7F4A7C15u;
      const u32x4 sv = {asu(lane ? al[1] : al[0]), epoch, asu(lane ? be[1] : be[0]), epoch};
      st128c(rWS, sv, lane < 2 ? WS_STATS + (8 * tg + 2 * w + lane) * 16 : OOB);
    }
    PSTAMP(3);
    u32 kv[57];
    {
      asm volatile("s_waitcnt vmcnt(2)" : "+v"(q_lar), "+v"(q_aim), "+v"(q_cre), "+v"(q_cim), "+v"(q_cre2), "+v"(q_cim2), "+v"(q_ldt), "+v"(q_dsk)::"memory");
      const float dt = rbf(expf(bf1(q_ldt)));
      const float zr = -rbf(expf(bf1(q_lar))) * dt, zi = bf1(q_aim) * dt;
      f32x2 C = f32x2{bf1(q_cre), bf1(q_cim)} * sp2(2.0f * dt);
      const float bd = wave_sum((__builtin_fabsf(C.x) + __builtin_fabsf(C.y)) + (__builtin_fabsf(bf1(q_cre2)) + __builtin_fabsf(bf1(q_cim2))) * (2.0f * dt));
      int se = 267 - (int)((asu(bd) >> 23) & 0xff);
      se = se < 1 ? 1 : (se > 253 ? 253 : se);
      C *= sp2(asf((u32)se << 23));
      inv_sk = asf((u32)(254 - se) << 23);
      const float e1 = zr * 1.44269504089f;
      const float ph = zi * 0.15915494309f;
      const float pl = __builtin_fmaf(zi, 0.15915494309f, -ph) + zi * 6.4206383e-9f;
      auto cexp = [&](float pw) {
        const float m = __builtin_amdgcn_exp2f(e1 * pw);
        const float a = __builtin_amdgcn_fractf(ph * pw) + pl * pw;
        return f32x2{__builtin_amdgcn_cosf(a), __builtin_amdgcn_sinf(a)} * sp2(m);
      };
      f32x2 P[8], Y[16], E[5], CE[17];
      P[1] = cexp(1.f); Y[1] = cexp(8.f); E[0] = cexp(128.f);
      Y[0] = f32x2{1.f, 0.f}; CE[0] = C;
      cmul2(P[2], P[1], P[1], Y[2], Y[1], Y[1]); cmul2(E[1], E[0], E[0], CE[1], C, E[0]);
      cmul2(P[3], P[2], P[1], P[4], P[2], P[2]); cmul2(Y[3], Y[2], Y[1], Y[4], Y[2], Y[2]);
      cmul2(E[2], E[1], E[1], CE[2], CE[0], E[1]); cmul2(CE[3], CE[1], E[1], P[5], P[4], P[1]);
      cmul2(P[6], P[4], P[2], P[7], P[4], P[3]); cmul2(Y[5], Y[4], Y[1], Y[6], Y[4], Y[2]);
      cmul2(Y[7], Y[4], Y[3], Y[8], Y[4], Y[4]); cmul2(E[3], E[2], E[2], CE[4], CE[0], E[2]);
      cmul2(CE[5], CE[1], E[2], CE[6], CE[2], E[2]); cmul2(CE[7], CE[3], E[2], E[4], E[3], E[3]);
      cmul2(Y[9], Y[8], Y[1], Y[10], Y[8], Y[2]); cmul2(Y[11], Y[8], Y[3], Y[12], Y[8], Y[4]);
      cmul2(Y[13], Y[8], Y[5], Y[14], Y[8], Y[6]); cmul2(Y[15], Y[8], Y[7], CE[16], C, E[4]);
#pragma unroll
      for (int i = 0; i < 16; ++i) kv[i] = pk_f16_conj(Y[i]);
      kv[16] = pk_f16(CE[0]); kv[24] = pk_f16(CE[1]);
#pragma unroll
      for (int i = 1; i < 8; ++i) {
        f32x2 a, c;
        cmul2(a, CE[0], P[i], c, CE[1], P[i]);
        kv[16 + i] = pk_f16(a); kv[24 + i] = pk_f16(c);
      }
      kv[32] = pk_f16(CE[2]); kv[36] = pk_f16(CE[3]);
#pragma unroll
      for (int i = 1; i < 4; ++i) {
        f32x2 a, c;
        cmul2(a, CE[2], P[2 * i], c, CE[3], P[2 * i]);
        kv[32 + i] = pk_f16(a); kv[36 + i] = pk_f16(c);
      }
      kv[40] = pk_f16(CE[4]); kv[42] = pk_f16(CE[5]); kv[44] = pk_f16(CE[6]); kv[46] = pk_f16(CE[7]);
      {
        f32x2 a, c;
        cmul2(a, CE[4], P[4], c, CE[5], P[4]); kv[41] = pk_f16(a); kv[43] = pk_f16(c);
        cmul2(a, CE[6], P[4], c, CE[7], P[4]); kv[45] = pk_f16(a); kv[47] = pk_f16(c);
      }
      cmul2(CE[8], CE[0], E[3], CE[9], CE[1], E[3]); cmul2(CE[10], CE[2], E[3], CE[11], CE[3], E[3]);
      cmul2(CE[12], CE[4], E[3], CE[13], CE[5], E[3]); cmul2(CE[14], CE[6], E[3], CE[15], CE[7], E[3]);
#pragma unroll
      for (int i = 0; i < 9; ++i) kv[48 + i] = pk_f16(CE[8 + i]);
#pragma unroll
      for (int i = 0; i < 57; ++i) tie(kv[i]);
    }
    PSTAMP(4);
    const u32 xa0 = lb + (32 * w + c31) * 128 + ((hh ^ ((c31 >> 1) & 7)) * 16);
    const u32 wa0 = lb + SLOT_W + (8 * hh + q4) * 192 + b16 * 32 + 8 * p4;
    u32 xa[2][4], wa[2];
#pragma unroll
    for (int s = 0; s < 4; ++s) { xa[0][s] = xa0 ^ (s * 32); xa[1][s] = xa[0][s] + 2 * SLOT; }
    wa[0] = wa0; wa[1] = wa0 + 2 * SLOT;
    u32x4 xf[2];
    u32x2 wl[2][3], wh[2][3];
    auto rd = [&](auto S, auto R) {
      constexpr int s = S, rr = R, buf = s & 1, kc = s >> 2, sl = kc & 3, so = (sl & 1) * SLOT, sp = s & 3;
      if constexpr (rr == 0) xf[buf] = a_ds128<so>(xa[sl >> 1][sp]);
      else {
        constexpr int u = (rr - 1) >> 1, hf = (rr - 1) & 1, off = so + u * 64 + (sp * 16 + hf * 4) * 192;
        if constexpr (hf == 0) wl[buf][u] = a_dstr<off>(wa[sl >> 1]); else wh[buf][u] = a_dstr<off>(wa[sl >> 1]);
      }
    };
    PSTAMP(5); PSTAMP(6);
    bar();
    PSTAMP(7); PDUMP();
    CSTAMP(0);
    sfor<7>([&](auto R) { rd(ic<0>{}, R); });
    sfor<32>([&](auto S) {
      constexpr int s = S, buf = s & 1, sp = s & 3, kc = s >> 2;
      constexpr bool last = s == 31, brk = sp == 2 && kc < 7;
      sfor<3>([&](auto U) {
        constexpr int u = U;
        if constexpr (u == 0) wait_lgkm<4>();
        else if constexpr (u == 1) {
          wait_lgkm<(last ? 0 : 3)>();
          if constexpr (brk) {
            constexpr int n = kc + 1;
            CSTAMP(2 * n - 1);
            asm volatile("s_barrier" ::: "memory");
            CSTAMP(2 * n);
            if constexpr (n == 6) stv = ld128c(sWS, WS_STATS + (128 * mt + 32 * w + c31) * 16, 0);
          }
        }
        mfma32_vv<s == 0>(acc1[u], cat4(wl[buf][u], wh[buf][u]), xf[buf]);
        if constexpr (!last) {
          if constexpr (u == 0) sfor<3>([&](auto R) { rd(ic<s + 1>{}, R); });
          if constexpr (u == 1) sfor<2>([&](auto R) { rd(ic<s + 1>{}, ic<3 + R>{}); });
          if constexpr (u == 2) sfor<2>([&](auto R) { rd(ic<s + 1>{}, ic<5 + R>{}); });
        }
      });
    });
    asm volatile("s_nop 7\n\ts_nop 3" : "+v"(acc1[0]), "+v"(acc1[1]), "+v"(acc1[2]));
    CSTAMP(15); CDUMP(16, 0);
    STAMP(2);
    bar();
    {
      const u32 tl = S_TAB + kch * TAB_CH + (64 * (w & 1) + lane) * 4;
#pragma unroll
      for (int i = 0; i < 57; ++i) lds_st<u32>(smem, tl + i * TAB_ROW, kv[i]);
    }
    asm volatile("s_waitcnt vmcnt(0)" : "+v"(stv) :: "memory");
  } else {
    const u32 wc0 = lb + SLOT_W + (32 * (w >> 1) + 8 * g + q4) * 192 + 3 * (w & 1) * 32 + 8 * p4;
    u32x4 ones = {0x3f803f80u, 0x3f803f80u, 0x3f803f80u, 0x3f803f80u};
    tie(ones);
    u32x2 cl[3], ch[3];
    auto cs_rd = [&](auto KC) {
      const u32 a = wc0 + (KC & 3) * SLOT;
      sfor<3>([&](auto I) { cl[I] = a_dstr<I * 32>(a); ch[I] = a_dstr<I * 32 + 4 * 192>(a); });
      FENCE();
    };
    auto cs_sum = [&](auto KC) {
      FENCE();
      wait_lgkm<0>();
      sfor<3>([&](auto I) { mfma16_bf<KC == 0>(csp[I], cat4(cl[I], ch[I]), ones); });
    };
    stage(ic<0>{}); stage(ic<1>{}); stage(ic<2>{});
    __builtin_amdgcn_s_setprio(1);
    u32 eword = load_eword();
    prep_ix();
    asm volatile("s_waitcnt lgkmcnt(0)" : "+s"(eword)::"memory");
    epoch = eword * 0x9E3779B1u + 0x7F4A7C15u;
    PSTAMP(1); PSTAMP(2); PSTAMP(3); PSTAMP(4); PSTAMP(5);
    wait_vm<14>();
    PSTAMP(6);
    bar();
    PSTAMP(7); PDUMP();
    CSTAMP(0);
    ampk = __builtin_amdgcn_s_memrealtime();
    cs_rd(ic<0>{}); stage(ic<3>{}); cs_sum(ic<0>{});
    wait_vm<14>();
    CSTAMP(1);
    bar();
    CSTAMP(2);
    cs_rd(ic<1>{});
#pragma unroll
    for (int i = 0; i < 2; ++i) {
      const u32 a = S_LN + q4 * LN_ROW + ((2 * w + i) * 64 + 16 * g + 4 * p4) * 2;
      st128c(rWS, cat4(lds_tr(smem, a), lds_tr(smem, a + 4 * LN_ROW)), WS_XN + tg * 8192 + ((2 * w + i) * 64 + lane) * 16);
    }
    b1v = ld64(mk_srd(p.b1, FF * 2), lane < 24 ? lane * 8 : OOB, ustart * 64);
    cs_sum(ic<1>{});
    wait_vm<10>();
    CSTAMP(3);
    bar();
    CSTAMP(4);
    cs_rd(ic<2>{}); stage(ic<4>{}); cs_sum(ic<2>{});
    wait_vm<10>();
    CSTAMP(5);
    bar();
    CSTAMP(6);
    cs_rd(ic<3>{}); stage(ic<5>{}); cs_sum(ic<3>{});
    asm volatile("s_waitcnt vmcnt(7)" : "+v"(b1v) :: "memory");
    st32c(rWS, epoch, lane == 0 ? WS_LNF + (tg * 4 + w) * 4 : OOB);
    CSTAMP(7);
    bar();
    CSTAMP(8);
    lds_st<f32x4>(smem, lane < 24 ? S_B1 + lane * 16 : S_MISC, f32x4{bflo(b1v.x), bfhi(b1v.x), bflo(b1v.y), bfhi(b1v.y)});
    cs_rd(ic<4>{}); stage(ic<6>{}); cs_sum(ic<4>{});
    wait_vm<8>();
    CSTAMP(9);
    bar();
    CSTAMP(10);
    cs_rd(ic<5>{}); stage(ic<7>{}); cs_sum(ic<5>{});
    wait_vm<7>();
    CSTAMP(11);
    bar();
    CSTAMP(12);
    cs_rd(ic<6>{});
    {
#pragma unroll
      for (int i = 0; i < 2; ++i) {
        const int ln = (2 * w + i) * 8 + (lane >> 3);
        st128c(rWS, u32x4{POISON, POISON, POISON, POISON}, WS_A + ((ln >> 1) * 512 + 2 * cp + (ln & 1)) * 128 + (lane & 7) * 16);
      }
    }
#pragma unroll
    for (int i = 0; i < 2; ++i) {
      const int ix = min(i * 256 + t8, 343);
      touch((const char*)p.w2 + (43 * r + (ix >> 3)) * 1024 + (ix & 7) * 128, lb + S_DUMMY);
    }
    cs_sum(ic<6>{});
    wait_vm<4>();
    CSTAMP(13);
    bar();
    CSTAMP(14);
    cs_rd(ic<7>{}); cs_sum(ic<7>{});
    asm volatile("s_nop 7\n\ts_nop 1" : "+v"(csp[0]), "+v"(csp[1]), "+v"(csp[2]));
#pragma unroll
    for (int i = 0; i < 3; ++i) lds_st<f32x4>(smem, c15 == 0 ? S_CSP + ((w >> 1) * 96 + (3 * (w & 1) + i) * 16 + 4 * g) * 4 : S_MISC, csp[i]);
    __builtin_amdgcn_s_setprio(0);
    CSTAMP(15); CDUMP(16, 20);
    STAMP(2);
    bar();
    const int f = t8 < 96 ? t8 : 0;
    lds_st<float>(smem, t8 < 96 ? S_CS + t8 * 4 : S_MISC, lds_ld<float>(smem, S_CSP + f * 4) + lds_ld<float>(smem, S_CSP + 384 + f * 4));
  }
  if (side == 0) {
#pragma unroll 1
    for (int it = 0; it < SPIN_MAX; ++it) {
      if (__all(stv.y == epoch && stv.w == epoch)) break;
      __builtin_amdgcn_s_sleep(1);
      stv = ld128c(sWS, WS_STATS + (128 * mt + 32 * w + c31) * 16, 0);
      asm volatile("s_waitcnt vmcnt(0)" : "+v"(stv)::"memory");
    }
  }
  bar();
  CSTAMP(0);
  STAMP(3);
  const u32 vw2 = (lane >> 3) * 1024 + ((((lane & 7) >> 1) ^ ((lane >> 3) & 2)) * 32) + (lane & 1) * 16;
  const u32 vw2p[2] = {vw2, vw2 + 7168};
  const u32 vw2z[2] = {kh ? OOB : vw2p[0], kh ? OOB : vw2p[1]};
  const u32 w2_lo = lb + kh * W2_HALF + 2 * mh * 1024, w2_so = (kh * 22 * 32 + 16 * mh) * 1024 + nt2 * 128;
  auto st2_piece = [&](auto N) {
    constexpr int j = N >> 1, i = N & 1;
    FENCE();
    if constexpr (i == 0) { dma_m0(w2_lo + (j % W2_NS) * 4096); asm volatile("s_nop 0"); }
    dma_go<i * 1024>(sW2, j == 21 ? vw2z[i] : vw2p[i], w2_so + j * 32 * 1024);
    FENCE();
  };

  if (side == 0) {
    __builtin_amdgcn_s_setprio(2);
    const f32x2 al = sp2(asf(stv.x)), nb = sp2(-asf(stv.z));
    const u32 hso = WS_H + ((mt * 4 + w) * NUNIT + ustart) * 2048 + lane * 16;
    f32x2 v[24], t[24];
    sfor<12>([&](auto Q) {
      const f32x4 bi = lds_ld<f32x4>(smem, S_B1 + hh * 16 + Q * 32), cs = lds_ld<f32x4>(smem, S_CS + hh * 16 + Q * 32);
      t[2 * Q] = fma2(nb, f32x2{cs.x, cs.y}, f32x2{bi.x, bi.y}); t[2 * Q + 1] = fma2(nb, f32x2{cs.z, cs.w}, f32x2{bi.z, bi.w});
    });
    FENCE();
#pragma unroll
    for (int q = 0; q < 12; ++q) {
      const f32x16& a = acc1[q >> 2];
      const int o = 4 * (q & 3);
      v[2 * q] = fma2(al, f32x2{a[o], a[o + 1]}, t[2 * q]); v[2 * q + 1] = fma2(al, f32x2{a[o + 2], a[o + 3]}, t[2 * q + 1]);
    }
    FENCE();
#pragma unroll
    for (int k = 0; k < 24; ++k) t[k] = v[k] * v[k];
    FENCE();
#pragma unroll
    for (int k = 0; k < 24; ++k) t[k] = fma2(t[k], sp2(-0.10294323970f), sp2(-2.3022081986f));
    FENCE();
#pragma unroll
    for (int k = 0; k < 24; ++k) t[k] = v[k] * t[k];
    FENCE();
#pragma unroll
    for (int k = 0; k < 24; ++k) t[k] = f32x2{__builtin_amdgcn_exp2f(t[k].x), __builtin_amdgcn_exp2f(t[k].y)};
    FENCE();
    CSTAMP(1);
    bar();
    CSTAMP(2);
#pragma unroll
    for (int k = 0; k < 24; ++k) t[k] += sp2(1.0f);
    FENCE();
#pragma unroll
    for (int k = 0; k < 24; ++k) t[k] = f32x2{__builtin_amdgcn_rcpf(t[k].x), __builtin_amdgcn_rcpf(t[k].y)};
    FENCE();
#pragma unroll
    for (int k = 0; k < 24; ++k) v[k] *= t[k];
    FENCE();
#pragma unroll
    for (int i = 0; i < 6; ++i)
      st128c(rWS, u32x4{pk_bf16(v[4 * i]), pk_bf16(v[4 * i + 1]), pk_bf16(v[4 * i + 2]), pk_bf16(v[4 * i + 3])},
             ustart + (i >> 1) < unext ? hso + i * 1024 : OOB);
    __builtin_amdgcn_s_setprio(0);
    CSTAMP(3);
    STAMP(4);
    wait_vm<0>();
    st32c(rWS, epoch, lane == 0 ? WS_HF + ((mt * 4 + w) * 16 + nt) * 4 : OOB);
  } else {
    wait_vm<2>();
    st32c(rWS, epoch, lane == 0 ? WS_PF + (cp * 4 + w) * 4 : OOB);
    const u32 lnfo = WS_LNF + (t8 * 4 + (g8 >> 1)) * 4;
    u32 lnf = ld32c(sWS, lnfo, 0);
    const u32 kc = S_KC + kch * KC_CH;
    const u32 rd = S_TAB + kch * TAB_CH + c15 * TAB_ROW + g * 16;
    u32x4 yf[8];
#pragma unroll
    for (int s = 0; s < 8; ++s) yf[s] = lds_ld<u32x4>(smem, rd + s * 64);
    auto tile = [&](int t) {
      const u32 ra = S_TAB + kch * TAB_CH + (16 + min(16 * t + c15, 40)) * TAB_ROW + g * 16;
      u32x4 xf[8];
#pragma unroll
      for (int s = 0; s < 8; ++s) xf[s] = lds_ld<u32x4>(smem, ra + s * 64);
      f32x4 a0 = {0, 0, 0, 0}, a1 = {0, 0, 0, 0};
#pragma unroll
      for (int s = 0; s < 4; ++s) { a0 = mfma_h(xf[2 * s], yf[2 * s], a0); a1 = mfma_h(xf[2 * s + 1], yf[2 * s + 1], a1); }
      return a0 + a1;
    };
    const f32x4 va = tile(mh), vb = tile(2);
    sfor<8>([&](auto N) { st2_piece(N); });
    asm volatile("s_waitcnt vmcnt(8)" : "+v"(lnf)::"memory");
    spin_flag(sWS, lnfo, epoch, lnf, true);
    u32x4 ug0 = ld128c(sWS, WS_XN + t8 * 8192 + cp * 32, 0), ug1 = ld128c(sWS, WS_XN + t8 * 8192 + cp * 32 + 16, 0);
    CSTAMP(1);
    bar();
    CSTAMP(2);
    {
      const u32 h01 = pk_f16(f32x2{va[0], va[1]}), h23 = pk_f16(f32x2{va[2], va[3]});
      if (mh == 0) {
        lds_st<u32x2>(smem, kc + (128 * (g >> 1) + 8 * c15 + 4 * (g & 1)) * 2, u32x2{h01, h23});
        const u32 k01 = pk_f16(f32x2{vb[0], vb[1]}), k23 = pk_f16(f32x2{vb[2], vb[3]});
        const u32 t2 = g < 2 ? 512 + 64 * g + c15 : 644, st2 = g < 2 ? 16 : 0;
        lds_st<u16>(smem, kc + ((g == 2 && c15 == 0) ? 640 : t2) * 2, (u16)k01);
        lds_st<u16>(smem, kc + (t2 + st2) * 2, (u16)(k01 >> 16));
        lds_st<u16>(smem, kc + (t2 + 2 * st2) * 2, (u16)k23);
        lds_st<u16>(smem, kc + (t2 + 3 * st2) * 2, (u16)(k23 >> 16));
      } else {
        const u32 t1a = g < 2 ? 256 + 64 * g + 4 * c15 : 384 + 64 * (g - 2) + 2 * c15;
        lds_st<u32>(smem, kc + t1a * 2, h01);
        lds_st<u32>(smem, kc + (t1a + (g < 2 ? 2 : 32)) * 2, h23);
        if (lane == 0) lds_st<u16>(smem, kc + 641 * 2, (u16)0);
      }
    }
    side_sync4(smem, w, epoch + 1);
#pragma unroll
    for (int ch = 0; ch < 2; ++ch) {
      u32 e[9];
#pragma unroll
      for (int i = 0; i < 9; ++i) e[i] = lds_ld<u16>(smem, S_KC + ch * KC_CH + ((i & 1) ? ixp[i >> 1] >> 16 : ixp[i >> 1] & 0xffffu));
      const u32x4 a0 = {e[0] | (e[1] << 16), e[2] | (e[3] << 16), e[4] | (e[5] << 16), e[6] | (e[7] << 16)};
      const u32x4 a1 = {e[1] | (e[2] << 16), e[3] | (e[4] << 16), e[5] | (e[6] << 16), e[7] | (e[8] << 16)};
      lds_st<u32x4>(smem, S_G + ch * G_CH + t8 * 16, a0);
      lds_st<u32x4>(smem, S_G + ch * G_CH + G_PAR + t8 * 16, a1);
    }
    const u32x4 z4 = {0, 0, 0, 0};
    if (t8 < 8) lds_st<u32x4>(smem, S_G + (t8 >> 2) * G_CH + ((t8 >> 1) & 1) * G_PAR + 4096 + (t8 & 1) * 16, z4);
    if (t8 < 60) lds_st<u32x4>(smem, S_U + (t8 / 30) * U_CH + (t8 % 30) * 16, z4);
    asm volatile("s_waitcnt vmcnt(0)" : "+v"(ug0), "+v"(ug1)::"memory");
    lds_st<u32x4>(smem, S_U + 480 + t8 * 16, ug0);
    lds_st<u32x4>(smem, S_U + U_CH + 480 + t8 * 16, ug1);
    CSTAMP(3);
    STAMP(4);
  }

  const int ch0 = 64 * nt2 + 32 * kh + 4 * hh;
  const u32 xto = (64 * mt2 + (t8 >> 3)) * 1024 + nt2 * 128 + (t8 & 7) * 16;
  f32x16 acc[2];
  if (side == 0) {
    bar();
    CSTAMP(4); CDUMP(5, 38);
    STAMP(5);
    const int par = w & 1;
    const u32 ub = lb + S_U + kch * U_CH + 64 - 64 * par + 32 * c15 + 16 * g;
    const u32 gb0 = lb + S_G + kch * G_CH + ((c15 & 1) ? 0 : G_PAR) + 4 * (1015 - (c15 >> 1) - 16 * par + 4 * g - 32 * 31);
    const u32 gb[4] = {gb0, gb0 + 1024, gb0 + 2048, gb0 + 3072};
    u32x4 bq[32];
    u32x2 al[32], ah[32];
    f32x4 y[8];
    auto rdB = [&](auto M, auto T) { constexpr int q = 4 * T - M + 3; bq[q] = a_ds128<128 * q>(ub); };
    auto rdA = [&](auto M, auto H) {
      constexpr int k = (31 - M) >> 3, o = 32 * ((31 - M) & 7);
      if constexpr (H == 0) al[M] = a_ds2x32<o, o + 1>(gb[k]); else ah[M] = a_ds2x32<o + 2, o + 3>(gb[k]);
    };
    sfor<2>([&](auto M) { sfor<8>([&](auto T) { rdB(M, T); }); rdA(M, ic<0>{}); rdA(M, ic<1>{}); });
    sfor<32>([&](auto M) {
      constexpr int m = M, t0 = m >> 2, n = 8 - t0;
      wait_lgkm<(m <= 2 ? 10 : (m == 3 ? 4 : (m == 4 ? 6 : (2 * (31 - m) < 8 ? 2 * (31 - m) : 8))))>();
      sfor<n>([&](auto I) {
        constexpr int i = I, t = t0 + i;
        mfma16_h<m == 0>(y[t], cat4(al[m], ah[m]), bq[4 * t - m + 3]);
        if constexpr (m < 2) {
          rdB(ic<m + 2>{}, I);
          if constexpr (i >= 6) rdA(ic<m + 2>{}, ic<i - 6>{});
        } else if constexpr (m <= 4) {
          if constexpr (i < 4) rdA(ic<2 * m + (i >> 1)>{}, ic<i & 1>{});
        } else if constexpr (m + 5 < 32) {
          if constexpr (i < 2) rdA(ic<m + 5>{}, I);
        }
      });
    });
    asm volatile("s_nop 7\n\ts_nop 1" : "+v"(y[0]), "+v"(y[1]), "+v"(y[2]), "+v"(y[3]), "+v"(y[4]), "+v"(y[5]), "+v"(y[6]), "+v"(y[7]));
    STAMP(6);
    CSTAMP(0);
    auto send = [&](auto PAR) {
      FENCE();
#pragma unroll
      for (int i = 0; i < 4; ++i) lds_st<f32x4>(smem, S_EXC + w * 4096 + i * 1024 + lane * 16, y[4 * (1 - PAR) + i]);
      FENCE();
    };
    if (par) send(ic<1>{}); else send(ic<0>{});
    CSTAMP(1);
    bar();
    CSTAMP(2);
    const float dsk = bf1(q_dsk);
    u32x2 ao[4];
    auto finish = [&](auto PAR) {
      FENCE();
#pragma unroll
      for (int i = 0; i < 4; ++i) {
        const f32x4 o = lds_ld<f32x4>(smem, S_EXC + (w ^ 1) * 4096 + i * 1024 + lane * 16);
        const u32x2 uu = lds_ld<u32x2>(smem, S_U + kch * U_CH + (240 + 256 * (4 * PAR + i) + 16 * c15 + 4 * g) * 2);
        const f16x2 u01 = ash2(uu.x), u23 = ash2(uu.y);
        const float uf[4] = {(float)u01.x, (float)u01.y, (float)u23.x, (float)u23.y};
        float v[4];
#pragma unroll
        for (int e = 0; e < 4; ++e) v[e] = ffma1(uf[e], dsk, fmul1(fadd1(y[4 * PAR + i][e], o[e]), inv_sk));
        ao[i] = u32x2{cvt_bf2(v[0], v[1]), cvt_bf2(v[2], v[3])};
      }
      FENCE();
    };
    if (par) finish(ic<1>{}); else finish(ic<0>{});
    CSTAMP(3);
#pragma unroll
    for (int i = 0; i < 4; ++i) {
      u32x2 o = ao[i];
      o.x = o.x == POISON ? 0xffc0ffc0u : o.x;
      o.y = o.y == POISON ? 0xffc0ffc0u : o.y;
      st64c(rWS, o, WS_A + ((4 * (4 * par + i) + (c15 >> 2)) * 512 + kd) * 128 + ((c15 & 3) * 16 + 4 * g) * 2);
    }
    STAMP(7);
    u32x4 xt0 = ld128(sX, xto, 0), xt1 = ld128(sX, xto + 32 * 1024, 0);
    u32 b2w = ld16(mk_srd(p.b2, DM * 2), t8 < 64 ? (64 * nt2 + t8) * 2 : OOB, 0);
    const u32 pfo = WS_PF + ((32 * nt2 + (lane & 31)) * 4 + (mt2 >> 3)) * 4;
    u32 pfl = ld32c(sWS, pfo, 0);
    CSTAMP(4);
    bar();
    CSTAMP(5);
    const u32 ato = WS_A + (mt2 * 512 + nt2 * 64) * 128 + t8 * 16;
    asm volatile("s_waitcnt vmcnt(0)" : "+v"(xt0), "+v"(xt1), "+v"(b2w), "+v"(pfl)::"memory");
    spin_flag(sWS, pfo, epoch, pfl, true);
    const int row0 = t8 >> 3, sl = t8 & 7;
    {
      const u32 o = S_XT + row0 * 128 + ((sl ^ (row0 & 7)) * 16);
      lds_st<u32x4>(smem, o, xt0);
      lds_st<u32x4>(smem, o + 32 * 128, xt1);
      lds_st<float>(smem, t8 < 64 ? S_B2 + t8 * 4 : S_MISC, bf1(b2w));
    }
    CSTAMP(6);
    bar();
    CSTAMP(7);
    u32x4 at0 = ld128c(sWS, ato, 0), at1 = ld128c(sWS, ato + 4096, 0);
    asm volatile("s_waitcnt vmcnt(0)" : "+v"(at0), "+v"(at1)::"memory");
#pragma unroll 1
    for (int it = 0; it < SPIN_MAX; ++it) {
      const u32 m0 = max(max(at0.x, at0.y), max(at0.z, at0.w)), m1 = max(max(at1.x, at1.y), max(at1.z, at1.w));
      if (!__any(max(m0, m1) == POISON)) break;
      at0 = ld128c(sWS, ato, 0); at1 = ld128c(sWS, ato + 4096, 0);
      asm volatile("s_waitcnt vmcnt(0)" : "+v"(at0), "+v"(at1)::"memory");
    }
    CSTAMP(8);
    {
      const u32 o = S_AT + row0 * 128 + (((sl >> 1) ^ (row0 & 2)) * 32) + (sl & 1) * 16;
      lds_st<u32x4>(smem, o, at0);
      lds_st<u32x4>(smem, o + 32 * 128, at1);
    }
    CSTAMP(9);
    bar();
    CSTAMP(10); CDUMP2(11, 0);
  } else {
    u32x4 hf[22][2];
    const u32 hb = WS_H + ((mt2 * 2 + mh) * NUNIT + kh * 22) * 2048;
    const u32 hvo = lane * 16, hvz = kh ? OOB : hvo;
    auto ldh = [&](auto J, auto S) { hf[J][S] = ld128c(sWS, J == 21 ? hvz : hvo, hb + (J * 2 + S) * 1024); };
    bar();
    CSTAMP(4); CDUMP(5, 48);
    STAMP(5);
    st32c(rWS, epoch, (b == 0 && t8 == 0) ? WS_E : OOB);
    sfor<20>([&](auto N) { st2_piece(ic<8 + N>{}); });
    {
      const u32 hfo = WS_HF + (((mt2 >> 1) * 4 + 2 * (mt2 & 1) + mh) * 16 + c15) * 4;
      u32 hfl = ld32c(sWS, hfo, 0);
      asm volatile("s_waitcnt vmcnt(0)" : "+v"(hfl)::"memory");
      side_sync4(smem, w, epoch + 2);
      spin_flag(sWS, hfo, epoch, hfl, kh ? c15 >= 8 : c15 <= 8);
    }
    STAMP(6);
    sfor<G2_LEAD>([&](auto J) { ldh(J, ic<0>{}); ldh(J, ic<1>{}); });
    STAMP(7);
    u32 wa[2];
#pragma unroll
    for (int n = 0; n < 2; ++n) wa[n] = lb + kh * W2_HALF + (4 * hh + q4) * 128 + (((2 * n + b16) ^ (q4 & 2)) * 32) + 8 * p4;
    u32x2 wl[2][4], wh[2][4];
    auto rd = [&](auto J, auto I, auto H) {
      constexpr int j = J, i = I, off = (j % W2_NS) * 4096 + ((i >> 1) * 16 + H * 8) * 128;
      if constexpr (H == 0) wl[j & 1][i] = a_dstr<off>(wa[i & 1]); else wh[j & 1][i] = a_dstr<off>(wa[i & 1]);
    };
    CSTAMP(0);
    sfor<4>([&](auto I) { rd(ic<0>{}, I, ic<0>{}); rd(ic<0>{}, I, ic<1>{}); });
    sfor<22>([&](auto J) {
      constexpr int j = J;
      constexpr bool last = j == 21, brk = j == 7 || j == 13 || j == 17;
      if constexpr (j == 13) wait_vm<g2_ops_before(13) - g2_ops_before(9)>();
      if constexpr (j == 17) wait_vm<g2_ops_before(17) - g2_ops_before(11)>();
      if constexpr (brk) CSTAMP(j == 7 ? 1 : (j == 13 ? 3 : 5));
      if constexpr (brk) bar();
      if constexpr (brk) CSTAMP(j == 7 ? 2 : (j == 13 ? 4 : 6));
      wait_vm<g2_wait_h(j)>();
      tie(hf[j][0]); tie(hf[j][1]);
      sfor<4>([&](auto I) {
        constexpr int i = I;
        constexpr bool stg = j >= 7 && j <= 10;
        constexpr int ju = stg ? 14 + 2 * (j - 7) + (i >> 1) : 14, pi = i & 1;
        if constexpr (!brk) wait_lgkm<(last ? 6 - 2 * i : 6)>();
        if constexpr (stg && pi == 0) dma_m0(w2_lo + (ju % W2_NS) * 4096);
        mfma32_vv<j == 0 && i < 2>(acc[i & 1], cat4(wl[j & 1][i], wh[j & 1][i]), hf[j][i >> 1]);
        if constexpr ((i & 1) == 0 && j + G2_LEAD <= 21) ldh(ic<j + G2_LEAD>{}, ic<(i >> 1)>{});
        if constexpr (stg) dma_go<pi * 1024>(sW2, ju == 21 ? vw2z[pi] : vw2p[pi], w2_so + ju * 32 * 1024);
        if constexpr (!last) { rd(ic<j + 1>{}, I, ic<0>{}); rd(ic<j + 1>{}, I, ic<1>{}); }
      });
    });
    asm volatile("s_nop 7\n\ts_nop 3" : "+v"(acc[0]), "+v"(acc[1]));
    CSTAMP(7);
    STAMP(8);
    auto send2 = [&](auto KH) {
      FENCE();
#pragma unroll
      for (int q = 0; q < 4; ++q) {
        const f32x16& as = acc[1 - KH];
        lds_st<f32x4>(smem, S_EX + w * 4096 + q * 1024 + lane * 16, f32x4{as[4 * q], as[4 * q + 1], as[4 * q + 2], as[4 * q + 3]});
      }
      FENCE();
    };
    if (kh) send2(ic<1>{}); else send2(ic<0>{});
    CSTAMP(8);
    bar();
    CSTAMP(9);
    const int tl = 32 * mh + c31;
    auto fin2 = [&](auto KH) {
      FENCE();
      const f32x16& ak = acc[KH];
      f32x4 pv[4], bb[4];
      u32x2 av[4], xv[4];
      u32 po[4];
#pragma unroll
      for (int q = 0; q < 4; ++q) {
        const int row = 32 * KH + 8 * q + 4 * hh + q4;
        po[q] = tl * 128 + (((4 * KH + q) ^ (tl & 7)) * 16) + hh * 8;
        pv[q] = lds_ld<f32x4>(smem, S_EX + (w ^ 2) * 4096 + q * 1024 + lane * 16);
        bb[q] = lds_ld<f32x4>(smem, S_B2 + (32 * KH + 8 * q + 4 * hh) * 4);
        av[q] = lds_tr(smem, S_AT + row * 128 + (((2 * mh + b16) ^ (row & 2)) * 32) + 8 * p4);
        xv[q] = lds_ld<u32x2>(smem, S_XT + po[q]);
      }
      FENCE();
      f32x2 o[8];
#pragma unroll
      for (int q = 0; q < 4; ++q) {
        o[2 * q] = f32x2{ak[4 * q], ak[4 * q + 1]} + f32x2{bb[q].x, bb[q].y};
        o[2 * q + 1] = f32x2{ak[4 * q + 2], ak[4 * q + 3]} + f32x2{bb[q].z, bb[q].w};
      }
      FENCE();
#pragma unroll
      for (int q = 0; q < 4; ++q) { o[2 * q] += f32x2{pv[q].x, pv[q].y}; o[2 * q + 1] += f32x2{pv[q].z, pv[q].w}; }
      FENCE();
#pragma unroll
      for (int q = 0; q < 4; ++q) { o[2 * q] += bf2(xv[q].x) + bf2(av[q].x); o[2 * q + 1] += bf2(xv[q].y) + bf2(av[q].y); }
      FENCE();
#pragma unroll
      for (int q = 0; q < 4; ++q) lds_st<u32x2>(smem, S_OT + po[q], u32x2{pk_bf16(o[2 * q]), pk_bf16(o[2 * q + 1])});
      FENCE();
    };
    if (kh) fin2(ic<1>{}); else fin2(ic<0>{});
    CSTAMP(10); CDUMP2(11, 16);
  }
  bar();
  {
    const int row = tid >> 3, sl = tid & 7;
    const u32x4 o = lds_ld<u32x4>(smem, S_OT + row * 128 + ((sl ^ (row & 7)) * 16));
    __builtin_amdgcn_raw_buffer_store_b128(o, rOUT, (64 * mt2 + row) * 1024 + nt2 * 128 + sl * 16, 0, 0);
  }
  { const u64 te = __builtin_amdgcn_s_memrealtime(); u64 until = te + 20 * (ampk - amp0);
    while (__builtin_amdgcn_s_memrealtime() < until) __builtin_amdgcn_s_sleep(2); }
}

extern "C" void kernel_launch(void* const* d_in, const int* in_sizes, int n_in, void* d_out, int out_size, void* d_ws,
                              size_t ws_size, hipStream_t stream) {
  Params p;
  p.x = (const u16*)d_in[0]; p.lar = (const u16*)d_in[1]; p.aim = (const u16*)d_in[2]; p.cre = (const u16*)d_in[3];
  p.cim = (const u16*)d_in[4]; p.ldt = (const u16*)d_in[5]; p.dsk = (const u16*)d_in[6];
  p.w1 = (const u16*)d_in[8]; p.b1 = (const u16*)d_in[9]; p.w2 = (const u16*)d_in[10]; p.b2 = (const u16*)d_in[11];
  p.out = (u16*)d_out; p.ws = (char*)d_ws;
  s4d_mixer_fused<<<256, 512, 0, stream>>>(p);
}
```

```cpp
#include <hip/hip_runtime.h>
#include <stdint.h>
#include <utility>

namespace {
typedef unsigned short u16;
typedef unsigned u32;
typedef unsigned long long u64;
typedef float f32x2 __attribute__((ext_vector_type(2)));
typedef float f32x4 __attribute__((ext_vector_type(4)));
typedef float f32x16 __attribute__((ext_vector_type(16)));
typedef u32 u32x2 __attribute__((ext_vector_type(2)));
typedef u32 u32x4 __attribute__((ext_vector_type(4)));
typedef short s16x4 __attribute__((ext_vector_type(4)));
typedef __bf16 bf16x2 __attribute__((ext_vector_type(2)));
typedef _Float16 f16x2 __attribute__((ext_vector_type(2)));
typedef _Float16 f16x8 __attribute__((ext_vector_type(8)));
#define LAS __attribute__((address_space(3)))
#define DEV __device__ __forceinline__
typedef __amdgpu_buffer_rsrc_t rsrc_t;

constexpr int SEQ = 2048, DM = 512, NMODE = 128, FF = 1376, NUNIT = 43;
constexpr u32 WS_E = 0, WS_LNF = 0x1000  , WS_HF = 0x2000  , WS_PF = 0x5000  , WS_STATS = 0x8000, WS_XN = 0x10000, WS_A = 0x210000, WS_H = 0x410000,
              WS_END = WS_H + 64 * NUNIT * 2048, WS_STAMP = 0xA00000;
constexpr u32 SLOT = 28672, SLOT_W = 16384;
constexpr u32 W2_NS = 14, W2_HALF = W2_NS * 4096;
constexpr u32 LDS_BYTES_ = 163840;
constexpr u32 S0 = 114688;
constexpr u32 S_DUMMY = LDS_BYTES_ - 256;
constexpr u32 S_SYNC = LDS_BYTES_ - 272;
constexpr u32 S_LN = S0, LN_ROW = 1088;
constexpr u32 TAB_ROW = 544, TAB_CH = 57 * TAB_ROW;
constexpr u32 S_KC = LDS_BYTES_ - 272 - 2592, KC_CH = 1296;
constexpr u32 S_TAB = S_KC - 2 * TAB_CH;
constexpr u32 S_CSP = S_KC;
constexpr u32 S_B1 = S_CSP + 768;
constexpr u32 S_MISC = S_B1 + 384;
constexpr u32 S_CS = S_MISC + 64;
constexpr u32 S_G = S0, G_PAR = 4160, G_CH = 8320;
constexpr u32 S_U = S0 + 16640, U_CH = 4608;
constexpr u32 S_EX = S0, S_EXC = S0 + 25856;
constexpr u32 S_AT = S0 + 16384, S_XT = S0 + 24576, S_OT = S0 + 32768, S_B2 = S0 + 40960;
constexpr u32 LDS_BYTES = 163840;
constexpr u32 OOB = 0x80000000u;
constexpr u32 POISON = 0xffffffffu;
constexpr int CODE_LINES = 200;
constexpr int SPIN_MAX = 1 << 13;

#define STAMP(k) do {} while (0)
#define CSTAMP(i) do {} while (0)
#define PSTAMP(i) do {} while (0)
#define PDUMP() do {} while (0)
#define CDUMP(n, base) do {} while (0)
#define CDUMP2(n, base) do {} while (0)

struct Params {
  const u16 *x, *lar, *aim, *cre, *cim, *ldt, *dsk, *w1, *b1, *w2, *b2;
  u16* out;
  char* ws;
};

DEV float asf(u32 v) { return __builtin_bit_cast(float, v); }
DEV u32 asu(float v) { return __builtin_bit_cast(u32, v); }
DEV f16x2 ash2(u32 v) { return __builtin_bit_cast(f16x2, v); }
DEV float bflo(u32 v) { return asf(v << 16); }
DEV float bfhi(u32 v) { return asf(v & 0xffff0000u); }
DEV f32x2 bf2(u32 v) { return f32x2{bflo(v), bfhi(v)}; }
DEV u32 pk_bf16(f32x2 v) { return __builtin_bit_cast(u32, __builtin_convertvector(v, bf16x2)); }
DEV u32 pk_f16(f32x2 v) { return __builtin_bit_cast(u32, __builtin_convertvector(v, f16x2)); }
DEV float rbf(float a) { return bflo(pk_bf16(f32x2{a, a})); }
DEV float bf1(u32 v) { return asf(v << 16); }
DEV f32x2 sp2(float a) { return f32x2{a, a}; }
DEV f32x2 fma2(f32x2 a, f32x2 b, f32x2 c) { return __builtin_elementwise_fma(a, b, c); }

template <int N> DEV void wait_vm() { asm volatile("s_waitcnt vmcnt(%0)" ::"n"(N) : "memory"); }
template <int N> DEV void wait_lgkm() { asm volatile("s_waitcnt lgkmcnt(%0)" ::"n"(N)); }
DEV void bar() { asm volatile("s_waitcnt lgkmcnt(0)\n\ts_barrier" ::: "memory"); }
DEV u32x4 mk_srd(const void* base, u32 bytes) {
  const u64 b = (u64)base;
  u32x4 s;
  s.x = __builtin_amdgcn_readfirstlane((u32)b);
  s.y = __builtin_amdgcn_readfirstlane((u32)(b >> 32)) & 0xffffu;
  s.z = bytes;
  s.w = 0x00020000u;
  return s;
}
DEV rsrc_t mk_rsrc(const void* base, u32 bytes) { return __builtin_amdgcn_make_buffer_rsrc((void*)base, 0, bytes, 0x00020000); }

#define BLOAD(INS, TY, NAME, FLAGS, CON)                                                                     \
  DEV TY NAME(u32x4 srd, u32 voff, u32 soff) {                                                               \
    TY r;                                                                                                    \
    asm volatile(INS " %0, %1, %2, %3 offen" FLAGS : CON(r) : "v"(voff), "s"(srd), "s"(soff) : "memory");     \
    return r;                                                                                                \
  }
BLOAD("buffer_load_dwordx4", u32x4, ld128, "", "=v")
BLOAD("buffer_load_dwordx4", u32x4, ld128c, " sc1", "=v")
BLOAD("buffer_load_dwordx4", u32x4, ld128c_a, " sc1", "=a")
BLOAD("buffer_load_dwordx2", u32x2, ld64, "", "=v")
BLOAD("buffer_load_dword", u32, ld32c, " sc1", "=v")
BLOAD("buffer_load_ushort", u32, ld16, "", "=v")
DEV void touch(const void* a, u32 lds_dummy) { asm volatile("s_mov_b32 m0, %1\n\ts_nop 0\n\tglobal_load_lds_dword %0, off" ::"v"(a), "s"(lds_dummy) : "memory"); }
template <class T> DEV void tie(T& v) { asm volatile("" : "+v"(v)); }

DEV void dma_m0(u32 lds_addr) { asm volatile("s_mov_b32 m0, %0" ::"s"(lds_addr) : "memory"); }
template <int IOFF> DEV void dma_go(u32x4 srd, u32 voff, u32 soff) {
  asm volatile("buffer_load_dwordx4 %0, %1, %2 offen offset:%3 lds" ::"v"(voff), "s"(srd), "s"(soff), "n"(IOFF) : "memory");
}
DEV void st128c(rsrc_t r, u32x4 v, u32 voff) { __builtin_amdgcn_raw_buffer_store_b128(v, r, voff, 0, 16); }
DEV void st64c(rsrc_t r, u32x2 v, u32 voff) { __builtin_amdgcn_raw_buffer_store_b64(v, r, voff, 0, 16); }
DEV void st32c(rsrc_t r, u32 v, u32 voff) { __builtin_amdgcn_raw_buffer_store_b32(v, r, voff, 0, 16); }
DEV void st64(rsrc_t r, u32x2 v, u32 voff) { __builtin_amdgcn_raw_buffer_store_b64(v, r, voff, 0, 0); }

template <class T> DEV T lds_ld(const char* smem, u32 off) { return *(const LAS T*)(smem + off); }
template <class T> DEV void lds_st(char* smem, u32 off, T v) { *(LAS T*)(smem + off) = v; }
DEV u32x2 lds_tr(const char* smem, u32 off) { return __builtin_bit_cast(u32x2, __builtin_amdgcn_ds_read_tr16_b64_v4i16((LAS s16x4*)(smem + off))); }

template <int I> using ic = std::integral_constant<int, I>;
template <class F, int... Is> DEV void sfor_impl(F&& f, std::integer_sequence<int, Is...>) { (f(ic<Is>{}), ...); }
template <int N, class F> DEV void sfor(F&& f) { sfor_impl(f, std::make_integer_sequence<int, N>{}); }
template <int OFF> DEV u32x4 a_ds128(u32 addr) { u32x4 r; asm volatile("ds_read_b128 %0, %1 offset:%2" : "=v"(r) : "v"(addr), "n"(OFF)); return r; }
template <int OFF> DEV u32x2 a_dstr(u32 addr) { u32x2 r; asm volatile("ds_read_b64_tr_b16 %0, %1 offset:%2" : "=v"(r) : "v"(addr), "n"(OFF)); return r; }
template <int O0, int O1> DEV u32x2 a_ds2x32(u32 addr) { u32x2 r; asm volatile("ds_read2_b32 %0, %1 offset0:%2 offset1:%3" : "=v"(r) : "v"(addr), "n"(O0), "n"(O1)); return r; }
DEV u32x4 cat4(u32x2 a, u32x2 b) { return __builtin_shufflevector(a, b, 0, 1, 2, 3); }
template <bool INIT> DEV void mfma32_vv(f32x16& c, u32x4 a, u32x4 b) {
  if constexpr (INIT) asm volatile("v_mfma_f32_32x32x16_bf16 %0, %1, %2, 0" : "=&v"(c) : "v"(a), "v"(b));
  else asm volatile("v_mfma_f32_32x32x16_bf16 %0, %1, %2, %0" : "+v"(c) : "v"(a), "v"(b));
}
template <bool INIT> DEV void mfma32_va(f32x16& c, u32x4 a, u32x4 b) {
  if constexpr (INIT) asm volatile("v_mfma_f32_32x32x16_bf16 %0, %1, %2, 0" : "=&v"(c) : "v"(a), "a"(b));
  else asm volatile("v_mfma_f32_32x32x16_bf16 %0, %1, %2, %0" : "+v"(c) : "v"(a), "a"(b));
}
template <bool INIT> DEV void mfma16_h(f32x4& c, u32x4 a, u32x4 b) {
  if constexpr (INIT) asm volatile("v_mfma_f32_16x16x32_f16 %0, %1, %2, 0" : "=&v"(c) : "v"(a), "v"(b));
  else asm volatile("v_mfma_f32_16x16x32_f16 %0, %1, %2, %0" : "+v"(c) : "v"(a), "v"(b));
}
DEV f32x4 mfma_h(u32x4 a, u32x4 b, f32x4 c) {
  return __builtin_amdgcn_mfma_f32_16x16x32_f16(__builtin_bit_cast(f16x8, a), __builtin_bit_cast(f16x8, b), c, 0, 0, 0);
}
template <bool INIT> DEV void mfma16_bf(f32x4& c, u32x4 a, u32x4 b) {
  if constexpr (INIT) asm volatile("v_mfma_f32_16x16x32_bf16 %0, %1, %2, 0" : "=&v"(c) : "v"(a), "v"(b));
  else asm volatile("v_mfma_f32_16x16x32_bf16 %0, %1, %2, %0" : "+v"(c) : "v"(a), "v"(b));
}
#define FENCE() __builtin_amdgcn_sched_barrier(0)

DEV void wave_sum4(float& a, float& b, float& c, float& d) {
#define S4(CTRL) "v_add_f32_dpp %0, %0, %0 " CTRL "\n\tv_add_f32_dpp %1, %1, %1 " CTRL "\n\tv_add_f32_dpp %2, %2, %2 " CTRL "\n\tv_add_f32_dpp %3, %3, %3 " CTRL "\n\t"
  asm volatile("s_nop 1\n\t" S4("row_shr:1 row_mask:0xf bank_mask:0xf bound_ctrl:1") S4("row_shr:2 row_mask:0xf bank_mask:0xf bound_ctrl:1")
               S4("row_shr:4 row_mask:0xf bank_mask:0xf bound_ctrl:1") S4("row_shr:8 row_mask:0xf bank_mask:0xf bound_ctrl:1")
               S4("row_bcast:15 row_mask:0xa bank_mask:0xf") S4("row_bcast:31 row_mask:0xc bank_mask:0xf") "s_nop 0"
               : "+v"(a), "+v"(b), "+v"(c), "+v"(d));
#undef S4
  a = asf(__builtin_amdgcn_readlane(asu(a), 63)); b = asf(__builtin_amdgcn_readlane(asu(b), 63));
  c = asf(__builtin_amdgcn_readlane(asu(c), 63)); d = asf(__builtin_amdgcn_readlane(asu(d), 63));
}
DEV void wave_sum2(float& a, float& b) {
#define S2(CTRL) "v_add_f32_dpp %0, %0, %0 " CTRL "\n\tv_add_f32_dpp %1, %1, %1 " CTRL "\n\ts_nop 0\n\t"
  asm volatile("s_nop 1\n\t" S2("row_shr:1 row_mask:0xf bank_mask:0xf bound_ctrl:1") S2("row_shr:2 row_mask:0xf bank_mask:0xf bound_ctrl:1")
               S2("row_shr:4 row_mask:0xf bank_mask:0xf bound_ctrl:1") S2("row_shr:8 row_mask:0xf bank_mask:0xf bound_ctrl:1")
               S2("row_bcast:15 row_mask:0xa bank_mask:0xf") S2("row_bcast:31 row_mask:0xc bank_mask:0xf")
               : "+v"(a), "+v"(b));
#undef S2
  a = asf(__builtin_amdgcn_readlane(asu(a), 63)); b = asf(__builtin_amdgcn_readlane(asu(b), 63));
}
template <int CTRL, int RM> DEV float dpp_add(float v) { return v + asf(__builtin_amdgcn_update_dpp(0, asu(v), CTRL, RM, 0xf, true)); }
DEV float wave_sum(float v) {
  v = dpp_add<0x111, 0xf>(v); v = dpp_add<0x112, 0xf>(v); v = dpp_add<0x114, 0xf>(v); v = dpp_add<0x118, 0xf>(v);
  v = dpp_add<0x142, 0xa>(v); v = dpp_add<0x143, 0xc>(v);
  return asf(__builtin_amdgcn_readlane(asu(v), 63));
}
DEV void cmul2(f32x2& r0, f32x2 a0, f32x2 b0, f32x2& r1, f32x2 a1, f32x2 b1) {
  f32x2 t0, t1;
  asm("v_pk_mul_f32 %0, %4, %5 op_sel_hi:[1,0]\n\tv_pk_mul_f32 %1, %6, %7 op_sel_hi:[1,0]\n\t"
      "v_pk_fma_f32 %2, %4, %5, %0 op_sel:[1,1,0] op_sel_hi:[0,1,1] neg_lo:[1,0,0]\n\t"
      "v_pk_fma_f32 %3, %6, %7, %1 op_sel:[1,1,0] op_sel_hi:[0,1,1] neg_lo:[1,0,0]"
      : "=&v"(t0), "=&v"(t1), "=&v"(r0), "=&v"(r1) : "v"(a0), "v"(b0), "v"(a1), "v"(b1));
}
DEV f32x2 cmul(f32x2 a, f32x2 b) {
  f32x2 t, r;
  asm("v_pk_mul_f32 %0, %2, %3 op_sel_hi:[1,0]\n\tv_pk_fma_f32 %1, %2, %3, %0 op_sel:[1,1,0] op_sel_hi:[0,1,1] neg_lo:[1,0,0]"
      : "=&v"(t), "=v"(r) : "v"(a), "v"(b));
  return r;
}
DEV u32 cvt_bf2(float a, float b) { u32 r; asm("v_cvt_pk_bf16_f32 %0, %1, %2" : "=v"(r) : "v"(a), "v"(b)); return r; }
DEV u32 cvt_h2(float a, float b) { u32 r; asm("v_cvt_pk_f16_f32 %0, %1, %2" : "=v"(r) : "v"(a), "v"(b)); return r; }
DEV float fadd1(float a, float b) { float r; asm("v_add_f32 %0, %1, %2" : "=v"(r) : "v"(a), "v"(b)); return r; }
DEV float fsub1(float a, float b) { float r; asm("v_sub_f32 %0, %1, %2" : "=v"(r) : "v"(a), "v"(b)); return r; }
DEV float fmul1(float a, float b) { float r; asm("v_mul_f32 %0, %1, %2" : "=v"(r) : "v"(a), "v"(b)); return r; }
DEV float ffma1(float a, float b, float c) { float r; asm("v_fma_f32 %0, %1, %2, %3" : "=v"(r) : "v"(a), "v"(b), "v"(c)); return r; }
DEV float rbf1(float a) { return bflo(cvt_bf2(a, a)); }
DEV u32 pk_f16_conj(f32x2 v) { u32 r; asm("v_cvt_pk_f16_f32 %0, %1, -%2" : "=v"(r) : "v"(v.x), "v"(v.y)); return r; }
DEV f32x2 gelu2(f32x2 v) {
  const f32x2 s = v * fma2(v * v, sp2(-0.10294323970f), sp2(-2.3022081986f));
  const f32x2 d = f32x2{__builtin_amdgcn_exp2f(s.x), __builtin_amdgcn_exp2f(s.y)} + sp2(1.0f);
  return v * f32x2{__builtin_amdgcn_rcpf(d.x), __builtin_amdgcn_rcpf(d.y)};
}
DEV void side_sync4(char* smem, int w, u32 tag) {
  asm volatile("s_waitcnt lgkmcnt(0)" ::: "memory");
  lds_st<u32>(smem, S_SYNC + w * 4, tag);
#pragma unroll 1
  for (int it = 0; it < (1 << 16); ++it) {
    asm volatile("" ::: "memory");
    const u32x4 v = lds_ld<u32x4>(smem, S_SYNC);
    if (__builtin_amdgcn_readfirstlane((v.x == tag && v.y == tag && v.z == tag && v.w == tag) ? 1u : 0u)) break;
  }
  asm volatile("" ::: "memory");
}
DEV void spin_flag(u32x4 srd, u32 voff, u32 epoch, u32 val, bool need) {
#pragma unroll 1
  for (int it = 0; it < SPIN_MAX; ++it) {
    if (__all(!need || val == epoch)) break;
    __builtin_amdgcn_s_sleep(1);
    val = ld32c(srd, voff, 0);
    asm volatile("s_waitcnt vmcnt(0)" : "+v"(val)::"memory");
  }
}

constexpr int G2_LEAD = 8;
constexpr int g2_unit_ops(int j) { return (j + G2_LEAD <= 21 ? 2 : 0) + ((j >= 7 && j <= 10) ? 4 : 0); }
constexpr int g2_ops_before(int j) { int n = 2 * G2_LEAD; for (int i = 0; i < j; ++i) n += g2_unit_ops(i); return n; }
constexpr int g2_pos_h(int j) {
  if (j < G2_LEAD) return 2 * j + 2;
  const int i = j - G2_LEAD;
  return g2_ops_before(i) + 2 + ((i >= 7 && i <= 10) ? 2 : 0);
}
constexpr int g2_wait_h(int j) { return g2_ops_before(j) - g2_pos_h(j); }

}

__global__ __launch_bounds__(512, 2) void s4d_mixer_fused(Params p) {
  __shared__ __attribute__((aligned(16))) char smem[LDS_BYTES];
  u64 pc0;
  asm volatile("s_getpc_b64 %0" : "=s"(pc0));
  const int tid = threadIdx.x, lane = tid & 63, t8 = tid & 255;
  const int wid = __builtin_amdgcn_readfirstlane(tid >> 6);
  const int side = wid >> 2, w = wid & 3;
  const int b = blockIdx.x, g8 = b & 7, r = b >> 3;
  const int mt = 4 * (g8 >> 1) + (r >> 3), nt = 8 * (g8 & 1) + (r & 7);
  const int ustart = (nt * NUNIT) >> 4, unext = ((nt + 1) * NUNIT) >> 4;
  const int tg = 16 * mt + nt;
  const int cp = 32 * g8 + r;
  const int mt2 = 4 * g8 + (r >> 3), nt2 = r & 7;
  const int c15 = lane & 15, g = lane >> 4;
  const int c31 = lane & 31, hh = lane >> 5, b16 = (lane >> 4) & 1;
  const int q4 = (lane & 15) >> 2, p4 = lane & 3;
  const int kch = w >> 1, kd = 2 * cp + kch, kh = w >> 1, mh = w & 1;

  const u32x4 sX = mk_srd(p.x, SEQ * DM * 2), sWS = mk_srd(p.ws, WS_END);
  const rsrc_t rWS = mk_rsrc(p.ws, WS_END), rOUT = mk_rsrc(p.out, SEQ * DM * 2);
  const u32x4 sW1 = mk_srd(p.w1, DM * FF * 2), sW2 = mk_srd(p.w2, FF * DM * 2);
  const u32 lb = (u32)(uintptr_t)(LAS char*)smem;
  STAMP(0);
  PSTAMP(0);

  auto load_eword = [&]() { u32 e; asm volatile("s_load_dword %0, %1, 0x0" : "=s"(e) : "s"(p.ws) : "memory"); return e; };
  u32 vx[4], vw[3];
#pragma unroll
  for (int j = 0; j < 4; ++j) vx[j] = (8 * j + (lane >> 3)) * 1024 - j * 1024 + (((lane & 7) ^ ((4 * j + (lane >> 4)) & 7)) * 16);
#pragma unroll
  for (int j = 0; j < 3; ++j) {
    const int P = (3 * w + j) * 64 + lane, k = P / 12, sl = P - 12 * k;
    vw[j] = k * (FF * 2) + sl * 16 - j * 1024;
  }
  const u32 x_so = (mt * 128 + 32 * w) * 1024, x_lo = lb + 32 * w * 128, w_so = ustart * 64, w_lo = lb + SLOT_W + 3 * w * 1024;
  auto st1_m0 = [&](auto KC, auto J) {
    if constexpr (J == 0) dma_m0(x_lo + (KC & 3) * SLOT);
    if constexpr (J == 4) dma_m0(w_lo + (KC & 3) * SLOT);
  };
  auto st1_go = [&](auto KC, auto J) {
    if constexpr (J < 4) dma_go<J * 1024>(sX, vx[J], x_so + KC * 128); else dma_go<(J - 4) * 1024>(sW1, vw[J - 4], w_so + KC * 64 * (FF * 2));
  };
  auto stage = [&](auto KC) { sfor<7>([&](auto J) { if constexpr (J == 0 || J == 4) { st1_m0(KC, J); asm volatile("s_nop 0"); } st1_go(KC, J); }); };
  u32 ixp[5];
  auto prep_ix = [&]() {
#pragma unroll
    for (int e = 0; e < 9; ++e) {
      const int l = 2047 - (8 * t8 + e);
      const u32 rb = pk_bf16(sp2((float)l)) & 0xffffu;
      const u32 ix = (l < 0 ? 641u : (l < 128 ? (u32)l : rb - 17024u)) * 2;
      if (e & 1) ixp[e >> 1] |= ix << 16; else ixp[e >> 1] = ix;
    }
#pragma unroll
    for (int e = 0; e < 5; ++e) tie(ixp[e]);
  };
  u32 epoch;
  u32 q_dsk;
  u32x2 b1v;
  f32x16 acc1[3];
  f32x4 csp[3];
  u32x4 stv;
  float inv_sk;
  if (side == 0) {
    u32x4 xr0 = ld128(sX, lane * 16, (8 * tg + 2 * w) * 1024), xr1 = ld128(sX, lane * 16, (8 * tg + 2 * w + 1) * 1024);
    u32 q_lar, q_aim, q_cre, q_cim, q_cre2, q_cim2, q_ldt;
    {
      const u32 vo = (64 * (w & 1) + lane) * 2, so = kd * NMODE * 2;
      const u32x4 sCr = mk_srd(p.cre, DM * NMODE * 2), sCi = mk_srd(p.cim, DM * NMODE * 2);
      q_lar = ld16(mk_srd(p.lar, DM * NMODE * 2), vo, so); q_aim = ld16(mk_srd(p.aim, DM * NMODE * 2), vo, so);
      q_cre = ld16(sCr, vo, so); q_cim = ld16(sCi, vo, so);
      q_cre2 = ld16(sCr, vo ^ 128, so); q_cim2 = ld16(sCi, vo ^ 128, so);
      q_ldt = ld16(mk_srd(p.ldt, DM * 2), 0, kd * 2); q_dsk = ld16(mk_srd(p.dsk, DM * 2), 0, kd * 2);
    }
    {
      const char* a;
      if (t8 < 96) a = (const char*)p.x + (512 * (g8 >> 1) + 16 * r + t8 / 6) * 1024 + (2 + t8 % 6) * 128;
      else if (t8 < 192) a = (const char*)p.w1 + (256 + 8 * r + (t8 - 96) / 12) * (FF * 2) + (g8 & 1) * 1280 + ((t8 - 96) % 12) * 120;
      else a = (const char*)pc0 + ((r * 64 + lane) % CODE_LINES) * 128;
      touch(a, lb + S_DUMMY);
    }
    u32 eword = load_eword();
    asm volatile("s_waitcnt vmcnt(9)" : "+v"(xr0), "+v"(xr1)::"memory");
    PSTAMP(1);
    STAMP(1);
    {
      const u32x4 xr[2] = {xr0, xr1};
      f32x2 x[2][4];
      float x0[2], sm[2], sq[2];
#pragma unroll
      for (int t = 0; t < 2; ++t) {
#pragma unroll
        for (int i = 0; i < 4; ++i) x[t][i] = bf2(xr[t][i]);
        x0[t] = asf(__builtin_amdgcn_readfirstlane(asu(x[t][0].x)));
        f32x2 s = {0.f, 0.f}, q = {0.f, 0.f};
#pragma unroll
        for (int i = 0; i < 4; ++i) { const f32x2 d = x[t][i] - sp2(x0[t]); s += d; q = fma2(d, d, q); }
        sm[t] = s.x + s.y; sq[t] = q.x + q.y;
      }
      wave_sum4(sm[0], sq[0], sm[1], sq[1]);
      float m1[2], r1[2], s2[2], q2[2];
#pragma unroll
      for (int t = 0; t < 2; ++t) {
        const float md = sm[t] * (1.0f / DM);
        const float var = fmaxf(__builtin_fmaf(-md, md, sq[t] * (1.0f / DM)), 0.f);
        m1[t] = rbf(x0[t] + md);
        r1[t] = __builtin_amdgcn_rcpf(rbf(__builtin_amdgcn_sqrtf(rbf(rbf(var) + 1e-5f))));
      }
#pragma unroll
      for (int t = 0; t < 2; ++t) {
        f32x2 s = {0.f, 0.f}, q = {0.f, 0.f};
        u32x4 hv;
#pragma unroll
        for (int i = 0; i < 4; ++i) {
          const f32x2 n = bf2(pk_bf16(bf2(pk_bf16(x[t][i] - sp2(m1[t]))) * sp2(r1[t])));
          s += n; q = fma2(n, n, q);
          hv[i] = pk_f16(n);
        }
        lds_st<u32x4>(smem, S_LN + (2 * w + t) * LN_ROW + lane * 16, hv);
        s2[t] = s.x + s.y; q2[t] = q.x + q.y;
      }
      wave_sum4(s2[0], q2[0], s2[1], q2[1]);
      float al[2], be[2];
#pragma unroll
      for (int t = 0; t < 2; ++t) {
        const float mean2 = s2[t] * (1.0f / DM);
        const float var2 = fmaxf(__builtin_fmaf(-mean2, mean2, q2[t] * (1.0f / DM)), 0.f);
        const float r2 = __builtin_amdgcn_rcpf(rbf(__builtin_amdgcn_sqrtf(rbf(rbf(var2) + 1e-5f))));
        al[t] = r1[t] * r2;
        be[t] = __builtin_fmaf(m1[t], r1[t], rbf(mean2)) * r2;
      }
      PSTAMP(2);
      asm volatile("s_waitcnt lgkmcnt(0)" : "+s"(eword)::"memory");
      epoch = eword * 0x9E3779B1u + 0x7F4A7C15u;
      const u32x4 sv = {asu(lane ? al[1] : al[0]), epoch, asu(lane ? be[1] : be[0]), epoch};
      st128c(rWS, sv, lane < 2 ? WS_STATS + (8 * tg + 2 * w + lane) * 16 : OOB);
    }
    PSTAMP(3);
    u32 kv[57];
    {
      asm volatile("s_waitcnt vmcnt(2)" : "+v"(q_lar), "+v"(q_aim), "+v"(q_cre), "+v"(q_cim), "+v"(q_cre2), "+v"(q_cim2), "+v"(q_ldt), "+v"(q_dsk)::"memory");
      const float dt = rbf(expf(bf1(q_ldt)));
      const float zr = -rbf(expf(bf1(q_lar))) * dt, zi = bf1(q_aim) * dt;
      f32x2 C = f32x2{bf1(q_cre), bf1(q_cim)} * sp2(2.0f * dt);
      const float bd = wave_sum((__builtin_fabsf(C.x) + __builtin_fabsf(C.y)) + (__builtin_fabsf(bf1(q_cre2)) + __builtin_fabsf(bf1(q_cim2))) * (2.0f * dt));
      int se = 267 - (int)((asu(bd) >> 23) & 0xff);
      se = se < 1 ? 1 : (se > 253 ? 253 : se);
      C *= sp2(asf((u32)se << 23));
      inv_sk = asf((u32)(254 - se) << 23);
      const float e1 = zr * 1.44269504089f;
      const float ph = zi * 0.15915494309f;
      const float pl = __builtin_fmaf(zi, 0.15915494309f, -ph) + zi * 6.4206383e-9f;
      auto cexp = [&](float pw) {
        const float m = __builtin_amdgcn_exp2f(e1 * pw);
        const float a = __builtin_amdgcn_fractf(ph * pw) + pl * pw;
        return f32x2{__builtin_amdgcn_cosf(a), __builtin_amdgcn_sinf(a)} * sp2(m);
      };
      f32x2 P[8], Y[16], E[5], CE[17];
      P[1] = cexp(1.f); Y[1] = cexp(8.f); E[0] = cexp(128.f);
      Y[0] = f32x2{1.f, 0.f}; CE[0] = C;
      cmul2(P[2], P[1], P[1], Y[2], Y[1], Y[1]); cmul2(E[1], E[0], E[0], CE[1], C, E[0]);
      cmul2(P[3], P[2], P[1], P[4], P[2], P[2]); cmul2(Y[3], Y[2], Y[1], Y[4], Y[2], Y[2]);
      cmul2(E[2], E[1], E[1], CE[2], CE[0], E[1]); cmul2(CE[3], CE[1], E[1], P[5], P[4], P[1]);
      cmul2(P[6], P[4], P[2], P[7], P[4], P[3]); cmul2(Y[5], Y[4], Y[1], Y[6], Y[4], Y[2]);
      cmul2(Y[7], Y[4], Y[3], Y[8], Y[4], Y[4]); cmul2(E[3], E[2], E[2], CE[4], CE[0], E[2]);
      cmul2(CE[5], CE[1], E[2], CE[6], CE[2], E[2]); cmul2(CE[7], CE[3], E[2], E[4], E[3], E[3]);
      cmul2(Y[9], Y[8], Y[1], Y[10], Y[8], Y[2]); cmul2(Y[11], Y[8], Y[3], Y[12], Y[8], Y[4]);
      cmul2(Y[13], Y[8], Y[5], Y[14], Y[8], Y[6]); cmul2(Y[15], Y[8], Y[7], CE[16], C, E[4]);
#pragma unroll
      for (int i = 0; i < 16; ++i) kv[i] = pk_f16_conj(Y[i]);
      kv[16] = pk_f16(CE[0]); kv[24] = pk_f16(CE[1]);
#pragma unroll
      for (int i = 1; i < 8; ++i) {
        f32x2 a, c;
        cmul2(a, CE[0], P[i], c, CE[1], P[i]);
        kv[16 + i] = pk_f16(a); kv[24 + i] = pk_f16(c);
      }
      kv[32] = pk_f16(CE[2]); kv[36] = pk_f16(CE[3]);
#pragma unroll
      for (int i = 1; i < 4; ++i) {
        f32x2 a, c;
        cmul2(a, CE[2], P[2 * i], c, CE[3], P[2 * i]);
        kv[32 + i] = pk_f16(a); kv[36 + i] = pk_f16(c);
      }
      kv[40] = pk_f16(CE[4]); kv[42] = pk_f16(CE[5]); kv[44] = pk_f16(CE[6]); kv[46] = pk_f16(CE[7]);
      {
        f32x2 a, c;
        cmul2(a, CE[4], P[4], c, CE[5], P[4]); kv[41] = pk_f16(a); kv[43] = pk_f16(c);
        cmul2(a, CE[6], P[4], c, CE[7], P[4]); kv[45] = pk_f16(a); kv[47] = pk_f16(c);
      }
      cmul2(CE[8], CE[0], E[3], CE[9], CE[1], E[3]); cmul2(CE[10], CE[2], E[3], CE[11], CE[3], E[3]);
      cmul2(CE[12], CE[4], E[3], CE[13], CE[5], E[3]); cmul2(CE[14], CE[6], E[3], CE[15], CE[7], E[3]);
#pragma unroll
      for (int i = 0; i < 9; ++i) kv[48 + i] = pk_f16(CE[8 + i]);
#pragma unroll
      for (int i = 0; i < 57; ++i) tie(kv[i]);
    }
    PSTAMP(4);
    const u32 xa0 = lb + (32 * w + c31) * 128 + ((hh ^ ((c31 >> 1) & 7)) * 16);
    const u32 wa0 = lb + SLOT_W + (8 * hh + q4) * 192 + b16 * 32 + 8 * p4;
    u32 xa[2][4], wa[2];
#pragma unroll
    for (int s = 0; s < 4; ++s) { xa[0][s] = xa0 ^ (s * 32); xa[1][s] = xa[0][s] + 2 * SLOT; }
    wa[0] = wa0; wa[1] = wa0 + 2 * SLOT;
    u32x4 xf[2];
    u32x2 wl[2][3], wh[2][3];
    auto rd = [&](auto S, auto R) {
      constexpr int s = S, rr = R, buf = s & 1, kc = s >> 2, sl = kc & 3, so = (sl & 1) * SLOT, sp = s & 3;
      if constexpr (rr == 0) xf[buf] = a_ds128<so>(xa[sl >> 1][sp]);
      else {
        constexpr int u = (rr - 1) >> 1, hf = (rr - 1) & 1, off = so + u * 64 + (sp * 16 + hf * 4) * 192;
        if constexpr (hf == 0) wl[buf][u] = a_dstr<off>(wa[sl >> 1]); else wh[buf][u] = a_dstr<off>(wa[sl >> 1]);
      }
    };
    PSTAMP(5); PSTAMP(6);
    bar();
    PSTAMP(7); PDUMP();
    CSTAMP(0);
    sfor<7>([&](auto R) { rd(ic<0>{}, R); });
    sfor<32>([&](auto S) {
      constexpr int s = S, buf = s & 1, sp = s & 3, kc = s >> 2;
      constexpr bool last = s == 31, brk = sp == 2 && kc < 7;
      sfor<3>([&](auto U) {
        constexpr int u = U;
        if constexpr (u == 0) wait_lgkm<4>();
        else if constexpr (u == 1) {
          wait_lgkm<(last ? 0 : 3)>();
          if constexpr (brk) {
            constexpr int n = kc + 1;
            CSTAMP(2 * n - 1);
            asm volatile("s_barrier" ::: "memory");
            CSTAMP(2 * n);
            if constexpr (n == 6) stv = ld128c(sWS, WS_STATS + (128 * mt + 32 * w + c31) * 16, 0);
          }
        }
        mfma32_vv<s == 0>(acc1[u], cat4(wl[buf][u], wh[buf][u]), xf[buf]);
        if constexpr (!last) {
          if constexpr (u == 0) sfor<3>([&](auto R) { rd(ic<s + 1>{}, R); });
          if constexpr (u == 1) sfor<2>([&](auto R) { rd(ic<s + 1>{}, ic<3 + R>{}); });
          if constexpr (u == 2) sfor<2>([&](auto R) { rd(ic<s + 1>{}, ic<5 + R>{}); });
        }
      });
    });
    asm volatile("s_nop 7\n\ts_nop 3" : "+v"(acc1[0]), "+v"(acc1[1]), "+v"(acc1[2]));
    CSTAMP(15); CDUMP(16, 0);
    STAMP(2);
    bar();
    {
      const u32 tl = S_TAB + kch * TAB_CH + (64 * (w & 1) + lane) * 4;
#pragma unroll
      for (int i = 0; i < 57; ++i) lds_st<u32>(smem, tl + i * TAB_ROW, kv[i]);
    }
    asm volatile("s_waitcnt vmcnt(0)" : "+v"(stv) :: "memory");
  } else {
    const u32 wc0 = lb + SLOT_W + (32 * (w >> 1) + 8 * g + q4) * 192 + 3 * (w & 1) * 32 + 8 * p4;
    u32x4 ones = {0x3f803f80u, 0x3f803f80u, 0x3f803f80u, 0x3f803f80u};
    tie(ones);
    u32x2 cl[3], ch[3];
    auto cs_rd = [&](auto KC) {
      const u32 a = wc0 + (KC & 3) * SLOT;
      sfor<3>([&](auto I) { cl[I] = a_dstr<I * 32>(a); ch[I] = a_dstr<I * 32 + 4 * 192>(a); });
      FENCE();
    };
    auto cs_sum = [&](auto KC) {
      FENCE();
      wait_lgkm<0>();
      sfor<3>([&](auto I) { mfma16_bf<KC == 0>(csp[I], cat4(cl[I], ch[I]), ones); });
    };
    stage(ic<0>{}); stage(ic<1>{}); stage(ic<2>{});
    __builtin_amdgcn_s_setprio(1);
    u32 eword = load_eword();
    prep_ix();
    asm volatile("s_waitcnt lgkmcnt(0)" : "+s"(eword)::"memory");
    epoch = eword * 0x9E3779B1u + 0x7F4A7C15u;
    PSTAMP(1); PSTAMP(2); PSTAMP(3); PSTAMP(4); PSTAMP(5);
    wait_vm<14>();
    PSTAMP(6);
    bar();
    PSTAMP(7); PDUMP();
    CSTAMP(0);
    STAMP(1);
    cs_rd(ic<0>{}); stage(ic<3>{}); cs_sum(ic<0>{});
    wait_vm<14>();
    CSTAMP(1);
    bar();
    CSTAMP(2);
    cs_rd(ic<1>{});
#pragma unroll
    for (int i = 0; i < 2; ++i) {
      const u32 a = S_LN + q4 * LN_ROW + ((2 * w + i) * 64 + 16 * g + 4 * p4) * 2;
      st128c(rWS, cat4(lds_tr(smem, a), lds_tr(smem, a + 4 * LN_ROW)), WS_XN + tg * 8192 + ((2 * w + i) * 64 + lane) * 16);
    }
    b1v = ld64(mk_srd(p.b1, FF * 2), lane < 24 ? lane * 8 : OOB, ustart * 64);
    cs_sum(ic<1>{});
    wait_vm<10>();
    CSTAMP(3);
    bar();
    CSTAMP(4);
    cs_rd(ic<2>{}); stage(ic<4>{}); cs_sum(ic<2>{});
    wait_vm<10>();
    CSTAMP(5);
    bar();
    CSTAMP(6);
    cs_rd(ic<3>{}); stage(ic<5>{}); cs_sum(ic<3>{});
    asm volatile("s_waitcnt vmcnt(7)" : "+v"(b1v) :: "memory");
    st32c(rWS, epoch, lane == 0 ? WS_LNF + (tg * 4 + w) * 4 : OOB);
    CSTAMP(7);
    bar();
    CSTAMP(8);
    lds_st<f32x4>(smem, lane < 24 ? S_B1 + lane * 16 : S_MISC, f32x4{bflo(b1v.x), bfhi(b1v.x), bflo(b1v.y), bfhi(b1v.y)});
    cs_rd(ic<4>{}); stage(ic<6>{}); cs_sum(ic<4>{});
    wait_vm<8>();
    CSTAMP(9);
    bar();
    CSTAMP(10);
    cs_rd(ic<5>{}); stage(ic<7>{}); cs_sum(ic<5>{});
    wait_vm<7>();
    CSTAMP(11);
    bar();
    CSTAMP(12);
    cs_rd(ic<6>{});
    {
#pragma unroll
      for (int i = 0; i < 2; ++i) {
        const int ln = (2 * w + i) * 8 + (lane >> 3);
        st128c(rWS, u32x4{POISON, POISON, POISON, POISON}, WS_A + ((ln >> 1) * 512 + 2 * cp + (ln & 1)) * 128 + (lane & 7) * 16);
      }
    }
#pragma unroll
    for (int i = 0; i < 2; ++i) {
      const int ix = min(i * 256 + t8, 343);
      touch((const char*)p.w2 + (43 * r + (ix >> 3)) * 1024 + (ix & 7) * 128, lb + S_DUMMY);
    }
    cs_sum(ic<6>{});
    wait_vm<4>();
    CSTAMP(13);
    bar();
    CSTAMP(14);
    cs_rd(ic<7>{}); cs_sum(ic<7>{});
    asm volatile("s_nop 7\n\ts_nop 1" : "+v"(csp[0]), "+v"(csp[1]), "+v"(csp[2]));
#pragma unroll
    for (int i = 0; i < 3; ++i) lds_st<f32x4>(smem, c15 == 0 ? S_CSP + ((w >> 1) * 96 + (3 * (w & 1) + i) * 16 + 4 * g) * 4 : S_MISC, csp[i]);
    __builtin_amdgcn_s_setprio(0);
    CSTAMP(15); CDUMP(16, 20);
    STAMP(2);
    bar();
    const int f = t8 < 96 ? t8 : 0;
    lds_st<float>(smem, t8 < 96 ? S_CS + t8 * 4 : S_MISC, lds_ld<float>(smem, S_CSP + f * 4) + lds_ld<float>(smem, S_CSP + 384 + f * 4));
  }
  if (side == 0) {
#pragma unroll 1
    for (int it = 0; it < SPIN_MAX; ++it) {
      if (__all(stv.y == epoch && stv.w == epoch)) break;
      __builtin_amdgcn_s_sleep(1);
      stv = ld128c(sWS, WS_STATS + (128 * mt + 32 * w + c31) * 16, 0);
      asm volatile("s_waitcnt vmcnt(0)" : "+v"(stv)::"memory");
    }
  }
  bar();
  CSTAMP(0);
  STAMP(3);
  const u32 vw2 = (lane >> 3) * 1024 + ((((lane & 7) >> 1) ^ ((lane >> 3) & 2)) * 32) + (lane & 1) * 16;
  const u32 vw2p[2] = {vw2, vw2 + 7168};
  const u32 vw2z[2] = {kh ? OOB : vw2p[0], kh ? OOB : vw2p[1]};
  const u32 w2_lo = lb + kh * W2_HALF + 2 * mh * 1024, w2_so = (kh * 22 * 32 + 16 * mh) * 1024 + nt2 * 128;
  auto st2_piece = [&](auto N) {
    constexpr int j = N >> 1, i = N & 1;
    FENCE();
    if constexpr (i == 0) { dma_m0(w2_lo + (j % W2_NS) * 4096); asm volatile("s_nop 0"); }
    dma_go<i * 1024>(sW2, j == 21 ? vw2z[i] : vw2p[i], w2_so + j * 32 * 1024);
    FENCE();
  };

  if (side == 0) {
    __builtin_amdgcn_s_setprio(2);
    const f32x2 al = sp2(asf(stv.x)), nb = sp2(-asf(stv.z));
    const u32 hso = WS_H + ((mt * 4 + w) * NUNIT + ustart) * 2048 + lane * 16;
    f32x2 v[24], t[24];
    sfor<12>([&](auto Q) {
      const f32x4 bi = lds_ld<f32x4>(smem, S_B1 + hh * 16 + Q * 32), cs = lds_ld<f32x4>(smem, S_CS + hh * 16 + Q * 32);
      t[2 * Q] = fma2(nb, f32x2{cs.x, cs.y}, f32x2{bi.x, bi.y}); t[2 * Q + 1] = fma2(nb, f32x2{cs.z, cs.w}, f32x2{bi.z, bi.w});
    });
    FENCE();
#pragma unroll
    for (int q = 0; q < 12; ++q) {
      const f32x16& a = acc1[q >> 2];
      const int o = 4 * (q & 3);
      v[2 * q] = fma2(al, f32x2{a[o], a[o + 1]}, t[2 * q]); v[2 * q + 1] = fma2(al, f32x2{a[o + 2], a[o + 3]}, t[2 * q + 1]);
    }
    FENCE();
#pragma unroll
    for (int k = 0; k < 24; ++k) t[k] = v[k] * v[k];
    FENCE();
#pragma unroll
    for (int k = 0; k < 24; ++k) t[k] = fma2(t[k], sp2(-0.10294323970f), sp2(-2.3022081986f));
    FENCE();
#pragma unroll
    for (int k = 0; k < 24; ++k) t[k] = v[k] * t[k];
    FENCE();
#pragma unroll
    for (int k = 0; k < 24; ++k) t[k] = f32x2{__builtin_amdgcn_exp2f(t[k].x), __builtin_amdgcn_exp2f(t[k].y)};
    FENCE();
    CSTAMP(1);
    bar();
    CSTAMP(2);
#pragma unroll
    for (int k = 0; k < 24; ++k) t[k] += sp2(1.0f);
    FENCE();
#pragma unroll
    for (int k = 0; k < 24; ++k) t[k] = f32x2{__builtin_amdgcn_rcpf(t[k].x), __builtin_amdgcn_rcpf(t[k].y)};
    FENCE();
#pragma unroll
    for (int k = 0; k < 24; ++k) v[k] *= t[k];
    FENCE();
#pragma unroll
    for (int i = 0; i < 6; ++i)
      st128c(rWS, u32x4{pk_bf16(v[4 * i]), pk_bf16(v[4 * i + 1]), pk_bf16(v[4 * i + 2]), pk_bf16(v[4 * i + 3])},
             ustart + (i >> 1) < unext ? hso + i * 1024 : OOB);
    __builtin_amdgcn_s_setprio(0);
    CSTAMP(3);
    STAMP(4);
    wait_vm<0>();
    st32c(rWS, epoch, lane == 0 ? WS_HF + ((mt * 4 + w) * 16 + nt) * 4 : OOB);
  } else {
    wait_vm<2>();
    st32c(rWS, epoch, lane == 0 ? WS_PF + (cp * 4 + w) * 4 : OOB);
    const u32 lnfo = WS_LNF + (t8 * 4 + (g8 >> 1)) * 4;
    u32 lnf = ld32c(sWS, lnfo, 0);
    const u32 kc = S_KC + kch * KC_CH;
    const u32 rd = S_TAB + kch * TAB_CH + c15 * TAB_ROW + g * 16;
    u32x4 yf[8];
#pragma unroll
    for (int s = 0; s < 8; ++s) yf[s] = lds_ld<u32x4>(smem, rd + s * 64);
    auto tile = [&](int t) {
      const u32 ra = S_TAB + kch * TAB_CH + (16 + min(16 * t + c15, 40)) * TAB_ROW + g * 16;
      u32x4 xf[8];
#pragma unroll
      for (int s = 0; s < 8; ++s) xf[s] = lds_ld<u32x4>(smem, ra + s * 64);
      f32x4 a0 = {0, 0, 0, 0}, a1 = {0, 0, 0, 0};
#pragma unroll
      for (int s = 0; s < 4; ++s) { a0 = mfma_h(xf[2 * s], yf[2 * s], a0); a1 = mfma_h(xf[2 * s + 1], yf[2 * s + 1], a1); }
      return a0 + a1;
    };
    const f32x4 va = tile(mh), vb = tile(2);
    sfor<8>([&](auto N) { st2_piece(N); });
    asm volatile("s_waitcnt vmcnt(8)" : "+v"(lnf)::"memory");
    spin_flag(sWS, lnfo, epoch, lnf, true);
    u32x4 ug0 = ld128c(sWS, WS_XN + t8 * 8192 + cp * 32, 0), ug1 = ld128c(sWS, WS_XN + t8 * 8192 + cp * 32 + 16, 0);
    CSTAMP(1);
    bar();
    CSTAMP(2);
    {
      const u32 h01 = pk_f16(f32x2{va[0], va[1]}), h23 = pk_f16(f32x2{va[2], va[3]});
      if (mh == 0) {
        lds_st<u32x2>(smem, kc + (128 * (g >> 1) + 8 * c15 + 4 * (g & 1)) * 2, u32x2{h01, h23});
        const u32 k01 = pk_f16(f32x2{vb[0], vb[1]}), k23 = pk_f16(f32x2{vb[2], vb[3]});
        const u32 t2 = g < 2 ? 512 + 64 * g + c15 : 644, st2 = g < 2 ? 16 : 0;
        lds_st<u16>(smem, kc + ((g == 2 && c15 == 0) ? 640 : t2) * 2, (u16)k01);
        lds_st<u16>(smem, kc + (t2 + st2) * 2, (u16)(k01 >> 16));
        lds_st<u16>(smem, kc + (t2 + 2 * st2) * 2, (u16)k23);
        lds_st<u16>(smem, kc + (t2 + 3 * st2) * 2, (u16)(k23 >> 16));
      } else {
        const u32 t1a = g < 2 ? 256 + 64 * g + 4 * c15 : 384 + 64 * (g - 2) + 2 * c15;
        lds_st<u32>(smem, kc + t1a * 2, h01);
        lds_st<u32>(smem, kc + (t1a + (g < 2 ? 2 : 32)) * 2, h23);
        if (lane == 0) lds_st<u16>(smem, kc + 641 * 2, (u16)0);
      }
    }
    side_sync4(smem, w, epoch + 1);
#pragma unroll
    for (int ch = 0; ch < 2; ++ch) {
      u32 e[9];
#pragma unroll
      for (int i = 0; i < 9; ++i) e[i] = lds_ld<u16>(smem, S_KC + ch * KC_CH + ((i & 1) ? ixp[i >> 1] >> 16 : ixp[i >> 1] & 0xffffu));
      const u32x4 a0 = {e[0] | (e[1] << 16), e[2] | (e[3] << 16), e[4] | (e[5] << 16), e[6] | (e[7] << 16)};
      const u32x4 a1 = {e[1] | (e[2] << 16), e[3] | (e[4] << 16), e[5] | (e[6] << 16), e[7] | (e[8] << 16)};
      lds_st<u32x4>(smem, S_G + ch * G_CH + t8 * 16, a0);
      lds_st<u32x4>(smem, S_G + ch * G_CH + G_PAR + t8 * 16, a1);
    }
    const u32x4 z4 = {0, 0, 0, 0};
    if (t8 < 8) lds_st<u32x4>(smem, S_G + (t8 >> 2) * G_CH + ((t8 >> 1) & 1) * G_PAR + 4096 + (t8 & 1) * 16, z4);
    if (t8 < 60) lds_st<u32x4>(smem, S_U + (t8 / 30) * U_CH + (t8 % 30) * 16, z4);
    asm volatile("s_waitcnt vmcnt(0)" : "+v"(ug0), "+v"(ug1)::"memory");
    lds_st<u32x4>(smem, S_U + 480 + t8 * 16, ug0);
    lds_st<u32x4>(smem, S_U + U_CH + 480 + t8 * 16, ug1);
    CSTAMP(3);
    STAMP(4);
  }

  const int ch0 = 64 * nt2 + 32 * kh + 4 * hh;
  const u32 xto = (64 * mt2 + (t8 >> 3)) * 1024 + nt2 * 128 + (t8 & 7) * 16;
  f32x16 acc[2];
  if (side == 0) {
    bar();
    CSTAMP(4); CDUMP(5, 38);
    STAMP(5);
    const int par = w & 1;
    const u32 ub = lb + S_U + kch * U_CH + 64 - 64 * par + 32 * c15 + 16 * g;
    const u32 gb0 = lb + S_G + kch * G_CH + ((c15 & 1) ? 0 : G_PAR) + 4 * (1015 - (c15 >> 1) - 16 * par + 4 * g - 32 * 31);
    const u32 gb[4] = {gb0, gb0 + 1024, gb0 + 2048, gb0 + 3072};
    u32x4 bq[32];
    u32x2 al[32], ah[32];
    f32x4 y[8];
    auto rdB = [&](auto M, auto T) { constexpr int q = 4 * T - M + 3; bq[q] = a_ds128<128 * q>(ub); };
    auto rdA = [&](auto M, auto H) {
      constexpr int k = (31 - M) >> 3, o = 32 * ((31 - M) & 7);
      if constexpr (H == 0) al[M] = a_ds2x32<o, o + 1>(gb[k]); else ah[M] = a_ds2x32<o + 2, o + 3>(gb[k]);
    };
    sfor<2>([&](auto M) { sfor<8>([&](auto T) { rdB(M, T); }); rdA(M, ic<0>{}); rdA(M, ic<1>{}); });
    sfor<32>([&](auto M) {
      constexpr int m = M, t0 = m >> 2, n = 8 - t0;
      wait_lgkm<(m <= 2 ? 10 : (m == 3 ? 4 : (m == 4 ? 6 : (2 * (31 - m) < 8 ? 2 * (31 - m) : 8))))>();
      sfor<n>([&](auto I) {
        constexpr int i = I, t = t0 + i;
        mfma16_h<m == 0>(y[t], cat4(al[m], ah[m]), bq[4 * t - m + 3]);
        if constexpr (m < 2) {
          rdB(ic<m + 2>{}, I);
          if constexpr (i >= 6) rdA(ic<m + 2>{}, ic<i - 6>{});
        } else if constexpr (m <= 4) {
          if constexpr (i < 4) rdA(ic<2 * m + (i >> 1)>{}, ic<i & 1>{});
        } else if constexpr (m + 5 < 32) {
          if constexpr (i < 2) rdA(ic<m + 5>{}, I);
        }
      });
    });
    asm volatile("s_nop 7\n\ts_nop 1" : "+v"(y[0]), "+v"(y[1]), "+v"(y[2]), "+v"(y[3]), "+v"(y[4]), "+v"(y[5]), "+v"(y[6]), "+v"(y[7]));
    STAMP(6);
    CSTAMP(0);
    auto send = [&](auto PAR) {
      FENCE();
#pragma unroll
      for (int i = 0; i < 4; ++i) lds_st<f32x4>(smem, S_EXC + w * 4096 + i * 1024 + lane * 16, y[4 * (1 - PAR) + i]);
      FENCE();
    };
    if (par) send(ic<1>{}); else send(ic<0>{});
    CSTAMP(1);
    bar();
    CSTAMP(2);
    const float dsk = bf1(q_dsk);
    u32x2 ao[4];
    auto finish = [&](auto PAR) {
      FENCE();
#pragma unroll
      for (int i = 0; i < 4; ++i) {
        const f32x4 o = lds_ld<f32x4>(smem, S_EXC + (w ^ 1) * 4096 + i * 1024 + lane * 16);
        const u32x2 uu = lds_ld<u32x2>(smem, S_U + kch * U_CH + (240 + 256 * (4 * PAR + i) + 16 * c15 + 4 * g) * 2);
        const f16x2 u01 = ash2(uu.x), u23 = ash2(uu.y);
        const float uf[4] = {(float)u01.x, (float)u01.y, (float)u23.x, (float)u23.y};
        float v[4];
#pragma unroll
        for (int e = 0; e < 4; ++e) v[e] = ffma1(uf[e], dsk, fmul1(fadd1(y[4 * PAR + i][e], o[e]), inv_sk));
        ao[i] = u32x2{cvt_bf2(v[0], v[1]), cvt_bf2(v[2], v[3])};
      }
      FENCE();
    };
    if (par) finish(ic<1>{}); else finish(ic<0>{});
    CSTAMP(3);
#pragma unroll
    for (int i = 0; i < 4; ++i) {
      u32x2 o = ao[i];
      o.x = o.x == POISON ? 0xffc0ffc0u : o.x;
      o.y = o.y == POISON ? 0xffc0ffc0u : o.y;
      st64c(rWS, o, WS_A + ((4 * (4 * par + i) + (c15 >> 2)) * 512 + kd) * 128 + ((c15 & 3) * 16 + 4 * g) * 2);
    }
    STAMP(7);
    u32x4 xt0 = ld128(sX, xto, 0), xt1 = ld128(sX, xto + 32 * 1024, 0);
    u32 b2w = ld16(mk_srd(p.b2, DM * 2), t8 < 64 ? (64 * nt2 + t8) * 2 : OOB, 0);
    const u32 pfo = WS_PF + ((32 * nt2 + (lane & 31)) * 4 + (mt2 >> 3)) * 4;
    u32 pfl = ld32c(sWS, pfo, 0);
    CSTAMP(4);
    bar();
    CSTAMP(5);
    const u32 ato = WS_A + (mt2 * 512 + nt2 * 64) * 128 + t8 * 16;
    asm volatile("s_waitcnt vmcnt(0)" : "+v"(xt0), "+v"(xt1), "+v"(b2w), "+v"(pfl)::"memory");
    spin_flag(sWS, pfo, epoch, pfl, true);
    const int row0 = t8 >> 3, sl = t8 & 7;
    {
      const u32 o = S_XT + row0 * 128 + ((sl ^ (row0 & 7)) * 16);
      lds_st<u32x4>(smem, o, xt0);
      lds_st<u32x4>(smem, o + 32 * 128, xt1);
      lds_st<float>(smem, t8 < 64 ? S_B2 + t8 * 4 : S_MISC, bf1(b2w));
    }
    CSTAMP(6);
    bar();
    CSTAMP(7);
    u32x4 at0 = ld128c(sWS, ato, 0), at1 = ld128c(sWS, ato + 4096, 0);
    asm volatile("s_waitcnt vmcnt(0)" : "+v"(at0), "+v"(at1)::"memory");
#pragma unroll 1
    for (int it = 0; it < SPIN_MAX; ++it) {
      const u32 m0 = max(max(at0.x, at0.y), max(at0.z, at0.w)), m1 = max(max(at1.x, at1.y), max(at1.z, at1.w));
      if (!__any(max(m0, m1) == POISON)) break;
      at0 = ld128c(sWS, ato, 0); at1 = ld128c(sWS, ato + 4096, 0);
      asm volatile("s_waitcnt vmcnt(0)" : "+v"(at0), "+v"(at1)::"memory");
    }
    CSTAMP(8);
    {
      const u32 o = S_AT + row0 * 128 + (((sl >> 1) ^ (row0 & 2)) * 32) + (sl & 1) * 16;
      lds_st<u32x4>(smem, o, at0);
      lds_st<u32x4>(smem, o + 32 * 128, at1);
    }
    CSTAMP(9);
    bar();
    CSTAMP(10); CDUMP2(11, 0);
  } else {
    u32x4 hf[22][2];
    const u32 hb = WS_H + ((mt2 * 2 + mh) * NUNIT + kh * 22) * 2048;
    const u32 hvo = lane * 16, hvz = kh ? OOB : hvo;
    auto ldh = [&](auto J, auto S) { hf[J][S] = ld128c(sWS, J == 21 ? hvz : hvo, hb + (J * 2 + S) * 1024); };
    bar();
    CSTAMP(4); CDUMP(5, 48);
    STAMP(5);
    st32c(rWS, epoch, (b == 0 && t8 == 0) ? WS_E : OOB);
    sfor<20>([&](auto N) { st2_piece(ic<8 + N>{}); });
    {
      const u32 hfo = WS_HF + (((mt2 >> 1) * 4 + 2 * (mt2 & 1) + mh) * 16 + c15) * 4;
      u32 hfl = ld32c(sWS, hfo, 0);
      asm volatile("s_waitcnt vmcnt(0)" : "+v"(hfl)::"memory");
      side_sync4(smem, w, epoch + 2);
      spin_flag(sWS, hfo, epoch, hfl, kh ? c15 >= 8 : c15 <= 8);
    }
    STAMP(6);
    sfor<G2_LEAD>([&](auto J) { ldh(J, ic<0>{}); ldh(J, ic<1>{}); });
    STAMP(7);
    u32 wa[2];
#pragma unroll
    for (int n = 0; n < 2; ++n) wa[n] = lb + kh * W2_HALF + (4 * hh + q4) * 128 + (((2 * n + b16) ^ (q4 & 2)) * 32) + 8 * p4;
    u32x2 wl[2][4], wh[2][4];
    auto rd = [&](auto J, auto I, auto H) {
      constexpr int j = J, i = I, off = (j % W2_NS) * 4096 + ((i >> 1) * 16 + H * 8) * 128;
      if constexpr (H == 0) wl[j & 1][i] = a_dstr<off>(wa[i & 1]); else wh[j & 1][i] = a_dstr<off>(wa[i & 1]);
    };
    CSTAMP(0);
    sfor<4>([&](auto I) { rd(ic<0>{}, I, ic<0>{}); rd(ic<0>{}, I, ic<1>{}); });
    sfor<22>([&](auto J) {
      constexpr int j = J;
      constexpr bool last = j == 21, brk = j == 7 || j == 13 || j == 17;
      if constexpr (j == 13) wait_vm<g2_ops_before(13) - g2_ops_before(9)>();
      if constexpr (j == 17) wait_vm<g2_ops_before(17) - g2_ops_before(11)>();
      if constexpr (brk) CSTAMP(j == 7 ? 1 : (j == 13 ? 3 : 5));
      if constexpr (brk) bar();
      if constexpr (brk) CSTAMP(j == 7 ? 2 : (j == 13 ? 4 : 6));
      wait_vm<g2_wait_h(j)>();
      tie(hf[j][0]); tie(hf[j][1]);
      sfor<4>([&](auto I) {
        constexpr int i = I;
        constexpr bool stg = j >= 7 && j <= 10;
        constexpr int ju = stg ? 14 + 2 * (j - 7) + (i >> 1) : 14, pi = i & 1;
        if constexpr (!brk) wait_lgkm<(last ? 6 - 2 * i : 6)>();
        if constexpr (stg && pi == 0) dma_m0(w2_lo + (ju % W2_NS) * 4096);
        mfma32_vv<j == 0 && i < 2>(acc[i & 1], cat4(wl[j & 1][i], wh[j & 1][i]), hf[j][i >> 1]);
        if constexpr ((i & 1) == 0 && j + G2_LEAD <= 21) ldh(ic<j + G2_LEAD>{}, ic<(i >> 1)>{});
        if constexpr (stg) dma_go<pi * 1024>(sW2, ju == 21 ? vw2z[pi] : vw2p[pi], w2_so + ju * 32 * 1024);
        if constexpr (!last) { rd(ic<j + 1>{}, I, ic<0>{}); rd(ic<j + 1>{}, I, ic<1>{}); }
      });
    });
    asm volatile("s_nop 7\n\ts_nop 3" : "+v"(acc[0]), "+v"(acc[1]));
    CSTAMP(7);
    STAMP(8);
    auto send2 = [&](auto KH) {
      FENCE();
#pragma unroll
      for (int q = 0; q < 4; ++q) {
        const f32x16& as = acc[1 - KH];
        lds_st<f32x4>(smem, S_EX + w * 4096 + q * 1024 + lane * 16, f32x4{as[4 * q], as[4 * q + 1], as[4 * q + 2], as[4 * q + 3]});
      }
      FENCE();
    };
    if (kh) send2(ic<1>{}); else send2(ic<0>{});
    CSTAMP(8);
    bar();
    CSTAMP(9);
    const int tl = 32 * mh + c31;
    auto fin2 = [&](auto KH) {
      FENCE();
      const f32x16& ak = acc[KH];
      f32x4 pv[4], bb[4];
      u32x2 av[4], xv[4];
      u32 po[4];
#pragma unroll
      for (int q = 0; q < 4; ++q) {
        const int row = 32 * KH + 8 * q + 4 * hh + q4;
        po[q] = tl * 128 + (((4 * KH + q) ^ (tl & 7)) * 16) + hh * 8;
        pv[q] = lds_ld<f32x4>(smem, S_EX + (w ^ 2) * 4096 + q * 1024 + lane * 16);
        bb[q] = lds_ld<f32x4>(smem, S_B2 + (32 * KH + 8 * q + 4 * hh) * 4);
        av[q] = lds_tr(smem, S_AT + row * 128 + (((2 * mh + b16) ^ (row & 2)) * 32) + 8 * p4);
        xv[q] = lds_ld<u32x2>(smem, S_XT + po[q]);
      }
      FENCE();
      f32x2 o[8];
#pragma unroll
      for (int q = 0; q < 4; ++q) {
        o[2 * q] = f32x2{ak[4 * q], ak[4 * q + 1]} + f32x2{bb[q].x, bb[q].y};
        o[2 * q + 1] = f32x2{ak[4 * q + 2], ak[4 * q + 3]} + f32x2{bb[q].z, bb[q].w};
      }
      FENCE();
#pragma unroll
      for (int q = 0; q < 4; ++q) { o[2 * q] += f32x2{pv[q].x, pv[q].y}; o[2 * q + 1] += f32x2{pv[q].z, pv[q].w}; }
      FENCE();
#pragma unroll
      for (int q = 0; q < 4; ++q) { o[2 * q] += bf2(xv[q].x) + bf2(av[q].x); o[2 * q + 1] += bf2(xv[q].y) + bf2(av[q].y); }
      FENCE();
#pragma unroll
      for (int q = 0; q < 4; ++q) lds_st<u32x2>(smem, S_OT + po[q], u32x2{pk_bf16(o[2 * q]), pk_bf16(o[2 * q + 1])});
      FENCE();
    };
    if (kh) fin2(ic<1>{}); else fin2(ic<0>{});
    CSTAMP(10); CDUMP2(11, 16);
  }
  bar();
  {
    const int row = tid >> 3, sl = tid & 7;
    const u32x4 o = lds_ld<u32x4>(smem, S_OT + row * 128 + ((sl ^ (row & 7)) * 16));
    __builtin_amdgcn_raw_buffer_store_b128(o, rOUT, (64 * mt2 + row) * 1024 + nt2 * 128 + sl * 16, 0, 0);
  }
  STAMP(9);
}

extern "C" void kernel_launch(void* const* d_in, const int* in_sizes, int n_in, void* d_out, int out_size, void* d_ws,
                              size_t ws_size, hipStream_t stream) {
  Params p;
  p.x = (const u16*)d_in[0]; p.lar = (const u16*)d_in[1]; p.aim = (const u16*)d_in[2]; p.cre = (const u16*)d_in[3];
  p.cim = (const u16*)d_in[4]; p.ldt = (const u16*)d_in[5]; p.dsk = (const u16*)d_in[6];
  p.w1 = (const u16*)d_in[8]; p.b1 = (const u16*)d_in[9]; p.w2 = (const u16*)d_in[10]; p.b2 = (const u16*)d_in[11];
  p.out = (u16*)d_out; p.ws = (char*)d_ws;
  s4d_mixer_fused<<<256, 512, 0, stream>>>(p);
}
```
